```python
import torch

def _softmax(x, axis=-1):
    return torch.softmax(x, dim=axis)
def _gelu(x):
    return torch.nn.functional.gelu(x, approximate="tanh")
def _silu(x):
    return torch.nn.functional.silu(x)
def _sigmoid(x):
    return torch.sigmoid(x)
def _layernorm(x, axis=-1, eps=1e-5):
    m = x.mean(dim=axis, keepdim=True)
    v = x.var(dim=axis, keepdim=True, unbiased=False)
    return (x - m) / torch.sqrt(v + eps)
def _scatter_add(tgt, idx, src, axis=0):
    return tgt.clone().index_add_(axis, idx, src)
def _segment_sum(src, seg, n):
    out = torch.zeros((n,) + src.shape[1:], dtype=src.dtype)
    return out.index_add_(0, seg, src)
def _pad(x, pw):
    flat = tuple(v for pair in reversed(pw) for v in pair)
    return torch.nn.functional.pad(x, flat)

kernel_name = 'standalone__mha_block_cached__near_ridge'

def setup_inputs():

    _w = (lambda a: (lambda t: t.to(torch.bfloat16) if t.is_floating_point() else t)(torch.as_tensor(a)))
    B, Sq, D = 8, 304, 512
    H, Sk, Dh = 32, 1024, 128
    return {
        "x": _w((lambda *s: torch.randn(*s, dtype=torch.bfloat16) * 0.8)(B, Sq, D)),
        "k_cache": _w((lambda *s: torch.randn(*s, dtype=torch.bfloat16) * 0.8)(B, H, Sk, Dh)),
        "v_cache": _w((lambda *s: torch.randn(*s, dtype=torch.bfloat16) * 0.05)(B, H, Sk, Dh)),
        "wq": _w((lambda *s: torch.randn(*s, dtype=torch.bfloat16) * 0.8)(D, H * Dh) * (D ** -0.5)),
        "wo": _w((lambda *s: torch.randn(*s, dtype=torch.bfloat16) * 0.05)(H * Dh, D) * ((H * Dh) ** -0.5)),
    }

def reference(x, k_cache, v_cache, wq, wo):
    B, Sq, D = x.shape
    H, Dh = 32, 128
    q = (lambda x, *d: x.permute(*d))((x @ wq).reshape(B, Sq, H, Dh), 0, 2, 1, 3)
    scores = torch.einsum("bhqd,bhkd->bhqk", q, k_cache) * (Dh ** -0.5)
    p = _softmax(scores, axis=-1)
    o = torch.einsum("bhqk,bhkd->bhqd", p, v_cache)
    return (lambda x, *d: x.permute(*d))(o, 0, 2, 1, 3).reshape(B, Sq, H * Dh) @ wo
```

```cpp
#include <hip/hip_runtime.h>
#include <hip/hip_bf16.h>

__global__ __launch_bounds__(256, 1) void mha_block_kernel(const void* x, const void* k, const void* v, const void* wq, const void* wo, void* out, void* ws, void* dbg) {
  __shared__ __attribute__((aligned(16))) char smem[163840];
  const unsigned wid = __builtin_amdgcn_readfirstlane(threadIdx.x >> 6);
  asm volatile(
    "s_load_dwordx16 s[68:83], %0, 0\n\t"
    "s_getreg_b32 s60, hwreg(HW_REG_HW_ID, 4, 2)\n\t"
    "s_lshl2_add_u32 s60, %2, s60\n\t"
    "s_lshr_b32 s60, 0xb34c, s60\n\t"
    "s_and_b32 s48, s60, 2\n\t"
    "L_start_%=:\n\t"
    "s_xor_b32 s40, %2, s48\n\t"
    "v_mbcnt_lo_u32_b32 v249, -1, 0\n\t"
    "s_lshr_b32 s96, %1, 5\n\t"
    "s_getpc_b64 s[6:7]\n\t"
    "s_lshl_b32 s60, s96, 2\n\t"
    "s_add_u32 s60, s60, s40\n\t"
    "s_mul_i32 s60, s60, 2560\n\t"
    "v_lshl_add_u32 v20, v249, 7, s60\n\t"
    "s_mov_b64 exec, 0xfffff\n\t"
    "global_load_dword v21, v20, s[6:7]\n\t"
    "s_mov_b64 exec, -1\n\t"
    "v_mbcnt_hi_u32_b32 v249, -1, v249\n\t"
    "v_bfe_u32 v1, v249, 4, 1\n\t"
    "v_mul_u32_u24 v2, 3, v1\n\t"
    "v_and_b32 v3, 3, v249\n\t"
    "v_xor_b32 v4, v3, v2\n\t"
    "v_lshlrev_b32 v4, 4, v4\n\t"
    "v_lshrrev_b32 v5, 5, v249\n\t"
    "v_lshl_add_u32 v0, v5, 6, v4\n\t"
    "v_bfe_u32 v6, v249, 2, 3\n\t"
    "v_lshl_add_u32 v255, v6, 10, v0\n\t"
    "v_add_u32 v201, 6144, v255\n\t"
    "v_bfe_u32 v7, v249, 2, 2\n\t"
    "v_lshl_add_u32 v7, v1, 4, v7\n\t"
    "v_lshl_add_u32 v248, v7, 13, v0\n\t"
    "v_add_u32 v200, 63488, v248\n\t"
    "s_and_b32 s60, %1, 1\n\t"
    "s_bfe_u32 s61, %1, 0x20003\n\t"
    "s_lshl2_add_u32 s41, s60, s61\n\t"
    "s_bfe_u32 s95, %1, 0x20001\n\t"
    "s_lshl3_add_u32 s42, s95, s96\n\t"
    "s_lshl_b32 s51, s40, 13\n\t"
    "s_lshl1_add_u32 s52, s51, 98304\n\t"
    "s_mov_b32 s23, 0x20000\n\t"
    "s_mov_b32 s27, 0x20000\n\t"
    "s_lshr_b32 s63, s40, 1\n\t"
    "s_mul_i32 s60, s63, 0x110\n\t"
    "s_sub_u32 s60, 0x863, s60\n\t"
    "s_lshl_b32 s61, s95, 2\n\t"
    "s_lshr_b32 s60, s60, s61\n\t"
    "s_and_b32 s60, s60, 15\n\t"
    "s_and_b32 s61, s40, 1\n\t"
    "s_add_u32 s45, s60, s61\n\t"
    "s_sub_u32 s46, 10, s63\n\t"
    "s_lshl_b32 s90, s45, 14\n\t"
    "s_mul_i32 s60, s41, 311296\n\t"
    "s_mul_i32 s61, s63, 163840\n\t"
    "s_add_u32 s60, s60, s61\n\t"
    "s_lshl_b32 s61, s42, 8\n\t"
    "s_lshl_b32 s62, s40, 18\n\t"
    "s_add_u32 s61, s61, s62\n\t"
    "s_sub_u32 s26, 0x400000, s61\n\t"
    "s_sub_u32 s22, 2490368, s60\n\t"
    "s_waitcnt lgkmcnt(0)\n\t"
    "s_add_u32 s20, s68, s60\n\t"
    "s_addc_u32 s21, s69, 0\n\t"
    "s_add_u32 s24, s74, s61\n\t"
    "s_addc_u32 s25, s75, 0\n\t"
    "s_add_u32 m0, s51, 32768\n\t"
    "s_mov_b32 s53, 0\n\t"
    "buffer_load_dwordx4 v248, s[24:27], s53 offen offset:0 lds\n\t"
    "buffer_load_dwordx4 v200, s[24:27], s53 offen offset:2048 lds\n\t"
    "s_add_u32 m0, s51, 36864\n\t"
    "s_mov_b32 s53, 32768\n\t"
    "buffer_load_dwordx4 v248, s[24:27], s53 offen offset:0 lds\n\t"
    "buffer_load_dwordx4 v200, s[24:27], s53 offen offset:2048 lds\n\t"
    "s_add_u32 m0, s52, 0\n\t"
    "s_add_u32 s63, s90, 0\n\t"
    "buffer_load_dwordx4 v255, s[20:23], s63 offen offset:0 lds\n\t"
    "buffer_load_dwordx4 v201, s[20:23], s63 offen offset:2048 lds\n\t"
    "s_add_u32 m0, s52, 896\n\t"
    "s_nop 0\n\t"
    "buffer_load_dwordx4 v255, s[20:23], s63 offen offset:128 lds\n\t"
    "buffer_load_dwordx4 v201, s[20:23], s63 offen offset:2176 lds\n\t"
    "v_lshrrev_b32 v12, 3, v249\n\t"
    "v_bfe_u32 v13, v249, 1, 2\n\t"
    "v_lshl_add_u32 v13, s41, 2, v13\n\t"
    "v_lshl_add_u32 v13, v12, 7, v13\n\t"
    "v_and_b32 v14, 1, v249\n\t"
    "v_lshlrev_b32 v14, 7, v14\n\t"
    "v_lshl_add_u32 v204, v13, 13, v14\n\t"
    "s_and_b32 s62, s40, 1\n\t"
    "s_lshl1_add_u32 s62, s42, s62\n\t"
    "s_mul_i32 s62, s62, 5\n\t"
    "v_and_b32 v13, 7, v249\n\t"
    "v_min_u32 v13, 4, v13\n\t"
    "v_add_u32 v13, s62, v13\n\t"
    "v_and_b32 v14, 1, v13\n\t"
    "v_lshl_add_u32 v14, v12, 1, v14\n\t"
    "v_lshlrev_b32 v14, 7, v14\n\t"
    "v_lshrrev_b32 v13, 1, v13\n\t"
    "v_lshl_add_u32 v205, v13, 10, v14\n\t"
    "s_mov_b32 exec_lo, 255\n\t"
    "s_mov_b32 exec_hi, 0\n\t"
    "buffer_load_dword v176, v204, s[24:27], 0 offen\n\t"
    "buffer_load_dword v177, v205, s[20:23], 0 offen\n\t"
    "s_mov_b64 exec, -1\n\t"
    "s_add_u32 m0, s51, 33792\n\t"
    "s_mov_b32 s53, 128\n\t"
    "buffer_load_dwordx4 v248, s[24:27], s53 offen offset:0 lds\n\t"
    "buffer_load_dwordx4 v200, s[24:27], s53 offen offset:2048 lds\n\t"
    "s_add_u32 m0, s51, 37888\n\t"
    "s_mov_b32 s53, 32896\n\t"
    "buffer_load_dwordx4 v248, s[24:27], s53 offen offset:0 lds\n\t"
    "buffer_load_dwordx4 v200, s[24:27], s53 offen offset:2048 lds\n\t"
    "s_mov_b32 exec_lo, 65280\n\t"
    "s_mov_b32 exec_hi, 0\n\t"
    "buffer_load_dword v178, v204, s[24:27], 0 offen\n\t"
    "buffer_load_dword v179, v205, s[20:23], 0 offen\n\t"
    "s_mov_b64 exec, -1\n\t"
    "v_lshl_add_u32 v8, v1, 1, v5\n\t"
    "v_bfe_u32 v9, v249, 2, 1\n\t"
    "v_mul_u32_u24 v9, 3, v9\n\t"
    "v_xor_b32 v9, v9, v8\n\t"
    "v_lshlrev_b32 v9, 4, v9\n\t"
    "v_and_b32 v10, 7, v249\n\t"
    "v_lshl_add_u32 v9, v10, 6, v9\n\t"
    "v_bfe_u32 v10, v249, 3, 1\n\t"
    "v_lshl_add_u32 v252, v10, 11, v9\n\t"
    "v_add_u32 v246, s52, v252\n\t"
    "v_lshlrev_b32 v250, 4, v8\n\t"
    "v_bfe_u32 v10, v249, 1, 1\n\t"
    "v_xor_b32 v10, v10, v2\n\t"
    "v_lshlrev_b32 v10, 4, v10\n\t"
    "v_and_b32 v11, 1, v249\n\t"
    "v_lshl_add_u32 v10, v11, 3, v10\n\t"
    "v_lshl_add_u32 v10, v6, 6, v10\n\t"
    "v_lshl_add_u32 v244, v5, 11, v10\n\t"
    "v_xor_b32 v245, 32, v244\n\t"
    "v_add_u32 v253, 0x10000, v244\n\t"
    "v_add_u32 v254, 0x10000, v245\n\t"
    "v_lshl_add_u32 v247, v6, 8, v0\n\t"
    "s_add_u32 s61, s45, 2\n\t"
    "s_sub_u32 s62, s61, s46\n\t"
    "s_cselect_b32 s61, s61, s62\n\t"
    "s_lshl_b32 s91, s61, 14\n\t"
    "s_add_u32 s61, s45, 4\n\t"
    "s_sub_u32 s62, s61, s46\n\t"
    "s_cselect_b32 s61, s61, s62\n\t"
    "s_lshl_b32 s92, s61, 14\n\t"
    "s_add_u32 s61, s45, 6\n\t"
    "s_sub_u32 s62, s61, s46\n\t"
    "s_cselect_b32 s61, s61, s62\n\t"
    "s_lshl_b32 s93, s61, 14\n\t"
    "s_add_u32 s61, s45, 8\n\t"
    "s_sub_u32 s62, s61, s46\n\t"
    "s_cselect_b32 s61, s61, s62\n\t"
    "s_lshl_b32 s94, s61, 14\n\t"
    "s_add_u32 m0, s52, 4096\n\t"
    "s_add_u32 s63, s91, 0\n\t"
    "buffer_load_dwordx4 v255, s[20:23], s63 offen offset:0 lds\n\t"
    "buffer_load_dwordx4 v201, s[20:23], s63 offen offset:2048 lds\n\t"
    "s_add_u32 m0, s52, 4992\n\t"
    "s_nop 0\n\t"
    "buffer_load_dwordx4 v255, s[20:23], s63 offen offset:128 lds\n\t"
    "buffer_load_dwordx4 v201, s[20:23], s63 offen offset:2176 lds\n\t"
    "s_lshl_b32 s60, %2, 2\n\t"
    "s_add_u32 s60, s60, 65536\n\t"
    "v_mov_b32 v12, s60\n\t"
    "v_mov_b32 v13, s48\n\t"
    "ds_write_b32 v12, v13\n\t"
    "v_mov_b32 v184, 65536\n\t"
    "s_cmp_eq_u32 s40, 3\n\t"
    "s_cbranch_scc1 L_w3_%=\n\t"
    "s_waitcnt vmcnt(16) lgkmcnt(0)\n\t"
    "s_barrier\n\t"
    "ds_read_b128 v[184:187], v184\n\t"
    "ds_read_b64_tr_b16 a[0:1], v244 offset:32768\n\t"
    "ds_read_b64_tr_b16 a[2:3], v244 offset:36864\n\t"
    "ds_read_b64_tr_b16 a[4:5], v244 offset:40960\n\t"
    "ds_read_b64_tr_b16 a[6:7], v244 offset:45056\n\t"
    "ds_read_b64_tr_b16 a[8:9], v244 offset:49152\n\t"
    "ds_read_b64_tr_b16 a[10:11], v244 offset:53248\n\t"
    "ds_read_b64_tr_b16 a[12:13], v244 offset:57344\n\t"
    "ds_read_b64_tr_b16 a[14:15], v244 offset:61440\n\t"
    "ds_read_b64_tr_b16 a[28:29], v245 offset:57344\n\t"
    "ds_read_b64_tr_b16 a[30:31], v245 offset:61440\n\t"
    "ds_read_b64_tr_b16 a[24:25], v245 offset:49152\n\t"
    "ds_read_b64_tr_b16 a[26:27], v245 offset:53248\n\t"
    "ds_read_b64_tr_b16 a[20:21], v245 offset:40960\n\t"
    "ds_read_b64_tr_b16 a[22:23], v245 offset:45056\n\t"
    "ds_read_b64_tr_b16 a[16:17], v245 offset:32768\n\t"
    "ds_read_b64_tr_b16 a[18:19], v245 offset:36864\n\t"
    "s_waitcnt vmcnt(12)\n\t"
    "ds_read_b128 v[212:215], v246 offset:0\n\t"
    "ds_read_b128 v[216:219], v246 offset:512\n\t"
    "ds_read_b128 v[220:223], v246 offset:1024\n\t"
    "ds_read_b128 v[224:227], v246 offset:1536\n\t"
    "s_waitcnt lgkmcnt(0)\n\t"
    "v_mfma_f32_16x16x32_bf16 v[0:3], a[0:3], v[212:215], 0\n\t"
    "ds_read_b64_tr_b16 a[32:33], v244 offset:33280\n\t"
    "ds_read_b64_tr_b16 a[34:35], v244 offset:37376\n\t"
    "v_mfma_f32_16x16x32_bf16 v[0:3], a[4:7], v[216:219], v[0:3]\n\t"
    "ds_read_b64_tr_b16 a[36:37], v244 offset:41472\n\t"
    "ds_read_b64_tr_b16 a[38:39], v244 offset:45568\n\t"
    "v_mfma_f32_16x16x32_bf16 v[0:3], a[8:11], v[220:223], v[0:3]\n\t"
    "s_waitcnt lgkmcnt(8)\n\t"
    "ds_read_b64_tr_b16 a[40:41], v244 offset:49664\n\t"
    "ds_read_b64_tr_b16 a[42:43], v244 offset:53760\n\t"
    "v_mfma_f32_16x16x32_bf16 v[0:3], a[12:15], v[224:227], v[0:3]\n\t"
    "ds_read_b64_tr_b16 a[44:45], v244 offset:57856\n\t"
    "ds_read_b64_tr_b16 a[46:47], v244 offset:61952\n\t"
    "v_mfma_f32_16x16x32_bf16 v[4:7], a[28:31], v[224:227], 0\n\t"
    "ds_read_b64_tr_b16 a[60:61], v245 offset:57856\n\t"
    "ds_read_b64_tr_b16 a[62:63], v245 offset:61952\n\t"
    "v_mfma_f32_16x16x32_bf16 v[4:7], a[24:27], v[220:223], v[4:7]\n\t"
    "ds_read_b64_tr_b16 a[56:57], v245 offset:49664\n\t"
    "ds_read_b64_tr_b16 a[58:59], v245 offset:53760\n\t"
    "v_mfma_f32_16x16x32_bf16 v[4:7], a[20:23], v[216:219], v[4:7]\n\t"
    "s_waitcnt lgkmcnt(4)\n\t"
    "ds_read_b64_tr_b16 a[52:53], v245 offset:41472\n\t"
    "ds_read_b64_tr_b16 a[54:55], v245 offset:45568\n\t"
    "v_mfma_f32_16x16x32_bf16 v[4:7], a[16:19], v[212:215], v[4:7]\n\t"
    "ds_read_b64_tr_b16 a[48:49], v245 offset:33280\n\t"
    "ds_read_b64_tr_b16 a[50:51], v245 offset:37376\n\t"
    "v_mfma_f32_16x16x32_bf16 v[8:11], a[32:35], v[212:215], 0\n\t"
    "s_waitcnt vmcnt(6)\n\t"
    "s_barrier\n\t"
    "ds_read_b64_tr_b16 a[64:65], v244 offset:33792\n\t"
    "ds_read_b64_tr_b16 a[66:67], v244 offset:37888\n\t"
    "v_mfma_f32_16x16x32_bf16 v[8:11], a[36:39], v[216:219], v[8:11]\n\t"
    "ds_read_b64_tr_b16 a[68:69], v244 offset:41984\n\t"
    "ds_read_b64_tr_b16 a[70:71], v244 offset:46080\n\t"
    "v_mfma_f32_16x16x32_bf16 v[8:11], a[40:43], v[220:223], v[8:11]\n\t"
    "s_waitcnt lgkmcnt(4)\n\t"
    "ds_read_b64_tr_b16 a[72:73], v244 offset:50176\n\t"
    "ds_read_b64_tr_b16 a[74:75], v244 offset:54272\n\t"
    "v_mfma_f32_16x16x32_bf16 v[8:11], a[44:47], v[224:227], v[8:11]\n\t"
    "s_add_u32 s63, s92, 0\n\t"
    "ds_read_b64_tr_b16 a[76:77], v244 offset:58368\n\t"
    "ds_read_b64_tr_b16 a[78:79], v244 offset:62464\n\t"
    "v_mfma_f32_16x16x32_bf16 v[12:15], a[60:63], v[224:227], 0\n\t"
    "s_add_u32 m0, s52, 8192\n\t"
    "ds_read_b64_tr_b16 a[92:93], v245 offset:58368\n\t"
    "ds_read_b64_tr_b16 a[94:95], v245 offset:62464\n\t"
    "v_mfma_f32_16x16x32_bf16 v[12:15], a[56:59], v[220:223], v[12:15]\n\t"
    "buffer_load_dwordx4 v255, s[20:23], s63 offen offset:0 lds\n\t"
    "buffer_load_dwordx4 v201, s[20:23], s63 offen offset:2048 lds\n\t"
    "ds_read_b64_tr_b16 a[88:89], v245 offset:50176\n\t"
    "ds_read_b64_tr_b16 a[90:91], v245 offset:54272\n\t"
    "v_mfma_f32_16x16x32_bf16 v[12:15], a[52:55], v[216:219], v[12:15]\n\t"
    "s_waitcnt lgkmcnt(4)\n\t"
    "ds_read_b64_tr_b16 a[84:85], v245 offset:41984\n\t"
    "ds_read_b64_tr_b16 a[86:87], v245 offset:46080\n\t"
    "v_mfma_f32_16x16x32_bf16 v[12:15], a[48:51], v[212:215], v[12:15]\n\t"
    "ds_read_b64_tr_b16 a[80:81], v245 offset:33792\n\t"
    "ds_read_b64_tr_b16 a[82:83], v245 offset:37888\n\t"
    "v_mfma_f32_16x16x32_bf16 v[16:19], a[64:67], v[212:215], 0\n\t"
    "ds_read_b64_tr_b16 a[96:97], v244 offset:34304\n\t"
    "ds_read_b64_tr_b16 a[98:99], v244 offset:38400\n\t"
    "v_mfma_f32_16x16x32_bf16 v[16:19], a[68:71], v[216:219], v[16:19]\n\t"
    "ds_read_b64_tr_b16 a[100:101], v244 offset:42496\n\t"
    "ds_read_b64_tr_b16 a[102:103], v244 offset:46592\n\t"
    "v_mfma_f32_16x16x32_bf16 v[16:19], a[72:75], v[220:223], v[16:19]\n\t"
    "s_waitcnt lgkmcnt(4)\n\t"
    "ds_read_b64_tr_b16 a[104:105], v244 offset:50688\n\t"
    "ds_read_b64_tr_b16 a[106:107], v244 offset:54784\n\t"
    "v_mfma_f32_16x16x32_bf16 v[16:19], a[76:79], v[224:227], v[16:19]\n\t"
    "ds_read_b64_tr_b16 a[108:109], v244 offset:58880\n\t"
    "ds_read_b64_tr_b16 a[110:111], v244 offset:62976\n\t"
    "v_mfma_f32_16x16x32_bf16 v[20:23], a[92:95], v[224:227], 0\n\t"
    "s_add_u32 m0, s52, 9088\n\t"
    "ds_read_b64_tr_b16 a[124:125], v245 offset:58880\n\t"
    "ds_read_b64_tr_b16 a[126:127], v245 offset:62976\n\t"
    "v_mfma_f32_16x16x32_bf16 v[20:23], a[88:91], v[220:223], v[20:23]\n\t"
    "buffer_load_dwordx4 v255, s[20:23], s63 offen offset:128 lds\n\t"
    "buffer_load_dwordx4 v201, s[20:23], s63 offen offset:2176 lds\n\t"
    "ds_read_b64_tr_b16 a[120:121], v245 offset:50688\n\t"
    "ds_read_b64_tr_b16 a[122:123], v245 offset:54784\n\t"
    "v_mfma_f32_16x16x32_bf16 v[20:23], a[84:87], v[216:219], v[20:23]\n\t"
    "s_waitcnt lgkmcnt(4)\n\t"
    "ds_read_b64_tr_b16 a[116:117], v245 offset:42496\n\t"
    "ds_read_b64_tr_b16 a[118:119], v245 offset:46592\n\t"
    "v_mfma_f32_16x16x32_bf16 v[20:23], a[80:83], v[212:215], v[20:23]\n\t"
    "s_waitcnt vmcnt(4)\n\t"
    "ds_read_b64_tr_b16 a[112:113], v245 offset:34304\n\t"
    "ds_read_b64_tr_b16 a[114:115], v245 offset:38400\n\t"
    "v_mfma_f32_16x16x32_bf16 v[24:27], a[96:99], v[212:215], 0\n\t"
    "ds_read_b128 v[228:231], v246 offset:4096\n\t"
    "ds_read_b128 v[232:235], v246 offset:4608\n\t"
    "v_mfma_f32_16x16x32_bf16 v[24:27], a[100:103], v[216:219], v[24:27]\n\t"
    "ds_read_b128 v[236:239], v246 offset:5120\n\t"
    "ds_read_b128 v[240:243], v246 offset:5632\n\t"
    "v_mfma_f32_16x16x32_bf16 v[24:27], a[104:107], v[220:223], v[24:27]\n\t"
    "s_waitcnt lgkmcnt(4)\n\t"
    "v_add3_u32 v184, v184, v185, v186\n\t"
    "v_add_u32 v184, v184, v187\n\t"
    "v_mfma_f32_16x16x32_bf16 v[24:27], a[108:111], v[224:227], v[24:27]\n\t"
    "s_add_u32 s63, s93, 0\n\t"
    "v_and_b32 v184, 6, v184\n\t"
    "v_cmp_ne_u32 vcc, 0, v184\n\t"
    "v_mfma_f32_16x16x32_bf16 v[28:31], a[124:127], v[224:227], 0\n\t"
    "s_add_u32 m0, s52, 12288\n\t"
    "s_cbranch_vccnz L_again_%=\n\t"
    "v_mfma_f32_16x16x32_bf16 v[28:31], a[120:123], v[220:223], v[28:31]\n\t"
    "buffer_load_dwordx4 v255, s[20:23], s63 offen offset:0 lds\n\t"
    "buffer_load_dwordx4 v201, s[20:23], s63 offen offset:2048 lds\n\t"
    "v_mfma_f32_16x16x32_bf16 v[28:31], a[116:119], v[216:219], v[28:31]\n\t"
    "s_nop 0\n\t"
    "s_add_u32 m0, s52, 13184\n\t"
    "v_mfma_f32_16x16x32_bf16 v[28:31], a[112:115], v[212:215], v[28:31]\n\t"
    "buffer_load_dwordx4 v255, s[20:23], s63 offen offset:128 lds\n\t"
    "buffer_load_dwordx4 v201, s[20:23], s63 offen offset:2176 lds\n\t"
    "s_waitcnt lgkmcnt(0)\n\t"
    "v_mfma_f32_16x16x32_bf16 v[32:35], a[0:3], v[228:231], 0\n\t"
    "s_mov_b32 s53, 1048576\n\t"
    "v_mfma_f32_16x16x32_bf16 v[32:35], a[4:7], v[232:235], v[32:35]\n\t"
    "s_add_u32 m0, s51, 0\n\t"
    "v_mfma_f32_16x16x32_bf16 v[32:35], a[8:11], v[236:239], v[32:35]\n\t"
    "buffer_load_dwordx4 v248, s[24:27], s53 offen offset:0 lds\n\t"
    "v_mfma_f32_16x16x32_bf16 v[32:35], a[12:15], v[240:243], v[32:35]\n\t"
    "buffer_load_dwordx4 v200, s[24:27], s53 offen offset:2048 lds\n\t"
    "v_mfma_f32_16x16x32_bf16 v[36:39], a[28:31], v[240:243], 0\n\t"
    "s_mov_b32 s53, 1081344\n\t"
    "v_mfma_f32_16x16x32_bf16 v[36:39], a[24:27], v[236:239], v[36:39]\n\t"
    "s_add_u32 m0, s51, 4096\n\t"
    "v_mfma_f32_16x16x32_bf16 v[36:39], a[20:23], v[232:235], v[36:39]\n\t"
    "buffer_load_dwordx4 v248, s[24:27], s53 offen offset:0 lds\n\t"
    "v_mfma_f32_16x16x32_bf16 v[36:39], a[16:19], v[228:231], v[36:39]\n\t"
    "buffer_load_dwordx4 v200, s[24:27], s53 offen offset:2048 lds\n\t"
    "v_mfma_f32_16x16x32_bf16 v[40:43], a[32:35], v[228:231], 0\n\t"
    "s_add_u32 s63, s94, 0\n\t"
    "v_mfma_f32_16x16x32_bf16 v[40:43], a[36:39], v[232:235], v[40:43]\n\t"
    "s_add_u32 m0, s52, 0\n\t"
    "v_mfma_f32_16x16x32_bf16 v[40:43], a[40:43], v[236:239], v[40:43]\n\t"
    "buffer_load_dwordx4 v255, s[20:23], s63 offen offset:0 lds\n\t"
    "v_mfma_f32_16x16x32_bf16 v[40:43], a[44:47], v[240:243], v[40:43]\n\t"
    "buffer_load_dwordx4 v201, s[20:23], s63 offen offset:2048 lds\n\t"
    "v_mfma_f32_16x16x32_bf16 v[44:47], a[60:63], v[240:243], 0\n\t"
    "v_mfma_f32_16x16x32_bf16 v[44:47], a[56:59], v[236:239], v[44:47]\n\t"
    "v_mfma_f32_16x16x32_bf16 v[44:47], a[52:55], v[232:235], v[44:47]\n\t"
    "s_waitcnt vmcnt(10)\n\t"
    "ds_read_b128 v[212:215], v246 offset:8192\n\t"
    "v_mfma_f32_16x16x32_bf16 v[44:47], a[48:51], v[228:231], v[44:47]\n\t"
    "ds_read_b128 v[216:219], v246 offset:8704\n\t"
    "v_mfma_f32_16x16x32_bf16 v[48:51], a[64:67], v[228:231], 0\n\t"
    "ds_read_b128 v[220:223], v246 offset:9216\n\t"
    "v_mfma_f32_16x16x32_bf16 v[48:51], a[68:71], v[232:235], v[48:51]\n\t"
    "ds_read_b128 v[224:227], v246 offset:9728\n\t"
    "v_mfma_f32_16x16x32_bf16 v[48:51], a[72:75], v[236:239], v[48:51]\n\t"
    "v_mfma_f32_16x16x32_bf16 v[48:51], a[76:79], v[240:243], v[48:51]\n\t"
    "s_add_u32 m0, s52, 896\n\t"
    "v_mfma_f32_16x16x32_bf16 v[52:55], a[92:95], v[240:243], 0\n\t"
    "buffer_load_dwordx4 v255, s[20:23], s63 offen offset:128 lds\n\t"
    "v_mfma_f32_16x16x32_bf16 v[52:55], a[88:91], v[236:239], v[52:55]\n\t"
    "buffer_load_dwordx4 v201, s[20:23], s63 offen offset:2176 lds\n\t"
    "v_mfma_f32_16x16x32_bf16 v[52:55], a[84:87], v[232:235], v[52:55]\n\t"
    "v_mfma_f32_16x16x32_bf16 v[52:55], a[80:83], v[228:231], v[52:55]\n\t"
    "v_mfma_f32_16x16x32_bf16 v[56:59], a[96:99], v[228:231], 0\n\t"
    "v_mfma_f32_16x16x32_bf16 v[56:59], a[100:103], v[232:235], v[56:59]\n\t"
    "v_mfma_f32_16x16x32_bf16 v[56:59], a[104:107], v[236:239], v[56:59]\n\t"
    "v_mfma_f32_16x16x32_bf16 v[56:59], a[108:111], v[240:243], v[56:59]\n\t"
    "v_mfma_f32_16x16x32_bf16 v[60:63], a[124:127], v[240:243], 0\n\t"
    "v_mfma_f32_16x16x32_bf16 v[60:63], a[120:123], v[236:239], v[60:63]\n\t"
    "v_mfma_f32_16x16x32_bf16 v[60:63], a[116:119], v[232:235], v[60:63]\n\t"
    "s_waitcnt lgkmcnt(0)\n\t"
    "v_mfma_f32_16x16x32_bf16 v[60:63], a[112:115], v[228:231], v[60:63]\n\t"
    "v_mfma_f32_16x16x32_bf16 v[64:67], a[0:3], v[212:215], 0\n\t"
    "s_mov_b32 s53, 1048704\n\t"
    "v_mfma_f32_16x16x32_bf16 v[64:67], a[4:7], v[216:219], v[64:67]\n\t"
    "s_add_u32 m0, s51, 1024\n\t"
    "v_mfma_f32_16x16x32_bf16 v[64:67], a[8:11], v[220:223], v[64:67]\n\t"
    "buffer_load_dwordx4 v248, s[24:27], s53 offen offset:0 lds\n\t"
    "v_mfma_f32_16x16x32_bf16 v[64:67], a[12:15], v[224:227], v[64:67]\n\t"
    "buffer_load_dwordx4 v200, s[24:27], s53 offen offset:2048 lds\n\t"
    "v_mfma_f32_16x16x32_bf16 v[68:71], a[28:31], v[224:227], 0\n\t"
    "s_mov_b32 s53, 1081472\n\t"
    "v_mfma_f32_16x16x32_bf16 v[68:71], a[24:27], v[220:223], v[68:71]\n\t"
    "s_add_u32 m0, s51, 5120\n\t"
    "v_mfma_f32_16x16x32_bf16 v[68:71], a[20:23], v[216:219], v[68:71]\n\t"
    "buffer_load_dwordx4 v248, s[24:27], s53 offen offset:0 lds\n\t"
    "v_mfma_f32_16x16x32_bf16 v[68:71], a[16:19], v[212:215], v[68:71]\n\t"
    "buffer_load_dwordx4 v200, s[24:27], s53 offen offset:2048 lds\n\t"
    "v_mfma_f32_16x16x32_bf16 v[72:75], a[32:35], v[212:215], 0\n\t"
    "s_add_u32 s63, s90, 256\n\t"
    "v_mfma_f32_16x16x32_bf16 v[72:75], a[36:39], v[216:219], v[72:75]\n\t"
    "s_add_u32 m0, s52, 4096\n\t"
    "v_mfma_f32_16x16x32_bf16 v[72:75], a[40:43], v[220:223], v[72:75]\n\t"
    "buffer_load_dwordx4 v255, s[20:23], s63 offen offset:0 lds\n\t"
    "v_mfma_f32_16x16x32_bf16 v[72:75], a[44:47], v[224:227], v[72:75]\n\t"
    "buffer_load_dwordx4 v201, s[20:23], s63 offen offset:2048 lds\n\t"
    "v_mfma_f32_16x16x32_bf16 v[76:79], a[60:63], v[224:227], 0\n\t"
    "s_mov_b32 s31, 0x20000\n\t"
    "v_mfma_f32_16x16x32_bf16 v[76:79], a[56:59], v[220:223], v[76:79]\n\t"
    "s_mov_b32 s19, 0x20000\n\t"
    "v_mfma_f32_16x16x32_bf16 v[76:79], a[52:55], v[216:219], v[76:79]\n\t"
    "s_waitcnt vmcnt(14)\n\t"
    "ds_read_b128 v[228:231], v246 offset:12288\n\t"
    "v_mfma_f32_16x16x32_bf16 v[76:79], a[48:51], v[212:215], v[76:79]\n\t"
    "s_mov_b32 s39, 0x20000\n\t"
    "ds_read_b128 v[232:235], v246 offset:12800\n\t"
    "s_mov_b32 exec_lo, 16711680\n\t"
    "s_mov_b32 exec_hi, 0\n\t"
    "buffer_load_dword v176, v204, s[24:27], 0 offen\n\t"
    "buffer_load_dword v177, v205, s[20:23], 0 offen\n\t"
    "s_mov_b64 exec, -1\n\t"
    "v_mfma_f32_16x16x32_bf16 v[80:83], a[64:67], v[212:215], 0\n\t"
    "s_mov_b32 s15, 0x20000\n\t"
    "ds_read_b128 v[236:239], v246 offset:13312\n\t"
    "v_mfma_f32_16x16x32_bf16 v[80:83], a[68:71], v[216:219], v[80:83]\n\t"
    "s_lshl_b32 s97, s40, 12\n\t"
    "ds_read_b128 v[240:243], v246 offset:13824\n\t"
    "v_mfma_f32_16x16x32_bf16 v[80:83], a[72:75], v[220:223], v[80:83]\n\t"
    "s_lshl_b32 s98, s40, 14\n\t"
    "v_mfma_f32_16x16x32_bf16 v[80:83], a[76:79], v[224:227], v[80:83]\n\t"
    "s_add_u32 m0, s52, 4992\n\t"
    "v_mfma_f32_16x16x32_bf16 v[84:87], a[92:95], v[224:227], 0\n\t"
    "buffer_load_dwordx4 v255, s[20:23], s63 offen offset:128 lds\n\t"
    "v_mfma_f32_16x16x32_bf16 v[84:87], a[88:91], v[220:223], v[84:87]\n\t"
    "buffer_load_dwordx4 v201, s[20:23], s63 offen offset:2176 lds\n\t"
    "v_mfma_f32_16x16x32_bf16 v[84:87], a[84:87], v[216:219], v[84:87]\n\t"
    "s_lshl_b32 s60, s41, 5\n\t"
    "v_mfma_f32_16x16x32_bf16 v[84:87], a[80:83], v[212:215], v[84:87]\n\t"
    "s_add_u32 s60, s60, s42\n\t"
    "v_mfma_f32_16x16x32_bf16 v[88:91], a[96:99], v[212:215], 0\n\t"
    "s_lshl_b32 s60, s60, 18\n\t"
    "v_mfma_f32_16x16x32_bf16 v[88:91], a[100:103], v[216:219], v[88:91]\n\t"
    "s_add_u32 s28, s70, s60\n\t"
    "s_addc_u32 s29, s71, 0\n\t"
    "v_mfma_f32_16x16x32_bf16 v[88:91], a[104:107], v[220:223], v[88:91]\n\t"
    "s_mov_b32 s30, 0x40000\n\t"
    "v_mfma_f32_16x16x32_bf16 v[88:91], a[108:111], v[224:227], v[88:91]\n\t"
    "s_add_u32 s16, s72, s60\n\t"
    "s_addc_u32 s17, s73, 0\n\t"
    "v_mfma_f32_16x16x32_bf16 v[92:95], a[124:127], v[224:227], 0\n\t"
    "s_mov_b32 s18, 0x40000\n\t"
    "v_mfma_f32_16x16x32_bf16 v[92:95], a[120:123], v[220:223], v[92:95]\n\t"
    "s_add_u32 s12, s80, 50331648\n\t"
    "s_addc_u32 s13, s81, 0\n\t"
    "v_mfma_f32_16x16x32_bf16 v[92:95], a[116:119], v[216:219], v[92:95]\n\t"
    "s_waitcnt vmcnt(14) lgkmcnt(0)\n\t"
    "v_mfma_f32_16x16x32_bf16 v[92:95], a[112:115], v[212:215], v[92:95]\n\t"
    "s_barrier\n\t"
    "v_mfma_f32_16x16x32_bf16 v[96:99], a[0:3], v[228:231], 0\n\t"
    "s_add_u32 s63, s91, 256\n\t"
    "ds_read_b64_tr_b16 a[128:129], v244 offset:0\n\t"
    "ds_read_b64_tr_b16 a[130:131], v244 offset:4096\n\t"
    "v_mfma_f32_16x16x32_bf16 v[96:99], a[4:7], v[232:235], v[96:99]\n\t"
    "s_add_u32 m0, s52, 8192\n\t"
    "ds_read_b64_tr_b16 a[132:133], v244 offset:8192\n\t"
    "v_mfma_f32_16x16x32_bf16 v[96:99], a[8:11], v[236:239], v[96:99]\n\t"
    "buffer_load_dwordx4 v255, s[20:23], s63 offen offset:0 lds\n\t"
    "v_mfma_f32_16x16x32_bf16 v[96:99], a[12:15], v[240:243], v[96:99]\n\t"
    "buffer_load_dwordx4 v201, s[20:23], s63 offen offset:2048 lds\n\t"
    "v_mfma_f32_16x16x32_bf16 v[100:103], a[28:31], v[240:243], 0\n\t"
    "s_mov_b32 s14, 2097152\n\t"
    "ds_read_b64_tr_b16 a[134:135], v244 offset:12288\n\t"
    "ds_read_b64_tr_b16 a[136:137], v244 offset:16384\n\t"
    "v_mfma_f32_16x16x32_bf16 v[100:103], a[24:27], v[236:239], v[100:103]\n\t"
    "s_add_u32 m0, s52, 9088\n\t"
    "ds_read_b64_tr_b16 a[138:139], v244 offset:20480\n\t"
    "v_mfma_f32_16x16x32_bf16 v[100:103], a[20:23], v[232:235], v[100:103]\n\t"
    "buffer_load_dwordx4 v255, s[20:23], s63 offen offset:128 lds\n\t"
    "v_mfma_f32_16x16x32_bf16 v[100:103], a[16:19], v[228:231], v[100:103]\n\t"
    "buffer_load_dwordx4 v201, s[20:23], s63 offen offset:2176 lds\n\t"
    "v_mfma_f32_16x16x32_bf16 v[104:107], a[32:35], v[228:231], 0\n\t"
    "s_add_u32 s63, s92, 256\n\t"
    "ds_read_b64_tr_b16 a[140:141], v244 offset:24576\n\t"
    "ds_read_b64_tr_b16 a[142:143], v244 offset:28672\n\t"
    "v_mfma_f32_16x16x32_bf16 v[104:107], a[36:39], v[232:235], v[104:107]\n\t"
    "s_add_u32 m0, s52, 12288\n\t"
    "ds_read_b64_tr_b16 a[156:157], v245 offset:24576\n\t"
    "v_mfma_f32_16x16x32_bf16 v[104:107], a[40:43], v[236:239], v[104:107]\n\t"
    "buffer_load_dwordx4 v255, s[20:23], s63 offen offset:0 lds\n\t"
    "v_mfma_f32_16x16x32_bf16 v[104:107], a[44:47], v[240:243], v[104:107]\n\t"
    "buffer_load_dwordx4 v201, s[20:23], s63 offen offset:2048 lds\n\t"
    "v_mfma_f32_16x16x32_bf16 v[108:111], a[60:63], v[240:243], 0\n\t"
    "s_lshl_b32 s64, s41, 2\n\t"
    "ds_read_b64_tr_b16 a[158:159], v245 offset:28672\n\t"
    "ds_read_b64_tr_b16 a[152:153], v245 offset:16384\n\t"
    "v_mfma_f32_16x16x32_bf16 v[108:111], a[56:59], v[236:239], v[108:111]\n\t"
    "s_add_u32 s64, s64, s95\n\t"
    "ds_read_b64_tr_b16 a[154:155], v245 offset:20480\n\t"
    "v_mfma_f32_16x16x32_bf16 v[108:111], a[52:55], v[232:235], v[108:111]\n\t"
    "s_waitcnt vmcnt(16)\n\t"
    "ds_read_b128 v[212:215], v246 offset:0\n\t"
    "ds_read_b64_tr_b16 a[148:149], v245 offset:8192\n\t"
    "v_mfma_f32_16x16x32_bf16 v[108:111], a[48:51], v[228:231], v[108:111]\n\t"
    "s_mul_i32 s64, s64, 4096\n\t"
    "ds_read_b128 v[216:219], v246 offset:512\n\t"
    "v_mfma_f32_16x16x32_bf16 v[112:115], a[64:67], v[228:231], 0\n\t"
    "s_lshl_b32 s65, s41, 3\n\t"
    "ds_read_b128 v[220:223], v246 offset:1024\n\t"
    "ds_read_b64_tr_b16 a[150:151], v245 offset:12288\n\t"
    "v_mfma_f32_16x16x32_bf16 v[112:115], a[68:71], v[232:235], v[112:115]\n\t"
    "s_add_u32 s65, s65, s96\n\t"
    "ds_read_b128 v[224:227], v246 offset:1536\n\t"
    "v_mfma_f32_16x16x32_bf16 v[112:115], a[72:75], v[236:239], v[112:115]\n\t"
    "s_lshl_b32 s65, s65, 2\n\t"
    "ds_read_b64_tr_b16 a[144:145], v245 offset:0\n\t"
    "ds_read_b64_tr_b16 a[146:147], v245 offset:4096\n\t"
    "v_mfma_f32_16x16x32_bf16 v[112:115], a[76:79], v[240:243], v[112:115]\n\t"
    "s_add_u32 m0, s52, 13184\n\t"
    "ds_read_b64_tr_b16 a[160:161], v244 offset:512\n\t"
    "v_mfma_f32_16x16x32_bf16 v[116:119], a[92:95], v[240:243], 0\n\t"
    "buffer_load_dwordx4 v255, s[20:23], s63 offen offset:128 lds\n\t"
    "v_mfma_f32_16x16x32_bf16 v[116:119], a[88:91], v[236:239], v[116:119]\n\t"
    "buffer_load_dwordx4 v201, s[20:23], s63 offen offset:2176 lds\n\t"
    "v_mfma_f32_16x16x32_bf16 v[116:119], a[84:87], v[232:235], v[116:119]\n\t"
    "s_add_u32 s65, s65, s40\n\t"
    "ds_read_b64_tr_b16 a[162:163], v244 offset:4608\n\t"
    "ds_read_b64_tr_b16 a[164:165], v244 offset:8704\n\t"
    "v_mfma_f32_16x16x32_bf16 v[116:119], a[80:83], v[228:231], v[116:119]\n\t"
    "s_lshl_b32 s65, s65, 7\n\t"
    "ds_read_b64_tr_b16 a[166:167], v244 offset:12800\n\t"
    "v_mfma_f32_16x16x32_bf16 v[120:123], a[96:99], v[228:231], 0\n\t"
    "s_add_u32 s65, s65, 1048576\n\t"
    "ds_read_b64_tr_b16 a[168:169], v244 offset:16896\n\t"
    "ds_read_b64_tr_b16 a[170:171], v244 offset:20992\n\t"
    "v_mfma_f32_16x16x32_bf16 v[120:123], a[100:103], v[232:235], v[120:123]\n\t"
    "s_lshl2_add_u32 s60, s41, s95\n\t"
    "ds_read_b64_tr_b16 a[172:173], v244 offset:25088\n\t"
    "v_mfma_f32_16x16x32_bf16 v[120:123], a[104:107], v[236:239], v[120:123]\n\t"
    "s_lshr_b32 s61, s40, 1\n\t"
    "ds_read_b64_tr_b16 a[174:175], v244 offset:29184\n\t"
    "ds_read_b64_tr_b16 a[188:189], v245 offset:25088\n\t"
    "v_mfma_f32_16x16x32_bf16 v[120:123], a[108:111], v[240:243], v[120:123]\n\t"
    "s_lshl1_add_u32 s60, s60, s61\n\t"
    "ds_read_b64_tr_b16 a[190:191], v245 offset:29184\n\t"
    "v_mfma_f32_16x16x32_bf16 v[124:127], a[124:127], v[240:243], 0\n\t"
    "s_mul_i32 s60, s60, 10\n\t"
    "ds_read_b64_tr_b16 a[184:185], v245 offset:16896\n\t"
    "ds_read_b64_tr_b16 a[186:187], v245 offset:20992\n\t"
    "v_mfma_f32_16x16x32_bf16 v[124:127], a[120:123], v[236:239], v[124:127]\n\t"
    "s_and_b32 s61, s40, 1\n\t"
    "ds_read_b64_tr_b16 a[180:181], v245 offset:8704\n\t"
    "v_mfma_f32_16x16x32_bf16 v[124:127], a[116:119], v[232:235], v[124:127]\n\t"
    "s_waitcnt lgkmcnt(15)\n\t"
    "ds_read_b64_tr_b16 a[182:183], v245 offset:12800\n\t"
    "ds_read_b64_tr_b16 a[176:177], v245 offset:512\n\t"
    "v_mfma_f32_16x16x32_bf16 v[124:127], a[112:115], v[228:231], v[124:127]\n\t"
    "ds_read_b64_tr_b16 a[178:179], v245 offset:4608\n\t"
    "s_waitcnt vmcnt(14)\n\t"
    "s_barrier\n\t"
    "v_mfma_f32_16x16x32_bf16 v[128:131], a[0:3], v[212:215], 0\n\t"
    "s_add_u32 s63, s93, 256\n\t"
    "ds_read_b64_tr_b16 a[192:193], v244 offset:1024\n\t"
    "v_mfma_f32_16x16x32_bf16 v[128:131], a[4:7], v[216:219], v[128:131]\n\t"
    "s_add_u32 m0, s52, 0\n\t"
    "ds_read_b64_tr_b16 a[194:195], v244 offset:5120\n\t"
    "v_mfma_f32_16x16x32_bf16 v[128:131], a[8:11], v[220:223], v[128:131]\n\t"
    "buffer_load_dwordx4 v255, s[20:23], s63 offen offset:0 lds\n\t"
    "v_mfma_f32_16x16x32_bf16 v[128:131], a[12:15], v[224:227], v[128:131]\n\t"
    "buffer_load_dwordx4 v201, s[20:23], s63 offen offset:2048 lds\n\t"
    "v_mfma_f32_16x16x32_bf16 v[132:135], a[28:31], v[224:227], 0\n\t"
    "s_add_u32 s60, s60, s61\n\t"
    "ds_read_b64_tr_b16 a[196:197], v244 offset:9216\n\t"
    "v_mfma_f32_16x16x32_bf16 v[132:135], a[24:27], v[220:223], v[132:135]\n\t"
    "s_add_u32 m0, s52, 896\n\t"
    "ds_read_b64_tr_b16 a[198:199], v244 offset:13312\n\t"
    "v_mfma_f32_16x16x32_bf16 v[132:135], a[20:23], v[216:219], v[132:135]\n\t"
    "buffer_load_dwordx4 v255, s[20:23], s63 offen offset:128 lds\n\t"
    "v_mfma_f32_16x16x32_bf16 v[132:135], a[16:19], v[212:215], v[132:135]\n\t"
    "buffer_load_dwordx4 v201, s[20:23], s63 offen offset:2176 lds\n\t"
    "v_mfma_f32_16x16x32_bf16 v[136:139], a[32:35], v[212:215], 0\n\t"
    "s_lshl3_add_u32 s60, s60, s96\n\t"
    "ds_read_b64_tr_b16 a[200:201], v244 offset:17408\n\t"
    "v_mfma_f32_16x16x32_bf16 v[136:139], a[36:39], v[216:219], v[136:139]\n\t"
    "s_lshl_b32 s60, s60, 12\n\t"
    "ds_read_b64_tr_b16 a[202:203], v244 offset:21504\n\t"
    "v_mfma_f32_16x16x32_bf16 v[136:139], a[40:43], v[220:223], v[136:139]\n\t"
    "s_add_u32 s36, s80, s60\n\t"
    "s_addc_u32 s37, s81, 0\n\t"
    "ds_read_b64_tr_b16 a[204:205], v244 offset:25600\n\t"
    "v_mfma_f32_16x16x32_bf16 v[136:139], a[44:47], v[224:227], v[136:139]\n\t"
    "s_mov_b32 s38, 266240\n\t"
    "ds_read_b64_tr_b16 a[206:207], v244 offset:29696\n\t"
    "v_mfma_f32_16x16x32_bf16 v[140:143], a[60:63], v[224:227], 0\n\t"
    "s_mov_b32 s84, 0\n\t"
    "ds_read_b64_tr_b16 a[220:221], v245 offset:25600\n\t"
    "v_mfma_f32_16x16x32_bf16 v[140:143], a[56:59], v[220:223], v[140:143]\n\t"
    "s_mov_b32 s85, 65536\n\t"
    "ds_read_b64_tr_b16 a[222:223], v245 offset:29696\n\t"
    "v_mfma_f32_16x16x32_bf16 v[140:143], a[52:55], v[216:219], v[140:143]\n\t"
    "s_waitcnt vmcnt(12)\n\t"
    "ds_read_b128 v[228:231], v246 offset:4096\n\t"
    "v_mfma_f32_16x16x32_bf16 v[140:143], a[48:51], v[212:215], v[140:143]\n\t"
    "s_mov_b32 s86, 131072\n\t"
    "ds_read_b128 v[232:235], v246 offset:4608\n\t"
    "v_mfma_f32_16x16x32_bf16 v[144:147], a[64:67], v[212:215], 0\n\t"
    "s_mov_b32 s87, 196608\n\t"
    "ds_read_b128 v[236:239], v246 offset:5120\n\t"
    "v_mfma_f32_16x16x32_bf16 v[144:147], a[68:71], v[216:219], v[144:147]\n\t"
    "s_mov_b32 s88, 262144\n\t"
    "ds_read_b128 v[240:243], v246 offset:5632\n\t"
    "v_mfma_f32_16x16x32_bf16 v[144:147], a[72:75], v[220:223], v[144:147]\n\t"
    "s_mov_b32 s89, 0x3e0293ee\n\t"
    "ds_read_b64_tr_b16 a[216:217], v245 offset:17408\n\t"
    "v_mfma_f32_16x16x32_bf16 v[144:147], a[76:79], v[224:227], v[144:147]\n\t"
    "s_mov_b32 s58, 0\n\t"
    "ds_read_b64_tr_b16 a[218:219], v245 offset:21504\n\t"
    "v_mfma_f32_16x16x32_bf16 v[148:151], a[92:95], v[224:227], 0\n\t"
    "ds_read_b64_tr_b16 a[212:213], v245 offset:9216\n\t"
    "v_mfma_f32_16x16x32_bf16 v[148:151], a[88:91], v[220:223], v[148:151]\n\t"
    "ds_read_b64_tr_b16 a[214:215], v245 offset:13312\n\t"
    "v_mfma_f32_16x16x32_bf16 v[148:151], a[84:87], v[216:219], v[148:151]\n\t"
    "ds_read_b64_tr_b16 a[208:209], v245 offset:1024\n\t"
    "v_mfma_f32_16x16x32_bf16 v[148:151], a[80:83], v[212:215], v[148:151]\n\t"
    "ds_read_b64_tr_b16 a[210:211], v245 offset:5120\n\t"
    "v_mfma_f32_16x16x32_bf16 v[152:155], a[96:99], v[212:215], 0\n\t"
    "ds_read_b64_tr_b16 a[224:225], v244 offset:1536\n\t"
    "v_mfma_f32_16x16x32_bf16 v[152:155], a[100:103], v[216:219], v[152:155]\n\t"
    "ds_read_b64_tr_b16 a[226:227], v244 offset:5632\n\t"
    "v_mfma_f32_16x16x32_bf16 v[152:155], a[104:107], v[220:223], v[152:155]\n\t"
    "ds_read_b64_tr_b16 a[228:229], v244 offset:9728\n\t"
    "v_mfma_f32_16x16x32_bf16 v[152:155], a[108:111], v[224:227], v[152:155]\n\t"
    "ds_read_b64_tr_b16 a[230:231], v244 offset:13824\n\t"
    "v_mfma_f32_16x16x32_bf16 v[156:159], a[124:127], v[224:227], 0\n\t"
    "ds_read_b64_tr_b16 a[232:233], v244 offset:17920\n\t"
    "v_mfma_f32_16x16x32_bf16 v[156:159], a[120:123], v[220:223], v[156:159]\n\t"
    "ds_read_b64_tr_b16 a[234:235], v244 offset:22016\n\t"
    "v_mfma_f32_16x16x32_bf16 v[156:159], a[116:119], v[216:219], v[156:159]\n\t"
    "s_waitcnt lgkmcnt(12)\n\t"
    "ds_read_b64_tr_b16 a[236:237], v244 offset:26112\n\t"
    "v_mfma_f32_16x16x32_bf16 v[156:159], a[112:115], v[212:215], v[156:159]\n\t"
    "s_waitcnt lgkmcnt(15)\n\t"
    "ds_read_b64_tr_b16 a[238:239], v244 offset:30208\n\t"
    "v_mfma_f32_16x16x32_bf16 v[0:3], a[128:131], v[228:231], v[0:3]\n\t"
    "s_mov_b32 s53, 2097152\n\t"
    "ds_read_b64_tr_b16 a[252:253], v245 offset:26112\n\t"
    "v_mfma_f32_16x16x32_bf16 v[0:3], a[132:135], v[232:235], v[0:3]\n\t"
    "s_add_u32 m0, s51, 65536\n\t"
    "ds_read_b64_tr_b16 a[254:255], v245 offset:30208\n\t"
    "v_mfma_f32_16x16x32_bf16 v[0:3], a[136:139], v[236:239], v[0:3]\n\t"
    "buffer_load_dwordx4 v248, s[24:27], s53 offen offset:0 lds\n\t"
    "v_mfma_f32_16x16x32_bf16 v[0:3], a[140:143], v[240:243], v[0:3]\n\t"
    "buffer_load_dwordx4 v200, s[24:27], s53 offen offset:2048 lds\n\t"
    "v_mfma_f32_16x16x32_bf16 v[4:7], a[156:159], v[240:243], v[4:7]\n\t"
    "s_mov_b32 s53, 2129920\n\t"
    "ds_read_b64_tr_b16 a[248:249], v245 offset:17920\n\t"
    "v_mfma_f32_16x16x32_bf16 v[4:7], a[152:155], v[236:239], v[4:7]\n\t"
    "s_add_u32 m0, s51, 69632\n\t"
    "ds_read_b64_tr_b16 a[250:251], v245 offset:22016\n\t"
    "v_mfma_f32_16x16x32_bf16 v[4:7], a[148:151], v[232:235], v[4:7]\n\t"
    "buffer_load_dwordx4 v248, s[24:27], s53 offen offset:0 lds\n\t"
    "v_mfma_f32_16x16x32_bf16 v[4:7], a[144:147], v[228:231], v[4:7]\n\t"
    "buffer_load_dwordx4 v200, s[24:27], s53 offen offset:2048 lds\n\t"
    "v_mfma_f32_16x16x32_bf16 v[8:11], a[160:163], v[228:231], v[8:11]\n\t"
    "s_add_u32 s63, s94, 256\n\t"
    "ds_read_b64_tr_b16 a[244:245], v245 offset:9728\n\t"
    "v_mfma_f32_16x16x32_bf16 v[8:11], a[164:167], v[232:235], v[8:11]\n\t"
    "s_add_u32 m0, s52, 4096\n\t"
    "ds_read_b64_tr_b16 a[246:247], v245 offset:13824\n\t"
    "v_mfma_f32_16x16x32_bf16 v[8:11], a[168:171], v[236:239], v[8:11]\n\t"
    "buffer_load_dwordx4 v255, s[20:23], s63 offen offset:0 lds\n\t"
    "v_mfma_f32_16x16x32_bf16 v[8:11], a[172:175], v[240:243], v[8:11]\n\t"
    "buffer_load_dwordx4 v201, s[20:23], s63 offen offset:2048 lds\n\t"
    "v_mfma_f32_16x16x32_bf16 v[12:15], a[188:191], v[240:243], v[12:15]\n\t"
    "ds_read_b64_tr_b16 a[240:241], v245 offset:1536\n\t"
    "v_mfma_f32_16x16x32_bf16 v[12:15], a[184:187], v[236:239], v[12:15]\n\t"
    "s_waitcnt lgkmcnt(7)\n\t"
    "ds_read_b64_tr_b16 a[242:243], v245 offset:5632\n\t"
    "v_mfma_f32_16x16x32_bf16 v[12:15], a[180:183], v[232:235], v[12:15]\n\t"
    "s_waitcnt vmcnt(14)\n\t"
    "ds_read_b128 v[212:215], v246 offset:8192\n\t"
    "v_mfma_f32_16x16x32_bf16 v[12:15], a[176:179], v[228:231], v[12:15]\n\t"
    "ds_read_b128 v[216:219], v246 offset:8704\n\t"
    "v_mfma_f32_16x16x32_bf16 v[16:19], a[192:195], v[228:231], v[16:19]\n\t"
    "ds_read_b128 v[220:223], v246 offset:9216\n\t"
    "v_mfma_f32_16x16x32_bf16 v[16:19], a[196:199], v[232:235], v[16:19]\n\t"
    "ds_read_b128 v[224:227], v246 offset:9728\n\t"
    "v_mfma_f32_16x16x32_bf16 v[16:19], a[200:203], v[236:239], v[16:19]\n\t"
    "v_mfma_f32_16x16x32_bf16 v[16:19], a[204:207], v[240:243], v[16:19]\n\t"
    "s_add_u32 m0, s52, 4992\n\t"
    "v_mfma_f32_16x16x32_bf16 v[20:23], a[220:223], v[240:243], v[20:23]\n\t"
    "buffer_load_dwordx4 v255, s[20:23], s63 offen offset:128 lds\n\t"
    "v_mfma_f32_16x16x32_bf16 v[20:23], a[216:219], v[236:239], v[20:23]\n\t"
    "buffer_load_dwordx4 v201, s[20:23], s63 offen offset:2176 lds\n\t"
    "v_mfma_f32_16x16x32_bf16 v[20:23], a[212:215], v[232:235], v[20:23]\n\t"
    "v_mfma_f32_16x16x32_bf16 v[20:23], a[208:211], v[228:231], v[20:23]\n\t"
    "v_mfma_f32_16x16x32_bf16 v[24:27], a[224:227], v[228:231], v[24:27]\n\t"
    "v_mfma_f32_16x16x32_bf16 v[24:27], a[228:231], v[232:235], v[24:27]\n\t"
    "v_mfma_f32_16x16x32_bf16 v[24:27], a[232:235], v[236:239], v[24:27]\n\t"
    "s_waitcnt lgkmcnt(4)\n\t"
    "v_mfma_f32_16x16x32_bf16 v[24:27], a[236:239], v[240:243], v[24:27]\n\t"
    "v_mfma_f32_16x16x32_bf16 v[28:31], a[252:255], v[240:243], v[28:31]\n\t"
    "v_mfma_f32_16x16x32_bf16 v[28:31], a[248:251], v[236:239], v[28:31]\n\t"
    "v_mfma_f32_16x16x32_bf16 v[28:31], a[244:247], v[232:235], v[28:31]\n\t"
    "s_waitcnt lgkmcnt(0)\n\t"
    "v_mfma_f32_16x16x32_bf16 v[28:31], a[240:243], v[228:231], v[28:31]\n\t"
    "v_mfma_f32_16x16x32_bf16 v[32:35], a[128:131], v[212:215], v[32:35]\n\t"
    "s_mov_b32 s53, 2097280\n\t"
    "v_mfma_f32_16x16x32_bf16 v[32:35], a[132:135], v[216:219], v[32:35]\n\t"
    "s_add_u32 m0, s51, 66560\n\t"
    "v_mfma_f32_16x16x32_bf16 v[32:35], a[136:139], v[220:223], v[32:35]\n\t"
    "buffer_load_dwordx4 v248, s[24:27], s53 offen offset:0 lds\n\t"
    "v_mfma_f32_16x16x32_bf16 v[32:35], a[140:143], v[224:227], v[32:35]\n\t"
    "buffer_load_dwordx4 v200, s[24:27], s53 offen offset:2048 lds\n\t"
    "v_mfma_f32_16x16x32_bf16 v[36:39], a[156:159], v[224:227], v[36:39]\n\t"
    "s_mov_b32 s53, 2130048\n\t"
    "v_mfma_f32_16x16x32_bf16 v[36:39], a[152:155], v[220:223], v[36:39]\n\t"
    "s_add_u32 m0, s51, 70656\n\t"
    "v_mfma_f32_16x16x32_bf16 v[36:39], a[148:151], v[216:219], v[36:39]\n\t"
    "buffer_load_dwordx4 v248, s[24:27], s53 offen offset:0 lds\n\t"
    "v_mfma_f32_16x16x32_bf16 v[36:39], a[144:147], v[212:215], v[36:39]\n\t"
    "buffer_load_dwordx4 v200, s[24:27], s53 offen offset:2048 lds\n\t"
    "v_mfma_f32_16x16x32_bf16 v[40:43], a[160:163], v[212:215], v[40:43]\n\t"
    "s_add_u32 s63, s90, 512\n\t"
    "v_mfma_f32_16x16x32_bf16 v[40:43], a[164:167], v[216:219], v[40:43]\n\t"
    "s_add_u32 m0, s52, 8192\n\t"
    "v_mfma_f32_16x16x32_bf16 v[40:43], a[168:171], v[220:223], v[40:43]\n\t"
    "buffer_load_dwordx4 v255, s[20:23], s63 offen offset:0 lds\n\t"
    "v_mfma_f32_16x16x32_bf16 v[40:43], a[172:175], v[224:227], v[40:43]\n\t"
    "buffer_load_dwordx4 v201, s[20:23], s63 offen offset:2048 lds\n\t"
    "v_mfma_f32_16x16x32_bf16 v[44:47], a[188:191], v[224:227], v[44:47]\n\t"
    "v_mfma_f32_16x16x32_bf16 v[44:47], a[184:187], v[220:223], v[44:47]\n\t"
    "v_mfma_f32_16x16x32_bf16 v[44:47], a[180:183], v[216:219], v[44:47]\n\t"
    "s_waitcnt vmcnt(18)\n\t"
    "ds_read_b128 v[228:231], v246 offset:12288\n\t"
    "v_mfma_f32_16x16x32_bf16 v[44:47], a[176:179], v[212:215], v[44:47]\n\t"
    "ds_read_b128 v[232:235], v246 offset:12800\n\t"
    "s_mov_b32 exec_lo, 4278190080\n\t"
    "s_mov_b32 exec_hi, 0\n\t"
    "buffer_load_dword v178, v204, s[24:27], 0 offen\n\t"
    "buffer_load_dword v179, v205, s[20:23], 0 offen\n\t"
    "s_mov_b64 exec, -1\n\t"
    "v_mfma_f32_16x16x32_bf16 v[48:51], a[192:195], v[212:215], v[48:51]\n\t"
    "ds_read_b128 v[236:239], v246 offset:13312\n\t"
    "v_mfma_f32_16x16x32_bf16 v[48:51], a[196:199], v[216:219], v[48:51]\n\t"
    "ds_read_b128 v[240:243], v246 offset:13824\n\t"
    "v_mfma_f32_16x16x32_bf16 v[48:51], a[200:203], v[220:223], v[48:51]\n\t"
    "v_mfma_f32_16x16x32_bf16 v[48:51], a[204:207], v[224:227], v[48:51]\n\t"
    "s_add_u32 m0, s52, 9088\n\t"
    "v_mfma_f32_16x16x32_bf16 v[52:55], a[220:223], v[224:227], v[52:55]\n\t"
    "buffer_load_dwordx4 v255, s[20:23], s63 offen offset:128 lds\n\t"
    "v_mfma_f32_16x16x32_bf16 v[52:55], a[216:219], v[220:223], v[52:55]\n\t"
    "buffer_load_dwordx4 v201, s[20:23], s63 offen offset:2176 lds\n\t"
    "v_mfma_f32_16x16x32_bf16 v[52:55], a[212:215], v[216:219], v[52:55]\n\t"
    "v_mfma_f32_16x16x32_bf16 v[52:55], a[208:211], v[212:215], v[52:55]\n\t"
    "v_mfma_f32_16x16x32_bf16 v[56:59], a[224:227], v[212:215], v[56:59]\n\t"
    "v_mfma_f32_16x16x32_bf16 v[56:59], a[228:231], v[216:219], v[56:59]\n\t"
    "v_mfma_f32_16x16x32_bf16 v[56:59], a[232:235], v[220:223], v[56:59]\n\t"
    "v_mfma_f32_16x16x32_bf16 v[56:59], a[236:239], v[224:227], v[56:59]\n\t"
    "v_mfma_f32_16x16x32_bf16 v[60:63], a[252:255], v[224:227], v[60:63]\n\t"
    "v_mfma_f32_16x16x32_bf16 v[60:63], a[248:251], v[220:223], v[60:63]\n\t"
    "v_mfma_f32_16x16x32_bf16 v[60:63], a[244:247], v[216:219], v[60:63]\n\t"
    "s_waitcnt lgkmcnt(0)\n\t"
    "v_mfma_f32_16x16x32_bf16 v[60:63], a[240:243], v[212:215], v[60:63]\n\t"
    "v_mfma_f32_16x16x32_bf16 v[64:67], a[128:131], v[228:231], v[64:67]\n\t"
    "s_add_u32 s63, s91, 512\n\t"
    "v_mfma_f32_16x16x32_bf16 v[64:67], a[132:135], v[232:235], v[64:67]\n\t"
    "s_add_u32 m0, s52, 12288\n\t"
    "v_mfma_f32_16x16x32_bf16 v[64:67], a[136:139], v[236:239], v[64:67]\n\t"
    "buffer_load_dwordx4 v255, s[20:23], s63 offen offset:0 lds\n\t"
    "v_mfma_f32_16x16x32_bf16 v[64:67], a[140:143], v[240:243], v[64:67]\n\t"
    "buffer_load_dwordx4 v201, s[20:23], s63 offen offset:2048 lds\n\t"
    "v_mfma_f32_16x16x32_bf16 v[68:71], a[156:159], v[240:243], v[68:71]\n\t"
    "v_mfma_f32_16x16x32_bf16 v[68:71], a[152:155], v[236:239], v[68:71]\n\t"
    "s_add_u32 m0, s52, 13184\n\t"
    "v_mfma_f32_16x16x32_bf16 v[68:71], a[148:151], v[232:235], v[68:71]\n\t"
    "buffer_load_dwordx4 v255, s[20:23], s63 offen offset:128 lds\n\t"
    "v_mfma_f32_16x16x32_bf16 v[68:71], a[144:147], v[228:231], v[68:71]\n\t"
    "buffer_load_dwordx4 v201, s[20:23], s63 offen offset:2176 lds\n\t"
    "v_mfma_f32_16x16x32_bf16 v[72:75], a[160:163], v[228:231], v[72:75]\n\t"
    "v_mfma_f32_16x16x32_bf16 v[72:75], a[164:167], v[232:235], v[72:75]\n\t"
    "v_mfma_f32_16x16x32_bf16 v[72:75], a[168:171], v[236:239], v[72:75]\n\t"
    "buffer_load_dwordx4 v[160:163], v250, s[28:31], 0 offen offset:0\n\t"
    "v_mfma_f32_16x16x32_bf16 v[72:75], a[172:175], v[240:243], v[72:75]\n\t"
    "buffer_load_dwordx4 v[164:167], v250, s[28:31], 0 offen offset:64\n\t"
    "v_mfma_f32_16x16x32_bf16 v[76:79], a[188:191], v[240:243], v[76:79]\n\t"
    "v_mfma_f32_16x16x32_bf16 v[76:79], a[184:187], v[236:239], v[76:79]\n\t"
    "v_mfma_f32_16x16x32_bf16 v[76:79], a[180:183], v[232:235], v[76:79]\n\t"
    "s_waitcnt vmcnt(24)\n\t"
    "ds_read_b128 v[212:215], v246 offset:0\n\t"
    "v_mfma_f32_16x16x32_bf16 v[76:79], a[176:179], v[228:231], v[76:79]\n\t"
    "ds_read_b128 v[216:219], v246 offset:512\n\t"
    "v_mfma_f32_16x16x32_bf16 v[80:83], a[192:195], v[228:231], v[80:83]\n\t"
    "ds_read_b128 v[220:223], v246 offset:1024\n\t"
    "v_mfma_f32_16x16x32_bf16 v[80:83], a[196:199], v[232:235], v[80:83]\n\t"
    "ds_read_b128 v[224:227], v246 offset:1536\n\t"
    "v_mfma_f32_16x16x32_bf16 v[80:83], a[200:203], v[236:239], v[80:83]\n\t"
    "v_mfma_f32_16x16x32_bf16 v[80:83], a[204:207], v[240:243], v[80:83]\n\t"
    "v_mfma_f32_16x16x32_bf16 v[84:87], a[220:223], v[240:243], v[84:87]\n\t"
    "buffer_load_dwordx4 v[168:171], v250, s[28:31], 0 offen offset:128\n\t"
    "v_mfma_f32_16x16x32_bf16 v[84:87], a[216:219], v[236:239], v[84:87]\n\t"
    "buffer_load_dwordx4 v[172:175], v250, s[28:31], 0 offen offset:192\n\t"
    "v_mfma_f32_16x16x32_bf16 v[84:87], a[212:215], v[232:235], v[84:87]\n\t"
    "v_mfma_f32_16x16x32_bf16 v[84:87], a[208:211], v[228:231], v[84:87]\n\t"
    "v_mul_u32_u24 v202, 128, v249\n\t"
    "v_add_u32 v202, s64, v202\n\t"
    "v_and_b32 v203, 3, v249\n\t"
    "s_mov_b32 exec_lo, 0\n\t"
    "v_lshl_add_u32 v202, v203, 2, s65\n\t"
    "s_mov_b32 exec_lo, -1\n\t"
    "v_mfma_f32_16x16x32_bf16 v[88:91], a[224:227], v[228:231], v[88:91]\n\t"
    "v_mfma_f32_16x16x32_bf16 v[88:91], a[228:231], v[232:235], v[88:91]\n\t"
    "buffer_load_dword v211, v202, s[12:15], 0 offen sc1\n\t"
    "v_mfma_f32_16x16x32_bf16 v[88:91], a[232:235], v[236:239], v[88:91]\n\t"
    "v_mfma_f32_16x16x32_bf16 v[88:91], a[236:239], v[240:243], v[88:91]\n\t"
    "v_mfma_f32_16x16x32_bf16 v[92:95], a[252:255], v[240:243], v[92:95]\n\t"
    "v_mfma_f32_16x16x32_bf16 v[92:95], a[248:251], v[236:239], v[92:95]\n\t"
    "v_mfma_f32_16x16x32_bf16 v[92:95], a[244:247], v[232:235], v[92:95]\n\t"
    "s_waitcnt vmcnt(15) lgkmcnt(0)\n\t"
    "v_mfma_f32_16x16x32_bf16 v[92:95], a[240:243], v[228:231], v[92:95]\n\t"
    "s_barrier\n\t"
    "v_mfma_f32_16x16x32_bf16 v[96:99], a[128:131], v[212:215], v[96:99]\n\t"
    "s_add_u32 s63, s92, 512\n\t"
    "ds_read_b64_tr_b16 a[0:1], v253 offset:0\n\t"
    "ds_read_b64_tr_b16 a[2:3], v253 offset:4096\n\t"
    "v_mfma_f32_16x16x32_bf16 v[96:99], a[132:135], v[216:219], v[96:99]\n\t"
    "s_add_u32 m0, s52, 0\n\t"
    "ds_read_b64_tr_b16 a[4:5], v253 offset:8192\n\t"
    "v_mfma_f32_16x16x32_bf16 v[96:99], a[136:139], v[220:223], v[96:99]\n\t"
    "buffer_load_dwordx4 v255, s[20:23], s63 offen offset:0 lds\n\t"
    "v_mfma_f32_16x16x32_bf16 v[96:99], a[140:143], v[224:227], v[96:99]\n\t"
    "buffer_load_dwordx4 v201, s[20:23], s63 offen offset:2048 lds\n\t"
    "v_mfma_f32_16x16x32_bf16 v[100:103], a[156:159], v[224:227], v[100:103]\n\t"
    "ds_read_b64_tr_b16 a[6:7], v253 offset:12288\n\t"
    "v_mfma_f32_16x16x32_bf16 v[100:103], a[152:155], v[220:223], v[100:103]\n\t"
    "s_add_u32 m0, s52, 896\n\t"
    "ds_read_b64_tr_b16 a[8:9], v253 offset:16384\n\t"
    "ds_read_b64_tr_b16 a[10:11], v253 offset:20480\n\t"
    "v_mfma_f32_16x16x32_bf16 v[100:103], a[148:151], v[216:219], v[100:103]\n\t"
    "buffer_load_dwordx4 v255, s[20:23], s63 offen offset:128 lds\n\t"
    "v_mfma_f32_16x16x32_bf16 v[100:103], a[144:147], v[212:215], v[100:103]\n\t"
    "buffer_load_dwordx4 v201, s[20:23], s63 offen offset:2176 lds\n\t"
    "v_mfma_f32_16x16x32_bf16 v[104:107], a[160:163], v[212:215], v[104:107]\n\t"
    "ds_read_b64_tr_b16 a[12:13], v253 offset:24576\n\t"
    "v_mfma_f32_16x16x32_bf16 v[104:107], a[164:167], v[216:219], v[104:107]\n\t"
    "ds_read_b64_tr_b16 a[14:15], v253 offset:28672\n\t"
    "v_mfma_f32_16x16x32_bf16 v[104:107], a[168:171], v[220:223], v[104:107]\n\t"
    "ds_read_b64_tr_b16 a[28:29], v254 offset:24576\n\t"
    "ds_read_b64_tr_b16 a[30:31], v254 offset:28672\n\t"
    "v_mfma_f32_16x16x32_bf16 v[104:107], a[172:175], v[224:227], v[104:107]\n\t"
    "ds_read_b64_tr_b16 a[24:25], v254 offset:16384\n\t"
    "v_mfma_f32_16x16x32_bf16 v[108:111], a[188:191], v[224:227], v[108:111]\n\t"
    "ds_read_b64_tr_b16 a[26:27], v254 offset:20480\n\t"
    "v_mfma_f32_16x16x32_bf16 v[108:111], a[184:187], v[220:223], v[108:111]\n\t"
    "ds_read_b64_tr_b16 a[20:21], v254 offset:8192\n\t"
    "ds_read_b64_tr_b16 a[22:23], v254 offset:12288\n\t"
    "v_mfma_f32_16x16x32_bf16 v[108:111], a[180:183], v[216:219], v[108:111]\n\t"
    "s_waitcnt vmcnt(23)\n\t"
    "ds_read_b128 v[228:231], v246 offset:4096\n\t"
    "v_mfma_f32_16x16x32_bf16 v[108:111], a[176:179], v[212:215], v[108:111]\n\t"
    "ds_read_b128 v[232:235], v246 offset:4608\n\t"
    "v_mfma_f32_16x16x32_bf16 v[112:115], a[192:195], v[212:215], v[112:115]\n\t"
    "ds_read_b128 v[236:239], v246 offset:5120\n\t"
    "ds_read_b64_tr_b16 a[16:17], v254 offset:0\n\t"
    "v_mfma_f32_16x16x32_bf16 v[112:115], a[196:199], v[216:219], v[112:115]\n\t"
    "ds_read_b128 v[240:243], v246 offset:5632\n\t"
    "v_mfma_f32_16x16x32_bf16 v[112:115], a[200:203], v[220:223], v[112:115]\n\t"
    "ds_read_b64_tr_b16 a[18:19], v254 offset:4096\n\t"
    "v_mfma_f32_16x16x32_bf16 v[112:115], a[204:207], v[224:227], v[112:115]\n\t"
    "ds_read_b64_tr_b16 a[32:33], v253 offset:512\n\t"
    "ds_read_b64_tr_b16 a[34:35], v253 offset:4608\n\t"
    "v_mfma_f32_16x16x32_bf16 v[116:119], a[220:223], v[224:227], v[116:119]\n\t"
    "ds_read_b64_tr_b16 a[36:37], v253 offset:8704\n\t"
    "v_mfma_f32_16x16x32_bf16 v[116:119], a[216:219], v[220:223], v[116:119]\n\t"
    "ds_read_b64_tr_b16 a[38:39], v253 offset:12800\n\t"
    "v_mfma_f32_16x16x32_bf16 v[116:119], a[212:215], v[216:219], v[116:119]\n\t"
    "ds_read_b64_tr_b16 a[40:41], v253 offset:16896\n\t"
    "ds_read_b64_tr_b16 a[42:43], v253 offset:20992\n\t"
    "v_mfma_f32_16x16x32_bf16 v[116:119], a[208:211], v[212:215], v[116:119]\n\t"
    "ds_read_b64_tr_b16 a[44:45], v253 offset:25088\n\t"
    "v_mfma_f32_16x16x32_bf16 v[120:123], a[224:227], v[212:215], v[120:123]\n\t"
    "ds_read_b64_tr_b16 a[46:47], v253 offset:29184\n\t"
    "v_mfma_f32_16x16x32_bf16 v[120:123], a[228:231], v[216:219], v[120:123]\n\t"
    "ds_read_b64_tr_b16 a[60:61], v254 offset:25088\n\t"
    "ds_read_b64_tr_b16 a[62:63], v254 offset:29184\n\t"
    "v_mfma_f32_16x16x32_bf16 v[120:123], a[232:235], v[220:223], v[120:123]\n\t"
    "ds_read_b64_tr_b16 a[56:57], v254 offset:16896\n\t"
    "v_mfma_f32_16x16x32_bf16 v[120:123], a[236:239], v[224:227], v[120:123]\n\t"
    "ds_read_b64_tr_b16 a[58:59], v254 offset:20992\n\t"
    "v_mfma_f32_16x16x32_bf16 v[124:127], a[252:255], v[224:227], v[124:127]\n\t"
    "ds_read_b64_tr_b16 a[52:53], v254 offset:8704\n\t"
    "v_mfma_f32_16x16x32_bf16 v[124:127], a[248:251], v[220:223], v[124:127]\n\t"
    "ds_read_b64_tr_b16 a[54:55], v254 offset:12800\n\t"
    "v_mfma_f32_16x16x32_bf16 v[124:127], a[244:247], v[216:219], v[124:127]\n\t"
    "s_waitcnt lgkmcnt(15)\n\t"
    "ds_read_b64_tr_b16 a[48:49], v254 offset:512\n\t"
    "v_mfma_f32_16x16x32_bf16 v[124:127], a[240:243], v[212:215], v[124:127]\n\t"
    "ds_read_b64_tr_b16 a[50:51], v254 offset:4608\n\t"
    "v_mfma_f32_16x16x32_bf16 v[128:131], a[128:131], v[228:231], v[128:131]\n\t"
    "s_add_u32 s63, s93, 512\n\t"
    "ds_read_b64_tr_b16 a[64:65], v253 offset:1024\n\t"
    "v_mfma_f32_16x16x32_bf16 v[128:131], a[132:135], v[232:235], v[128:131]\n\t"
    "s_add_u32 m0, s52, 4096\n\t"
    "ds_read_b64_tr_b16 a[66:67], v253 offset:5120\n\t"
    "v_mfma_f32_16x16x32_bf16 v[128:131], a[136:139], v[236:239], v[128:131]\n\t"
    "buffer_load_dwordx4 v255, s[20:23], s63 offen offset:0 lds\n\t"
    "v_mfma_f32_16x16x32_bf16 v[128:131], a[140:143], v[240:243], v[128:131]\n\t"
    "buffer_load_dwordx4 v201, s[20:23], s63 offen offset:2048 lds\n\t"
    "v_mfma_f32_16x16x32_bf16 v[132:135], a[156:159], v[240:243], v[132:135]\n\t"
    "ds_read_b64_tr_b16 a[68:69], v253 offset:9216\n\t"
    "v_mfma_f32_16x16x32_bf16 v[132:135], a[152:155], v[236:239], v[132:135]\n\t"
    "s_add_u32 m0, s52, 4992\n\t"
    "ds_read_b64_tr_b16 a[70:71], v253 offset:13312\n\t"
    "v_mfma_f32_16x16x32_bf16 v[132:135], a[148:151], v[232:235], v[132:135]\n\t"
    "buffer_load_dwordx4 v255, s[20:23], s63 offen offset:128 lds\n\t"
    "v_mfma_f32_16x16x32_bf16 v[132:135], a[144:147], v[228:231], v[132:135]\n\t"
    "buffer_load_dwordx4 v201, s[20:23], s63 offen offset:2176 lds\n\t"
    "v_mfma_f32_16x16x32_bf16 v[136:139], a[160:163], v[228:231], v[136:139]\n\t"
    "ds_read_b64_tr_b16 a[72:73], v253 offset:17408\n\t"
    "v_mfma_f32_16x16x32_bf16 v[136:139], a[164:167], v[232:235], v[136:139]\n\t"
    "ds_read_b64_tr_b16 a[74:75], v253 offset:21504\n\t"
    "v_mfma_f32_16x16x32_bf16 v[136:139], a[168:171], v[236:239], v[136:139]\n\t"
    "ds_read_b64_tr_b16 a[76:77], v253 offset:25600\n\t"
    "v_mfma_f32_16x16x32_bf16 v[136:139], a[172:175], v[240:243], v[136:139]\n\t"
    "ds_read_b64_tr_b16 a[78:79], v253 offset:29696\n\t"
    "v_mfma_f32_16x16x32_bf16 v[140:143], a[188:191], v[240:243], v[140:143]\n\t"
    "ds_read_b64_tr_b16 a[92:93], v254 offset:25600\n\t"
    "v_mfma_f32_16x16x32_bf16 v[140:143], a[184:187], v[236:239], v[140:143]\n\t"
    "ds_read_b64_tr_b16 a[94:95], v254 offset:29696\n\t"
    "v_mfma_f32_16x16x32_bf16 v[140:143], a[180:183], v[232:235], v[140:143]\n\t"
    "s_waitcnt vmcnt(17)\n\t"
    "ds_read_b128 v[212:215], v246 offset:8192\n\t"
    "v_mfma_f32_16x16x32_bf16 v[140:143], a[176:179], v[228:231], v[140:143]\n\t"
    "ds_read_b128 v[216:219], v246 offset:8704\n\t"
    "v_mfma_f32_16x16x32_bf16 v[144:147], a[192:195], v[228:231], v[144:147]\n\t"
    "ds_read_b128 v[220:223], v246 offset:9216\n\t"
    "v_mfma_f32_16x16x32_bf16 v[144:147], a[196:199], v[232:235], v[144:147]\n\t"
    "ds_read_b128 v[224:227], v246 offset:9728\n\t"
    "v_mfma_f32_16x16x32_bf16 v[144:147], a[200:203], v[236:239], v[144:147]\n\t"
    "ds_read_b64_tr_b16 a[88:89], v254 offset:17408\n\t"
    "v_mfma_f32_16x16x32_bf16 v[144:147], a[204:207], v[240:243], v[144:147]\n\t"
    "ds_read_b64_tr_b16 a[90:91], v254 offset:21504\n\t"
    "v_mfma_f32_16x16x32_bf16 v[148:151], a[220:223], v[240:243], v[148:151]\n\t"
    "ds_read_b64_tr_b16 a[84:85], v254 offset:9216\n\t"
    "v_mfma_f32_16x16x32_bf16 v[148:151], a[216:219], v[236:239], v[148:151]\n\t"
    "ds_read_b64_tr_b16 a[86:87], v254 offset:13312\n\t"
    "v_mfma_f32_16x16x32_bf16 v[148:151], a[212:215], v[232:235], v[148:151]\n\t"
    "ds_read_b64_tr_b16 a[80:81], v254 offset:1024\n\t"
    "v_mfma_f32_16x16x32_bf16 v[148:151], a[208:211], v[228:231], v[148:151]\n\t"
    "ds_read_b64_tr_b16 a[82:83], v254 offset:5120\n\t"
    "v_mfma_f32_16x16x32_bf16 v[152:155], a[224:227], v[228:231], v[152:155]\n\t"
    "ds_read_b64_tr_b16 a[96:97], v253 offset:1536\n\t"
    "v_mfma_f32_16x16x32_bf16 v[152:155], a[228:231], v[232:235], v[152:155]\n\t"
    "ds_read_b64_tr_b16 a[98:99], v253 offset:5632\n\t"
    "v_mfma_f32_16x16x32_bf16 v[152:155], a[232:235], v[236:239], v[152:155]\n\t"
    "ds_read_b64_tr_b16 a[100:101], v253 offset:9728\n\t"
    "v_mfma_f32_16x16x32_bf16 v[152:155], a[236:239], v[240:243], v[152:155]\n\t"
    "ds_read_b64_tr_b16 a[102:103], v253 offset:13824\n\t"
    "v_mfma_f32_16x16x32_bf16 v[156:159], a[252:255], v[240:243], v[156:159]\n\t"
    "ds_read_b64_tr_b16 a[104:105], v253 offset:17920\n\t"
    "v_mfma_f32_16x16x32_bf16 v[156:159], a[248:251], v[236:239], v[156:159]\n\t"
    "ds_read_b64_tr_b16 a[106:107], v253 offset:22016\n\t"
    "v_mfma_f32_16x16x32_bf16 v[156:159], a[244:247], v[232:235], v[156:159]\n\t"
    "s_waitcnt lgkmcnt(12)\n\t"
    "ds_read_b64_tr_b16 a[108:109], v253 offset:26112\n\t"
    "v_mfma_f32_16x16x32_bf16 v[156:159], a[240:243], v[228:231], v[156:159]\n\t"
    "s_waitcnt lgkmcnt(15)\n\t"
    "ds_read_b64_tr_b16 a[110:111], v253 offset:30208\n\t"
    "v_mfma_f32_16x16x32_bf16 v[0:3], a[0:3], v[212:215], v[0:3]\n\t"
    "s_mov_b32 s53, 3145728\n\t"
    "ds_read_b64_tr_b16 a[124:125], v254 offset:26112\n\t"
    "v_mfma_f32_16x16x32_bf16 v[0:3], a[4:7], v[216:219], v[0:3]\n\t"
    "s_add_u32 m0, s51, 32768\n\t"
    "ds_read_b64_tr_b16 a[126:127], v254 offset:30208\n\t"
    "v_mfma_f32_16x16x32_bf16 v[0:3], a[8:11], v[220:223], v[0:3]\n\t"
    "buffer_load_dwordx4 v248, s[24:27], s53 offen offset:0 lds\n\t"
    "v_mfma_f32_16x16x32_bf16 v[0:3], a[12:15], v[224:227], v[0:3]\n\t"
    "buffer_load_dwordx4 v200, s[24:27], s53 offen offset:2048 lds\n\t"
    "v_mfma_f32_16x16x32_bf16 v[4:7], a[28:31], v[224:227], v[4:7]\n\t"
    "s_mov_b32 s53, 3178496\n\t"
    "ds_read_b64_tr_b16 a[120:121], v254 offset:17920\n\t"
    "v_mfma_f32_16x16x32_bf16 v[4:7], a[24:27], v[220:223], v[4:7]\n\t"
    "s_add_u32 m0, s51, 36864\n\t"
    "ds_read_b64_tr_b16 a[122:123], v254 offset:22016\n\t"
    "v_mfma_f32_16x16x32_bf16 v[4:7], a[20:23], v[216:219], v[4:7]\n\t"
    "buffer_load_dwordx4 v248, s[24:27], s53 offen offset:0 lds\n\t"
    "v_mfma_f32_16x16x32_bf16 v[4:7], a[16:19], v[212:215], v[4:7]\n\t"
    "buffer_load_dwordx4 v200, s[24:27], s53 offen offset:2048 lds\n\t"
    "v_mfma_f32_16x16x32_bf16 v[8:11], a[32:35], v[212:215], v[8:11]\n\t"
    "s_add_u32 s63, s94, 512\n\t"
    "ds_read_b64_tr_b16 a[116:117], v254 offset:9728\n\t"
    "v_mfma_f32_16x16x32_bf16 v[8:11], a[36:39], v[216:219], v[8:11]\n\t"
    "s_add_u32 m0, s52, 8192\n\t"
    "ds_read_b64_tr_b16 a[118:119], v254 offset:13824\n\t"
    "v_mfma_f32_16x16x32_bf16 v[8:11], a[40:43], v[220:223], v[8:11]\n\t"
    "buffer_load_dwordx4 v255, s[20:23], s63 offen offset:0 lds\n\t"
    "v_mfma_f32_16x16x32_bf16 v[8:11], a[44:47], v[224:227], v[8:11]\n\t"
    "buffer_load_dwordx4 v201, s[20:23], s63 offen offset:2048 lds\n\t"
    "v_mfma_f32_16x16x32_bf16 v[12:15], a[60:63], v[224:227], v[12:15]\n\t"
    "ds_read_b64_tr_b16 a[112:113], v254 offset:1536\n\t"
    "v_mfma_f32_16x16x32_bf16 v[12:15], a[56:59], v[220:223], v[12:15]\n\t"
    "s_waitcnt lgkmcnt(7)\n\t"
    "ds_read_b64_tr_b16 a[114:115], v254 offset:5632\n\t"
    "v_mfma_f32_16x16x32_bf16 v[12:15], a[52:55], v[216:219], v[12:15]\n\t"
    "s_waitcnt vmcnt(19)\n\t"
    "ds_read_b128 v[228:231], v246 offset:12288\n\t"
    "v_mfma_f32_16x16x32_bf16 v[12:15], a[48:51], v[212:215], v[12:15]\n\t"
    "ds_read_b128 v[232:235], v246 offset:12800\n\t"
    "v_mfma_f32_16x16x32_bf16 v[16:19], a[64:67], v[212:215], v[16:19]\n\t"
    "ds_read_b128 v[236:239], v246 offset:13312\n\t"
    "v_mfma_f32_16x16x32_bf16 v[16:19], a[68:71], v[216:219], v[16:19]\n\t"
    "ds_read_b128 v[240:243], v246 offset:13824\n\t"
    "v_mfma_f32_16x16x32_bf16 v[16:19], a[72:75], v[220:223], v[16:19]\n\t"
    "v_mfma_f32_16x16x32_bf16 v[16:19], a[76:79], v[224:227], v[16:19]\n\t"
    "s_add_u32 m0, s52, 9088\n\t"
    "v_mfma_f32_16x16x32_bf16 v[20:23], a[92:95], v[224:227], v[20:23]\n\t"
    "buffer_load_dwordx4 v255, s[20:23], s63 offen offset:128 lds\n\t"
    "v_mfma_f32_16x16x32_bf16 v[20:23], a[88:91], v[220:223], v[20:23]\n\t"
    "buffer_load_dwordx4 v201, s[20:23], s63 offen offset:2176 lds\n\t"
    "v_mfma_f32_16x16x32_bf16 v[20:23], a[84:87], v[216:219], v[20:23]\n\t"
    "v_mfma_f32_16x16x32_bf16 v[20:23], a[80:83], v[212:215], v[20:23]\n\t"
    "v_mfma_f32_16x16x32_bf16 v[24:27], a[96:99], v[212:215], v[24:27]\n\t"
    "v_mfma_f32_16x16x32_bf16 v[24:27], a[100:103], v[216:219], v[24:27]\n\t"
    "v_mfma_f32_16x16x32_bf16 v[24:27], a[104:107], v[220:223], v[24:27]\n\t"
    "s_waitcnt lgkmcnt(4)\n\t"
    "v_mfma_f32_16x16x32_bf16 v[24:27], a[108:111], v[224:227], v[24:27]\n\t"
    "v_mfma_f32_16x16x32_bf16 v[28:31], a[124:127], v[224:227], v[28:31]\n\t"
    "v_mfma_f32_16x16x32_bf16 v[28:31], a[120:123], v[220:223], v[28:31]\n\t"
    "v_mfma_f32_16x16x32_bf16 v[28:31], a[116:119], v[216:219], v[28:31]\n\t"
    "s_waitcnt lgkmcnt(0)\n\t"
    "v_mfma_f32_16x16x32_bf16 v[28:31], a[112:115], v[212:215], v[28:31]\n\t"
    "v_mfma_f32_16x16x32_bf16 v[32:35], a[0:3], v[228:231], v[32:35]\n\t"
    "s_mov_b32 s53, 3145856\n\t"
    "v_mfma_f32_16x16x32_bf16 v[32:35], a[4:7], v[232:235], v[32:35]\n\t"
    "s_add_u32 m0, s51, 33792\n\t"
    "v_mfma_f32_16x16x32_bf16 v[32:35], a[8:11], v[236:239], v[32:35]\n\t"
    "buffer_load_dwordx4 v248, s[24:27], s53 offen offset:0 lds\n\t"
    "v_mfma_f32_16x16x32_bf16 v[32:35], a[12:15], v[240:243], v[32:35]\n\t"
    "buffer_load_dwordx4 v200, s[24:27], s53 offen offset:2048 lds\n\t"
    "v_mfma_f32_16x16x32_bf16 v[36:39], a[28:31], v[240:243], v[36:39]\n\t"
    "s_mov_b32 s53, 3178624\n\t"
    "v_mfma_f32_16x16x32_bf16 v[36:39], a[24:27], v[236:239], v[36:39]\n\t"
    "s_add_u32 m0, s51, 37888\n\t"
    "v_mfma_f32_16x16x32_bf16 v[36:39], a[20:23], v[232:235], v[36:39]\n\t"
    "buffer_load_dwordx4 v248, s[24:27], s53 offen offset:0 lds\n\t"
    "v_mfma_f32_16x16x32_bf16 v[36:39], a[16:19], v[228:231], v[36:39]\n\t"
    "buffer_load_dwordx4 v200, s[24:27], s53 offen offset:2048 lds\n\t"
    "v_mfma_f32_16x16x32_bf16 v[40:43], a[32:35], v[228:231], v[40:43]\n\t"
    "s_add_u32 s63, s90, 768\n\t"
    "v_mfma_f32_16x16x32_bf16 v[40:43], a[36:39], v[232:235], v[40:43]\n\t"
    "s_add_u32 m0, s52, 12288\n\t"
    "v_mfma_f32_16x16x32_bf16 v[40:43], a[40:43], v[236:239], v[40:43]\n\t"
    "buffer_load_dwordx4 v255, s[20:23], s63 offen offset:0 lds\n\t"
    "v_mfma_f32_16x16x32_bf16 v[40:43], a[44:47], v[240:243], v[40:43]\n\t"
    "buffer_load_dwordx4 v201, s[20:23], s63 offen offset:2048 lds\n\t"
    "v_mfma_f32_16x16x32_bf16 v[44:47], a[60:63], v[240:243], v[44:47]\n\t"
    "s_waitcnt vmcnt(23)\n\t"
    "v_xor_b32 v160, 0x80008000, v160\n\t"
    "v_xor_b32 v161, 0x80008000, v161\n\t"
    "v_mfma_f32_16x16x32_bf16 v[44:47], a[56:59], v[236:239], v[44:47]\n\t"
    "v_xor_b32 v162, 0x80008000, v162\n\t"
    "v_xor_b32 v163, 0x80008000, v163\n\t"
    "v_mfma_f32_16x16x32_bf16 v[44:47], a[52:55], v[232:235], v[44:47]\n\t"
    "s_waitcnt vmcnt(18)\n\t"
    "ds_read_b128 v[212:215], v246 offset:0\n\t"
    "v_xor_b32 v164, 0x80008000, v164\n\t"
    "v_mfma_f32_16x16x32_bf16 v[44:47], a[48:51], v[228:231], v[44:47]\n\t"
    "ds_read_b128 v[216:219], v246 offset:512\n\t"
    "v_xor_b32 v165, 0x80008000, v165\n\t"
    "v_mfma_f32_16x16x32_bf16 v[48:51], a[64:67], v[228:231], v[48:51]\n\t"
    "ds_read_b128 v[220:223], v246 offset:1024\n\t"
    "v_xor_b32 v166, 0x80008000, v166\n\t"
    "v_mfma_f32_16x16x32_bf16 v[48:51], a[68:71], v[232:235], v[48:51]\n\t"
    "ds_read_b128 v[224:227], v246 offset:1536\n\t"
    "v_xor_b32 v167, 0x80008000, v167\n\t"
    "v_mfma_f32_16x16x32_bf16 v[48:51], a[72:75], v[236:239], v[48:51]\n\t"
    "v_xor_b32 v168, 0x80008000, v168\n\t"
    "v_xor_b32 v169, 0x80008000, v169\n\t"
    "v_mfma_f32_16x16x32_bf16 v[48:51], a[76:79], v[240:243], v[48:51]\n\t"
    "s_add_u32 m0, s52, 13184\n\t"
    "v_xor_b32 v170, 0x80008000, v170\n\t"
    "v_xor_b32 v171, 0x80008000, v171\n\t"
    "v_mfma_f32_16x16x32_bf16 v[52:55], a[92:95], v[240:243], v[52:55]\n\t"
    "buffer_load_dwordx4 v255, s[20:23], s63 offen offset:128 lds\n\t"
    "v_mfma_f32_16x16x32_bf16 v[52:55], a[88:91], v[236:239], v[52:55]\n\t"
    "buffer_load_dwordx4 v201, s[20:23], s63 offen offset:2176 lds\n\t"
    "v_mfma_f32_16x16x32_bf16 v[52:55], a[84:87], v[232:235], v[52:55]\n\t"
    "v_xor_b32 v172, 0x80008000, v172\n\t"
    "v_xor_b32 v173, 0x80008000, v173\n\t"
    "v_mfma_f32_16x16x32_bf16 v[52:55], a[80:83], v[228:231], v[52:55]\n\t"
    "v_xor_b32 v174, 0x80008000, v174\n\t"
    "v_xor_b32 v175, 0x80008000, v175\n\t"
    "v_mfma_f32_16x16x32_bf16 v[56:59], a[96:99], v[228:231], v[56:59]\n\t"
    "v_mfma_f32_16x16x32_bf16 v[56:59], a[100:103], v[232:235], v[56:59]\n\t"
    "v_mfma_f32_16x16x32_bf16 v[56:59], a[104:107], v[236:239], v[56:59]\n\t"
    "v_mfma_f32_16x16x32_bf16 v[56:59], a[108:111], v[240:243], v[56:59]\n\t"
    "v_mfma_f32_16x16x32_bf16 v[60:63], a[124:127], v[240:243], v[60:63]\n\t"
    "v_mfma_f32_16x16x32_bf16 v[60:63], a[120:123], v[236:239], v[60:63]\n\t"
    "v_mfma_f32_16x16x32_bf16 v[60:63], a[116:119], v[232:235], v[60:63]\n\t"
    "s_waitcnt lgkmcnt(0)\n\t"
    "v_mfma_f32_16x16x32_bf16 v[60:63], a[112:115], v[228:231], v[60:63]\n\t"
    "v_mfma_f32_16x16x32_bf16 v[64:67], a[0:3], v[212:215], v[64:67]\n\t"
    "s_add_u32 s63, s91, 768\n\t"
    "v_mfma_f32_16x16x32_bf16 v[64:67], a[4:7], v[216:219], v[64:67]\n\t"
    "s_add_u32 m0, s52, 0\n\t"
    "v_mfma_f32_16x16x32_bf16 v[64:67], a[8:11], v[220:223], v[64:67]\n\t"
    "buffer_load_dwordx4 v255, s[20:23], s63 offen offset:0 lds\n\t"
    "v_mfma_f32_16x16x32_bf16 v[64:67], a[12:15], v[224:227], v[64:67]\n\t"
    "buffer_load_dwordx4 v201, s[20:23], s63 offen offset:2048 lds\n\t"
    "v_mfma_f32_16x16x32_bf16 v[68:71], a[28:31], v[224:227], v[68:71]\n\t"
    "v_mfma_f32_16x16x32_bf16 v[68:71], a[24:27], v[220:223], v[68:71]\n\t"
    "s_add_u32 m0, s52, 896\n\t"
    "v_mfma_f32_16x16x32_bf16 v[68:71], a[20:23], v[216:219], v[68:71]\n\t"
    "buffer_load_dwordx4 v255, s[20:23], s63 offen offset:128 lds\n\t"
    "v_mfma_f32_16x16x32_bf16 v[68:71], a[16:19], v[212:215], v[68:71]\n\t"
    "buffer_load_dwordx4 v201, s[20:23], s63 offen offset:2176 lds\n\t"
    "v_mfma_f32_16x16x32_bf16 v[72:75], a[32:35], v[212:215], v[72:75]\n\t"
    "v_mov_b32 v180, 0xc1a00000\n\t"
    "v_mov_b32 v181, 0xc1a00000\n\t"
    "v_mfma_f32_16x16x32_bf16 v[72:75], a[36:39], v[216:219], v[72:75]\n\t"
    "v_mov_b32 v182, 0xc1a00000\n\t"
    "v_mov_b32 v183, 0xc1a00000\n\t"
    "v_mfma_f32_16x16x32_bf16 v[72:75], a[40:43], v[220:223], v[72:75]\n\t"
    "v_mov_b32 v184, 0xc1a00000\n\t"
    "v_mov_b32 v185, 0xc1a00000\n\t"
    "v_mfma_f32_16x16x32_bf16 v[72:75], a[44:47], v[224:227], v[72:75]\n\t"
    "v_mov_b32 v186, 0xc1a00000\n\t"
    "v_mov_b32 v187, 0xc1a00000\n\t"
    "v_mfma_f32_16x16x32_bf16 v[76:79], a[60:63], v[224:227], v[76:79]\n\t"
    "v_mov_b32 v188, 0xc1a00000\n\t"
    "v_mov_b32 v189, 0xc1a00000\n\t"
    "v_mfma_f32_16x16x32_bf16 v[76:79], a[56:59], v[220:223], v[76:79]\n\t"
    "v_mov_b32 v190, 0xc1a00000\n\t"
    "v_mov_b32 v191, 0xc1a00000\n\t"
    "v_mfma_f32_16x16x32_bf16 v[76:79], a[52:55], v[216:219], v[76:79]\n\t"
    "s_waitcnt vmcnt(20)\n\t"
    "ds_read_b128 v[228:231], v246 offset:4096\n\t"
    "v_mov_b32 v192, 0xc1a00000\n\t"
    "v_mfma_f32_16x16x32_bf16 v[76:79], a[48:51], v[212:215], v[76:79]\n\t"
    "ds_read_b128 v[232:235], v246 offset:4608\n\t"
    "v_mov_b32 v193, 0xc1a00000\n\t"
    "v_mfma_f32_16x16x32_bf16 v[80:83], a[64:67], v[212:215], v[80:83]\n\t"
    "ds_read_b128 v[236:239], v246 offset:5120\n\t"
    "v_mov_b32 v194, 0xc1a00000\n\t"
    "v_mfma_f32_16x16x32_bf16 v[80:83], a[68:71], v[216:219], v[80:83]\n\t"
    "ds_read_b128 v[240:243], v246 offset:5632\n\t"
    "v_mov_b32 v195, 0xc1a00000\n\t"
    "v_mfma_f32_16x16x32_bf16 v[80:83], a[72:75], v[220:223], v[80:83]\n\t"
    "v_mov_b32 v196, 0xc1a00000\n\t"
    "v_mov_b32 v197, 0xc1a00000\n\t"
    "v_mfma_f32_16x16x32_bf16 v[80:83], a[76:79], v[224:227], v[80:83]\n\t"
    "v_mov_b32 v198, 0xc1a00000\n\t"
    "v_mov_b32 v199, 0xc1a00000\n\t"
    "v_mfma_f32_16x16x32_bf16 v[84:87], a[92:95], v[224:227], v[84:87]\n\t"
    "v_mfma_f32_16x16x32_bf16 v[84:87], a[88:91], v[220:223], v[84:87]\n\t"
    "v_mfma_f32_16x16x32_bf16 v[84:87], a[84:87], v[216:219], v[84:87]\n\t"
    "v_mfma_f32_16x16x32_bf16 v[84:87], a[80:83], v[212:215], v[84:87]\n\t"
    "v_mfma_f32_16x16x32_bf16 v[88:91], a[96:99], v[212:215], v[88:91]\n\t"
    "v_mfma_f32_16x16x32_bf16 v[88:91], a[100:103], v[216:219], v[88:91]\n\t"
    "v_mfma_f32_16x16x32_bf16 v[88:91], a[104:107], v[220:223], v[88:91]\n\t"
    "v_mfma_f32_16x16x32_bf16 v[88:91], a[108:111], v[224:227], v[88:91]\n\t"
    "v_mfma_f32_16x16x32_bf16 v[92:95], a[124:127], v[224:227], v[92:95]\n\t"
    "v_mfma_f32_16x16x32_bf16 v[92:95], a[120:123], v[220:223], v[92:95]\n\t"
    "v_mfma_f32_16x16x32_bf16 v[92:95], a[116:119], v[216:219], v[92:95]\n\t"
    "s_waitcnt vmcnt(8) lgkmcnt(0)\n\t"
    "v_mfma_f32_16x16x32_bf16 v[92:95], a[112:115], v[212:215], v[92:95]\n\t"
    "s_barrier\n\t"
    "v_mfma_f32_16x16x32_bf16 v[96:99], a[0:3], v[228:231], v[96:99]\n\t"
    "s_add_u32 s63, s92, 768\n\t"
    "ds_read_b64_tr_b16 a[128:129], v244 offset:32768\n\t"
    "ds_read_b64_tr_b16 a[130:131], v244 offset:36864\n\t"
    "v_mfma_f32_16x16x32_bf16 v[96:99], a[4:7], v[232:235], v[96:99]\n\t"
    "s_add_u32 m0, s52, 4096\n\t"
    "ds_read_b64_tr_b16 a[132:133], v244 offset:40960\n\t"
    "v_mfma_f32_16x16x32_bf16 v[96:99], a[8:11], v[236:239], v[96:99]\n\t"
    "buffer_load_dwordx4 v255, s[20:23], s63 offen offset:0 lds\n\t"
    "v_mfma_f32_16x16x32_bf16 v[96:99], a[12:15], v[240:243], v[96:99]\n\t"
    "buffer_load_dwordx4 v201, s[20:23], s63 offen offset:2048 lds\n\t"
    "v_mfma_f32_16x16x32_bf16 v[100:103], a[28:31], v[240:243], v[100:103]\n\t"
    "ds_read_b64_tr_b16 a[134:135], v244 offset:45056\n\t"
    "v_mfma_f32_16x16x32_bf16 v[100:103], a[24:27], v[236:239], v[100:103]\n\t"
    "s_add_u32 m0, s52, 4992\n\t"
    "ds_read_b64_tr_b16 a[136:137], v244 offset:49152\n\t"
    "ds_read_b64_tr_b16 a[138:139], v244 offset:53248\n\t"
    "v_mfma_f32_16x16x32_bf16 v[100:103], a[20:23], v[232:235], v[100:103]\n\t"
    "buffer_load_dwordx4 v255, s[20:23], s63 offen offset:128 lds\n\t"
    "v_mfma_f32_16x16x32_bf16 v[100:103], a[16:19], v[228:231], v[100:103]\n\t"
    "buffer_load_dwordx4 v201, s[20:23], s63 offen offset:2176 lds\n\t"
    "v_mfma_f32_16x16x32_bf16 v[104:107], a[32:35], v[228:231], v[104:107]\n\t"
    "ds_read_b64_tr_b16 a[140:141], v244 offset:57344\n\t"
    "v_mfma_f32_16x16x32_bf16 v[104:107], a[36:39], v[232:235], v[104:107]\n\t"
    "ds_read_b64_tr_b16 a[142:143], v244 offset:61440\n\t"
    "v_mfma_f32_16x16x32_bf16 v[104:107], a[40:43], v[236:239], v[104:107]\n\t"
    "ds_read_b64_tr_b16 a[156:157], v245 offset:57344\n\t"
    "ds_read_b64_tr_b16 a[158:159], v245 offset:61440\n\t"
    "v_mfma_f32_16x16x32_bf16 v[104:107], a[44:47], v[240:243], v[104:107]\n\t"
    "ds_read_b64_tr_b16 a[152:153], v245 offset:49152\n\t"
    "v_mfma_f32_16x16x32_bf16 v[108:111], a[60:63], v[240:243], v[108:111]\n\t"
    "ds_read_b64_tr_b16 a[154:155], v245 offset:53248\n\t"
    "v_mfma_f32_16x16x32_bf16 v[108:111], a[56:59], v[236:239], v[108:111]\n\t"
    "ds_read_b64_tr_b16 a[148:149], v245 offset:40960\n\t"
    "ds_read_b64_tr_b16 a[150:151], v245 offset:45056\n\t"
    "v_mfma_f32_16x16x32_bf16 v[108:111], a[52:55], v[232:235], v[108:111]\n\t"
    "s_waitcnt vmcnt(16)\n\t"
    "ds_read_b128 v[212:215], v246 offset:8192\n\t"
    "v_mfma_f32_16x16x32_bf16 v[108:111], a[48:51], v[228:231], v[108:111]\n\t"
    "ds_read_b128 v[216:219], v246 offset:8704\n\t"
    "v_mfma_f32_16x16x32_bf16 v[112:115], a[64:67], v[228:231], v[112:115]\n\t"
    "ds_read_b128 v[220:223], v246 offset:9216\n\t"
    "ds_read_b64_tr_b16 a[144:145], v245 offset:32768\n\t"
    "v_mfma_f32_16x16x32_bf16 v[112:115], a[68:71], v[232:235], v[112:115]\n\t"
    "ds_read_b128 v[224:227], v246 offset:9728\n\t"
    "v_mfma_f32_16x16x32_bf16 v[112:115], a[72:75], v[236:239], v[112:115]\n\t"
    "ds_read_b64_tr_b16 a[146:147], v245 offset:36864\n\t"
    "v_mfma_f32_16x16x32_bf16 v[112:115], a[76:79], v[240:243], v[112:115]\n\t"
    "ds_read_b64_tr_b16 a[160:161], v244 offset:33280\n\t"
    "ds_read_b64_tr_b16 a[162:163], v244 offset:37376\n\t"
    "v_mfma_f32_16x16x32_bf16 v[116:119], a[92:95], v[240:243], v[116:119]\n\t"
    "ds_read_b64_tr_b16 a[164:165], v244 offset:41472\n\t"
    "v_mfma_f32_16x16x32_bf16 v[116:119], a[88:91], v[236:239], v[116:119]\n\t"
    "ds_read_b64_tr_b16 a[166:167], v244 offset:45568\n\t"
    "v_mfma_f32_16x16x32_bf16 v[116:119], a[84:87], v[232:235], v[116:119]\n\t"
    "ds_read_b64_tr_b16 a[168:169], v244 offset:49664\n\t"
    "ds_read_b64_tr_b16 a[170:171], v244 offset:53760\n\t"
    "v_mfma_f32_16x16x32_bf16 v[116:119], a[80:83], v[228:231], v[116:119]\n\t"
    "ds_read_b64_tr_b16 a[172:173], v244 offset:57856\n\t"
    "v_mfma_f32_16x16x32_bf16 v[120:123], a[96:99], v[228:231], v[120:123]\n\t"
    "ds_read_b64_tr_b16 a[174:175], v244 offset:61952\n\t"
    "v_mfma_f32_16x16x32_bf16 v[120:123], a[100:103], v[232:235], v[120:123]\n\t"
    "ds_read_b64_tr_b16 a[188:189], v245 offset:57856\n\t"
    "ds_read_b64_tr_b16 a[190:191], v245 offset:61952\n\t"
    "v_mfma_f32_16x16x32_bf16 v[120:123], a[104:107], v[236:239], v[120:123]\n\t"
    "ds_read_b64_tr_b16 a[184:185], v245 offset:49664\n\t"
    "v_mfma_f32_16x16x32_bf16 v[120:123], a[108:111], v[240:243], v[120:123]\n\t"
    "ds_read_b64_tr_b16 a[186:187], v245 offset:53760\n\t"
    "v_mfma_f32_16x16x32_bf16 v[124:127], a[124:127], v[240:243], v[124:127]\n\t"
    "ds_read_b64_tr_b16 a[180:181], v245 offset:41472\n\t"
    "v_mfma_f32_16x16x32_bf16 v[124:127], a[120:123], v[236:239], v[124:127]\n\t"
    "ds_read_b64_tr_b16 a[182:183], v245 offset:45568\n\t"
    "v_mfma_f32_16x16x32_bf16 v[124:127], a[116:119], v[232:235], v[124:127]\n\t"
    "s_waitcnt lgkmcnt(15)\n\t"
    "ds_read_b64_tr_b16 a[176:177], v245 offset:33280\n\t"
    "v_mfma_f32_16x16x32_bf16 v[124:127], a[112:115], v[228:231], v[124:127]\n\t"
    "ds_read_b64_tr_b16 a[178:179], v245 offset:37376\n\t"
    "v_mfma_f32_16x16x32_bf16 v[128:131], a[0:3], v[212:215], v[128:131]\n\t"
    "s_add_u32 s63, s93, 768\n\t"
    "ds_read_b64_tr_b16 a[192:193], v244 offset:33792\n\t"
    "v_mfma_f32_16x16x32_bf16 v[128:131], a[4:7], v[216:219], v[128:131]\n\t"
    "s_add_u32 m0, s52, 8192\n\t"
    "ds_read_b64_tr_b16 a[194:195], v244 offset:37888\n\t"
    "v_mfma_f32_16x16x32_bf16 v[128:131], a[8:11], v[220:223], v[128:131]\n\t"
    "buffer_load_dwordx4 v255, s[20:23], s63 offen offset:0 lds\n\t"
    "v_mfma_f32_16x16x32_bf16 v[128:131], a[12:15], v[224:227], v[128:131]\n\t"
    "buffer_load_dwordx4 v201, s[20:23], s63 offen offset:2048 lds\n\t"
    "v_mfma_f32_16x16x32_bf16 v[132:135], a[28:31], v[224:227], v[132:135]\n\t"
    "ds_read_b64_tr_b16 a[196:197], v244 offset:41984\n\t"
    "v_mfma_f32_16x16x32_bf16 v[132:135], a[24:27], v[220:223], v[132:135]\n\t"
    "s_add_u32 m0, s52, 9088\n\t"
    "ds_read_b64_tr_b16 a[198:199], v244 offset:46080\n\t"
    "v_mfma_f32_16x16x32_bf16 v[132:135], a[20:23], v[216:219], v[132:135]\n\t"
    "buffer_load_dwordx4 v255, s[20:23], s63 offen offset:128 lds\n\t"
    "v_mfma_f32_16x16x32_bf16 v[132:135], a[16:19], v[212:215], v[132:135]\n\t"
    "buffer_load_dwordx4 v201, s[20:23], s63 offen offset:2176 lds\n\t"
    "v_mfma_f32_16x16x32_bf16 v[136:139], a[32:35], v[212:215], v[136:139]\n\t"
    "ds_read_b64_tr_b16 a[200:201], v244 offset:50176\n\t"
    "v_mfma_f32_16x16x32_bf16 v[136:139], a[36:39], v[216:219], v[136:139]\n\t"
    "ds_read_b64_tr_b16 a[202:203], v244 offset:54272\n\t"
    "v_mfma_f32_16x16x32_bf16 v[136:139], a[40:43], v[220:223], v[136:139]\n\t"
    "ds_read_b64_tr_b16 a[204:205], v244 offset:58368\n\t"
    "v_mfma_f32_16x16x32_bf16 v[136:139], a[44:47], v[224:227], v[136:139]\n\t"
    "ds_read_b64_tr_b16 a[206:207], v244 offset:62464\n\t"
    "v_mfma_f32_16x16x32_bf16 v[140:143], a[60:63], v[224:227], v[140:143]\n\t"
    "ds_read_b64_tr_b16 a[220:221], v245 offset:58368\n\t"
    "v_mfma_f32_16x16x32_bf16 v[140:143], a[56:59], v[220:223], v[140:143]\n\t"
    "ds_read_b64_tr_b16 a[222:223], v245 offset:62464\n\t"
    "v_mfma_f32_16x16x32_bf16 v[140:143], a[52:55], v[216:219], v[140:143]\n\t"
    "s_waitcnt vmcnt(12)\n\t"
    "ds_read_b128 v[228:231], v246 offset:12288\n\t"
    "v_mfma_f32_16x16x32_bf16 v[140:143], a[48:51], v[212:215], v[140:143]\n\t"
    "ds_read_b128 v[232:235], v246 offset:12800\n\t"
    "v_mfma_f32_16x16x32_bf16 v[144:147], a[64:67], v[212:215], v[144:147]\n\t"
    "ds_read_b128 v[236:239], v246 offset:13312\n\t"
    "v_mfma_f32_16x16x32_bf16 v[144:147], a[68:71], v[216:219], v[144:147]\n\t"
    "ds_read_b128 v[240:243], v246 offset:13824\n\t"
    "v_mfma_f32_16x16x32_bf16 v[144:147], a[72:75], v[220:223], v[144:147]\n\t"
    "ds_read_b64_tr_b16 a[216:217], v245 offset:50176\n\t"
    "v_mfma_f32_16x16x32_bf16 v[144:147], a[76:79], v[224:227], v[144:147]\n\t"
    "ds_read_b64_tr_b16 a[218:219], v245 offset:54272\n\t"
    "v_mfma_f32_16x16x32_bf16 v[148:151], a[92:95], v[224:227], v[148:151]\n\t"
    "ds_read_b64_tr_b16 a[212:213], v245 offset:41984\n\t"
    "v_mfma_f32_16x16x32_bf16 v[148:151], a[88:91], v[220:223], v[148:151]\n\t"
    "ds_read_b64_tr_b16 a[214:215], v245 offset:46080\n\t"
    "v_mfma_f32_16x16x32_bf16 v[148:151], a[84:87], v[216:219], v[148:151]\n\t"
    "ds_read_b64_tr_b16 a[208:209], v245 offset:33792\n\t"
    "v_mfma_f32_16x16x32_bf16 v[148:151], a[80:83], v[212:215], v[148:151]\n\t"
    "ds_read_b64_tr_b16 a[210:211], v245 offset:37888\n\t"
    "v_mfma_f32_16x16x32_bf16 v[152:155], a[96:99], v[212:215], v[152:155]\n\t"
    "ds_read_b64_tr_b16 a[224:225], v244 offset:34304\n\t"
    "v_mfma_f32_16x16x32_bf16 v[152:155], a[100:103], v[216:219], v[152:155]\n\t"
    "ds_read_b64_tr_b16 a[226:227], v244 offset:38400\n\t"
    "v_mfma_f32_16x16x32_bf16 v[152:155], a[104:107], v[220:223], v[152:155]\n\t"
    "ds_read_b64_tr_b16 a[228:229], v244 offset:42496\n\t"
    "v_mfma_f32_16x16x32_bf16 v[152:155], a[108:111], v[224:227], v[152:155]\n\t"
    "ds_read_b64_tr_b16 a[230:231], v244 offset:46592\n\t"
    "v_mfma_f32_16x16x32_bf16 v[156:159], a[124:127], v[224:227], v[156:159]\n\t"
    "ds_read_b64_tr_b16 a[232:233], v244 offset:50688\n\t"
    "v_mfma_f32_16x16x32_bf16 v[156:159], a[120:123], v[220:223], v[156:159]\n\t"
    "ds_read_b64_tr_b16 a[234:235], v244 offset:54784\n\t"
    "v_mfma_f32_16x16x32_bf16 v[156:159], a[116:119], v[216:219], v[156:159]\n\t"
    "s_waitcnt lgkmcnt(12)\n\t"
    "ds_read_b64_tr_b16 a[236:237], v244 offset:58880\n\t"
    "v_mfma_f32_16x16x32_bf16 v[156:159], a[112:115], v[212:215], v[156:159]\n\t"
    "s_waitcnt lgkmcnt(15)\n\t"
    "ds_read_b64_tr_b16 a[238:239], v244 offset:62976\n\t"
    "v_mfma_f32_16x16x32_bf16 v[0:3], a[128:131], v[228:231], v[0:3]\n\t"
    "s_add_u32 s63, s94, 768\n\t"
    "ds_read_b64_tr_b16 a[252:253], v245 offset:58880\n\t"
    "v_accvgpr_write_b32 a32, v160\n\t"
    "v_mfma_f32_16x16x32_bf16 v[0:3], a[132:135], v[232:235], v[0:3]\n\t"
    "s_add_u32 m0, s52, 12288\n\t"
    "ds_read_b64_tr_b16 a[254:255], v245 offset:62976\n\t"
    "v_accvgpr_write_b32 a33, v161\n\t"
    "v_mfma_f32_16x16x32_bf16 v[0:3], a[136:139], v[236:239], v[0:3]\n\t"
    "buffer_load_dwordx4 v255, s[20:23], s63 offen offset:0 lds\n\t"
    "v_mfma_f32_16x16x32_bf16 v[0:3], a[140:143], v[240:243], v[0:3]\n\t"
    "buffer_load_dwordx4 v201, s[20:23], s63 offen offset:2048 lds\n\t"
    "v_mfma_f32_16x16x32_bf16 v[4:7], a[156:159], v[240:243], v[4:7]\n\t"
    "ds_read_b64_tr_b16 a[248:249], v245 offset:50688\n\t"
    "v_accvgpr_write_b32 a34, v162\n\t"
    "v_mfma_f32_16x16x32_bf16 v[4:7], a[152:155], v[236:239], v[4:7]\n\t"
    "s_add_u32 m0, s52, 13184\n\t"
    "ds_read_b64_tr_b16 a[250:251], v245 offset:54784\n\t"
    "v_accvgpr_write_b32 a35, v163\n\t"
    "v_mfma_f32_16x16x32_bf16 v[4:7], a[148:151], v[232:235], v[4:7]\n\t"
    "buffer_load_dwordx4 v255, s[20:23], s63 offen offset:128 lds\n\t"
    "v_mfma_f32_16x16x32_bf16 v[4:7], a[144:147], v[228:231], v[4:7]\n\t"
    "buffer_load_dwordx4 v201, s[20:23], s63 offen offset:2176 lds\n\t"
    "v_mfma_f32_16x16x32_bf16 v[8:11], a[160:163], v[228:231], v[8:11]\n\t"
    "ds_read_b64_tr_b16 a[244:245], v245 offset:42496\n\t"
    "v_accvgpr_write_b32 a36, v164\n\t"
    "v_mfma_f32_16x16x32_bf16 v[8:11], a[164:167], v[232:235], v[8:11]\n\t"
    "ds_read_b64_tr_b16 a[246:247], v245 offset:46592\n\t"
    "v_accvgpr_write_b32 a37, v165\n\t"
    "v_mfma_f32_16x16x32_bf16 v[8:11], a[168:171], v[236:239], v[8:11]\n\t"
    "ds_read_b64_tr_b16 a[240:241], v245 offset:34304\n\t"
    "v_accvgpr_write_b32 a38, v166\n\t"
    "v_mfma_f32_16x16x32_bf16 v[8:11], a[172:175], v[240:243], v[8:11]\n\t"
    "ds_read_b64_tr_b16 a[242:243], v245 offset:38400\n\t"
    "v_accvgpr_write_b32 a39, v167\n\t"
    "v_mfma_f32_16x16x32_bf16 v[12:15], a[188:191], v[240:243], v[12:15]\n\t"
    "v_accvgpr_write_b32 a40, v168\n\t"
    "v_accvgpr_write_b32 a41, v169\n\t"
    "v_mfma_f32_16x16x32_bf16 v[12:15], a[184:187], v[236:239], v[12:15]\n\t"
    "s_waitcnt lgkmcnt(8)\n\t"
    "v_accvgpr_write_b32 a42, v170\n\t"
    "v_accvgpr_write_b32 a43, v171\n\t"
    "v_mfma_f32_16x16x32_bf16 v[12:15], a[180:183], v[232:235], v[12:15]\n\t"
    "s_waitcnt vmcnt(12)\n\t"
    "ds_read_b128 v[212:215], v246 offset:0\n\t"
    "v_accvgpr_write_b32 a44, v172\n\t"
    "v_mfma_f32_16x16x32_bf16 v[12:15], a[176:179], v[228:231], v[12:15]\n\t"
    "ds_read_b128 v[216:219], v246 offset:512\n\t"
    "v_accvgpr_write_b32 a45, v173\n\t"
    "v_mfma_f32_16x16x32_bf16 v[16:19], a[192:195], v[228:231], v[16:19]\n\t"
    "ds_read_b128 v[220:223], v246 offset:1024\n\t"
    "v_accvgpr_write_b32 a46, v174\n\t"
    "v_mfma_f32_16x16x32_bf16 v[16:19], a[196:199], v[232:235], v[16:19]\n\t"
    "ds_read_b128 v[224:227], v246 offset:1536\n\t"
    "v_accvgpr_write_b32 a47, v175\n\t"
    "v_mfma_f32_16x16x32_bf16 v[16:19], a[200:203], v[236:239], v[16:19]\n\t"
    "v_accvgpr_write_b32 a48, 0\n\t"
    "v_accvgpr_write_b32 a49, 0\n\t"
    "v_mfma_f32_16x16x32_bf16 v[16:19], a[204:207], v[240:243], v[16:19]\n\t"
    "v_accvgpr_write_b32 a50, 0\n\t"
    "v_accvgpr_write_b32 a51, 0\n\t"
    "v_mfma_f32_16x16x32_bf16 v[20:23], a[220:223], v[240:243], v[20:23]\n\t"
    "v_mov_b32 v209, 0\n\t"
    "v_mov_b32 v208, 0\n\t"
    "v_mfma_f32_16x16x32_bf16 v[20:23], a[216:219], v[236:239], v[20:23]\n\t"
    "v_mov_b32 v207, 0\n\t"
    "v_mov_b32 v206, 0\n\t"
    "v_mfma_f32_16x16x32_bf16 v[20:23], a[212:215], v[232:235], v[20:23]\n\t"
    "v_mov_b32 v205, 0\n\t"
    "v_mov_b32 v204, 0\n\t"
    "v_mfma_f32_16x16x32_bf16 v[20:23], a[208:211], v[228:231], v[20:23]\n\t"
    "v_mov_b32 v203, 0\n\t"
    "v_mov_b32 v202, 0\n\t"
    "v_mfma_f32_16x16x32_bf16 v[24:27], a[224:227], v[228:231], v[24:27]\n\t"
    "v_mov_b32 v201, 0\n\t"
    "v_mov_b32 v200, 0\n\t"
    "v_mfma_f32_16x16x32_bf16 v[24:27], a[228:231], v[232:235], v[24:27]\n\t"
    "v_mfma_f32_16x16x32_bf16 v[24:27], a[232:235], v[236:239], v[24:27]\n\t"
    "s_waitcnt lgkmcnt(4)\n\t"
    "v_mfma_f32_16x16x32_bf16 v[24:27], a[236:239], v[240:243], v[24:27]\n\t"
    "v_mfma_f32_16x16x32_bf16 v[28:31], a[252:255], v[240:243], v[28:31]\n\t"
    "v_mfma_f32_16x16x32_bf16 v[28:31], a[248:251], v[236:239], v[28:31]\n\t"
    "v_mfma_f32_16x16x32_bf16 v[28:31], a[244:247], v[232:235], v[28:31]\n\t"
    "s_waitcnt lgkmcnt(0)\n\t"
    "v_mfma_f32_16x16x32_bf16 v[28:31], a[240:243], v[228:231], v[28:31]\n\t"
    "v_mfma_f32_16x16x32_bf16 v[32:35], a[128:131], v[212:215], v[32:35]\n\t"
    "s_mov_b32 s71, 0x20000\n\t"
    "v_mul_f32 v0, s89, v0\n\t"
    "v_mul_f32 v1, s89, v1\n\t"
    "v_mfma_f32_16x16x32_bf16 v[32:35], a[132:135], v[216:219], v[32:35]\n\t"
    "s_mov_b32 s75, 0x20000\n\t"
    "v_mul_f32 v2, s89, v2\n\t"
    "v_mul_f32 v3, s89, v3\n\t"
    "v_mfma_f32_16x16x32_bf16 v[32:35], a[136:139], v[220:223], v[32:35]\n\t"
    "s_mov_b32 s5, 0x1f800000\n\t"
    "v_mul_f32 v4, s89, v4\n\t"
    "v_mul_f32 v5, s89, v5\n\t"
    "v_mfma_f32_16x16x32_bf16 v[32:35], a[140:143], v[224:227], v[32:35]\n\t"
    "s_lshr_b32 s45, s96, 2\n\t"
    "v_mul_f32 v6, s89, v6\n\t"
    "v_mul_f32 v7, s89, v7\n\t"
    "v_mfma_f32_16x16x32_bf16 v[36:39], a[156:159], v[224:227], v[36:39]\n\t"
    "s_sub_u32 s46, 10, s45\n\t"
    "v_cvt_pk_bf16_f32 v0, v0, v1\n\t"
    "v_cvt_pk_bf16_f32 v1, v2, v3\n\t"
    "v_mfma_f32_16x16x32_bf16 v[36:39], a[152:155], v[220:223], v[36:39]\n\t"
    "s_mul_i32 s60, s45, 0x110\n\t"
    "v_cvt_pk_bf16_f32 v2, v4, v5\n\t"
    "v_cvt_pk_bf16_f32 v3, v6, v7\n\t"
    "v_mfma_f32_16x16x32_bf16 v[36:39], a[148:151], v[216:219], v[36:39]\n\t"
    "s_sub_u32 s60, 0x863, s60\n\t"
    "v_mul_f32 v8, s89, v8\n\t"
    "v_mul_f32 v9, s89, v9\n\t"
    "v_mfma_f32_16x16x32_bf16 v[36:39], a[144:147], v[212:215], v[36:39]\n\t"
    "s_sub_u32 s8, 2, s45\n\t"
    "v_mul_f32 v10, s89, v10\n\t"
    "v_mul_f32 v11, s89, v11\n\t"
    "v_mfma_f32_16x16x32_bf16 v[40:43], a[160:163], v[212:215], v[40:43]\n\t"
    "s_lshl_b32 s61, s95, 2\n\t"
    "v_mul_f32 v12, s89, v12\n\t"
    "v_mul_f32 v13, s89, v13\n\t"
    "v_mfma_f32_16x16x32_bf16 v[40:43], a[164:167], v[216:219], v[40:43]\n\t"
    "s_lshr_b32 s60, s60, s61\n\t"
    "v_mul_f32 v14, s89, v14\n\t"
    "v_mul_f32 v15, s89, v15\n\t"
    "v_mfma_f32_16x16x32_bf16 v[40:43], a[168:171], v[220:223], v[40:43]\n\t"
    "s_and_b32 s47, s60, 15\n\t"
    "v_permlane16_swap_b32 v0, v2\n\t"
    "v_mfma_f32_16x16x32_bf16 v[40:43], a[172:175], v[224:227], v[40:43]\n\t"
    "s_add_u32 s60, s47, s46\n\t"
    "v_permlane16_swap_b32 v1, v3\n\t"
    "v_mfma_f32_16x16x32_bf16 v[44:47], a[188:191], v[224:227], v[44:47]\n\t"
    "s_sub_u32 s60, s60, 3\n\t"
    "v_cvt_pk_bf16_f32 v4, v8, v9\n\t"
    "v_cvt_pk_bf16_f32 v5, v10, v11\n\t"
    "v_mfma_f32_16x16x32_bf16 v[44:47], a[184:187], v[220:223], v[44:47]\n\t"
    "s_sub_u32 s61, s60, s46\n\t"
    "s_cselect_b32 s60, s60, s61\n\t"
    "v_cvt_pk_bf16_f32 v6, v12, v13\n\t"
    "v_cvt_pk_bf16_f32 v7, v14, v15\n\t"
    "v_mfma_f32_16x16x32_bf16 v[44:47], a[180:183], v[216:219], v[44:47]\n\t"
    "s_waitcnt vmcnt(8)\n\t"
    "ds_read_b128 v[228:231], v246 offset:4096\n\t"
    "v_mul_f32 v16, s89, v16\n\t"
    "v_mfma_f32_16x16x32_bf16 v[44:47], a[176:179], v[212:215], v[44:47]\n\t"
    "s_mul_i32 s49, s60, 16384\n\t"
    "ds_read_b128 v[232:235], v246 offset:4608\n\t"
    "v_mul_f32 v17, s89, v17\n\t"
    "v_mfma_f32_16x16x32_bf16 v[48:51], a[192:195], v[212:215], v[48:51]\n\t"
    "s_add_u32 s60, s47, s46\n\t"
    "ds_read_b128 v[236:239], v246 offset:5120\n\t"
    "v_mul_f32 v18, s89, v18\n\t"
    "v_mfma_f32_16x16x32_bf16 v[48:51], a[196:199], v[216:219], v[48:51]\n\t"
    "s_sub_u32 s60, s60, 2\n\t"
    "ds_read_b128 v[240:243], v246 offset:5632\n\t"
    "v_mul_f32 v19, s89, v19\n\t"
    "v_mfma_f32_16x16x32_bf16 v[48:51], a[200:203], v[220:223], v[48:51]\n\t"
    "s_sub_u32 s61, s60, s46\n\t"
    "s_cselect_b32 s60, s60, s61\n\t"
    "v_mul_f32 v20, s89, v20\n\t"
    "v_mul_f32 v21, s89, v21\n\t"
    "v_mfma_f32_16x16x32_bf16 v[48:51], a[204:207], v[224:227], v[48:51]\n\t"
    "s_mul_i32 s50, s60, 16384\n\t"
    "v_mul_f32 v22, s89, v22\n\t"
    "v_mul_f32 v23, s89, v23\n\t"
    "v_mfma_f32_16x16x32_bf16 v[52:55], a[220:223], v[224:227], v[52:55]\n\t"
    "s_add_u32 s60, s47, s46\n\t"
    "v_permlane16_swap_b32 v4, v6\n\t"
    "v_mfma_f32_16x16x32_bf16 v[52:55], a[216:219], v[220:223], v[52:55]\n\t"
    "s_sub_u32 s60, s60, 1\n\t"
    "v_permlane16_swap_b32 v5, v7\n\t"
    "v_mfma_f32_16x16x32_bf16 v[52:55], a[212:215], v[216:219], v[52:55]\n\t"
    "s_sub_u32 s61, s60, s46\n\t"
    "s_cselect_b32 s60, s60, s61\n\t"
    "v_cvt_pk_bf16_f32 v8, v16, v17\n\t"
    "v_cvt_pk_bf16_f32 v9, v18, v19\n\t"
    "v_mfma_f32_16x16x32_bf16 v[52:55], a[208:211], v[212:215], v[52:55]\n\t"
    "s_mul_i32 s59, s60, 16384\n\t"
    "v_cvt_pk_bf16_f32 v10, v20, v21\n\t"
    "v_cvt_pk_bf16_f32 v11, v22, v23\n\t"
    "v_mfma_f32_16x16x32_bf16 v[56:59], a[224:227], v[212:215], v[56:59]\n\t"
    "s_cmp_ge_u32 s95, s8\n\t"
    "s_cmov_b32 s49, s50\n\t"
    "v_mul_f32 v24, s89, v24\n\t"
    "v_mul_f32 v25, s89, v25\n\t"
    "v_mfma_f32_16x16x32_bf16 v[56:59], a[228:231], v[216:219], v[56:59]\n\t"
    "s_lshl_b32 s60, s41, 2\n\t"
    "v_mul_f32 v26, s89, v26\n\t"
    "v_mul_f32 v27, s89, v27\n\t"
    "v_mfma_f32_16x16x32_bf16 v[56:59], a[232:235], v[220:223], v[56:59]\n\t"
    "s_add_u32 s60, s60, s95\n\t"
    "v_mul_f32 v28, s89, v28\n\t"
    "v_mul_f32 v29, s89, v29\n\t"
    "v_mfma_f32_16x16x32_bf16 v[56:59], a[236:239], v[224:227], v[56:59]\n\t"
    "s_lshl_b32 s60, s60, 1\n\t"
    "v_mul_f32 v30, s89, v30\n\t"
    "v_mul_f32 v31, s89, v31\n\t"
    "v_mfma_f32_16x16x32_bf16 v[60:63], a[252:255], v[224:227], v[60:63]\n\t"
    "s_add_u32 s60, s60, s45\n\t"
    "v_permlane16_swap_b32 v8, v10\n\t"
    "v_mfma_f32_16x16x32_bf16 v[60:63], a[248:251], v[220:223], v[60:63]\n\t"
    "s_mul_i32 s60, s60, 327680\n\t"
    "v_permlane16_swap_b32 v9, v11\n\t"
    "v_mfma_f32_16x16x32_bf16 v[60:63], a[244:247], v[216:219], v[60:63]\n\t"
    "s_waitcnt lgkmcnt(0)\n\t"
    "v_cvt_pk_bf16_f32 v12, v24, v25\n\t"
    "v_cvt_pk_bf16_f32 v13, v26, v27\n\t"
    "v_mfma_f32_16x16x32_bf16 v[60:63], a[240:243], v[212:215], v[60:63]\n\t"
    "v_cvt_pk_bf16_f32 v14, v28, v29\n\t"
    "v_cvt_pk_bf16_f32 v15, v30, v31\n\t"
    "s_nop 1\n\t"
    "v_permlane16_swap_b32 v12, v14\n\t"
    "v_permlane16_swap_b32 v13, v15\n\t"
    "v_mfma_f32_16x16x32_bf16 v[64:67], a[128:131], v[228:231], v[64:67]\n\t"
    "s_add_u32 s60, s60, s51\n\t"
    "v_mul_f32 v32, s89, v32\n\t"
    "v_mul_f32 v33, s89, v33\n\t"
    "v_mfma_f32_16x16x32_bf16 v[64:67], a[132:135], v[232:235], v[64:67]\n\t"
    "s_add_u32 s20, s80, s60\n\t"
    "s_addc_u32 s21, s81, 0\n\t"
    "v_mul_f32 v34, s89, v34\n\t"
    "v_mul_f32 v35, s89, v35\n\t"
    "v_mfma_f32_16x16x32_bf16 v[64:67], a[136:139], v[236:239], v[64:67]\n\t"
    "s_mov_b32 s22, 303104\n\t"
    "v_mul_f32 v36, s89, v36\n\t"
    "v_mul_f32 v37, s89, v37\n\t"
    "v_mfma_f32_16x16x32_bf16 v[64:67], a[140:143], v[240:243], v[64:67]\n\t"
    "s_mul_i32 s90, s45, 131072\n\t"
    "v_mul_f32 v38, s89, v38\n\t"
    "v_mul_f32 v39, s89, v39\n\t"
    "v_mfma_f32_16x16x32_bf16 v[68:71], a[156:159], v[240:243], v[68:71]\n\t"
    "s_add_u32 s90, s90, s51\n\t"
    "v_cvt_pk_bf16_f32 v32, v32, v33\n\t"
    "v_cvt_pk_bf16_f32 v33, v34, v35\n\t"
    "v_mfma_f32_16x16x32_bf16 v[68:71], a[152:155], v[236:239], v[68:71]\n\t"
    "s_mul_i32 s91, s45, 4294868992\n\t"
    "v_cvt_pk_bf16_f32 v34, v36, v37\n\t"
    "v_cvt_pk_bf16_f32 v35, v38, v39\n\t"
    "v_mfma_f32_16x16x32_bf16 v[68:71], a[148:151], v[232:235], v[68:71]\n\t"
    "s_add_u32 s91, s91, s51\n\t"
    "v_mul_f32 v40, s89, v40\n\t"
    "v_mul_f32 v41, s89, v41\n\t"
    "v_mfma_f32_16x16x32_bf16 v[68:71], a[144:147], v[228:231], v[68:71]\n\t"
    "s_add_u32 s91, s91, 98304\n\t"
    "v_mul_f32 v42, s89, v42\n\t"
    "v_mul_f32 v43, s89, v43\n\t"
    "v_mfma_f32_16x16x32_bf16 v[72:75], a[160:163], v[228:231], v[72:75]\n\t"
    "s_mul_i32 s99, s45, 4294934528\n\t"
    "v_mul_f32 v44, s89, v44\n\t"
    "v_mul_f32 v45, s89, v45\n\t"
    "v_mfma_f32_16x16x32_bf16 v[72:75], a[164:167], v[232:235], v[72:75]\n\t"
    "s_add_u32 s99, s99, s51\n\t"
    "v_mul_f32 v46, s89, v46\n\t"
    "v_mul_f32 v47, s89, v47\n\t"
    "v_mfma_f32_16x16x32_bf16 v[72:75], a[168:171], v[236:239], v[72:75]\n\t"
    "s_add_u32 s99, s99, 131072\n\t"
    "v_permlane16_swap_b32 v32, v34\n\t"
    "v_mfma_f32_16x16x32_bf16 v[72:75], a[172:175], v[240:243], v[72:75]\n\t"
    "s_mov_b32 s48, 4294938624\n\t"
    "v_permlane16_swap_b32 v33, v35\n\t"
    "v_mfma_f32_16x16x32_bf16 v[76:79], a[188:191], v[240:243], v[76:79]\n\t"
    "s_mov_b32 s57, 1048576\n\t"
    "v_cvt_pk_bf16_f32 v36, v40, v41\n\t"
    "v_cvt_pk_bf16_f32 v37, v42, v43\n\t"
    "v_mfma_f32_16x16x32_bf16 v[76:79], a[184:187], v[236:239], v[76:79]\n\t"
    "s_lshl_b32 s60, s41, 3\n\t"
    "v_cvt_pk_bf16_f32 v38, v44, v45\n\t"
    "v_cvt_pk_bf16_f32 v39, v46, v47\n\t"
    "v_mfma_f32_16x16x32_bf16 v[76:79], a[180:183], v[232:235], v[76:79]\n\t"
    "s_waitcnt vmcnt(4)\n\t"
    "ds_read_b128 v[212:215], v246 offset:8192\n\t"
    "v_mul_f32 v48, s89, v48\n\t"
    "v_mfma_f32_16x16x32_bf16 v[76:79], a[176:179], v[228:231], v[76:79]\n\t"
    "s_add_u32 s60, s60, s96\n\t"
    "ds_read_b128 v[216:219], v246 offset:8704\n\t"
    "v_mul_f32 v49, s89, v49\n\t"
    "v_mfma_f32_16x16x32_bf16 v[80:83], a[192:195], v[228:231], v[80:83]\n\t"
    "s_mul_i32 s60, s60, 160\n\t"
    "ds_read_b128 v[220:223], v246 offset:9216\n\t"
    "v_mul_f32 v50, s89, v50\n\t"
    "v_mfma_f32_16x16x32_bf16 v[80:83], a[196:199], v[232:235], v[80:83]\n\t"
    "s_add_u32 s60, s60, s40\n\t"
    "ds_read_b128 v[224:227], v246 offset:9728\n\t"
    "v_mul_f32 v51, s89, v51\n\t"
    "v_mfma_f32_16x16x32_bf16 v[80:83], a[200:203], v[236:239], v[80:83]\n\t"
    "s_mul_i32 s60, s60, 1024\n\t"
    "v_mul_f32 v52, s89, v52\n\t"
    "v_mul_f32 v53, s89, v53\n\t"
    "v_mfma_f32_16x16x32_bf16 v[80:83], a[204:207], v[240:243], v[80:83]\n\t"
    "s_add_u32 s60, s60, 25165824\n\t"
    "v_mul_f32 v54, s89, v54\n\t"
    "v_mul_f32 v55, s89, v55\n\t"
    "v_mfma_f32_16x16x32_bf16 v[84:87], a[220:223], v[240:243], v[84:87]\n\t"
    "s_add_u32 s68, s80, s60\n\t"
    "s_addc_u32 s69, s81, 0\n\t"
    "v_permlane16_swap_b32 v36, v38\n\t"
    "v_mfma_f32_16x16x32_bf16 v[84:87], a[216:219], v[236:239], v[84:87]\n\t"
    "s_mov_b32 s70, 160768\n\t"
    "v_permlane16_swap_b32 v37, v39\n\t"
    "v_mfma_f32_16x16x32_bf16 v[84:87], a[212:215], v[232:235], v[84:87]\n\t"
    "s_mul_i32 s60, s41, 304\n\t"
    "v_cvt_pk_bf16_f32 v40, v48, v49\n\t"
    "v_cvt_pk_bf16_f32 v41, v50, v51\n\t"
    "v_mfma_f32_16x16x32_bf16 v[84:87], a[208:211], v[228:231], v[84:87]\n\t"
    "s_mul_i32 s61, s45, 160\n\t"
    "v_cvt_pk_bf16_f32 v42, v52, v53\n\t"
    "v_cvt_pk_bf16_f32 v43, v54, v55\n\t"
    "v_mfma_f32_16x16x32_bf16 v[88:91], a[224:227], v[228:231], v[88:91]\n\t"
    "s_add_u32 s60, s60, s61\n\t"
    "v_mul_f32 v56, s89, v56\n\t"
    "v_mul_f32 v57, s89, v57\n\t"
    "v_mfma_f32_16x16x32_bf16 v[88:91], a[228:231], v[232:235], v[88:91]\n\t"
    "s_lshl_b32 s60, s60, 10\n\t"
    "v_mul_f32 v58, s89, v58\n\t"
    "v_mul_f32 v59, s89, v59\n\t"
    "v_mfma_f32_16x16x32_bf16 v[88:91], a[232:235], v[236:239], v[88:91]\n\t"
    "s_and_b32 s61, s96, 3\n\t"
    "v_mul_f32 v60, s89, v60\n\t"
    "v_mul_f32 v61, s89, v61\n\t"
    "v_mfma_f32_16x16x32_bf16 v[88:91], a[236:239], v[240:243], v[88:91]\n\t"
    "s_lshl_b32 s61, s61, 2\n\t"
    "v_mul_f32 v62, s89, v62\n\t"
    "v_mul_f32 v63, s89, v63\n\t"
    "v_mfma_f32_16x16x32_bf16 v[92:95], a[252:255], v[240:243], v[92:95]\n\t"
    "s_add_u32 s61, s61, s40\n\t"
    "v_permlane16_swap_b32 v40, v42\n\t"
    "v_mfma_f32_16x16x32_bf16 v[92:95], a[248:251], v[236:239], v[92:95]\n\t"
    "s_lshl_b32 s61, s61, 6\n\t"
    "v_permlane16_swap_b32 v41, v43\n\t"
    "v_mfma_f32_16x16x32_bf16 v[92:95], a[244:247], v[232:235], v[92:95]\n\t"
    "s_add_u32 s60, s60, s61\n\t"
    "v_cvt_pk_bf16_f32 v44, v56, v57\n\t"
    "v_cvt_pk_bf16_f32 v45, v58, v59\n\t"
    "v_mfma_f32_16x16x32_bf16 v[92:95], a[240:243], v[228:231], v[92:95]\n\t"
    "s_add_u32 s72, s78, s60\n\t"
    "s_addc_u32 s73, s79, 0\n\t"
    "v_cvt_pk_bf16_f32 v46, v60, v61\n\t"
    "v_cvt_pk_bf16_f32 v47, v62, v63\n\t"
    "v_mfma_f32_16x16x32_bf16 v[180:183], a[32:35], v[0:3], v[180:183]\n\t"
    "s_nop 1\n\t"
    "v_permlane16_swap_b32 v44, v46\n\t"
    "v_mfma_f32_16x16x32_bf16 v[180:183], a[36:39], v[4:7], v[180:183]\n\t"
    "s_sub_u32 s74, 2490368, s60\n\t"
    "v_permlane16_swap_b32 v45, v47\n\t"
    "v_mfma_f32_16x16x32_bf16 v[180:183], a[40:43], v[8:11], v[180:183]\n\t"
    "s_waitcnt lgkmcnt(0)\n\t"
    "v_mfma_f32_16x16x32_bf16 v[180:183], a[44:47], v[12:15], v[180:183]\n\t"
    "v_mfma_f32_16x16x32_bf16 v[96:99], a[128:131], v[212:215], v[96:99]\n\t"
    "v_mul_f32 v64, s89, v64\n\t"
    "v_mul_f32 v65, s89, v65\n\t"
    "v_mfma_f32_16x16x32_bf16 v[96:99], a[132:135], v[216:219], v[96:99]\n\t"
    "v_mul_f32 v66, s89, v66\n\t"
    "v_mul_f32 v67, s89, v67\n\t"
    "v_mfma_f32_16x16x32_bf16 v[96:99], a[136:139], v[220:223], v[96:99]\n\t"
    "v_mul_f32 v68, s89, v68\n\t"
    "v_mul_f32 v69, s89, v69\n\t"
    "v_mfma_f32_16x16x32_bf16 v[96:99], a[140:143], v[224:227], v[96:99]\n\t"
    "v_mul_f32 v70, s89, v70\n\t"
    "v_mul_f32 v71, s89, v71\n\t"
    "v_mfma_f32_16x16x32_bf16 v[100:103], a[156:159], v[224:227], v[100:103]\n\t"
    "v_cvt_pk_bf16_f32 v64, v64, v65\n\t"
    "v_cvt_pk_bf16_f32 v65, v66, v67\n\t"
    "v_mfma_f32_16x16x32_bf16 v[100:103], a[152:155], v[220:223], v[100:103]\n\t"
    "v_cvt_pk_bf16_f32 v66, v68, v69\n\t"
    "v_cvt_pk_bf16_f32 v67, v70, v71\n\t"
    "v_mfma_f32_16x16x32_bf16 v[100:103], a[148:151], v[216:219], v[100:103]\n\t"
    "v_mul_f32 v72, s89, v72\n\t"
    "v_mul_f32 v73, s89, v73\n\t"
    "v_mfma_f32_16x16x32_bf16 v[100:103], a[144:147], v[212:215], v[100:103]\n\t"
    "v_mul_f32 v74, s89, v74\n\t"
    "v_mul_f32 v75, s89, v75\n\t"
    "v_mfma_f32_16x16x32_bf16 v[104:107], a[160:163], v[212:215], v[104:107]\n\t"
    "v_mul_f32 v76, s89, v76\n\t"
    "v_mul_f32 v77, s89, v77\n\t"
    "v_mfma_f32_16x16x32_bf16 v[104:107], a[164:167], v[216:219], v[104:107]\n\t"
    "v_mul_f32 v78, s89, v78\n\t"
    "v_mul_f32 v79, s89, v79\n\t"
    "v_mfma_f32_16x16x32_bf16 v[104:107], a[168:171], v[220:223], v[104:107]\n\t"
    "v_permlane16_swap_b32 v64, v66\n\t"
    "v_mfma_f32_16x16x32_bf16 v[104:107], a[172:175], v[224:227], v[104:107]\n\t"
    "v_permlane16_swap_b32 v65, v67\n\t"
    "v_mfma_f32_16x16x32_bf16 v[108:111], a[188:191], v[224:227], v[108:111]\n\t"
    "v_cvt_pk_bf16_f32 v68, v72, v73\n\t"
    "v_cvt_pk_bf16_f32 v69, v74, v75\n\t"
    "v_mfma_f32_16x16x32_bf16 v[108:111], a[184:187], v[220:223], v[108:111]\n\t"
    "v_cvt_pk_bf16_f32 v70, v76, v77\n\t"
    "v_cvt_pk_bf16_f32 v71, v78, v79\n\t"
    "v_mfma_f32_16x16x32_bf16 v[108:111], a[180:183], v[216:219], v[108:111]\n\t"
    "s_waitcnt vmcnt(0)\n\t"
    "ds_read_b128 v[228:231], v246 offset:12288\n\t"
    "v_mul_f32 v80, s89, v80\n\t"
    "v_mfma_f32_16x16x32_bf16 v[108:111], a[176:179], v[212:215], v[108:111]\n\t"
    "ds_read_b128 v[232:235], v246 offset:12800\n\t"
    "v_mul_f32 v81, s89, v81\n\t"
    "v_mfma_f32_16x16x32_bf16 v[112:115], a[192:195], v[212:215], v[112:115]\n\t"
    "ds_read_b128 v[236:239], v246 offset:13312\n\t"
    "v_mul_f32 v82, s89, v82\n\t"
    "v_mfma_f32_16x16x32_bf16 v[112:115], a[196:199], v[216:219], v[112:115]\n\t"
    "ds_read_b128 v[240:243], v246 offset:13824\n\t"
    "v_mul_f32 v83, s89, v83\n\t"
    "v_mfma_f32_16x16x32_bf16 v[112:115], a[200:203], v[220:223], v[112:115]\n\t"
    "v_mul_f32 v84, s89, v84\n\t"
    "v_mul_f32 v85, s89, v85\n\t"
    "v_mfma_f32_16x16x32_bf16 v[112:115], a[204:207], v[224:227], v[112:115]\n\t"
    "v_mul_f32 v86, s89, v86\n\t"
    "v_mul_f32 v87, s89, v87\n\t"
    "v_mfma_f32_16x16x32_bf16 v[116:119], a[220:223], v[224:227], v[116:119]\n\t"
    "v_permlane16_swap_b32 v68, v70\n\t"
    "v_mfma_f32_16x16x32_bf16 v[116:119], a[216:219], v[220:223], v[116:119]\n\t"
    "v_permlane16_swap_b32 v69, v71\n\t"
    "v_mfma_f32_16x16x32_bf16 v[116:119], a[212:215], v[216:219], v[116:119]\n\t"
    "v_cvt_pk_bf16_f32 v72, v80, v81\n\t"
    "v_cvt_pk_bf16_f32 v73, v82, v83\n\t"
    "v_mfma_f32_16x16x32_bf16 v[116:119], a[208:211], v[212:215], v[116:119]\n\t"
    "v_cvt_pk_bf16_f32 v74, v84, v85\n\t"
    "v_cvt_pk_bf16_f32 v75, v86, v87\n\t"
    "v_mfma_f32_16x16x32_bf16 v[120:123], a[224:227], v[212:215], v[120:123]\n\t"
    "v_mul_f32 v88, s89, v88\n\t"
    "v_mul_f32 v89, s89, v89\n\t"
    "v_mfma_f32_16x16x32_bf16 v[120:123], a[228:231], v[216:219], v[120:123]\n\t"
    "v_mul_f32 v90, s89, v90\n\t"
    "v_mul_f32 v91, s89, v91\n\t"
    "v_mfma_f32_16x16x32_bf16 v[120:123], a[232:235], v[220:223], v[120:123]\n\t"
    "v_mul_f32 v92, s89, v92\n\t"
    "v_mul_f32 v93, s89, v93\n\t"
    "v_mfma_f32_16x16x32_bf16 v[120:123], a[236:239], v[224:227], v[120:123]\n\t"
    "v_mul_f32 v94, s89, v94\n\t"
    "v_mul_f32 v95, s89, v95\n\t"
    "v_mfma_f32_16x16x32_bf16 v[124:127], a[252:255], v[224:227], v[124:127]\n\t"
    "v_permlane16_swap_b32 v72, v74\n\t"
    "v_mfma_f32_16x16x32_bf16 v[124:127], a[248:251], v[220:223], v[124:127]\n\t"
    "v_permlane16_swap_b32 v73, v75\n\t"
    "v_mfma_f32_16x16x32_bf16 v[124:127], a[244:247], v[216:219], v[124:127]\n\t"
    "v_cvt_pk_bf16_f32 v76, v88, v89\n\t"
    "v_cvt_pk_bf16_f32 v77, v90, v91\n\t"
    "v_mfma_f32_16x16x32_bf16 v[124:127], a[240:243], v[212:215], v[124:127]\n\t"
    "v_cvt_pk_bf16_f32 v78, v92, v93\n\t"
    "v_cvt_pk_bf16_f32 v79, v94, v95\n\t"
    "v_mfma_f32_16x16x32_bf16 v[184:187], a[32:35], v[32:35], v[184:187]\n\t"
    "s_nop 1\n\t"
    "v_permlane16_swap_b32 v76, v78\n\t"
    "v_mfma_f32_16x16x32_bf16 v[184:187], a[36:39], v[36:39], v[184:187]\n\t"
    "v_permlane16_swap_b32 v77, v79\n\t"
    "v_mfma_f32_16x16x32_bf16 v[184:187], a[40:43], v[40:43], v[184:187]\n\t"
    "s_waitcnt lgkmcnt(0)\n\t"
    "v_mfma_f32_16x16x32_bf16 v[184:187], a[44:47], v[44:47], v[184:187]\n\t"
    "v_mfma_f32_16x16x32_bf16 v[128:131], a[128:131], v[228:231], v[128:131]\n\t"
    "v_mul_f32 v96, s89, v96\n\t"
    "v_mul_f32 v97, s89, v97\n\t"
    "v_mfma_f32_16x16x32_bf16 v[128:131], a[132:135], v[232:235], v[128:131]\n\t"
    "v_mul_f32 v98, s89, v98\n\t"
    "v_mul_f32 v99, s89, v99\n\t"
    "v_mfma_f32_16x16x32_bf16 v[128:131], a[136:139], v[236:239], v[128:131]\n\t"
    "v_mul_f32 v100, s89, v100\n\t"
    "v_mul_f32 v101, s89, v101\n\t"
    "v_mfma_f32_16x16x32_bf16 v[128:131], a[140:143], v[240:243], v[128:131]\n\t"
    "v_mul_f32 v102, s89, v102\n\t"
    "v_mul_f32 v103, s89, v103\n\t"
    "v_mfma_f32_16x16x32_bf16 v[132:135], a[156:159], v[240:243], v[132:135]\n\t"
    "v_cvt_pk_bf16_f32 v96, v96, v97\n\t"
    "v_cvt_pk_bf16_f32 v97, v98, v99\n\t"
    "v_mfma_f32_16x16x32_bf16 v[132:135], a[152:155], v[236:239], v[132:135]\n\t"
    "v_cvt_pk_bf16_f32 v98, v100, v101\n\t"
    "v_cvt_pk_bf16_f32 v99, v102, v103\n\t"
    "v_mfma_f32_16x16x32_bf16 v[132:135], a[148:151], v[232:235], v[132:135]\n\t"
    "v_mul_f32 v104, s89, v104\n\t"
    "v_mul_f32 v105, s89, v105\n\t"
    "v_mfma_f32_16x16x32_bf16 v[132:135], a[144:147], v[228:231], v[132:135]\n\t"
    "v_mul_f32 v106, s89, v106\n\t"
    "v_mul_f32 v107, s89, v107\n\t"
    "v_mfma_f32_16x16x32_bf16 v[136:139], a[160:163], v[228:231], v[136:139]\n\t"
    "v_mul_f32 v108, s89, v108\n\t"
    "v_mul_f32 v109, s89, v109\n\t"
    "v_mfma_f32_16x16x32_bf16 v[136:139], a[164:167], v[232:235], v[136:139]\n\t"
    "v_mul_f32 v110, s89, v110\n\t"
    "v_mul_f32 v111, s89, v111\n\t"
    "v_mfma_f32_16x16x32_bf16 v[136:139], a[168:171], v[236:239], v[136:139]\n\t"
    "v_permlane16_swap_b32 v96, v98\n\t"
    "v_mfma_f32_16x16x32_bf16 v[136:139], a[172:175], v[240:243], v[136:139]\n\t"
    "v_permlane16_swap_b32 v97, v99\n\t"
    "v_mfma_f32_16x16x32_bf16 v[140:143], a[188:191], v[240:243], v[140:143]\n\t"
    "v_cvt_pk_bf16_f32 v100, v104, v105\n\t"
    "v_cvt_pk_bf16_f32 v101, v106, v107\n\t"
    "v_mfma_f32_16x16x32_bf16 v[140:143], a[184:187], v[236:239], v[140:143]\n\t"
    "v_cvt_pk_bf16_f32 v102, v108, v109\n\t"
    "v_cvt_pk_bf16_f32 v103, v110, v111\n\t"
    "v_mfma_f32_16x16x32_bf16 v[140:143], a[180:183], v[232:235], v[140:143]\n\t"
    "v_mul_f32 v112, s89, v112\n\t"
    "v_mul_f32 v113, s89, v113\n\t"
    "v_mfma_f32_16x16x32_bf16 v[140:143], a[176:179], v[228:231], v[140:143]\n\t"
    "v_mul_f32 v114, s89, v114\n\t"
    "v_mul_f32 v115, s89, v115\n\t"
    "v_mfma_f32_16x16x32_bf16 v[144:147], a[192:195], v[228:231], v[144:147]\n\t"
    "v_mul_f32 v116, s89, v116\n\t"
    "v_mul_f32 v117, s89, v117\n\t"
    "v_mfma_f32_16x16x32_bf16 v[144:147], a[196:199], v[232:235], v[144:147]\n\t"
    "v_mul_f32 v118, s89, v118\n\t"
    "v_mul_f32 v119, s89, v119\n\t"
    "v_mfma_f32_16x16x32_bf16 v[144:147], a[200:203], v[236:239], v[144:147]\n\t"
    "v_permlane16_swap_b32 v100, v102\n\t"
    "v_mfma_f32_16x16x32_bf16 v[144:147], a[204:207], v[240:243], v[144:147]\n\t"
    "s_barrier\n\t"
    "ds_read_b128 a[0:3], v252 offset:0\n\t"
    "v_mfma_f32_16x16x32_bf16 v[148:151], a[220:223], v[240:243], v[148:151]\n\t"
    "ds_read_b128 a[4:7], v252 offset:512\n\t"
    "v_mfma_f32_16x16x32_bf16 v[148:151], a[216:219], v[236:239], v[148:151]\n\t"
    "ds_read_b128 a[8:11], v252 offset:1024\n\t"
    "v_mfma_f32_16x16x32_bf16 v[148:151], a[212:215], v[232:235], v[148:151]\n\t"
    "ds_read_b128 a[12:15], v252 offset:1536\n\t"
    "v_mfma_f32_16x16x32_bf16 v[148:151], a[208:211], v[228:231], v[148:151]\n\t"
    "v_permlane16_swap_b32 v101, v103\n\t"
    "v_mfma_f32_16x16x32_bf16 v[152:155], a[224:227], v[228:231], v[152:155]\n\t"
    "v_cvt_pk_bf16_f32 v104, v112, v113\n\t"
    "v_cvt_pk_bf16_f32 v105, v114, v115\n\t"
    "v_mfma_f32_16x16x32_bf16 v[152:155], a[228:231], v[232:235], v[152:155]\n\t"
    "v_cvt_pk_bf16_f32 v106, v116, v117\n\t"
    "v_cvt_pk_bf16_f32 v107, v118, v119\n\t"
    "v_mfma_f32_16x16x32_bf16 v[152:155], a[232:235], v[236:239], v[152:155]\n\t"
    "v_mul_f32 v120, s89, v120\n\t"
    "v_mul_f32 v121, s89, v121\n\t"
    "v_mfma_f32_16x16x32_bf16 v[152:155], a[236:239], v[240:243], v[152:155]\n\t"
    "v_mul_f32 v122, s89, v122\n\t"
    "v_mul_f32 v123, s89, v123\n\t"
    "v_mfma_f32_16x16x32_bf16 v[156:159], a[252:255], v[240:243], v[156:159]\n\t"
    "v_mul_f32 v124, s89, v124\n\t"
    "v_mul_f32 v125, s89, v125\n\t"
    "v_mfma_f32_16x16x32_bf16 v[156:159], a[248:251], v[236:239], v[156:159]\n\t"
    "v_mul_f32 v126, s89, v126\n\t"
    "v_mul_f32 v127, s89, v127\n\t"
    "v_mfma_f32_16x16x32_bf16 v[156:159], a[244:247], v[232:235], v[156:159]\n\t"
    "v_permlane16_swap_b32 v104, v106\n\t"
    "v_mfma_f32_16x16x32_bf16 v[156:159], a[240:243], v[228:231], v[156:159]\n\t"
    "v_permlane16_swap_b32 v105, v107\n\t"
    "v_mfma_f32_16x16x32_bf16 v[188:191], a[32:35], v[64:67], v[188:191]\n\t"
    "v_cvt_pk_bf16_f32 v108, v120, v121\n\t"
    "v_cvt_pk_bf16_f32 v109, v122, v123\n\t"
    "v_mfma_f32_16x16x32_bf16 v[188:191], a[36:39], v[68:71], v[188:191]\n\t"
    "v_cvt_pk_bf16_f32 v110, v124, v125\n\t"
    "v_cvt_pk_bf16_f32 v111, v126, v127\n\t"
    "v_mfma_f32_16x16x32_bf16 v[188:191], a[40:43], v[72:75], v[188:191]\n\t"
    "s_nop 1\n\t"
    "v_permlane16_swap_b32 v108, v110\n\t"
    "v_mfma_f32_16x16x32_bf16 v[188:191], a[44:47], v[76:79], v[188:191]\n\t"
    "v_permlane16_swap_b32 v109, v111\n\t"
    "v_mfma_f32_16x16x32_bf16 v[192:195], a[32:35], v[96:99], v[192:195]\n\t"
    "s_lshl_b32 s60, s95, 20\n\t"
    "v_mul_f32 v128, s89, v128\n\t"
    "v_mul_f32 v129, s89, v129\n\t"
    "v_mfma_f32_16x16x32_bf16 v[192:195], a[36:39], v[100:103], v[192:195]\n\t"
    "s_and_b32 s61, s96, 3\n\t"
    "v_mul_f32 v130, s89, v130\n\t"
    "v_mul_f32 v131, s89, v131\n\t"
    "v_mfma_f32_16x16x32_bf16 v[192:195], a[40:43], v[104:107], v[192:195]\n\t"
    "s_waitcnt lgkmcnt(0)\n\t"
    "v_mul_f32 v132, s89, v132\n\t"
    "v_mul_f32 v133, s89, v133\n\t"
    "v_mfma_f32_16x16x32_bf16 v[192:195], a[44:47], v[108:111], v[192:195]\n\t"
    "s_lshl_b32 s61, s61, 8\n\t"
    "v_mul_f32 v134, s89, v134\n\t"
    "v_mul_f32 v135, s89, v135\n\t"
    "v_cvt_pk_bf16_f32 v128, v128, v129\n\t"
    "v_cvt_pk_bf16_f32 v129, v130, v131\n\t"
    "v_cvt_pk_bf16_f32 v130, v132, v133\n\t"
    "v_cvt_pk_bf16_f32 v131, v134, v135\n\t"
    "v_mul_f32 v136, s89, v136\n\t"
    "v_mul_f32 v137, s89, v137\n\t"
    "v_mul_f32 v138, s89, v138\n\t"
    "v_mul_f32 v139, s89, v139\n\t"
    "v_mul_f32 v140, s89, v140\n\t"
    "v_mul_f32 v141, s89, v141\n\t"
    "v_mul_f32 v142, s89, v142\n\t"
    "v_mul_f32 v143, s89, v143\n\t"
    "v_mfma_f32_16x16x32_bf16 v[160:163], a[0:3], v[0:3], v[180:183]\n\t"
    "s_add_u32 s60, s60, s61\n\t"
    "v_permlane16_swap_b32 v128, v130\n\t"
    "v_mfma_f32_16x16x32_bf16 v[160:163], a[4:7], v[4:7], v[160:163]\n\t"
    "s_add_u32 s24, s76, s60\n\t"
    "s_addc_u32 s25, s77, 0\n\t"
    "v_permlane16_swap_b32 v129, v131\n\t"
    "v_mfma_f32_16x16x32_bf16 v[160:163], a[8:11], v[8:11], v[160:163]\n\t"
    "s_sub_u32 s26, 0x100000, s61\n\t"
    "v_cvt_pk_bf16_f32 v132, v136, v137\n\t"
    "v_cvt_pk_bf16_f32 v133, v138, v139\n\t"
    "v_mfma_f32_16x16x32_bf16 v[160:163], a[12:15], v[12:15], v[160:163]\n\t"
    "s_mov_b32 s54, 7\n\t"
    "ds_read_b128 a[16:19], v252 offset:4096\n\t"
    "v_cvt_pk_bf16_f32 v134, v140, v141\n\t"
    "v_cvt_pk_bf16_f32 v135, v142, v143\n\t"
    "v_mul_f32 v144, s89, v144\n\t"
    "v_mul_f32 v145, s89, v145\n\t"
    "v_mul_f32 v146, s89, v146\n\t"
    "v_mul_f32 v147, s89, v147\n\t"
    "v_mul_f32 v148, s89, v148\n\t"
    "v_mul_f32 v149, s89, v149\n\t"
    "v_mul_f32 v150, s89, v150\n\t"
    "v_mul_f32 v151, s89, v151\n\t"
    "v_permlane16_swap_b32 v132, v134\n\t"
    "v_permlane16_swap_b32 v133, v135\n\t"
    "v_cvt_pk_bf16_f32 v136, v144, v145\n\t"
    "v_cvt_pk_bf16_f32 v137, v146, v147\n\t"
    "v_cvt_pk_bf16_f32 v138, v148, v149\n\t"
    "v_mfma_f32_16x16x32_bf16 v[164:167], a[12:15], v[44:47], v[184:187]\n\t"
    "s_lshl_b32 s93, s40, 9\n\t"
    "v_cvt_pk_bf16_f32 v139, v150, v151\n\t"
    "v_mul_f32 v152, s89, v152\n\t"
    "v_mfma_f32_16x16x32_bf16 v[164:167], a[8:11], v[40:43], v[164:167]\n\t"
    "s_sub_u32 s92, s93, 0x10000\n\t"
    "v_mul_f32 v153, s89, v153\n\t"
    "v_mul_f32 v154, s89, v154\n\t"
    "v_mfma_f32_16x16x32_bf16 v[164:167], a[4:7], v[36:39], v[164:167]\n\t"
    "s_add_u32 s94, s93, 0x10000\n\t"
    "v_mul_f32 v155, s89, v155\n\t"
    "v_mul_f32 v156, s89, v156\n\t"
    "v_mfma_f32_16x16x32_bf16 v[164:167], a[0:3], v[32:35], v[164:167]\n\t"
    "ds_read_b128 a[20:23], v252 offset:4608\n\t"
    "v_mul_f32 v157, s89, v157\n\t"
    "v_mul_f32 v158, s89, v158\n\t"
    "v_mul_f32 v159, s89, v159\n\t"
    "v_permlane16_swap_b32 v136, v138\n\t"
    "v_permlane16_swap_b32 v137, v139\n\t"
    "v_cvt_pk_bf16_f32 v140, v152, v153\n\t"
    "v_cvt_pk_bf16_f32 v141, v154, v155\n\t"
    "v_cvt_pk_bf16_f32 v142, v156, v157\n\t"
    "v_cvt_pk_bf16_f32 v143, v158, v159\n\t"
    "s_nop 1\n\t"
    "v_permlane16_swap_b32 v140, v142\n\t"
    "v_permlane16_swap_b32 v141, v143\n\t"
    "s_nop 1\n\t"
    "v_mfma_f32_16x16x32_bf16 v[196:199], a[32:35], v[128:131], v[196:199]\n\t"
    "v_mfma_f32_16x16x32_bf16 v[196:199], a[36:39], v[132:135], v[196:199]\n\t"
    "v_mfma_f32_16x16x32_bf16 v[196:199], a[40:43], v[136:139], v[196:199]\n\t"
    "v_mfma_f32_16x16x32_bf16 v[196:199], a[44:47], v[140:143], v[196:199]\n\t"
    "v_mfma_f32_16x16x32_bf16 v[168:171], a[0:3], v[64:67], v[188:191]\n\t"
    "v_exp_f32 v160, v160\n\t"
    "v_mfma_f32_16x16x32_bf16 v[168:171], a[4:7], v[68:71], v[168:171]\n\t"
    "v_exp_f32 v161, v161\n\t"
    "v_mfma_f32_16x16x32_bf16 v[168:171], a[8:11], v[72:75], v[168:171]\n\t"
    "v_exp_f32 v162, v162\n\t"
    "v_mfma_f32_16x16x32_bf16 v[168:171], a[12:15], v[76:79], v[168:171]\n\t"
    "ds_read_b128 a[24:27], v252 offset:5120\n\t"
    "v_exp_f32 v163, v163\n\t"
    "v_add_f32 v200, v200, v160\n\t"
    "v_add_f32 v201, v201, v161\n\t"
    "v_cvt_pk_bf16_f32 v16, v160, v161\n\t"
    "v_add_f32 v200, v200, v162\n\t"
    "v_add_f32 v201, v201, v163\n\t"
    "v_cvt_pk_bf16_f32 v17, v162, v163\n\t"
    "v_mfma_f32_16x16x32_bf16 v[172:175], a[12:15], v[108:111], v[192:195]\n\t"
    "v_exp_f32 v164, v164\n\t"
    "v_mfma_f32_16x16x32_bf16 v[172:175], a[8:11], v[104:107], v[172:175]\n\t"
    "v_exp_f32 v165, v165\n\t"
    "v_mfma_f32_16x16x32_bf16 v[172:175], a[4:7], v[100:103], v[172:175]\n\t"
    "v_exp_f32 v166, v166\n\t"
    "v_mfma_f32_16x16x32_bf16 v[172:175], a[0:3], v[96:99], v[172:175]\n\t"
    "ds_read_b128 a[28:31], v252 offset:5632\n\t"
    "v_exp_f32 v167, v167\n\t"
    "v_mfma_f32_32x32x16_bf16 a[96:111], a[48:51], a[48:51], 0\n\t"
    "v_add_f32 v202, v202, v164\n\t"
    "v_add_f32 v203, v203, v165\n\t"
    "v_cvt_pk_bf16_f32 v48, v164, v165\n\t"
    "v_add_f32 v202, v202, v166\n\t"
    "v_add_f32 v203, v203, v167\n\t"
    "v_cvt_pk_bf16_f32 v49, v166, v167\n\t"
    "v_mfma_f32_16x16x32_bf16 v[176:179], a[0:3], v[128:131], v[196:199]\n\t"
    "v_exp_f32 v168, v168\n\t"
    "v_mfma_f32_16x16x32_bf16 v[176:179], a[4:7], v[132:135], v[176:179]\n\t"
    "v_exp_f32 v169, v169\n\t"
    "v_mfma_f32_16x16x32_bf16 v[176:179], a[8:11], v[136:139], v[176:179]\n\t"
    "v_exp_f32 v170, v170\n\t"
    "v_mfma_f32_16x16x32_bf16 v[176:179], a[12:15], v[140:143], v[176:179]\n\t"
    "v_exp_f32 v171, v171\n\t"
    "v_mfma_f32_32x32x16_bf16 a[112:127], a[48:51], a[48:51], 0\n\t"
    "v_add_f32 v204, v204, v168\n\t"
    "v_add_f32 v205, v205, v169\n\t"
    "v_cvt_pk_bf16_f32 v80, v168, v169\n\t"
    "v_add_f32 v204, v204, v170\n\t"
    "v_add_f32 v205, v205, v171\n\t"
    "v_cvt_pk_bf16_f32 v81, v170, v171\n\t"
    "s_waitcnt lgkmcnt(0)\n\t"
    "v_mfma_f32_16x16x32_bf16 v[160:163], a[16:19], v[0:3], v[180:183]\n\t"
    "v_exp_f32 v172, v172\n\t"
    "v_mfma_f32_16x16x32_bf16 v[160:163], a[20:23], v[4:7], v[160:163]\n\t"
    "v_exp_f32 v173, v173\n\t"
    "v_mfma_f32_16x16x32_bf16 v[160:163], a[24:27], v[8:11], v[160:163]\n\t"
    "v_exp_f32 v174, v174\n\t"
    "v_mfma_f32_16x16x32_bf16 v[160:163], a[28:31], v[12:15], v[160:163]\n\t"
    "ds_read_b128 a[0:3], v252 offset:8192\n\t"
    "v_exp_f32 v175, v175\n\t"
    "v_mfma_f32_32x32x16_bf16 a[128:143], a[48:51], a[48:51], 0\n\t"
    "v_add_f32 v206, v206, v172\n\t"
    "v_add_f32 v207, v207, v173\n\t"
    "v_cvt_pk_bf16_f32 v112, v172, v173\n\t"
    "v_add_f32 v206, v206, v174\n\t"
    "v_add_f32 v207, v207, v175\n\t"
    "v_cvt_pk_bf16_f32 v113, v174, v175\n\t"
    "v_mfma_f32_16x16x32_bf16 v[164:167], a[28:31], v[44:47], v[184:187]\n\t"
    "v_exp_f32 v176, v176\n\t"
    "v_mfma_f32_16x16x32_bf16 v[164:167], a[24:27], v[40:43], v[164:167]\n\t"
    "v_exp_f32 v177, v177\n\t"
    "v_mfma_f32_16x16x32_bf16 v[164:167], a[20:23], v[36:39], v[164:167]\n\t"
    "v_exp_f32 v178, v178\n\t"
    "v_mfma_f32_16x16x32_bf16 v[164:167], a[16:19], v[32:35], v[164:167]\n\t"
    "ds_read_b128 a[4:7], v252 offset:8704\n\t"
    "v_exp_f32 v179, v179\n\t"
    "v_mfma_f32_32x32x16_bf16 a[144:159], a[48:51], a[48:51], 0\n\t"
    "v_add_f32 v208, v208, v176\n\t"
    "v_add_f32 v209, v209, v177\n\t"
    "v_cvt_pk_bf16_f32 v144, v176, v177\n\t"
    "v_add_f32 v208, v208, v178\n\t"
    "v_add_f32 v209, v209, v179\n\t"
    "v_cvt_pk_bf16_f32 v145, v178, v179\n\t"
    "v_mfma_f32_16x16x32_bf16 v[168:171], a[16:19], v[64:67], v[188:191]\n\t"
    "v_exp_f32 v160, v160\n\t"
    "v_mfma_f32_16x16x32_bf16 v[168:171], a[20:23], v[68:71], v[168:171]\n\t"
    "v_exp_f32 v161, v161\n\t"
    "v_mfma_f32_16x16x32_bf16 v[168:171], a[24:27], v[72:75], v[168:171]\n\t"
    "v_exp_f32 v162, v162\n\t"
    "v_mfma_f32_16x16x32_bf16 v[168:171], a[28:31], v[76:79], v[168:171]\n\t"
    "ds_read_b128 a[8:11], v252 offset:9216\n\t"
    "v_exp_f32 v163, v163\n\t"
    "v_mfma_f32_32x32x16_bf16 a[160:175], a[48:51], a[48:51], 0\n\t"
    "v_add_f32 v200, v200, v160\n\t"
    "v_add_f32 v201, v201, v161\n\t"
    "v_cvt_pk_bf16_f32 v18, v160, v161\n\t"
    "v_add_f32 v200, v200, v162\n\t"
    "v_add_f32 v201, v201, v163\n\t"
    "v_cvt_pk_bf16_f32 v19, v162, v163\n\t"
    "v_mfma_f32_16x16x32_bf16 v[172:175], a[28:31], v[108:111], v[192:195]\n\t"
    "v_exp_f32 v164, v164\n\t"
    "v_mfma_f32_16x16x32_bf16 v[172:175], a[24:27], v[104:107], v[172:175]\n\t"
    "v_exp_f32 v165, v165\n\t"
    "v_mfma_f32_16x16x32_bf16 v[172:175], a[20:23], v[100:103], v[172:175]\n\t"
    "v_exp_f32 v166, v166\n\t"
    "v_mfma_f32_16x16x32_bf16 v[172:175], a[16:19], v[96:99], v[172:175]\n\t"
    "ds_read_b128 a[12:15], v252 offset:9728\n\t"
    "v_exp_f32 v167, v167\n\t"
    "v_mfma_f32_32x32x16_bf16 a[176:191], a[48:51], a[48:51], 0\n\t"
    "v_add_f32 v202, v202, v164\n\t"
    "v_add_f32 v203, v203, v165\n\t"
    "v_cvt_pk_bf16_f32 v50, v164, v165\n\t"
    "v_add_f32 v202, v202, v166\n\t"
    "v_add_f32 v203, v203, v167\n\t"
    "v_cvt_pk_bf16_f32 v51, v166, v167\n\t"
    "v_mfma_f32_16x16x32_bf16 v[176:179], a[16:19], v[128:131], v[196:199]\n\t"
    "v_exp_f32 v168, v168\n\t"
    "v_mfma_f32_16x16x32_bf16 v[176:179], a[20:23], v[132:135], v[176:179]\n\t"
    "v_exp_f32 v169, v169\n\t"
    "v_mfma_f32_16x16x32_bf16 v[176:179], a[24:27], v[136:139], v[176:179]\n\t"
    "v_exp_f32 v170, v170\n\t"
    "v_mfma_f32_16x16x32_bf16 v[176:179], a[28:31], v[140:143], v[176:179]\n\t"
    "v_exp_f32 v171, v171\n\t"
    "v_mfma_f32_32x32x16_bf16 a[192:207], a[48:51], a[48:51], 0\n\t"
    "v_add_f32 v204, v204, v168\n\t"
    "v_add_f32 v205, v205, v169\n\t"
    "v_cvt_pk_bf16_f32 v82, v168, v169\n\t"
    "v_add_f32 v204, v204, v170\n\t"
    "v_add_f32 v205, v205, v171\n\t"
    "v_cvt_pk_bf16_f32 v83, v170, v171\n\t"
    "s_waitcnt lgkmcnt(0)\n\t"
    "v_mfma_f32_16x16x32_bf16 v[160:163], a[0:3], v[0:3], v[180:183]\n\t"
    "v_exp_f32 v172, v172\n\t"
    "v_mfma_f32_16x16x32_bf16 v[160:163], a[4:7], v[4:7], v[160:163]\n\t"
    "v_exp_f32 v173, v173\n\t"
    "v_mfma_f32_16x16x32_bf16 v[160:163], a[8:11], v[8:11], v[160:163]\n\t"
    "v_exp_f32 v174, v174\n\t"
    "v_mfma_f32_16x16x32_bf16 v[160:163], a[12:15], v[12:15], v[160:163]\n\t"
    "ds_read_b128 a[16:19], v252 offset:12288\n\t"
    "v_exp_f32 v175, v175\n\t"
    "v_mfma_f32_32x32x16_bf16 a[208:223], a[48:51], a[48:51], 0\n\t"
    "v_add_f32 v206, v206, v172\n\t"
    "v_add_f32 v207, v207, v173\n\t"
    "v_cvt_pk_bf16_f32 v114, v172, v173\n\t"
    "v_add_f32 v206, v206, v174\n\t"
    "v_add_f32 v207, v207, v175\n\t"
    "v_cvt_pk_bf16_f32 v115, v174, v175\n\t"
    "v_mfma_f32_16x16x32_bf16 v[164:167], a[12:15], v[44:47], v[184:187]\n\t"
    "v_exp_f32 v176, v176\n\t"
    "v_mfma_f32_16x16x32_bf16 v[164:167], a[8:11], v[40:43], v[164:167]\n\t"
    "v_exp_f32 v177, v177\n\t"
    "v_mfma_f32_16x16x32_bf16 v[164:167], a[4:7], v[36:39], v[164:167]\n\t"
    "v_exp_f32 v178, v178\n\t"
    "v_mfma_f32_16x16x32_bf16 v[164:167], a[0:3], v[32:35], v[164:167]\n\t"
    "ds_read_b128 a[20:23], v252 offset:12800\n\t"
    "v_exp_f32 v179, v179\n\t"
    "v_mfma_f32_32x32x16_bf16 a[224:239], a[48:51], a[48:51], 0\n\t"
    "v_add_f32 v208, v208, v176\n\t"
    "v_add_f32 v209, v209, v177\n\t"
    "v_cvt_pk_bf16_f32 v146, v176, v177\n\t"
    "v_add_f32 v208, v208, v178\n\t"
    "v_add_f32 v209, v209, v179\n\t"
    "v_cvt_pk_bf16_f32 v147, v178, v179\n\t"
    "v_mfma_f32_16x16x32_bf16 v[168:171], a[0:3], v[64:67], v[188:191]\n\t"
    "v_exp_f32 v160, v160\n\t"
    "v_mfma_f32_16x16x32_bf16 v[168:171], a[4:7], v[68:71], v[168:171]\n\t"
    "v_exp_f32 v161, v161\n\t"
    "v_mfma_f32_16x16x32_bf16 v[168:171], a[8:11], v[72:75], v[168:171]\n\t"
    "v_exp_f32 v162, v162\n\t"
    "v_mfma_f32_16x16x32_bf16 v[168:171], a[12:15], v[76:79], v[168:171]\n\t"
    "ds_read_b128 a[24:27], v252 offset:13312\n\t"
    "v_exp_f32 v163, v163\n\t"
    "v_mfma_f32_32x32x16_bf16 a[240:255], a[48:51], a[48:51], 0\n\t"
    "v_add_f32 v200, v200, v160\n\t"
    "v_add_f32 v201, v201, v161\n\t"
    "v_cvt_pk_bf16_f32 v20, v160, v161\n\t"
    "v_add_f32 v200, v200, v162\n\t"
    "v_add_f32 v201, v201, v163\n\t"
    "v_cvt_pk_bf16_f32 v21, v162, v163\n\t"
    "v_mfma_f32_16x16x32_bf16 v[172:175], a[12:15], v[108:111], v[192:195]\n\t"
    "v_exp_f32 v164, v164\n\t"
    "v_mfma_f32_16x16x32_bf16 v[172:175], a[8:11], v[104:107], v[172:175]\n\t"
    "v_exp_f32 v165, v165\n\t"
    "v_mfma_f32_16x16x32_bf16 v[172:175], a[4:7], v[100:103], v[172:175]\n\t"
    "v_exp_f32 v166, v166\n\t"
    "v_mfma_f32_16x16x32_bf16 v[172:175], a[0:3], v[96:99], v[172:175]\n\t"
    "ds_read_b128 a[28:31], v252 offset:13824\n\t"
    "v_exp_f32 v167, v167\n\t"
    "v_add_f32 v202, v202, v164\n\t"
    "v_add_f32 v203, v203, v165\n\t"
    "v_cvt_pk_bf16_f32 v52, v164, v165\n\t"
    "v_add_f32 v202, v202, v166\n\t"
    "v_add_f32 v203, v203, v167\n\t"
    "v_cvt_pk_bf16_f32 v53, v166, v167\n\t"
    "v_mfma_f32_16x16x32_bf16 v[176:179], a[0:3], v[128:131], v[196:199]\n\t"
    "v_exp_f32 v168, v168\n\t"
    "v_mfma_f32_16x16x32_bf16 v[176:179], a[4:7], v[132:135], v[176:179]\n\t"
    "v_exp_f32 v169, v169\n\t"
    "v_mfma_f32_16x16x32_bf16 v[176:179], a[8:11], v[136:139], v[176:179]\n\t"
    "v_exp_f32 v170, v170\n\t"
    "v_mfma_f32_16x16x32_bf16 v[176:179], a[12:15], v[140:143], v[176:179]\n\t"
    "v_exp_f32 v171, v171\n\t"
    "v_add_f32 v204, v204, v168\n\t"
    "v_add_f32 v205, v205, v169\n\t"
    "v_cvt_pk_bf16_f32 v84, v168, v169\n\t"
    "v_add_f32 v204, v204, v170\n\t"
    "v_add_f32 v205, v205, v171\n\t"
    "v_cvt_pk_bf16_f32 v85, v170, v171\n\t"
    "s_waitcnt lgkmcnt(0)\n\t"
    "v_mfma_f32_16x16x32_bf16 v[160:163], a[16:19], v[0:3], v[180:183]\n\t"
    "v_exp_f32 v172, v172\n\t"
    "v_mfma_f32_16x16x32_bf16 v[160:163], a[20:23], v[4:7], v[160:163]\n\t"
    "v_exp_f32 v173, v173\n\t"
    "v_mfma_f32_16x16x32_bf16 v[160:163], a[24:27], v[8:11], v[160:163]\n\t"
    "v_exp_f32 v174, v174\n\t"
    "v_mfma_f32_16x16x32_bf16 v[160:163], a[28:31], v[12:15], v[160:163]\n\t"
    "v_exp_f32 v175, v175\n\t"
    "v_add_f32 v206, v206, v172\n\t"
    "v_add_f32 v207, v207, v173\n\t"
    "v_cvt_pk_bf16_f32 v116, v172, v173\n\t"
    "v_add_f32 v206, v206, v174\n\t"
    "v_add_f32 v207, v207, v175\n\t"
    "v_cvt_pk_bf16_f32 v117, v174, v175\n\t"
    "v_mfma_f32_16x16x32_bf16 v[164:167], a[28:31], v[44:47], v[184:187]\n\t"
    "v_exp_f32 v176, v176\n\t"
    "v_mfma_f32_16x16x32_bf16 v[164:167], a[24:27], v[40:43], v[164:167]\n\t"
    "v_exp_f32 v177, v177\n\t"
    "v_mfma_f32_16x16x32_bf16 v[164:167], a[20:23], v[36:39], v[164:167]\n\t"
    "v_exp_f32 v178, v178\n\t"
    "v_mfma_f32_16x16x32_bf16 v[164:167], a[16:19], v[32:35], v[164:167]\n\t"
    "s_barrier\n\t"
    "ds_read_b128 a[0:3], v252 offset:16384\n\t"
    "ds_read_b128 a[4:7], v252 offset:16896\n\t"
    "v_exp_f32 v179, v179\n\t"
    "v_add_f32 v208, v208, v176\n\t"
    "v_add_f32 v209, v209, v177\n\t"
    "v_cvt_pk_bf16_f32 v148, v176, v177\n\t"
    "v_add_f32 v208, v208, v178\n\t"
    "v_add_f32 v209, v209, v179\n\t"
    "v_cvt_pk_bf16_f32 v149, v178, v179\n\t"
    "v_mfma_f32_16x16x32_bf16 v[168:171], a[16:19], v[64:67], v[188:191]\n\t"
    "v_exp_f32 v160, v160\n\t"
    "v_mfma_f32_16x16x32_bf16 v[168:171], a[20:23], v[68:71], v[168:171]\n\t"
    "v_exp_f32 v161, v161\n\t"
    "v_mfma_f32_16x16x32_bf16 v[168:171], a[24:27], v[72:75], v[168:171]\n\t"
    "v_exp_f32 v162, v162\n\t"
    "v_mfma_f32_16x16x32_bf16 v[168:171], a[28:31], v[76:79], v[168:171]\n\t"
    "ds_read_b128 a[8:11], v252 offset:17408\n\t"
    "ds_read_b128 a[12:15], v252 offset:17920\n\t"
    "v_exp_f32 v163, v163\n\t"
    "v_add_f32 v200, v200, v160\n\t"
    "v_add_f32 v201, v201, v161\n\t"
    "v_cvt_pk_bf16_f32 v22, v160, v161\n\t"
    "v_add_f32 v200, v200, v162\n\t"
    "v_add_f32 v201, v201, v163\n\t"
    "v_cvt_pk_bf16_f32 v23, v162, v163\n\t"
    "v_mfma_f32_16x16x32_bf16 v[172:175], a[28:31], v[108:111], v[192:195]\n\t"
    "v_exp_f32 v164, v164\n\t"
    "v_mfma_f32_16x16x32_bf16 v[172:175], a[24:27], v[104:107], v[172:175]\n\t"
    "v_exp_f32 v165, v165\n\t"
    "v_mfma_f32_16x16x32_bf16 v[172:175], a[20:23], v[100:103], v[172:175]\n\t"
    "v_exp_f32 v166, v166\n\t"
    "v_mfma_f32_16x16x32_bf16 v[172:175], a[16:19], v[96:99], v[172:175]\n\t"
    "v_exp_f32 v167, v167\n\t"
    "v_add_f32 v202, v202, v164\n\t"
    "v_add_f32 v203, v203, v165\n\t"
    "v_cvt_pk_bf16_f32 v54, v164, v165\n\t"
    "v_add_f32 v202, v202, v166\n\t"
    "v_add_f32 v203, v203, v167\n\t"
    "v_cvt_pk_bf16_f32 v55, v166, v167\n\t"
    "v_mfma_f32_16x16x32_bf16 v[176:179], a[16:19], v[128:131], v[196:199]\n\t"
    "v_exp_f32 v168, v168\n\t"
    "v_mfma_f32_16x16x32_bf16 v[176:179], a[20:23], v[132:135], v[176:179]\n\t"
    "v_exp_f32 v169, v169\n\t"
    "v_mfma_f32_16x16x32_bf16 v[176:179], a[24:27], v[136:139], v[176:179]\n\t"
    "v_exp_f32 v170, v170\n\t"
    "v_mfma_f32_16x16x32_bf16 v[176:179], a[28:31], v[140:143], v[176:179]\n\t"
    "ds_read_b64_tr_b16 a[16:17], v253 offset:0\n\t"
    "ds_read_b64_tr_b16 a[18:19], v253 offset:4096\n\t"
    "ds_read_b64_tr_b16 a[20:21], v253 offset:8192\n\t"
    "v_exp_f32 v171, v171\n\t"
    "v_add_f32 v204, v204, v168\n\t"
    "v_add_f32 v205, v205, v169\n\t"
    "v_cvt_pk_bf16_f32 v86, v168, v169\n\t"
    "v_add_f32 v204, v204, v170\n\t"
    "v_add_f32 v205, v205, v171\n\t"
    "v_cvt_pk_bf16_f32 v87, v170, v171\n\t"
    "v_exp_f32 v172, v172\n\t"
    "v_exp_f32 v173, v173\n\t"
    "v_exp_f32 v174, v174\n\t"
    "v_exp_f32 v175, v175\n\t"
    "v_add_f32 v206, v206, v172\n\t"
    "v_add_f32 v207, v207, v173\n\t"
    "v_cvt_pk_bf16_f32 v118, v172, v173\n\t"
    "s_waitcnt lgkmcnt(3)\n\t"
    ".p2align 6\n\t"
    "L_loopA_%=:\n\t"
    "ds_read_b64_tr_b16 a[22:23], v253 offset:12288\n\t"
    "v_add_f32 v206, v206, v174\n\t"
    "v_mfma_f32_16x16x32_bf16 v[160:163], a[0:3], v[0:3], v[180:183]\n\t"
    "v_exp_f32 v176, v176\n\t"
    "v_mfma_f32_16x16x32_bf16 v[160:163], a[4:7], v[4:7], v[160:163]\n\t"
    "v_add_f32 v207, v207, v175\n\t"
    "v_cvt_pk_bf16_f32 v119, v174, v175\n\t"
    "v_mfma_f32_16x16x32_bf16 v[160:163], a[8:11], v[8:11], v[160:163]\n\t"
    "v_exp_f32 v177, v177\n\t"
    "v_mfma_f32_16x16x32_bf16 v[160:163], a[12:15], v[12:15], v[160:163]\n\t"
    "ds_read_b64_tr_b16 a[24:25], v254 offset:0\n\t"
    "v_add_f32 v208, v208, v176\n\t"
    "v_mfma_f32_16x16x32_bf16 v[164:167], a[12:15], v[44:47], v[184:187]\n\t"
    "s_sub_u32 s54, s54, 1\n\t"
    "v_exp_f32 v178, v178\n\t"
    "v_mfma_f32_16x16x32_bf16 v[164:167], a[8:11], v[40:43], v[164:167]\n\t"
    "v_add_f32 v209, v209, v177\n\t"
    "v_cvt_pk_bf16_f32 v150, v176, v177\n\t"
    "v_mfma_f32_16x16x32_bf16 v[164:167], a[4:7], v[36:39], v[164:167]\n\t"
    "v_exp_f32 v179, v179\n\t"
    "v_mfma_f32_16x16x32_bf16 v[164:167], a[0:3], v[32:35], v[164:167]\n\t"
    "ds_read_b64_tr_b16 a[26:27], v254 offset:4096\n\t"
    "v_add_f32 v208, v208, v178\n\t"
    "v_mfma_f32_16x16x32_bf16 v[168:171], a[0:3], v[64:67], v[188:191]\n\t"
    "v_exp_f32 v160, v160\n\t"
    "v_mfma_f32_16x16x32_bf16 v[168:171], a[4:7], v[68:71], v[168:171]\n\t"
    "v_add_f32 v209, v209, v179\n\t"
    "v_cvt_pk_bf16_f32 v151, v178, v179\n\t"
    "v_mfma_f32_16x16x32_bf16 v[168:171], a[8:11], v[72:75], v[168:171]\n\t"
    "v_exp_f32 v161, v161\n\t"
    "v_mfma_f32_16x16x32_bf16 v[168:171], a[12:15], v[76:79], v[168:171]\n\t"
    "ds_read_b64_tr_b16 a[28:29], v254 offset:8192\n\t"
    "v_add_f32 v200, v200, v160\n\t"
    "v_mfma_f32_16x16x32_bf16 v[172:175], a[12:15], v[108:111], v[192:195]\n\t"
    "v_exp_f32 v162, v162\n\t"
    "v_mfma_f32_16x16x32_bf16 v[172:175], a[8:11], v[104:107], v[172:175]\n\t"
    "v_add_f32 v201, v201, v161\n\t"
    "v_cvt_pk_bf16_f32 v24, v160, v161\n\t"
    "v_mfma_f32_16x16x32_bf16 v[172:175], a[4:7], v[100:103], v[172:175]\n\t"
    "v_exp_f32 v163, v163\n\t"
    "v_mfma_f32_16x16x32_bf16 v[172:175], a[0:3], v[96:99], v[172:175]\n\t"
    "ds_read_b64_tr_b16 a[30:31], v254 offset:12288\n\t"
    "v_add_f32 v200, v200, v162\n\t"
    "v_mfma_f32_16x16x32_bf16 v[176:179], a[0:3], v[128:131], v[196:199]\n\t"
    "v_exp_f32 v164, v164\n\t"
    "v_mfma_f32_16x16x32_bf16 v[176:179], a[4:7], v[132:135], v[176:179]\n\t"
    "v_add_f32 v201, v201, v163\n\t"
    "v_cvt_pk_bf16_f32 v25, v162, v163\n\t"
    "v_mfma_f32_16x16x32_bf16 v[176:179], a[8:11], v[136:139], v[176:179]\n\t"
    "s_waitcnt lgkmcnt(4)\n\t"
    "v_exp_f32 v165, v165\n\t"
    "v_mfma_f32_16x16x32_bf16 v[176:179], a[12:15], v[140:143], v[176:179]\n\t"
    "ds_read_b128 a[0:3], v252 offset:20480\n\t"
    "v_add_f32 v202, v202, v164\n\t"
    "v_mfma_f32_16x16x32_bf16 a[96:99], a[16:19], v[16:19], a[96:99]\n\t"
    "v_exp_f32 v166, v166\n\t"
    "v_mfma_f32_16x16x32_bf16 a[96:99], a[20:23], v[20:23], a[96:99]\n\t"
    "v_add_f32 v203, v203, v165\n\t"
    "v_cvt_pk_bf16_f32 v56, v164, v165\n\t"
    "v_mfma_f32_16x16x32_bf16 a[128:131], a[20:23], v[52:55], a[128:131]\n\t"
    "v_exp_f32 v167, v167\n\t"
    "v_mfma_f32_16x16x32_bf16 a[128:131], a[16:19], v[48:51], a[128:131]\n\t"
    "ds_read_b128 a[4:7], v252 offset:20992\n\t"
    "v_add_f32 v202, v202, v166\n\t"
    "v_mfma_f32_16x16x32_bf16 a[160:163], a[16:19], v[80:83], a[160:163]\n\t"
    "v_exp_f32 v168, v168\n\t"
    "v_mfma_f32_16x16x32_bf16 a[160:163], a[20:23], v[84:87], a[160:163]\n\t"
    "ds_read_b128 a[8:11], v252 offset:21504\n\t"
    "v_add_f32 v203, v203, v167\n\t"
    "v_cvt_pk_bf16_f32 v57, v166, v167\n\t"
    "v_mfma_f32_16x16x32_bf16 a[192:195], a[20:23], v[116:119], a[192:195]\n\t"
    "v_exp_f32 v169, v169\n\t"
    "v_mfma_f32_16x16x32_bf16 a[192:195], a[16:19], v[112:115], a[192:195]\n\t"
    "ds_read_b128 a[12:15], v252 offset:22016\n\t"
    "v_add_f32 v204, v204, v168\n\t"
    "v_mfma_f32_16x16x32_bf16 a[224:227], a[16:19], v[144:147], a[224:227]\n\t"
    "s_waitcnt lgkmcnt(4)\n\t"
    "v_exp_f32 v170, v170\n\t"
    "v_mfma_f32_16x16x32_bf16 a[224:227], a[20:23], v[148:151], a[224:227]\n\t"
    "v_add_f32 v205, v205, v169\n\t"
    "v_cvt_pk_bf16_f32 v88, v168, v169\n\t"
    "v_mfma_f32_16x16x32_bf16 a[228:231], a[28:31], v[148:151], a[228:231]\n\t"
    "v_exp_f32 v171, v171\n\t"
    "v_mfma_f32_16x16x32_bf16 a[228:231], a[24:27], v[144:147], a[228:231]\n\t"
    "ds_read_b64_tr_b16 a[16:17], v253 offset:512\n\t"
    "v_add_f32 v204, v204, v170\n\t"
    "v_mfma_f32_16x16x32_bf16 a[196:199], a[24:27], v[112:115], a[196:199]\n\t"
    "v_exp_f32 v172, v172\n\t"
    "v_mfma_f32_16x16x32_bf16 a[196:199], a[28:31], v[116:119], a[196:199]\n\t"
    "ds_read_b64_tr_b16 a[18:19], v253 offset:4608\n\t"
    "v_add_f32 v205, v205, v171\n\t"
    "v_cvt_pk_bf16_f32 v89, v170, v171\n\t"
    "v_mfma_f32_16x16x32_bf16 a[164:167], a[28:31], v[84:87], a[164:167]\n\t"
    "v_exp_f32 v173, v173\n\t"
    "v_mfma_f32_16x16x32_bf16 a[164:167], a[24:27], v[80:83], a[164:167]\n\t"
    "ds_read_b64_tr_b16 a[20:21], v253 offset:8704\n\t"
    "v_add_f32 v206, v206, v172\n\t"
    "v_mfma_f32_16x16x32_bf16 a[132:135], a[24:27], v[48:51], a[132:135]\n\t"
    "v_exp_f32 v174, v174\n\t"
    "v_mfma_f32_16x16x32_bf16 a[132:135], a[28:31], v[52:55], a[132:135]\n\t"
    "v_add_f32 v207, v207, v173\n\t"
    "v_cvt_pk_bf16_f32 v120, v172, v173\n\t"
    "v_mfma_f32_16x16x32_bf16 a[100:103], a[28:31], v[20:23], a[100:103]\n\t"
    "s_waitcnt lgkmcnt(3)\n\t"
    "v_exp_f32 v175, v175\n\t"
    "v_mfma_f32_16x16x32_bf16 a[100:103], a[24:27], v[16:19], a[100:103]\n\t"
    "ds_read_b64_tr_b16 a[22:23], v253 offset:12800\n\t"
    "v_add_f32 v206, v206, v174\n\t"
    "v_mfma_f32_16x16x32_bf16 v[160:163], a[0:3], v[0:3], v[180:183]\n\t"
    "v_exp_f32 v176, v176\n\t"
    "v_mfma_f32_16x16x32_bf16 v[160:163], a[4:7], v[4:7], v[160:163]\n\t"
    "v_add_f32 v207, v207, v175\n\t"
    "v_cvt_pk_bf16_f32 v121, v174, v175\n\t"
    "v_mfma_f32_16x16x32_bf16 v[160:163], a[8:11], v[8:11], v[160:163]\n\t"
    "v_exp_f32 v177, v177\n\t"
    "v_mfma_f32_16x16x32_bf16 v[160:163], a[12:15], v[12:15], v[160:163]\n\t"
    "ds_read_b64_tr_b16 a[24:25], v254 offset:512\n\t"
    "v_add_f32 v208, v208, v176\n\t"
    "v_mfma_f32_16x16x32_bf16 v[164:167], a[12:15], v[44:47], v[184:187]\n\t"
    "v_exp_f32 v178, v178\n\t"
    "v_mfma_f32_16x16x32_bf16 v[164:167], a[8:11], v[40:43], v[164:167]\n\t"
    "v_add_f32 v209, v209, v177\n\t"
    "v_cvt_pk_bf16_f32 v152, v176, v177\n\t"
    "v_mfma_f32_16x16x32_bf16 v[164:167], a[4:7], v[36:39], v[164:167]\n\t"
    "v_exp_f32 v179, v179\n\t"
    "v_mfma_f32_16x16x32_bf16 v[164:167], a[0:3], v[32:35], v[164:167]\n\t"
    "ds_read_b64_tr_b16 a[26:27], v254 offset:4608\n\t"
    "v_add_f32 v208, v208, v178\n\t"
    "v_mfma_f32_16x16x32_bf16 v[168:171], a[0:3], v[64:67], v[188:191]\n\t"
    "v_exp_f32 v160, v160\n\t"
    "v_mfma_f32_16x16x32_bf16 v[168:171], a[4:7], v[68:71], v[168:171]\n\t"
    "v_add_f32 v209, v209, v179\n\t"
    "v_cvt_pk_bf16_f32 v153, v178, v179\n\t"
    "v_mfma_f32_16x16x32_bf16 v[168:171], a[8:11], v[72:75], v[168:171]\n\t"
    "v_exp_f32 v161, v161\n\t"
    "v_mfma_f32_16x16x32_bf16 v[168:171], a[12:15], v[76:79], v[168:171]\n\t"
    "ds_read_b64_tr_b16 a[28:29], v254 offset:8704\n\t"
    "v_add_f32 v200, v200, v160\n\t"
    "v_mfma_f32_16x16x32_bf16 v[172:175], a[12:15], v[108:111], v[192:195]\n\t"
    "v_exp_f32 v162, v162\n\t"
    "v_mfma_f32_16x16x32_bf16 v[172:175], a[8:11], v[104:107], v[172:175]\n\t"
    "v_add_f32 v201, v201, v161\n\t"
    "v_cvt_pk_bf16_f32 v26, v160, v161\n\t"
    "v_mfma_f32_16x16x32_bf16 v[172:175], a[4:7], v[100:103], v[172:175]\n\t"
    "v_exp_f32 v163, v163\n\t"
    "v_mfma_f32_16x16x32_bf16 v[172:175], a[0:3], v[96:99], v[172:175]\n\t"
    "ds_read_b64_tr_b16 a[30:31], v254 offset:12800\n\t"
    "v_add_f32 v200, v200, v162\n\t"
    "v_mfma_f32_16x16x32_bf16 v[176:179], a[0:3], v[128:131], v[196:199]\n\t"
    "v_exp_f32 v164, v164\n\t"
    "v_mfma_f32_16x16x32_bf16 v[176:179], a[4:7], v[132:135], v[176:179]\n\t"
    "v_add_f32 v201, v201, v163\n\t"
    "v_cvt_pk_bf16_f32 v27, v162, v163\n\t"
    "v_mfma_f32_16x16x32_bf16 v[176:179], a[8:11], v[136:139], v[176:179]\n\t"
    "s_waitcnt lgkmcnt(4)\n\t"
    "v_exp_f32 v165, v165\n\t"
    "v_mfma_f32_16x16x32_bf16 v[176:179], a[12:15], v[140:143], v[176:179]\n\t"
    "ds_read_b128 a[0:3], v252 offset:24576\n\t"
    "v_add_f32 v202, v202, v164\n\t"
    "v_mfma_f32_16x16x32_bf16 a[104:107], a[16:19], v[16:19], a[104:107]\n\t"
    "v_exp_f32 v166, v166\n\t"
    "v_mfma_f32_16x16x32_bf16 a[104:107], a[20:23], v[20:23], a[104:107]\n\t"
    "v_add_f32 v203, v203, v165\n\t"
    "v_cvt_pk_bf16_f32 v58, v164, v165\n\t"
    "v_mfma_f32_16x16x32_bf16 a[136:139], a[20:23], v[52:55], a[136:139]\n\t"
    "v_exp_f32 v167, v167\n\t"
    "v_mfma_f32_16x16x32_bf16 a[136:139], a[16:19], v[48:51], a[136:139]\n\t"
    "ds_read_b128 a[4:7], v252 offset:25088\n\t"
    "v_add_f32 v202, v202, v166\n\t"
    "v_mfma_f32_16x16x32_bf16 a[168:171], a[16:19], v[80:83], a[168:171]\n\t"
    "v_exp_f32 v168, v168\n\t"
    "v_mfma_f32_16x16x32_bf16 a[168:171], a[20:23], v[84:87], a[168:171]\n\t"
    "ds_read_b128 a[8:11], v252 offset:25600\n\t"
    "v_add_f32 v203, v203, v167\n\t"
    "v_cvt_pk_bf16_f32 v59, v166, v167\n\t"
    "v_mfma_f32_16x16x32_bf16 a[200:203], a[20:23], v[116:119], a[200:203]\n\t"
    "v_exp_f32 v169, v169\n\t"
    "v_mfma_f32_16x16x32_bf16 a[200:203], a[16:19], v[112:115], a[200:203]\n\t"
    "ds_read_b128 a[12:15], v252 offset:26112\n\t"
    "v_add_f32 v204, v204, v168\n\t"
    "v_mfma_f32_16x16x32_bf16 a[232:235], a[16:19], v[144:147], a[232:235]\n\t"
    "s_waitcnt lgkmcnt(4)\n\t"
    "v_exp_f32 v170, v170\n\t"
    "v_mfma_f32_16x16x32_bf16 a[232:235], a[20:23], v[148:151], a[232:235]\n\t"
    "v_add_f32 v205, v205, v169\n\t"
    "v_cvt_pk_bf16_f32 v90, v168, v169\n\t"
    "v_mfma_f32_16x16x32_bf16 a[236:239], a[28:31], v[148:151], a[236:239]\n\t"
    "v_exp_f32 v171, v171\n\t"
    "v_mfma_f32_16x16x32_bf16 a[236:239], a[24:27], v[144:147], a[236:239]\n\t"
    "ds_read_b64_tr_b16 a[16:17], v253 offset:1024\n\t"
    "v_add_f32 v204, v204, v170\n\t"
    "v_mfma_f32_16x16x32_bf16 a[204:207], a[24:27], v[112:115], a[204:207]\n\t"
    "v_exp_f32 v172, v172\n\t"
    "v_mfma_f32_16x16x32_bf16 a[204:207], a[28:31], v[116:119], a[204:207]\n\t"
    "ds_read_b64_tr_b16 a[18:19], v253 offset:5120\n\t"
    "v_add_f32 v205, v205, v171\n\t"
    "v_cvt_pk_bf16_f32 v91, v170, v171\n\t"
    "v_mfma_f32_16x16x32_bf16 a[172:175], a[28:31], v[84:87], a[172:175]\n\t"
    "v_exp_f32 v173, v173\n\t"
    "v_mfma_f32_16x16x32_bf16 a[172:175], a[24:27], v[80:83], a[172:175]\n\t"
    "ds_read_b64_tr_b16 a[20:21], v253 offset:9216\n\t"
    "v_add_f32 v206, v206, v172\n\t"
    "v_mfma_f32_16x16x32_bf16 a[140:143], a[24:27], v[48:51], a[140:143]\n\t"
    "v_exp_f32 v174, v174\n\t"
    "v_mfma_f32_16x16x32_bf16 a[140:143], a[28:31], v[52:55], a[140:143]\n\t"
    "v_add_f32 v207, v207, v173\n\t"
    "v_cvt_pk_bf16_f32 v122, v172, v173\n\t"
    "v_mfma_f32_16x16x32_bf16 a[108:111], a[28:31], v[20:23], a[108:111]\n\t"
    "s_waitcnt lgkmcnt(3)\n\t"
    "v_exp_f32 v175, v175\n\t"
    "v_mfma_f32_16x16x32_bf16 a[108:111], a[24:27], v[16:19], a[108:111]\n\t"
    "ds_read_b64_tr_b16 a[22:23], v253 offset:13312\n\t"
    "v_add_f32 v206, v206, v174\n\t"
    "v_mfma_f32_16x16x32_bf16 v[160:163], a[0:3], v[0:3], v[180:183]\n\t"
    "v_exp_f32 v176, v176\n\t"
    "v_mfma_f32_16x16x32_bf16 v[160:163], a[4:7], v[4:7], v[160:163]\n\t"
    "v_add_f32 v207, v207, v175\n\t"
    "v_cvt_pk_bf16_f32 v123, v174, v175\n\t"
    "v_mfma_f32_16x16x32_bf16 v[160:163], a[8:11], v[8:11], v[160:163]\n\t"
    "v_exp_f32 v177, v177\n\t"
    "v_mfma_f32_16x16x32_bf16 v[160:163], a[12:15], v[12:15], v[160:163]\n\t"
    "ds_read_b64_tr_b16 a[24:25], v254 offset:1024\n\t"
    "v_add_f32 v208, v208, v176\n\t"
    "v_mfma_f32_16x16x32_bf16 v[164:167], a[12:15], v[44:47], v[184:187]\n\t"
    "v_exp_f32 v178, v178\n\t"
    "v_mfma_f32_16x16x32_bf16 v[164:167], a[8:11], v[40:43], v[164:167]\n\t"
    "v_add_f32 v209, v209, v177\n\t"
    "v_cvt_pk_bf16_f32 v154, v176, v177\n\t"
    "v_mfma_f32_16x16x32_bf16 v[164:167], a[4:7], v[36:39], v[164:167]\n\t"
    "v_exp_f32 v179, v179\n\t"
    "v_mfma_f32_16x16x32_bf16 v[164:167], a[0:3], v[32:35], v[164:167]\n\t"
    "ds_read_b64_tr_b16 a[26:27], v254 offset:5120\n\t"
    "v_add_f32 v208, v208, v178\n\t"
    "v_mfma_f32_16x16x32_bf16 v[168:171], a[0:3], v[64:67], v[188:191]\n\t"
    "v_exp_f32 v160, v160\n\t"
    "v_mfma_f32_16x16x32_bf16 v[168:171], a[4:7], v[68:71], v[168:171]\n\t"
    "v_add_f32 v209, v209, v179\n\t"
    "v_cvt_pk_bf16_f32 v155, v178, v179\n\t"
    "v_mfma_f32_16x16x32_bf16 v[168:171], a[8:11], v[72:75], v[168:171]\n\t"
    "v_exp_f32 v161, v161\n\t"
    "v_mfma_f32_16x16x32_bf16 v[168:171], a[12:15], v[76:79], v[168:171]\n\t"
    "ds_read_b64_tr_b16 a[28:29], v254 offset:9216\n\t"
    "v_add_f32 v200, v200, v160\n\t"
    "v_mfma_f32_16x16x32_bf16 v[172:175], a[12:15], v[108:111], v[192:195]\n\t"
    "v_exp_f32 v162, v162\n\t"
    "v_mfma_f32_16x16x32_bf16 v[172:175], a[8:11], v[104:107], v[172:175]\n\t"
    "v_add_f32 v201, v201, v161\n\t"
    "v_cvt_pk_bf16_f32 v28, v160, v161\n\t"
    "v_mfma_f32_16x16x32_bf16 v[172:175], a[4:7], v[100:103], v[172:175]\n\t"
    "v_exp_f32 v163, v163\n\t"
    "v_mfma_f32_16x16x32_bf16 v[172:175], a[0:3], v[96:99], v[172:175]\n\t"
    "ds_read_b64_tr_b16 a[30:31], v254 offset:13312\n\t"
    "v_add_f32 v200, v200, v162\n\t"
    "v_mfma_f32_16x16x32_bf16 v[176:179], a[0:3], v[128:131], v[196:199]\n\t"
    "v_exp_f32 v164, v164\n\t"
    "v_mfma_f32_16x16x32_bf16 v[176:179], a[4:7], v[132:135], v[176:179]\n\t"
    "v_add_f32 v201, v201, v163\n\t"
    "v_cvt_pk_bf16_f32 v29, v162, v163\n\t"
    "v_mfma_f32_16x16x32_bf16 v[176:179], a[8:11], v[136:139], v[176:179]\n\t"
    "s_waitcnt lgkmcnt(4)\n\t"
    "v_exp_f32 v165, v165\n\t"
    "v_mfma_f32_16x16x32_bf16 v[176:179], a[12:15], v[140:143], v[176:179]\n\t"
    "ds_read_b128 a[0:3], v252 offset:28672\n\t"
    "v_add_f32 v202, v202, v164\n\t"
    "v_mfma_f32_16x16x32_bf16 a[112:115], a[16:19], v[16:19], a[112:115]\n\t"
    "v_exp_f32 v166, v166\n\t"
    "v_mfma_f32_16x16x32_bf16 a[112:115], a[20:23], v[20:23], a[112:115]\n\t"
    "v_add_f32 v203, v203, v165\n\t"
    "v_cvt_pk_bf16_f32 v60, v164, v165\n\t"
    "v_mfma_f32_16x16x32_bf16 a[144:147], a[20:23], v[52:55], a[144:147]\n\t"
    "v_exp_f32 v167, v167\n\t"
    "v_mfma_f32_16x16x32_bf16 a[144:147], a[16:19], v[48:51], a[144:147]\n\t"
    "ds_read_b128 a[4:7], v252 offset:29184\n\t"
    "v_add_f32 v202, v202, v166\n\t"
    "v_mfma_f32_16x16x32_bf16 a[176:179], a[16:19], v[80:83], a[176:179]\n\t"
    "v_exp_f32 v168, v168\n\t"
    "v_mfma_f32_16x16x32_bf16 a[176:179], a[20:23], v[84:87], a[176:179]\n\t"
    "ds_read_b128 a[8:11], v252 offset:29696\n\t"
    "v_add_f32 v203, v203, v167\n\t"
    "v_cvt_pk_bf16_f32 v61, v166, v167\n\t"
    "v_mfma_f32_16x16x32_bf16 a[208:211], a[20:23], v[116:119], a[208:211]\n\t"
    "v_exp_f32 v169, v169\n\t"
    "v_mfma_f32_16x16x32_bf16 a[208:211], a[16:19], v[112:115], a[208:211]\n\t"
    "ds_read_b128 a[12:15], v252 offset:30208\n\t"
    "v_add_f32 v204, v204, v168\n\t"
    "v_mfma_f32_16x16x32_bf16 a[240:243], a[16:19], v[144:147], a[240:243]\n\t"
    "s_waitcnt lgkmcnt(4)\n\t"
    "v_exp_f32 v170, v170\n\t"
    "v_mfma_f32_16x16x32_bf16 a[240:243], a[20:23], v[148:151], a[240:243]\n\t"
    "v_add_f32 v205, v205, v169\n\t"
    "v_cvt_pk_bf16_f32 v92, v168, v169\n\t"
    "v_mfma_f32_16x16x32_bf16 a[244:247], a[28:31], v[148:151], a[244:247]\n\t"
    "v_exp_f32 v171, v171\n\t"
    "v_mfma_f32_16x16x32_bf16 a[244:247], a[24:27], v[144:147], a[244:247]\n\t"
    "ds_read_b64_tr_b16 a[16:17], v253 offset:1536\n\t"
    "v_add_f32 v204, v204, v170\n\t"
    "v_mfma_f32_16x16x32_bf16 a[212:215], a[24:27], v[112:115], a[212:215]\n\t"
    "v_exp_f32 v172, v172\n\t"
    "v_mfma_f32_16x16x32_bf16 a[212:215], a[28:31], v[116:119], a[212:215]\n\t"
    "ds_read_b64_tr_b16 a[18:19], v253 offset:5632\n\t"
    "v_add_f32 v205, v205, v171\n\t"
    "v_cvt_pk_bf16_f32 v93, v170, v171\n\t"
    "v_mfma_f32_16x16x32_bf16 a[180:183], a[28:31], v[84:87], a[180:183]\n\t"
    "v_exp_f32 v173, v173\n\t"
    "v_mfma_f32_16x16x32_bf16 a[180:183], a[24:27], v[80:83], a[180:183]\n\t"
    "ds_read_b64_tr_b16 a[20:21], v253 offset:9728\n\t"
    "v_add_f32 v206, v206, v172\n\t"
    "v_mfma_f32_16x16x32_bf16 a[148:151], a[24:27], v[48:51], a[148:151]\n\t"
    "v_exp_f32 v174, v174\n\t"
    "v_mfma_f32_16x16x32_bf16 a[148:151], a[28:31], v[52:55], a[148:151]\n\t"
    "v_add_f32 v207, v207, v173\n\t"
    "v_cvt_pk_bf16_f32 v124, v172, v173\n\t"
    "v_mfma_f32_16x16x32_bf16 a[116:119], a[28:31], v[20:23], a[116:119]\n\t"
    "s_waitcnt lgkmcnt(3)\n\t"
    "v_exp_f32 v175, v175\n\t"
    "v_mfma_f32_16x16x32_bf16 a[116:119], a[24:27], v[16:19], a[116:119]\n\t"
    "ds_read_b64_tr_b16 a[22:23], v253 offset:13824\n\t"
    "v_add_f32 v206, v206, v174\n\t"
    "v_mfma_f32_16x16x32_bf16 v[160:163], a[0:3], v[0:3], v[180:183]\n\t"
    "v_exp_f32 v176, v176\n\t"
    "v_mfma_f32_16x16x32_bf16 v[160:163], a[4:7], v[4:7], v[160:163]\n\t"
    "v_add_f32 v207, v207, v175\n\t"
    "v_cvt_pk_bf16_f32 v125, v174, v175\n\t"
    "v_mfma_f32_16x16x32_bf16 v[160:163], a[8:11], v[8:11], v[160:163]\n\t"
    "v_exp_f32 v177, v177\n\t"
    "v_mfma_f32_16x16x32_bf16 v[160:163], a[12:15], v[12:15], v[160:163]\n\t"
    "ds_read_b64_tr_b16 a[24:25], v254 offset:1536\n\t"
    "v_add_f32 v208, v208, v176\n\t"
    "v_mfma_f32_16x16x32_bf16 v[164:167], a[12:15], v[44:47], v[184:187]\n\t"
    "v_exp_f32 v178, v178\n\t"
    "v_mfma_f32_16x16x32_bf16 v[164:167], a[8:11], v[40:43], v[164:167]\n\t"
    "v_add_f32 v209, v209, v177\n\t"
    "v_cvt_pk_bf16_f32 v156, v176, v177\n\t"
    "v_mfma_f32_16x16x32_bf16 v[164:167], a[4:7], v[36:39], v[164:167]\n\t"
    "v_exp_f32 v179, v179\n\t"
    "v_mfma_f32_16x16x32_bf16 v[164:167], a[0:3], v[32:35], v[164:167]\n\t"
    "ds_read_b64_tr_b16 a[26:27], v254 offset:5632\n\t"
    "v_add_f32 v208, v208, v178\n\t"
    "v_mfma_f32_16x16x32_bf16 v[168:171], a[0:3], v[64:67], v[188:191]\n\t"
    "v_exp_f32 v160, v160\n\t"
    "v_mfma_f32_16x16x32_bf16 v[168:171], a[4:7], v[68:71], v[168:171]\n\t"
    "v_add_f32 v209, v209, v179\n\t"
    "v_cvt_pk_bf16_f32 v157, v178, v179\n\t"
    "v_mfma_f32_16x16x32_bf16 v[168:171], a[8:11], v[72:75], v[168:171]\n\t"
    "v_exp_f32 v161, v161\n\t"
    "v_mfma_f32_16x16x32_bf16 v[168:171], a[12:15], v[76:79], v[168:171]\n\t"
    "ds_read_b64_tr_b16 a[28:29], v254 offset:9728\n\t"
    "v_add_f32 v200, v200, v160\n\t"
    "v_mfma_f32_16x16x32_bf16 v[172:175], a[12:15], v[108:111], v[192:195]\n\t"
    "v_exp_f32 v162, v162\n\t"
    "v_mfma_f32_16x16x32_bf16 v[172:175], a[8:11], v[104:107], v[172:175]\n\t"
    "v_add_f32 v201, v201, v161\n\t"
    "v_cvt_pk_bf16_f32 v30, v160, v161\n\t"
    "v_mfma_f32_16x16x32_bf16 v[172:175], a[4:7], v[100:103], v[172:175]\n\t"
    "v_exp_f32 v163, v163\n\t"
    "v_mfma_f32_16x16x32_bf16 v[172:175], a[0:3], v[96:99], v[172:175]\n\t"
    "ds_read_b64_tr_b16 a[30:31], v254 offset:13824\n\t"
    "v_add_f32 v200, v200, v162\n\t"
    "v_mfma_f32_16x16x32_bf16 v[176:179], a[0:3], v[128:131], v[196:199]\n\t"
    "v_exp_f32 v164, v164\n\t"
    "v_mfma_f32_16x16x32_bf16 v[176:179], a[4:7], v[132:135], v[176:179]\n\t"
    "v_add_f32 v201, v201, v163\n\t"
    "v_cvt_pk_bf16_f32 v31, v162, v163\n\t"
    "v_mfma_f32_16x16x32_bf16 v[176:179], a[8:11], v[136:139], v[176:179]\n\t"
    "s_waitcnt lgkmcnt(4)\n\t"
    "v_exp_f32 v165, v165\n\t"
    "v_mfma_f32_16x16x32_bf16 v[176:179], a[12:15], v[140:143], v[176:179]\n\t"
    "v_add_f32 v202, v202, v164\n\t"
    "v_xor_b32 v252, 0x8000, v252\n\t"
    "v_mfma_f32_16x16x32_bf16 a[120:123], a[16:19], v[16:19], a[120:123]\n\t"
    "v_exp_f32 v166, v166\n\t"
    "v_mfma_f32_16x16x32_bf16 a[120:123], a[20:23], v[20:23], a[120:123]\n\t"
    "v_add_f32 v203, v203, v165\n\t"
    "v_cvt_pk_bf16_f32 v62, v164, v165\n\t"
    "v_mfma_f32_16x16x32_bf16 a[152:155], a[20:23], v[52:55], a[152:155]\n\t"
    "s_waitcnt lgkmcnt(0)\n\t"
    "v_exp_f32 v167, v167\n\t"
    "v_mfma_f32_16x16x32_bf16 a[152:155], a[16:19], v[48:51], a[152:155]\n\t"
    "s_barrier\n\t"
    "ds_read_b128 a[0:3], v252 offset:0\n\t"
    "v_add_f32 v202, v202, v166\n\t"
    "v_mfma_f32_16x16x32_bf16 a[184:187], a[16:19], v[80:83], a[184:187]\n\t"
    "v_exp_f32 v168, v168\n\t"
    "v_mfma_f32_16x16x32_bf16 a[184:187], a[20:23], v[84:87], a[184:187]\n\t"
    "ds_read_b128 a[4:7], v252 offset:512\n\t"
    "v_add_f32 v203, v203, v167\n\t"
    "v_cvt_pk_bf16_f32 v63, v166, v167\n\t"
    "v_mfma_f32_16x16x32_bf16 a[216:219], a[20:23], v[116:119], a[216:219]\n\t"
    "v_exp_f32 v169, v169\n\t"
    "v_mfma_f32_16x16x32_bf16 a[216:219], a[16:19], v[112:115], a[216:219]\n\t"
    "ds_read_b128 a[8:11], v252 offset:1024\n\t"
    "v_add_f32 v204, v204, v168\n\t"
    "v_mfma_f32_16x16x32_bf16 a[248:251], a[16:19], v[144:147], a[248:251]\n\t"
    "v_exp_f32 v170, v170\n\t"
    "v_mfma_f32_16x16x32_bf16 a[248:251], a[20:23], v[148:151], a[248:251]\n\t"
    "ds_read_b128 a[12:15], v252 offset:1536\n\t"
    "v_add_f32 v205, v205, v169\n\t"
    "v_cvt_pk_bf16_f32 v94, v168, v169\n\t"
    "v_mfma_f32_16x16x32_bf16 a[252:255], a[28:31], v[148:151], a[252:255]\n\t"
    "v_exp_f32 v171, v171\n\t"
    "v_mfma_f32_16x16x32_bf16 a[252:255], a[24:27], v[144:147], a[252:255]\n\t"
    "ds_read_b64_tr_b16 a[16:17], v253 offset:16384\n\t"
    "v_add_f32 v204, v204, v170\n\t"
    "v_mfma_f32_16x16x32_bf16 a[220:223], a[24:27], v[112:115], a[220:223]\n\t"
    "v_exp_f32 v172, v172\n\t"
    "v_mfma_f32_16x16x32_bf16 a[220:223], a[28:31], v[116:119], a[220:223]\n\t"
    "ds_read_b64_tr_b16 a[18:19], v253 offset:20480\n\t"
    "v_add_f32 v205, v205, v171\n\t"
    "v_cvt_pk_bf16_f32 v95, v170, v171\n\t"
    "v_mfma_f32_16x16x32_bf16 a[188:191], a[28:31], v[84:87], a[188:191]\n\t"
    "v_exp_f32 v173, v173\n\t"
    "v_mfma_f32_16x16x32_bf16 a[188:191], a[24:27], v[80:83], a[188:191]\n\t"
    "ds_read_b64_tr_b16 a[20:21], v253 offset:24576\n\t"
    "v_add_f32 v206, v206, v172\n\t"
    "v_mfma_f32_16x16x32_bf16 a[156:159], a[24:27], v[48:51], a[156:159]\n\t"
    "s_cmp_eq_u32 s54, 0\n\t"
    "v_exp_f32 v174, v174\n\t"
    "v_mfma_f32_16x16x32_bf16 a[156:159], a[28:31], v[52:55], a[156:159]\n\t"
    "v_add_f32 v207, v207, v173\n\t"
    "v_cvt_pk_bf16_f32 v126, v172, v173\n\t"
    "v_mfma_f32_16x16x32_bf16 a[124:127], a[28:31], v[20:23], a[124:127]\n\t"
    "s_waitcnt lgkmcnt(3)\n\t"
    "v_exp_f32 v175, v175\n\t"
    "v_mfma_f32_16x16x32_bf16 a[124:127], a[24:27], v[16:19], a[124:127]\n\t"
    "s_cbranch_scc1 L_exitA_%=\n\t"
    "ds_read_b64_tr_b16 a[22:23], v253 offset:28672\n\t"
    "v_add_f32 v206, v206, v174\n\t"
    "v_mfma_f32_16x16x32_bf16 v[160:163], a[0:3], v[0:3], v[180:183]\n\t"
    "v_exp_f32 v176, v176\n\t"
    "v_mfma_f32_16x16x32_bf16 v[160:163], a[4:7], v[4:7], v[160:163]\n\t"
    "v_add_f32 v207, v207, v175\n\t"
    "v_cvt_pk_bf16_f32 v127, v174, v175\n\t"
    "v_mfma_f32_16x16x32_bf16 v[160:163], a[8:11], v[8:11], v[160:163]\n\t"
    "v_exp_f32 v177, v177\n\t"
    "v_mfma_f32_16x16x32_bf16 v[160:163], a[12:15], v[12:15], v[160:163]\n\t"
    "ds_read_b64_tr_b16 a[24:25], v254 offset:16384\n\t"
    "v_add_f32 v208, v208, v176\n\t"
    "v_mfma_f32_16x16x32_bf16 v[164:167], a[12:15], v[44:47], v[184:187]\n\t"
    "v_exp_f32 v178, v178\n\t"
    "v_mfma_f32_16x16x32_bf16 v[164:167], a[8:11], v[40:43], v[164:167]\n\t"
    "v_add_f32 v209, v209, v177\n\t"
    "v_cvt_pk_bf16_f32 v158, v176, v177\n\t"
    "v_mfma_f32_16x16x32_bf16 v[164:167], a[4:7], v[36:39], v[164:167]\n\t"
    "v_exp_f32 v179, v179\n\t"
    "v_mfma_f32_16x16x32_bf16 v[164:167], a[0:3], v[32:35], v[164:167]\n\t"
    "ds_read_b64_tr_b16 a[26:27], v254 offset:20480\n\t"
    "v_add_f32 v208, v208, v178\n\t"
    "v_mfma_f32_16x16x32_bf16 v[168:171], a[0:3], v[64:67], v[188:191]\n\t"
    "v_exp_f32 v160, v160\n\t"
    "v_mfma_f32_16x16x32_bf16 v[168:171], a[4:7], v[68:71], v[168:171]\n\t"
    "v_add_f32 v209, v209, v179\n\t"
    "v_cvt_pk_bf16_f32 v159, v178, v179\n\t"
    "v_mfma_f32_16x16x32_bf16 v[168:171], a[8:11], v[72:75], v[168:171]\n\t"
    "v_exp_f32 v161, v161\n\t"
    "v_mfma_f32_16x16x32_bf16 v[168:171], a[12:15], v[76:79], v[168:171]\n\t"
    "ds_read_b64_tr_b16 a[28:29], v254 offset:24576\n\t"
    "v_add_f32 v200, v200, v160\n\t"
    "v_mfma_f32_16x16x32_bf16 v[172:175], a[12:15], v[108:111], v[192:195]\n\t"
    "v_exp_f32 v162, v162\n\t"
    "v_mfma_f32_16x16x32_bf16 v[172:175], a[8:11], v[104:107], v[172:175]\n\t"
    "v_add_f32 v201, v201, v161\n\t"
    "v_cvt_pk_bf16_f32 v16, v160, v161\n\t"
    "v_mfma_f32_16x16x32_bf16 v[172:175], a[4:7], v[100:103], v[172:175]\n\t"
    "v_exp_f32 v163, v163\n\t"
    "v_mfma_f32_16x16x32_bf16 v[172:175], a[0:3], v[96:99], v[172:175]\n\t"
    "ds_read_b64_tr_b16 a[30:31], v254 offset:28672\n\t"
    "v_add_f32 v200, v200, v162\n\t"
    "v_mfma_f32_16x16x32_bf16 v[176:179], a[0:3], v[128:131], v[196:199]\n\t"
    "v_exp_f32 v164, v164\n\t"
    "v_mfma_f32_16x16x32_bf16 v[176:179], a[4:7], v[132:135], v[176:179]\n\t"
    "v_add_f32 v201, v201, v163\n\t"
    "v_cvt_pk_bf16_f32 v17, v162, v163\n\t"
    "v_mfma_f32_16x16x32_bf16 v[176:179], a[8:11], v[136:139], v[176:179]\n\t"
    "s_waitcnt lgkmcnt(4)\n\t"
    "v_exp_f32 v165, v165\n\t"
    "v_mfma_f32_16x16x32_bf16 v[176:179], a[12:15], v[140:143], v[176:179]\n\t"
    "ds_read_b128 a[0:3], v252 offset:4096\n\t"
    "v_add_f32 v202, v202, v164\n\t"
    "v_mfma_f32_16x16x32_bf16 a[96:99], a[16:19], v[24:27], a[96:99]\n\t"
    "v_exp_f32 v166, v166\n\t"
    "v_mfma_f32_16x16x32_bf16 a[96:99], a[20:23], v[28:31], a[96:99]\n\t"
    "v_add_f32 v203, v203, v165\n\t"
    "v_cvt_pk_bf16_f32 v48, v164, v165\n\t"
    "v_mfma_f32_16x16x32_bf16 a[128:131], a[20:23], v[60:63], a[128:131]\n\t"
    "v_exp_f32 v167, v167\n\t"
    "v_mfma_f32_16x16x32_bf16 a[128:131], a[16:19], v[56:59], a[128:131]\n\t"
    "ds_read_b128 a[4:7], v252 offset:4608\n\t"
    "v_add_f32 v202, v202, v166\n\t"
    "v_mfma_f32_16x16x32_bf16 a[160:163], a[16:19], v[88:91], a[160:163]\n\t"
    "v_exp_f32 v168, v168\n\t"
    "v_mfma_f32_16x16x32_bf16 a[160:163], a[20:23], v[92:95], a[160:163]\n\t"
    "ds_read_b128 a[8:11], v252 offset:5120\n\t"
    "v_add_f32 v203, v203, v167\n\t"
    "v_cvt_pk_bf16_f32 v49, v166, v167\n\t"
    "v_mfma_f32_16x16x32_bf16 a[192:195], a[20:23], v[124:127], a[192:195]\n\t"
    "v_exp_f32 v169, v169\n\t"
    "v_mfma_f32_16x16x32_bf16 a[192:195], a[16:19], v[120:123], a[192:195]\n\t"
    "ds_read_b128 a[12:15], v252 offset:5632\n\t"
    "v_add_f32 v204, v204, v168\n\t"
    "v_mfma_f32_16x16x32_bf16 a[224:227], a[16:19], v[152:155], a[224:227]\n\t"
    "s_waitcnt lgkmcnt(4)\n\t"
    "v_exp_f32 v170, v170\n\t"
    "v_mfma_f32_16x16x32_bf16 a[224:227], a[20:23], v[156:159], a[224:227]\n\t"
    "v_add_f32 v205, v205, v169\n\t"
    "v_cvt_pk_bf16_f32 v80, v168, v169\n\t"
    "v_mfma_f32_16x16x32_bf16 a[228:231], a[28:31], v[156:159], a[228:231]\n\t"
    "v_exp_f32 v171, v171\n\t"
    "v_mfma_f32_16x16x32_bf16 a[228:231], a[24:27], v[152:155], a[228:231]\n\t"
    "ds_read_b64_tr_b16 a[16:17], v253 offset:16896\n\t"
    "v_add_f32 v204, v204, v170\n\t"
    "v_mfma_f32_16x16x32_bf16 a[196:199], a[24:27], v[120:123], a[196:199]\n\t"
    "v_exp_f32 v172, v172\n\t"
    "v_mfma_f32_16x16x32_bf16 a[196:199], a[28:31], v[124:127], a[196:199]\n\t"
    "ds_read_b64_tr_b16 a[18:19], v253 offset:20992\n\t"
    "v_add_f32 v205, v205, v171\n\t"
    "v_cvt_pk_bf16_f32 v81, v170, v171\n\t"
    "v_mfma_f32_16x16x32_bf16 a[164:167], a[28:31], v[92:95], a[164:167]\n\t"
    "v_exp_f32 v173, v173\n\t"
    "v_mfma_f32_16x16x32_bf16 a[164:167], a[24:27], v[88:91], a[164:167]\n\t"
    "ds_read_b64_tr_b16 a[20:21], v253 offset:25088\n\t"
    "v_add_f32 v206, v206, v172\n\t"
    "v_mfma_f32_16x16x32_bf16 a[132:135], a[24:27], v[56:59], a[132:135]\n\t"
    "v_exp_f32 v174, v174\n\t"
    "v_mfma_f32_16x16x32_bf16 a[132:135], a[28:31], v[60:63], a[132:135]\n\t"
    "v_add_f32 v207, v207, v173\n\t"
    "v_cvt_pk_bf16_f32 v112, v172, v173\n\t"
    "v_mfma_f32_16x16x32_bf16 a[100:103], a[28:31], v[28:31], a[100:103]\n\t"
    "s_waitcnt lgkmcnt(3)\n\t"
    "v_exp_f32 v175, v175\n\t"
    "v_mfma_f32_16x16x32_bf16 a[100:103], a[24:27], v[24:27], a[100:103]\n\t"
    "ds_read_b64_tr_b16 a[22:23], v253 offset:29184\n\t"
    "v_add_f32 v206, v206, v174\n\t"
    "v_mfma_f32_16x16x32_bf16 v[160:163], a[0:3], v[0:3], v[180:183]\n\t"
    "v_exp_f32 v176, v176\n\t"
    "v_mfma_f32_16x16x32_bf16 v[160:163], a[4:7], v[4:7], v[160:163]\n\t"
    "v_add_f32 v207, v207, v175\n\t"
    "v_cvt_pk_bf16_f32 v113, v174, v175\n\t"
    "v_mfma_f32_16x16x32_bf16 v[160:163], a[8:11], v[8:11], v[160:163]\n\t"
    "v_exp_f32 v177, v177\n\t"
    "v_mfma_f32_16x16x32_bf16 v[160:163], a[12:15], v[12:15], v[160:163]\n\t"
    "ds_read_b64_tr_b16 a[24:25], v254 offset:16896\n\t"
    "v_add_f32 v208, v208, v176\n\t"
    "v_mfma_f32_16x16x32_bf16 v[164:167], a[12:15], v[44:47], v[184:187]\n\t"
    "v_exp_f32 v178, v178\n\t"
    "v_mfma_f32_16x16x32_bf16 v[164:167], a[8:11], v[40:43], v[164:167]\n\t"
    "v_add_f32 v209, v209, v177\n\t"
    "v_cvt_pk_bf16_f32 v144, v176, v177\n\t"
    "v_mfma_f32_16x16x32_bf16 v[164:167], a[4:7], v[36:39], v[164:167]\n\t"
    "v_exp_f32 v179, v179\n\t"
    "v_mfma_f32_16x16x32_bf16 v[164:167], a[0:3], v[32:35], v[164:167]\n\t"
    "ds_read_b64_tr_b16 a[26:27], v254 offset:20992\n\t"
    "v_add_f32 v208, v208, v178\n\t"
    "v_mfma_f32_16x16x32_bf16 v[168:171], a[0:3], v[64:67], v[188:191]\n\t"
    "v_exp_f32 v160, v160\n\t"
    "v_mfma_f32_16x16x32_bf16 v[168:171], a[4:7], v[68:71], v[168:171]\n\t"
    "v_add_f32 v209, v209, v179\n\t"
    "v_cvt_pk_bf16_f32 v145, v178, v179\n\t"
    "v_mfma_f32_16x16x32_bf16 v[168:171], a[8:11], v[72:75], v[168:171]\n\t"
    "v_exp_f32 v161, v161\n\t"
    "v_mfma_f32_16x16x32_bf16 v[168:171], a[12:15], v[76:79], v[168:171]\n\t"
    "ds_read_b64_tr_b16 a[28:29], v254 offset:25088\n\t"
    "v_add_f32 v200, v200, v160\n\t"
    "v_mfma_f32_16x16x32_bf16 v[172:175], a[12:15], v[108:111], v[192:195]\n\t"
    "v_exp_f32 v162, v162\n\t"
    "v_mfma_f32_16x16x32_bf16 v[172:175], a[8:11], v[104:107], v[172:175]\n\t"
    "v_add_f32 v201, v201, v161\n\t"
    "v_cvt_pk_bf16_f32 v18, v160, v161\n\t"
    "v_mfma_f32_16x16x32_bf16 v[172:175], a[4:7], v[100:103], v[172:175]\n\t"
    "v_exp_f32 v163, v163\n\t"
    "v_mfma_f32_16x16x32_bf16 v[172:175], a[0:3], v[96:99], v[172:175]\n\t"
    "ds_read_b64_tr_b16 a[30:31], v254 offset:29184\n\t"
    "v_add_f32 v200, v200, v162\n\t"
    "v_mfma_f32_16x16x32_bf16 v[176:179], a[0:3], v[128:131], v[196:199]\n\t"
    "v_exp_f32 v164, v164\n\t"
    "v_mfma_f32_16x16x32_bf16 v[176:179], a[4:7], v[132:135], v[176:179]\n\t"
    "v_add_f32 v201, v201, v163\n\t"
    "v_cvt_pk_bf16_f32 v19, v162, v163\n\t"
    "v_mfma_f32_16x16x32_bf16 v[176:179], a[8:11], v[136:139], v[176:179]\n\t"
    "s_waitcnt lgkmcnt(4)\n\t"
    "v_exp_f32 v165, v165\n\t"
    "v_mfma_f32_16x16x32_bf16 v[176:179], a[12:15], v[140:143], v[176:179]\n\t"
    "ds_read_b128 a[0:3], v252 offset:8192\n\t"
    "v_add_f32 v202, v202, v164\n\t"
    "v_mfma_f32_16x16x32_bf16 a[104:107], a[16:19], v[24:27], a[104:107]\n\t"
    "v_exp_f32 v166, v166\n\t"
    "v_mfma_f32_16x16x32_bf16 a[104:107], a[20:23], v[28:31], a[104:107]\n\t"
    "v_add_f32 v203, v203, v165\n\t"
    "v_cvt_pk_bf16_f32 v50, v164, v165\n\t"
    "v_mfma_f32_16x16x32_bf16 a[136:139], a[20:23], v[60:63], a[136:139]\n\t"
    "v_exp_f32 v167, v167\n\t"
    "v_mfma_f32_16x16x32_bf16 a[136:139], a[16:19], v[56:59], a[136:139]\n\t"
    "ds_read_b128 a[4:7], v252 offset:8704\n\t"
    "v_add_f32 v202, v202, v166\n\t"
    "v_mfma_f32_16x16x32_bf16 a[168:171], a[16:19], v[88:91], a[168:171]\n\t"
    "v_exp_f32 v168, v168\n\t"
    "v_mfma_f32_16x16x32_bf16 a[168:171], a[20:23], v[92:95], a[168:171]\n\t"
    "ds_read_b128 a[8:11], v252 offset:9216\n\t"
    "v_add_f32 v203, v203, v167\n\t"
    "v_cvt_pk_bf16_f32 v51, v166, v167\n\t"
    "v_mfma_f32_16x16x32_bf16 a[200:203], a[20:23], v[124:127], a[200:203]\n\t"
    "v_exp_f32 v169, v169\n\t"
    "v_mfma_f32_16x16x32_bf16 a[200:203], a[16:19], v[120:123], a[200:203]\n\t"
    "ds_read_b128 a[12:15], v252 offset:9728\n\t"
    "v_add_f32 v204, v204, v168\n\t"
    "v_mfma_f32_16x16x32_bf16 a[232:235], a[16:19], v[152:155], a[232:235]\n\t"
    "s_waitcnt lgkmcnt(4)\n\t"
    "v_exp_f32 v170, v170\n\t"
    "v_mfma_f32_16x16x32_bf16 a[232:235], a[20:23], v[156:159], a[232:235]\n\t"
    "v_add_f32 v205, v205, v169\n\t"
    "v_cvt_pk_bf16_f32 v82, v168, v169\n\t"
    "v_mfma_f32_16x16x32_bf16 a[236:239], a[28:31], v[156:159], a[236:239]\n\t"
    "v_exp_f32 v171, v171\n\t"
    "v_mfma_f32_16x16x32_bf16 a[236:239], a[24:27], v[152:155], a[236:239]\n\t"
    "ds_read_b64_tr_b16 a[16:17], v253 offset:17408\n\t"
    "v_add_f32 v204, v204, v170\n\t"
    "v_mfma_f32_16x16x32_bf16 a[204:207], a[24:27], v[120:123], a[204:207]\n\t"
    "v_exp_f32 v172, v172\n\t"
    "v_mfma_f32_16x16x32_bf16 a[204:207], a[28:31], v[124:127], a[204:207]\n\t"
    "ds_read_b64_tr_b16 a[18:19], v253 offset:21504\n\t"
    "v_add_f32 v205, v205, v171\n\t"
    "v_cvt_pk_bf16_f32 v83, v170, v171\n\t"
    "v_mfma_f32_16x16x32_bf16 a[172:175], a[28:31], v[92:95], a[172:175]\n\t"
    "v_exp_f32 v173, v173\n\t"
    "v_mfma_f32_16x16x32_bf16 a[172:175], a[24:27], v[88:91], a[172:175]\n\t"
    "ds_read_b64_tr_b16 a[20:21], v253 offset:25600\n\t"
    "v_add_f32 v206, v206, v172\n\t"
    "v_mfma_f32_16x16x32_bf16 a[140:143], a[24:27], v[56:59], a[140:143]\n\t"
    "v_exp_f32 v174, v174\n\t"
    "v_mfma_f32_16x16x32_bf16 a[140:143], a[28:31], v[60:63], a[140:143]\n\t"
    "v_add_f32 v207, v207, v173\n\t"
    "v_cvt_pk_bf16_f32 v114, v172, v173\n\t"
    "v_mfma_f32_16x16x32_bf16 a[108:111], a[28:31], v[28:31], a[108:111]\n\t"
    "s_waitcnt lgkmcnt(3)\n\t"
    "v_exp_f32 v175, v175\n\t"
    "v_mfma_f32_16x16x32_bf16 a[108:111], a[24:27], v[24:27], a[108:111]\n\t"
    "ds_read_b64_tr_b16 a[22:23], v253 offset:29696\n\t"
    "v_add_f32 v206, v206, v174\n\t"
    "v_mfma_f32_16x16x32_bf16 v[160:163], a[0:3], v[0:3], v[180:183]\n\t"
    "v_exp_f32 v176, v176\n\t"
    "v_mfma_f32_16x16x32_bf16 v[160:163], a[4:7], v[4:7], v[160:163]\n\t"
    "v_add_f32 v207, v207, v175\n\t"
    "v_cvt_pk_bf16_f32 v115, v174, v175\n\t"
    "v_mfma_f32_16x16x32_bf16 v[160:163], a[8:11], v[8:11], v[160:163]\n\t"
    "v_exp_f32 v177, v177\n\t"
    "v_mfma_f32_16x16x32_bf16 v[160:163], a[12:15], v[12:15], v[160:163]\n\t"
    "ds_read_b64_tr_b16 a[24:25], v254 offset:17408\n\t"
    "v_add_f32 v208, v208, v176\n\t"
    "v_mfma_f32_16x16x32_bf16 v[164:167], a[12:15], v[44:47], v[184:187]\n\t"
    "v_exp_f32 v178, v178\n\t"
    "v_mfma_f32_16x16x32_bf16 v[164:167], a[8:11], v[40:43], v[164:167]\n\t"
    "v_add_f32 v209, v209, v177\n\t"
    "v_cvt_pk_bf16_f32 v146, v176, v177\n\t"
    "v_mfma_f32_16x16x32_bf16 v[164:167], a[4:7], v[36:39], v[164:167]\n\t"
    "v_exp_f32 v179, v179\n\t"
    "v_mfma_f32_16x16x32_bf16 v[164:167], a[0:3], v[32:35], v[164:167]\n\t"
    "ds_read_b64_tr_b16 a[26:27], v254 offset:21504\n\t"
    "v_add_f32 v208, v208, v178\n\t"
    "v_mfma_f32_16x16x32_bf16 v[168:171], a[0:3], v[64:67], v[188:191]\n\t"
    "v_exp_f32 v160, v160\n\t"
    "v_mfma_f32_16x16x32_bf16 v[168:171], a[4:7], v[68:71], v[168:171]\n\t"
    "v_add_f32 v209, v209, v179\n\t"
    "v_cvt_pk_bf16_f32 v147, v178, v179\n\t"
    "v_mfma_f32_16x16x32_bf16 v[168:171], a[8:11], v[72:75], v[168:171]\n\t"
    "v_exp_f32 v161, v161\n\t"
    "v_mfma_f32_16x16x32_bf16 v[168:171], a[12:15], v[76:79], v[168:171]\n\t"
    "ds_read_b64_tr_b16 a[28:29], v254 offset:25600\n\t"
    "v_add_f32 v200, v200, v160\n\t"
    "v_mfma_f32_16x16x32_bf16 v[172:175], a[12:15], v[108:111], v[192:195]\n\t"
    "v_exp_f32 v162, v162\n\t"
    "v_mfma_f32_16x16x32_bf16 v[172:175], a[8:11], v[104:107], v[172:175]\n\t"
    "v_add_f32 v201, v201, v161\n\t"
    "v_cvt_pk_bf16_f32 v20, v160, v161\n\t"
    "v_mfma_f32_16x16x32_bf16 v[172:175], a[4:7], v[100:103], v[172:175]\n\t"
    "v_exp_f32 v163, v163\n\t"
    "v_mfma_f32_16x16x32_bf16 v[172:175], a[0:3], v[96:99], v[172:175]\n\t"
    "ds_read_b64_tr_b16 a[30:31], v254 offset:29696\n\t"
    "v_add_f32 v200, v200, v162\n\t"
    "v_mfma_f32_16x16x32_bf16 v[176:179], a[0:3], v[128:131], v[196:199]\n\t"
    "v_exp_f32 v164, v164\n\t"
    "v_mfma_f32_16x16x32_bf16 v[176:179], a[4:7], v[132:135], v[176:179]\n\t"
    "v_add_f32 v201, v201, v163\n\t"
    "v_cvt_pk_bf16_f32 v21, v162, v163\n\t"
    "v_mfma_f32_16x16x32_bf16 v[176:179], a[8:11], v[136:139], v[176:179]\n\t"
    "s_waitcnt lgkmcnt(4)\n\t"
    "v_exp_f32 v165, v165\n\t"
    "v_mfma_f32_16x16x32_bf16 v[176:179], a[12:15], v[140:143], v[176:179]\n\t"
    "ds_read_b128 a[0:3], v252 offset:12288\n\t"
    "v_add_f32 v202, v202, v164\n\t"
    "v_mfma_f32_16x16x32_bf16 a[112:115], a[16:19], v[24:27], a[112:115]\n\t"
    "v_exp_f32 v166, v166\n\t"
    "v_mfma_f32_16x16x32_bf16 a[112:115], a[20:23], v[28:31], a[112:115]\n\t"
    "v_add_f32 v203, v203, v165\n\t"
    "v_cvt_pk_bf16_f32 v52, v164, v165\n\t"
    "v_mfma_f32_16x16x32_bf16 a[144:147], a[20:23], v[60:63], a[144:147]\n\t"
    "v_exp_f32 v167, v167\n\t"
    "v_mfma_f32_16x16x32_bf16 a[144:147], a[16:19], v[56:59], a[144:147]\n\t"
    "ds_read_b128 a[4:7], v252 offset:12800\n\t"
    "v_add_f32 v202, v202, v166\n\t"
    "v_mfma_f32_16x16x32_bf16 a[176:179], a[16:19], v[88:91], a[176:179]\n\t"
    "v_exp_f32 v168, v168\n\t"
    "v_mfma_f32_16x16x32_bf16 a[176:179], a[20:23], v[92:95], a[176:179]\n\t"
    "ds_read_b128 a[8:11], v252 offset:13312\n\t"
    "v_add_f32 v203, v203, v167\n\t"
    "v_cvt_pk_bf16_f32 v53, v166, v167\n\t"
    "v_mfma_f32_16x16x32_bf16 a[208:211], a[20:23], v[124:127], a[208:211]\n\t"
    "v_exp_f32 v169, v169\n\t"
    "v_mfma_f32_16x16x32_bf16 a[208:211], a[16:19], v[120:123], a[208:211]\n\t"
    "ds_read_b128 a[12:15], v252 offset:13824\n\t"
    "v_add_f32 v204, v204, v168\n\t"
    "v_mfma_f32_16x16x32_bf16 a[240:243], a[16:19], v[152:155], a[240:243]\n\t"
    "s_waitcnt lgkmcnt(4)\n\t"
    "v_exp_f32 v170, v170\n\t"
    "v_mfma_f32_16x16x32_bf16 a[240:243], a[20:23], v[156:159], a[240:243]\n\t"
    "v_add_f32 v205, v205, v169\n\t"
    "v_cvt_pk_bf16_f32 v84, v168, v169\n\t"
    "v_mfma_f32_16x16x32_bf16 a[244:247], a[28:31], v[156:159], a[244:247]\n\t"
    "v_exp_f32 v171, v171\n\t"
    "v_mfma_f32_16x16x32_bf16 a[244:247], a[24:27], v[152:155], a[244:247]\n\t"
    "ds_read_b64_tr_b16 a[16:17], v253 offset:17920\n\t"
    "v_add_f32 v204, v204, v170\n\t"
    "v_mfma_f32_16x16x32_bf16 a[212:215], a[24:27], v[120:123], a[212:215]\n\t"
    "v_exp_f32 v172, v172\n\t"
    "v_mfma_f32_16x16x32_bf16 a[212:215], a[28:31], v[124:127], a[212:215]\n\t"
    "ds_read_b64_tr_b16 a[18:19], v253 offset:22016\n\t"
    "v_add_f32 v205, v205, v171\n\t"
    "v_cvt_pk_bf16_f32 v85, v170, v171\n\t"
    "v_mfma_f32_16x16x32_bf16 a[180:183], a[28:31], v[92:95], a[180:183]\n\t"
    "v_exp_f32 v173, v173\n\t"
    "v_mfma_f32_16x16x32_bf16 a[180:183], a[24:27], v[88:91], a[180:183]\n\t"
    "ds_read_b64_tr_b16 a[20:21], v253 offset:26112\n\t"
    "v_add_f32 v206, v206, v172\n\t"
    "v_mfma_f32_16x16x32_bf16 a[148:151], a[24:27], v[56:59], a[148:151]\n\t"
    "v_exp_f32 v174, v174\n\t"
    "v_mfma_f32_16x16x32_bf16 a[148:151], a[28:31], v[60:63], a[148:151]\n\t"
    "v_add_f32 v207, v207, v173\n\t"
    "v_cvt_pk_bf16_f32 v116, v172, v173\n\t"
    "v_mfma_f32_16x16x32_bf16 a[116:119], a[28:31], v[28:31], a[116:119]\n\t"
    "s_waitcnt lgkmcnt(3)\n\t"
    "v_exp_f32 v175, v175\n\t"
    "v_mfma_f32_16x16x32_bf16 a[116:119], a[24:27], v[24:27], a[116:119]\n\t"
    "ds_read_b64_tr_b16 a[22:23], v253 offset:30208\n\t"
    "v_add_f32 v206, v206, v174\n\t"
    "v_mfma_f32_16x16x32_bf16 v[160:163], a[0:3], v[0:3], v[180:183]\n\t"
    "v_exp_f32 v176, v176\n\t"
    "v_mfma_f32_16x16x32_bf16 v[160:163], a[4:7], v[4:7], v[160:163]\n\t"
    "v_add_f32 v207, v207, v175\n\t"
    "v_cvt_pk_bf16_f32 v117, v174, v175\n\t"
    "v_mfma_f32_16x16x32_bf16 v[160:163], a[8:11], v[8:11], v[160:163]\n\t"
    "v_exp_f32 v177, v177\n\t"
    "v_mfma_f32_16x16x32_bf16 v[160:163], a[12:15], v[12:15], v[160:163]\n\t"
    "ds_read_b64_tr_b16 a[24:25], v254 offset:17920\n\t"
    "v_add_f32 v208, v208, v176\n\t"
    "v_mfma_f32_16x16x32_bf16 v[164:167], a[12:15], v[44:47], v[184:187]\n\t"
    "v_exp_f32 v178, v178\n\t"
    "v_mfma_f32_16x16x32_bf16 v[164:167], a[8:11], v[40:43], v[164:167]\n\t"
    "v_add_f32 v209, v209, v177\n\t"
    "v_cvt_pk_bf16_f32 v148, v176, v177\n\t"
    "v_mfma_f32_16x16x32_bf16 v[164:167], a[4:7], v[36:39], v[164:167]\n\t"
    "v_exp_f32 v179, v179\n\t"
    "v_mfma_f32_16x16x32_bf16 v[164:167], a[0:3], v[32:35], v[164:167]\n\t"
    "ds_read_b64_tr_b16 a[26:27], v254 offset:22016\n\t"
    "v_add_f32 v208, v208, v178\n\t"
    "v_mfma_f32_16x16x32_bf16 v[168:171], a[0:3], v[64:67], v[188:191]\n\t"
    "v_exp_f32 v160, v160\n\t"
    "v_mfma_f32_16x16x32_bf16 v[168:171], a[4:7], v[68:71], v[168:171]\n\t"
    "v_add_f32 v209, v209, v179\n\t"
    "v_cvt_pk_bf16_f32 v149, v178, v179\n\t"
    "v_mfma_f32_16x16x32_bf16 v[168:171], a[8:11], v[72:75], v[168:171]\n\t"
    "v_exp_f32 v161, v161\n\t"
    "v_mfma_f32_16x16x32_bf16 v[168:171], a[12:15], v[76:79], v[168:171]\n\t"
    "ds_read_b64_tr_b16 a[28:29], v254 offset:26112\n\t"
    "v_add_f32 v200, v200, v160\n\t"
    "v_mfma_f32_16x16x32_bf16 v[172:175], a[12:15], v[108:111], v[192:195]\n\t"
    "v_exp_f32 v162, v162\n\t"
    "v_mfma_f32_16x16x32_bf16 v[172:175], a[8:11], v[104:107], v[172:175]\n\t"
    "v_add_f32 v201, v201, v161\n\t"
    "v_cvt_pk_bf16_f32 v22, v160, v161\n\t"
    "v_mfma_f32_16x16x32_bf16 v[172:175], a[4:7], v[100:103], v[172:175]\n\t"
    "v_exp_f32 v163, v163\n\t"
    "v_mfma_f32_16x16x32_bf16 v[172:175], a[0:3], v[96:99], v[172:175]\n\t"
    "ds_read_b64_tr_b16 a[30:31], v254 offset:30208\n\t"
    "v_add_f32 v200, v200, v162\n\t"
    "v_mfma_f32_16x16x32_bf16 v[176:179], a[0:3], v[128:131], v[196:199]\n\t"
    "v_exp_f32 v164, v164\n\t"
    "v_mfma_f32_16x16x32_bf16 v[176:179], a[4:7], v[132:135], v[176:179]\n\t"
    "v_add_f32 v201, v201, v163\n\t"
    "v_cvt_pk_bf16_f32 v23, v162, v163\n\t"
    "v_mfma_f32_16x16x32_bf16 v[176:179], a[8:11], v[136:139], v[176:179]\n\t"
    "s_waitcnt lgkmcnt(4)\n\t"
    "v_exp_f32 v165, v165\n\t"
    "v_mfma_f32_16x16x32_bf16 v[176:179], a[12:15], v[140:143], v[176:179]\n\t"
    "v_add_f32 v202, v202, v164\n\t"
    "v_xor_b32 v253, 0x8000, v253\n\t"
    "v_mfma_f32_16x16x32_bf16 a[120:123], a[16:19], v[24:27], a[120:123]\n\t"
    "v_exp_f32 v166, v166\n\t"
    "v_mfma_f32_16x16x32_bf16 a[120:123], a[20:23], v[28:31], a[120:123]\n\t"
    "v_add_f32 v203, v203, v165\n\t"
    "v_cvt_pk_bf16_f32 v54, v164, v165\n\t"
    "v_mfma_f32_16x16x32_bf16 a[152:155], a[20:23], v[60:63], a[152:155]\n\t"
    "s_waitcnt lgkmcnt(0)\n\t"
    "v_exp_f32 v167, v167\n\t"
    "v_mfma_f32_16x16x32_bf16 a[152:155], a[16:19], v[56:59], a[152:155]\n\t"
    "s_barrier\n\t"
    "ds_read_b128 a[0:3], v252 offset:16384\n\t"
    "v_add_f32 v202, v202, v166\n\t"
    "v_mfma_f32_16x16x32_bf16 a[184:187], a[16:19], v[88:91], a[184:187]\n\t"
    "v_exp_f32 v168, v168\n\t"
    "v_mfma_f32_16x16x32_bf16 a[184:187], a[20:23], v[92:95], a[184:187]\n\t"
    "ds_read_b128 a[4:7], v252 offset:16896\n\t"
    "v_add_f32 v203, v203, v167\n\t"
    "v_cvt_pk_bf16_f32 v55, v166, v167\n\t"
    "v_mfma_f32_16x16x32_bf16 a[216:219], a[20:23], v[124:127], a[216:219]\n\t"
    "v_exp_f32 v169, v169\n\t"
    "v_mfma_f32_16x16x32_bf16 a[216:219], a[16:19], v[120:123], a[216:219]\n\t"
    "ds_read_b128 a[8:11], v252 offset:17408\n\t"
    "v_add_f32 v204, v204, v168\n\t"
    "v_mfma_f32_16x16x32_bf16 a[248:251], a[16:19], v[152:155], a[248:251]\n\t"
    "v_exp_f32 v170, v170\n\t"
    "v_mfma_f32_16x16x32_bf16 a[248:251], a[20:23], v[156:159], a[248:251]\n\t"
    "ds_read_b128 a[12:15], v252 offset:17920\n\t"
    "v_add_f32 v205, v205, v169\n\t"
    "v_cvt_pk_bf16_f32 v86, v168, v169\n\t"
    "v_mfma_f32_16x16x32_bf16 a[252:255], a[28:31], v[156:159], a[252:255]\n\t"
    "v_exp_f32 v171, v171\n\t"
    "v_mfma_f32_16x16x32_bf16 a[252:255], a[24:27], v[152:155], a[252:255]\n\t"
    "ds_read_b64_tr_b16 a[16:17], v253 offset:0\n\t"
    "v_add_f32 v204, v204, v170\n\t"
    "v_xor_b32 v254, 0x8000, v254\n\t"
    "v_mfma_f32_16x16x32_bf16 a[220:223], a[24:27], v[120:123], a[220:223]\n\t"
    "v_exp_f32 v172, v172\n\t"
    "v_mfma_f32_16x16x32_bf16 a[220:223], a[28:31], v[124:127], a[220:223]\n\t"
    "ds_read_b64_tr_b16 a[18:19], v253 offset:4096\n\t"
    "v_add_f32 v205, v205, v171\n\t"
    "v_cvt_pk_bf16_f32 v87, v170, v171\n\t"
    "v_mfma_f32_16x16x32_bf16 a[188:191], a[28:31], v[92:95], a[188:191]\n\t"
    "v_exp_f32 v173, v173\n\t"
    "v_mfma_f32_16x16x32_bf16 a[188:191], a[24:27], v[88:91], a[188:191]\n\t"
    "ds_read_b64_tr_b16 a[20:21], v253 offset:8192\n\t"
    "v_add_f32 v206, v206, v172\n\t"
    "v_mfma_f32_16x16x32_bf16 a[156:159], a[24:27], v[56:59], a[156:159]\n\t"
    "v_exp_f32 v174, v174\n\t"
    "v_mfma_f32_16x16x32_bf16 a[156:159], a[28:31], v[60:63], a[156:159]\n\t"
    "v_add_f32 v207, v207, v173\n\t"
    "v_cvt_pk_bf16_f32 v118, v172, v173\n\t"
    "v_mfma_f32_16x16x32_bf16 a[124:127], a[28:31], v[28:31], a[124:127]\n\t"
    "s_waitcnt lgkmcnt(3)\n\t"
    "v_exp_f32 v175, v175\n\t"
    "v_mfma_f32_16x16x32_bf16 a[124:127], a[24:27], v[24:27], a[124:127]\n\t"
    "s_branch L_loopA_%=\n\t"
    "L_exitA_%=:\n\t"
    "ds_read_b64_tr_b16 a[22:23], v253 offset:28672\n\t"
    "v_add_f32 v206, v206, v174\n\t"
    "v_mfma_f32_16x16x32_bf16 v[160:163], a[0:3], v[0:3], v[180:183]\n\t"
    "v_add_f32 v207, v207, v175\n\t"
    "v_cvt_pk_bf16_f32 v127, v174, v175\n\t"
    "v_mfma_f32_16x16x32_bf16 v[160:163], a[4:7], v[4:7], v[160:163]\n\t"
    "v_add_u32 v253, s94, v253\n\t"
    "v_add_u32 v254, s94, v254\n\t"
    "v_mfma_f32_16x16x32_bf16 v[160:163], a[8:11], v[8:11], v[160:163]\n\t"
    "v_mfma_f32_16x16x32_bf16 v[160:163], a[12:15], v[12:15], v[160:163]\n\t"
    "s_waitcnt lgkmcnt(0)\n\t"
    "ds_read_b128 a[16:19], v252 offset:4096\n\t"
    "v_mfma_f32_16x16x32_bf16 v[164:167], a[12:15], v[44:47], v[184:187]\n\t"
    "v_exp_f32 v176, v176\n\t"
    "v_mfma_f32_16x16x32_bf16 v[164:167], a[8:11], v[40:43], v[164:167]\n\t"
    "v_exp_f32 v177, v177\n\t"
    "v_mfma_f32_16x16x32_bf16 v[164:167], a[4:7], v[36:39], v[164:167]\n\t"
    "v_exp_f32 v178, v178\n\t"
    "v_mfma_f32_16x16x32_bf16 v[164:167], a[0:3], v[32:35], v[164:167]\n\t"
    "ds_read_b128 a[20:23], v252 offset:4608\n\t"
    "ds_read_b64_tr_b16 a[32:33], v253 offset:0\n\t"
    "ds_read_b64_tr_b16 a[34:35], v253 offset:4096\n\t"
    "ds_read_b64_tr_b16 a[36:37], v253 offset:8192\n\t"
    "ds_read_b64_tr_b16 a[38:39], v253 offset:12288\n\t"
    "v_exp_f32 v179, v179\n\t"
    "v_add_f32 v208, v208, v176\n\t"
    "v_add_f32 v209, v209, v177\n\t"
    "v_cvt_pk_bf16_f32 v158, v176, v177\n\t"
    "v_add_f32 v208, v208, v178\n\t"
    "v_add_f32 v209, v209, v179\n\t"
    "v_cvt_pk_bf16_f32 v159, v178, v179\n\t"
    "v_mfma_f32_16x16x32_bf16 v[168:171], a[0:3], v[64:67], v[188:191]\n\t"
    "v_exp_f32 v160, v160\n\t"
    "v_mfma_f32_16x16x32_bf16 v[168:171], a[4:7], v[68:71], v[168:171]\n\t"
    "v_exp_f32 v161, v161\n\t"
    "v_mfma_f32_16x16x32_bf16 v[168:171], a[8:11], v[72:75], v[168:171]\n\t"
    "v_exp_f32 v162, v162\n\t"
    "v_mfma_f32_16x16x32_bf16 v[168:171], a[12:15], v[76:79], v[168:171]\n\t"
    "ds_read_b128 a[24:27], v252 offset:5120\n\t"
    "ds_read_b64_tr_b16 a[40:41], v254 offset:0\n\t"
    "ds_read_b64_tr_b16 a[42:43], v254 offset:4096\n\t"
    "ds_read_b64_tr_b16 a[44:45], v254 offset:8192\n\t"
    "ds_read_b64_tr_b16 a[46:47], v254 offset:12288\n\t"
    "v_exp_f32 v163, v163\n\t"
    "v_add_f32 v200, v200, v160\n\t"
    "v_add_f32 v201, v201, v161\n\t"
    "v_cvt_pk_bf16_f32 v16, v160, v161\n\t"
    "v_add_f32 v200, v200, v162\n\t"
    "v_add_f32 v201, v201, v163\n\t"
    "v_cvt_pk_bf16_f32 v17, v162, v163\n\t"
    "v_mfma_f32_16x16x32_bf16 v[172:175], a[12:15], v[108:111], v[192:195]\n\t"
    "v_exp_f32 v164, v164\n\t"
    "v_mfma_f32_16x16x32_bf16 v[172:175], a[8:11], v[104:107], v[172:175]\n\t"
    "v_exp_f32 v165, v165\n\t"
    "v_mfma_f32_16x16x32_bf16 v[172:175], a[4:7], v[100:103], v[172:175]\n\t"
    "v_exp_f32 v166, v166\n\t"
    "v_mfma_f32_16x16x32_bf16 v[172:175], a[0:3], v[96:99], v[172:175]\n\t"
    "ds_read_b128 a[28:31], v252 offset:5632\n\t"
    "ds_read_b64_tr_b16 a[48:49], v253 offset:16384\n\t"
    "ds_read_b64_tr_b16 a[50:51], v253 offset:20480\n\t"
    "ds_read_b64_tr_b16 a[52:53], v253 offset:24576\n\t"
    "ds_read_b64_tr_b16 a[54:55], v253 offset:28672\n\t"
    "v_exp_f32 v167, v167\n\t"
    "v_add_f32 v202, v202, v164\n\t"
    "v_add_f32 v203, v203, v165\n\t"
    "v_cvt_pk_bf16_f32 v48, v164, v165\n\t"
    "v_add_f32 v202, v202, v166\n\t"
    "v_add_f32 v203, v203, v167\n\t"
    "v_cvt_pk_bf16_f32 v49, v166, v167\n\t"
    "v_mfma_f32_16x16x32_bf16 v[176:179], a[0:3], v[128:131], v[196:199]\n\t"
    "v_exp_f32 v168, v168\n\t"
    "v_mfma_f32_16x16x32_bf16 v[176:179], a[4:7], v[132:135], v[176:179]\n\t"
    "v_exp_f32 v169, v169\n\t"
    "v_mfma_f32_16x16x32_bf16 v[176:179], a[8:11], v[136:139], v[176:179]\n\t"
    "v_exp_f32 v170, v170\n\t"
    "v_mfma_f32_16x16x32_bf16 v[176:179], a[12:15], v[140:143], v[176:179]\n\t"
    "ds_read_b64_tr_b16 a[56:57], v254 offset:16384\n\t"
    "ds_read_b64_tr_b16 a[58:59], v254 offset:20480\n\t"
    "ds_read_b64_tr_b16 a[60:61], v254 offset:24576\n\t"
    "ds_read_b64_tr_b16 a[62:63], v254 offset:28672\n\t"
    "v_exp_f32 v171, v171\n\t"
    "v_add_f32 v204, v204, v168\n\t"
    "v_add_f32 v205, v205, v169\n\t"
    "v_cvt_pk_bf16_f32 v80, v168, v169\n\t"
    "v_add_f32 v204, v204, v170\n\t"
    "v_add_f32 v205, v205, v171\n\t"
    "v_cvt_pk_bf16_f32 v81, v170, v171\n\t"
    "s_waitcnt lgkmcnt(8)\n\t"
    "v_mfma_f32_16x16x32_bf16 v[160:163], a[16:19], v[0:3], v[180:183]\n\t"
    "v_exp_f32 v172, v172\n\t"
    "v_mfma_f32_16x16x32_bf16 v[160:163], a[20:23], v[4:7], v[160:163]\n\t"
    "v_exp_f32 v173, v173\n\t"
    "v_mfma_f32_16x16x32_bf16 v[160:163], a[24:27], v[8:11], v[160:163]\n\t"
    "v_exp_f32 v174, v174\n\t"
    "v_mfma_f32_16x16x32_bf16 v[160:163], a[28:31], v[12:15], v[160:163]\n\t"
    "ds_read_b128 a[0:3], v252 offset:8192\n\t"
    "v_exp_f32 v175, v175\n\t"
    "v_add_f32 v206, v206, v172\n\t"
    "v_add_f32 v207, v207, v173\n\t"
    "v_cvt_pk_bf16_f32 v112, v172, v173\n\t"
    "v_add_f32 v206, v206, v174\n\t"
    "v_add_f32 v207, v207, v175\n\t"
    "v_cvt_pk_bf16_f32 v113, v174, v175\n\t"
    "v_mfma_f32_16x16x32_bf16 v[164:167], a[28:31], v[44:47], v[184:187]\n\t"
    "v_exp_f32 v176, v176\n\t"
    "v_mfma_f32_16x16x32_bf16 v[164:167], a[24:27], v[40:43], v[164:167]\n\t"
    "v_exp_f32 v177, v177\n\t"
    "v_mfma_f32_16x16x32_bf16 v[164:167], a[20:23], v[36:39], v[164:167]\n\t"
    "v_exp_f32 v178, v178\n\t"
    "v_mfma_f32_16x16x32_bf16 v[164:167], a[16:19], v[32:35], v[164:167]\n\t"
    "ds_read_b128 a[4:7], v252 offset:8704\n\t"
    "v_subrev_u32 v253, 0x20000, v253\n\t"
    "v_subrev_u32 v254, 0x20000, v254\n\t"
    "v_exp_f32 v179, v179\n\t"
    "v_add_f32 v208, v208, v176\n\t"
    "v_add_f32 v209, v209, v177\n\t"
    "v_cvt_pk_bf16_f32 v144, v176, v177\n\t"
    "v_add_f32 v208, v208, v178\n\t"
    "v_add_f32 v209, v209, v179\n\t"
    "v_cvt_pk_bf16_f32 v145, v178, v179\n\t"
    "v_mfma_f32_16x16x32_bf16 v[168:171], a[16:19], v[64:67], v[188:191]\n\t"
    "v_exp_f32 v160, v160\n\t"
    "v_mfma_f32_16x16x32_bf16 v[168:171], a[20:23], v[68:71], v[168:171]\n\t"
    "v_exp_f32 v161, v161\n\t"
    "v_mfma_f32_16x16x32_bf16 v[168:171], a[24:27], v[72:75], v[168:171]\n\t"
    "v_exp_f32 v162, v162\n\t"
    "v_mfma_f32_16x16x32_bf16 v[168:171], a[28:31], v[76:79], v[168:171]\n\t"
    "ds_read_b128 a[8:11], v252 offset:9216\n\t"
    "v_exp_f32 v163, v163\n\t"
    "v_add_f32 v200, v200, v160\n\t"
    "v_add_f32 v201, v201, v161\n\t"
    "v_cvt_pk_bf16_f32 v18, v160, v161\n\t"
    "v_add_f32 v200, v200, v162\n\t"
    "v_add_f32 v201, v201, v163\n\t"
    "v_cvt_pk_bf16_f32 v19, v162, v163\n\t"
    "v_mfma_f32_16x16x32_bf16 v[172:175], a[28:31], v[108:111], v[192:195]\n\t"
    "v_exp_f32 v164, v164\n\t"
    "v_mfma_f32_16x16x32_bf16 v[172:175], a[24:27], v[104:107], v[172:175]\n\t"
    "v_exp_f32 v165, v165\n\t"
    "v_mfma_f32_16x16x32_bf16 v[172:175], a[20:23], v[100:103], v[172:175]\n\t"
    "v_exp_f32 v166, v166\n\t"
    "v_mfma_f32_16x16x32_bf16 v[172:175], a[16:19], v[96:99], v[172:175]\n\t"
    "ds_read_b128 a[12:15], v252 offset:9728\n\t"
    "v_exp_f32 v167, v167\n\t"
    "v_add_f32 v202, v202, v164\n\t"
    "v_add_f32 v203, v203, v165\n\t"
    "v_cvt_pk_bf16_f32 v50, v164, v165\n\t"
    "v_add_f32 v202, v202, v166\n\t"
    "v_add_f32 v203, v203, v167\n\t"
    "v_cvt_pk_bf16_f32 v51, v166, v167\n\t"
    "v_mfma_f32_16x16x32_bf16 v[176:179], a[16:19], v[128:131], v[196:199]\n\t"
    "v_exp_f32 v168, v168\n\t"
    "v_mfma_f32_16x16x32_bf16 v[176:179], a[20:23], v[132:135], v[176:179]\n\t"
    "v_exp_f32 v169, v169\n\t"
    "v_mfma_f32_16x16x32_bf16 v[176:179], a[24:27], v[136:139], v[176:179]\n\t"
    "v_exp_f32 v170, v170\n\t"
    "v_mfma_f32_16x16x32_bf16 v[176:179], a[28:31], v[140:143], v[176:179]\n\t"
    "v_exp_f32 v171, v171\n\t"
    "v_add_f32 v204, v204, v168\n\t"
    "v_add_f32 v205, v205, v169\n\t"
    "v_cvt_pk_bf16_f32 v82, v168, v169\n\t"
    "v_add_f32 v204, v204, v170\n\t"
    "v_add_f32 v205, v205, v171\n\t"
    "v_cvt_pk_bf16_f32 v83, v170, v171\n\t"
    "s_waitcnt lgkmcnt(0)\n\t"
    "v_mfma_f32_16x16x32_bf16 v[160:163], a[0:3], v[0:3], v[180:183]\n\t"
    "v_exp_f32 v172, v172\n\t"
    "v_mfma_f32_16x16x32_bf16 v[160:163], a[4:7], v[4:7], v[160:163]\n\t"
    "s_add_u32 s53, s98, 393216\n\t"
    "v_exp_f32 v173, v173\n\t"
    "v_mfma_f32_16x16x32_bf16 v[160:163], a[8:11], v[8:11], v[160:163]\n\t"
    "s_add_u32 m0, s97, 65536\n\t"
    "v_exp_f32 v174, v174\n\t"
    "v_mfma_f32_16x16x32_bf16 v[160:163], a[12:15], v[12:15], v[160:163]\n\t"
    "buffer_load_dwordx4 v255, s[24:27], s53 offen offset:0 lds\n\t"
    "ds_read_b128 a[16:19], v252 offset:12288\n\t"
    "v_exp_f32 v175, v175\n\t"
    "v_add_f32 v206, v206, v172\n\t"
    "v_add_f32 v207, v207, v173\n\t"
    "v_cvt_pk_bf16_f32 v114, v172, v173\n\t"
    "v_add_f32 v206, v206, v174\n\t"
    "v_add_f32 v207, v207, v175\n\t"
    "v_cvt_pk_bf16_f32 v115, v174, v175\n\t"
    "v_mfma_f32_16x16x32_bf16 v[164:167], a[12:15], v[44:47], v[184:187]\n\t"
    "v_exp_f32 v176, v176\n\t"
    "v_mfma_f32_16x16x32_bf16 v[164:167], a[8:11], v[40:43], v[164:167]\n\t"
    "v_exp_f32 v177, v177\n\t"
    "v_mfma_f32_16x16x32_bf16 v[164:167], a[4:7], v[36:39], v[164:167]\n\t"
    "s_add_u32 m0, s97, 66432\n\t"
    "v_exp_f32 v178, v178\n\t"
    "v_mfma_f32_16x16x32_bf16 v[164:167], a[0:3], v[32:35], v[164:167]\n\t"
    "buffer_load_dwordx4 v255, s[24:27], s53 offen offset:128 lds\n\t"
    "ds_read_b128 a[20:23], v252 offset:12800\n\t"
    "v_exp_f32 v179, v179\n\t"
    "v_add_f32 v208, v208, v176\n\t"
    "v_add_f32 v209, v209, v177\n\t"
    "v_cvt_pk_bf16_f32 v146, v176, v177\n\t"
    "v_add_f32 v208, v208, v178\n\t"
    "v_add_f32 v209, v209, v179\n\t"
    "v_cvt_pk_bf16_f32 v147, v178, v179\n\t"
    "v_mfma_f32_16x16x32_bf16 v[168:171], a[0:3], v[64:67], v[188:191]\n\t"
    "v_exp_f32 v160, v160\n\t"
    "v_mfma_f32_16x16x32_bf16 v[168:171], a[4:7], v[68:71], v[168:171]\n\t"
    "s_add_u32 s53, s98, 401408\n\t"
    "v_exp_f32 v161, v161\n\t"
    "v_mfma_f32_16x16x32_bf16 v[168:171], a[8:11], v[72:75], v[168:171]\n\t"
    "s_add_u32 m0, s97, 67584\n\t"
    "v_exp_f32 v162, v162\n\t"
    "v_mfma_f32_16x16x32_bf16 v[168:171], a[12:15], v[76:79], v[168:171]\n\t"
    "buffer_load_dwordx4 v255, s[24:27], s53 offen offset:0 lds\n\t"
    "ds_read_b128 a[24:27], v252 offset:13312\n\t"
    "v_exp_f32 v163, v163\n\t"
    "v_add_f32 v200, v200, v160\n\t"
    "v_add_f32 v201, v201, v161\n\t"
    "v_cvt_pk_bf16_f32 v20, v160, v161\n\t"
    "v_add_f32 v200, v200, v162\n\t"
    "v_add_f32 v201, v201, v163\n\t"
    "v_cvt_pk_bf16_f32 v21, v162, v163\n\t"
    "v_mfma_f32_16x16x32_bf16 v[172:175], a[12:15], v[108:111], v[192:195]\n\t"
    "v_exp_f32 v164, v164\n\t"
    "v_mfma_f32_16x16x32_bf16 v[172:175], a[8:11], v[104:107], v[172:175]\n\t"
    "v_exp_f32 v165, v165\n\t"
    "v_mfma_f32_16x16x32_bf16 v[172:175], a[4:7], v[100:103], v[172:175]\n\t"
    "s_add_u32 m0, s97, 68480\n\t"
    "v_exp_f32 v166, v166\n\t"
    "v_mfma_f32_16x16x32_bf16 v[172:175], a[0:3], v[96:99], v[172:175]\n\t"
    "buffer_load_dwordx4 v255, s[24:27], s53 offen offset:128 lds\n\t"
    "ds_read_b128 a[28:31], v252 offset:13824\n\t"
    "v_exp_f32 v167, v167\n\t"
    "v_add_f32 v202, v202, v164\n\t"
    "v_add_f32 v203, v203, v165\n\t"
    "v_cvt_pk_bf16_f32 v52, v164, v165\n\t"
    "v_add_f32 v202, v202, v166\n\t"
    "v_add_f32 v203, v203, v167\n\t"
    "v_cvt_pk_bf16_f32 v53, v166, v167\n\t"
    "v_mfma_f32_16x16x32_bf16 v[176:179], a[0:3], v[128:131], v[196:199]\n\t"
    "v_exp_f32 v168, v168\n\t"
    "v_mfma_f32_16x16x32_bf16 v[176:179], a[4:7], v[132:135], v[176:179]\n\t"
    "v_exp_f32 v169, v169\n\t"
    "v_mfma_f32_16x16x32_bf16 v[176:179], a[8:11], v[136:139], v[176:179]\n\t"
    "v_exp_f32 v170, v170\n\t"
    "v_mfma_f32_16x16x32_bf16 v[176:179], a[12:15], v[140:143], v[176:179]\n\t"
    "v_exp_f32 v171, v171\n\t"
    "v_add_f32 v204, v204, v168\n\t"
    "v_add_f32 v205, v205, v169\n\t"
    "v_cvt_pk_bf16_f32 v84, v168, v169\n\t"
    "v_add_f32 v204, v204, v170\n\t"
    "v_add_f32 v205, v205, v171\n\t"
    "v_cvt_pk_bf16_f32 v85, v170, v171\n\t"
    "s_waitcnt lgkmcnt(0)\n\t"
    "v_mfma_f32_16x16x32_bf16 v[160:163], a[16:19], v[0:3], v[180:183]\n\t"
    "v_exp_f32 v172, v172\n\t"
    "v_mfma_f32_16x16x32_bf16 v[160:163], a[20:23], v[4:7], v[160:163]\n\t"
    "v_exp_f32 v173, v173\n\t"
    "v_mfma_f32_16x16x32_bf16 v[160:163], a[24:27], v[8:11], v[160:163]\n\t"
    "s_waitcnt lgkmcnt(8)\n\t"
    "v_exp_f32 v174, v174\n\t"
    "v_mfma_f32_16x16x32_bf16 v[160:163], a[28:31], v[12:15], v[160:163]\n\t"
    "s_barrier\n\t"
    "ds_read_b128 a[0:3], v252 offset:16384\n\t"
    "v_exp_f32 v175, v175\n\t"
    "v_add_f32 v206, v206, v172\n\t"
    "v_add_f32 v207, v207, v173\n\t"
    "v_cvt_pk_bf16_f32 v116, v172, v173\n\t"
    "v_add_f32 v206, v206, v174\n\t"
    "v_add_f32 v207, v207, v175\n\t"
    "v_cvt_pk_bf16_f32 v117, v174, v175\n\t"
    "v_mfma_f32_16x16x32_bf16 v[164:167], a[28:31], v[44:47], v[184:187]\n\t"
    "v_exp_f32 v176, v176\n\t"
    "v_mfma_f32_16x16x32_bf16 v[164:167], a[24:27], v[40:43], v[164:167]\n\t"
    "s_add_u32 s53, s98, 262144\n\t"
    "v_exp_f32 v177, v177\n\t"
    "v_mfma_f32_16x16x32_bf16 v[164:167], a[20:23], v[36:39], v[164:167]\n\t"
    "s_add_u32 m0, s97, 131072\n\t"
    "v_exp_f32 v178, v178\n\t"
    "v_mfma_f32_16x16x32_bf16 v[164:167], a[16:19], v[32:35], v[164:167]\n\t"
    "buffer_load_dwordx4 v255, s[24:27], s53 offen offset:0 lds\n\t"
    "ds_read_b128 a[4:7], v252 offset:16896\n\t"
    "v_exp_f32 v179, v179\n\t"
    "v_add_f32 v208, v208, v176\n\t"
    "v_add_f32 v209, v209, v177\n\t"
    "v_cvt_pk_bf16_f32 v148, v176, v177\n\t"
    "v_add_f32 v208, v208, v178\n\t"
    "v_add_f32 v209, v209, v179\n\t"
    "v_cvt_pk_bf16_f32 v149, v178, v179\n\t"
    "v_mfma_f32_16x16x32_bf16 v[168:171], a[16:19], v[64:67], v[188:191]\n\t"
    "v_exp_f32 v160, v160\n\t"
    "v_mfma_f32_16x16x32_bf16 v[168:171], a[20:23], v[68:71], v[168:171]\n\t"
    "v_exp_f32 v161, v161\n\t"
    "v_mfma_f32_16x16x32_bf16 v[168:171], a[24:27], v[72:75], v[168:171]\n\t"
    "s_add_u32 m0, s97, 131968\n\t"
    "v_exp_f32 v162, v162\n\t"
    "v_mfma_f32_16x16x32_bf16 v[168:171], a[28:31], v[76:79], v[168:171]\n\t"
    "buffer_load_dwordx4 v255, s[24:27], s53 offen offset:128 lds\n\t"
    "ds_read_b128 a[8:11], v252 offset:17408\n\t"
    "v_exp_f32 v163, v163\n\t"
    "v_add_f32 v200, v200, v160\n\t"
    "v_add_f32 v201, v201, v161\n\t"
    "v_cvt_pk_bf16_f32 v22, v160, v161\n\t"
    "v_add_f32 v200, v200, v162\n\t"
    "v_add_f32 v201, v201, v163\n\t"
    "v_cvt_pk_bf16_f32 v23, v162, v163\n\t"
    "v_mfma_f32_16x16x32_bf16 v[172:175], a[28:31], v[108:111], v[192:195]\n\t"
    "v_exp_f32 v164, v164\n\t"
    "v_mfma_f32_16x16x32_bf16 v[172:175], a[24:27], v[104:107], v[172:175]\n\t"
    "s_add_u32 s53, s98, 270336\n\t"
    "v_exp_f32 v165, v165\n\t"
    "v_mfma_f32_16x16x32_bf16 v[172:175], a[20:23], v[100:103], v[172:175]\n\t"
    "s_add_u32 m0, s97, 133120\n\t"
    "v_exp_f32 v166, v166\n\t"
    "v_mfma_f32_16x16x32_bf16 v[172:175], a[16:19], v[96:99], v[172:175]\n\t"
    "buffer_load_dwordx4 v255, s[24:27], s53 offen offset:0 lds\n\t"
    "ds_read_b128 a[12:15], v252 offset:17920\n\t"
    "v_exp_f32 v167, v167\n\t"
    "v_add_f32 v202, v202, v164\n\t"
    "v_add_f32 v203, v203, v165\n\t"
    "v_cvt_pk_bf16_f32 v54, v164, v165\n\t"
    "v_add_f32 v202, v202, v166\n\t"
    "v_add_f32 v203, v203, v167\n\t"
    "v_cvt_pk_bf16_f32 v55, v166, v167\n\t"
    "v_mfma_f32_16x16x32_bf16 v[176:179], a[16:19], v[128:131], v[196:199]\n\t"
    "v_exp_f32 v168, v168\n\t"
    "v_mfma_f32_16x16x32_bf16 v[176:179], a[20:23], v[132:135], v[176:179]\n\t"
    "v_exp_f32 v169, v169\n\t"
    "v_mfma_f32_16x16x32_bf16 v[176:179], a[24:27], v[136:139], v[176:179]\n\t"
    "s_add_u32 m0, s97, 134016\n\t"
    "v_exp_f32 v170, v170\n\t"
    "v_mfma_f32_16x16x32_bf16 v[176:179], a[28:31], v[140:143], v[176:179]\n\t"
    "buffer_load_dwordx4 v255, s[24:27], s53 offen offset:128 lds\n\t"
    "v_exp_f32 v171, v171\n\t"
    "v_add_f32 v204, v204, v168\n\t"
    "v_add_f32 v205, v205, v169\n\t"
    "v_cvt_pk_bf16_f32 v86, v168, v169\n\t"
    "v_add_f32 v204, v204, v170\n\t"
    "v_add_f32 v205, v205, v171\n\t"
    "v_cvt_pk_bf16_f32 v87, v170, v171\n\t"
    "s_waitcnt lgkmcnt(0)\n\t"
    "v_mfma_f32_16x16x32_bf16 v[160:163], a[0:3], v[0:3], v[180:183]\n\t"
    "v_exp_f32 v172, v172\n\t"
    "v_mfma_f32_16x16x32_bf16 v[160:163], a[4:7], v[4:7], v[160:163]\n\t"
    "s_add_u32 s53, s98, 327680\n\t"
    "v_exp_f32 v173, v173\n\t"
    "v_mfma_f32_16x16x32_bf16 v[160:163], a[8:11], v[8:11], v[160:163]\n\t"
    "s_add_u32 m0, s97, 147456\n\t"
    "v_exp_f32 v174, v174\n\t"
    "v_mfma_f32_16x16x32_bf16 v[160:163], a[12:15], v[12:15], v[160:163]\n\t"
    "buffer_load_dwordx4 v255, s[24:27], s53 offen offset:0 lds\n\t"
    "ds_read_b128 a[16:19], v252 offset:20480\n\t"
    "v_exp_f32 v175, v175\n\t"
    "v_add_f32 v206, v206, v172\n\t"
    "v_add_f32 v207, v207, v173\n\t"
    "v_cvt_pk_bf16_f32 v118, v172, v173\n\t"
    "v_add_f32 v206, v206, v174\n\t"
    "v_add_f32 v207, v207, v175\n\t"
    "v_cvt_pk_bf16_f32 v119, v174, v175\n\t"
    "v_mfma_f32_16x16x32_bf16 v[164:167], a[12:15], v[44:47], v[184:187]\n\t"
    "v_exp_f32 v176, v176\n\t"
    "v_mfma_f32_16x16x32_bf16 v[164:167], a[8:11], v[40:43], v[164:167]\n\t"
    "v_exp_f32 v177, v177\n\t"
    "v_mfma_f32_16x16x32_bf16 v[164:167], a[4:7], v[36:39], v[164:167]\n\t"
    "s_add_u32 m0, s97, 148352\n\t"
    "v_exp_f32 v178, v178\n\t"
    "v_mfma_f32_16x16x32_bf16 v[164:167], a[0:3], v[32:35], v[164:167]\n\t"
    "buffer_load_dwordx4 v255, s[24:27], s53 offen offset:128 lds\n\t"
    "ds_read_b128 a[20:23], v252 offset:20992\n\t"
    "v_exp_f32 v179, v179\n\t"
    "v_add_f32 v208, v208, v176\n\t"
    "v_add_f32 v209, v209, v177\n\t"
    "v_cvt_pk_bf16_f32 v150, v176, v177\n\t"
    "v_add_f32 v208, v208, v178\n\t"
    "v_add_f32 v209, v209, v179\n\t"
    "v_cvt_pk_bf16_f32 v151, v178, v179\n\t"
    "v_mfma_f32_16x16x32_bf16 v[168:171], a[0:3], v[64:67], v[188:191]\n\t"
    "v_exp_f32 v160, v160\n\t"
    "v_mfma_f32_16x16x32_bf16 v[168:171], a[4:7], v[68:71], v[168:171]\n\t"
    "s_add_u32 s53, s98, 335872\n\t"
    "v_exp_f32 v161, v161\n\t"
    "v_mfma_f32_16x16x32_bf16 v[168:171], a[8:11], v[72:75], v[168:171]\n\t"
    "s_add_u32 m0, s97, 149504\n\t"
    "v_exp_f32 v162, v162\n\t"
    "v_mfma_f32_16x16x32_bf16 v[168:171], a[12:15], v[76:79], v[168:171]\n\t"
    "buffer_load_dwordx4 v255, s[24:27], s53 offen offset:0 lds\n\t"
    "ds_read_b128 a[24:27], v252 offset:21504\n\t"
    "v_exp_f32 v163, v163\n\t"
    "v_add_f32 v200, v200, v160\n\t"
    "v_add_f32 v201, v201, v161\n\t"
    "v_cvt_pk_bf16_f32 v212, v160, v161\n\t"
    "v_add_f32 v200, v200, v162\n\t"
    "v_add_f32 v201, v201, v163\n\t"
    "v_cvt_pk_bf16_f32 v213, v162, v163\n\t"
    "v_mfma_f32_16x16x32_bf16 v[172:175], a[12:15], v[108:111], v[192:195]\n\t"
    "v_exp_f32 v164, v164\n\t"
    "v_mfma_f32_16x16x32_bf16 v[172:175], a[8:11], v[104:107], v[172:175]\n\t"
    "v_exp_f32 v165, v165\n\t"
    "v_mfma_f32_16x16x32_bf16 v[172:175], a[4:7], v[100:103], v[172:175]\n\t"
    "v_exp_f32 v166, v166\n\t"
    "v_mfma_f32_16x16x32_bf16 v[172:175], a[0:3], v[96:99], v[172:175]\n\t"
    "s_barrier\n\t"
    "ds_read_b128 a[28:31], v252 offset:22016\n\t"
    "ds_read_b64_tr_b16 a[64:65], v253 offset:0\n\t"
    "ds_read_b64_tr_b16 a[66:67], v253 offset:4096\n\t"
    "ds_read_b64_tr_b16 a[68:69], v253 offset:8192\n\t"
    "ds_read_b64_tr_b16 a[70:71], v253 offset:12288\n\t"
    "v_exp_f32 v167, v167\n\t"
    "v_add_f32 v202, v202, v164\n\t"
    "v_add_f32 v203, v203, v165\n\t"
    "v_cvt_pk_bf16_f32 v220, v164, v165\n\t"
    "v_add_f32 v202, v202, v166\n\t"
    "v_add_f32 v203, v203, v167\n\t"
    "v_cvt_pk_bf16_f32 v221, v166, v167\n\t"
    "v_mfma_f32_16x16x32_bf16 v[176:179], a[0:3], v[128:131], v[196:199]\n\t"
    "v_exp_f32 v168, v168\n\t"
    "v_mfma_f32_16x16x32_bf16 v[176:179], a[4:7], v[132:135], v[176:179]\n\t"
    "v_exp_f32 v169, v169\n\t"
    "v_mfma_f32_16x16x32_bf16 v[176:179], a[8:11], v[136:139], v[176:179]\n\t"
    "s_add_u32 m0, s97, 150400\n\t"
    "v_exp_f32 v170, v170\n\t"
    "v_mfma_f32_16x16x32_bf16 v[176:179], a[12:15], v[140:143], v[176:179]\n\t"
    "buffer_load_dwordx4 v255, s[24:27], s53 offen offset:128 lds\n\t"
    "ds_read_b64_tr_b16 a[72:73], v254 offset:0\n\t"
    "ds_read_b64_tr_b16 a[74:75], v254 offset:4096\n\t"
    "ds_read_b64_tr_b16 a[76:77], v254 offset:8192\n\t"
    "ds_read_b64_tr_b16 a[78:79], v254 offset:12288\n\t"
    "v_exp_f32 v171, v171\n\t"
    "v_add_f32 v204, v204, v168\n\t"
    "v_add_f32 v205, v205, v169\n\t"
    "v_cvt_pk_bf16_f32 v228, v168, v169\n\t"
    "v_add_f32 v204, v204, v170\n\t"
    "v_add_f32 v205, v205, v171\n\t"
    "v_cvt_pk_bf16_f32 v229, v170, v171\n\t"
    "s_waitcnt lgkmcnt(8)\n\t"
    "v_mfma_f32_16x16x32_bf16 v[160:163], a[16:19], v[0:3], v[180:183]\n\t"
    "v_exp_f32 v172, v172\n\t"
    "v_mfma_f32_16x16x32_bf16 v[160:163], a[20:23], v[4:7], v[160:163]\n\t"
    "v_exp_f32 v173, v173\n\t"
    "v_mfma_f32_16x16x32_bf16 v[160:163], a[24:27], v[8:11], v[160:163]\n\t"
    "v_exp_f32 v174, v174\n\t"
    "v_mfma_f32_16x16x32_bf16 v[160:163], a[28:31], v[12:15], v[160:163]\n\t"
    "ds_read_b128 a[0:3], v252 offset:24576\n\t"
    "ds_read_b64_tr_b16 a[80:81], v253 offset:16384\n\t"
    "ds_read_b64_tr_b16 a[82:83], v253 offset:20480\n\t"
    "ds_read_b64_tr_b16 a[84:85], v253 offset:24576\n\t"
    "ds_read_b64_tr_b16 a[86:87], v253 offset:28672\n\t"
    "v_exp_f32 v175, v175\n\t"
    "v_add_f32 v206, v206, v172\n\t"
    "v_add_f32 v207, v207, v173\n\t"
    "v_cvt_pk_bf16_f32 v236, v172, v173\n\t"
    "v_add_f32 v206, v206, v174\n\t"
    "v_add_f32 v207, v207, v175\n\t"
    "v_cvt_pk_bf16_f32 v237, v174, v175\n\t"
    "v_mfma_f32_16x16x32_bf16 v[164:167], a[28:31], v[44:47], v[184:187]\n\t"
    "v_exp_f32 v176, v176\n\t"
    "v_mfma_f32_16x16x32_bf16 v[164:167], a[24:27], v[40:43], v[164:167]\n\t"
    "v_exp_f32 v177, v177\n\t"
    "v_mfma_f32_16x16x32_bf16 v[164:167], a[20:23], v[36:39], v[164:167]\n\t"
    "v_exp_f32 v178, v178\n\t"
    "v_mfma_f32_16x16x32_bf16 v[164:167], a[16:19], v[32:35], v[164:167]\n\t"
    "ds_read_b128 a[4:7], v252 offset:25088\n\t"
    "ds_read_b64_tr_b16 a[88:89], v254 offset:16384\n\t"
    "ds_read_b64_tr_b16 a[90:91], v254 offset:20480\n\t"
    "ds_read_b64_tr_b16 a[92:93], v254 offset:24576\n\t"
    "ds_read_b64_tr_b16 a[94:95], v254 offset:28672\n\t"
    "v_exp_f32 v179, v179\n\t"
    "v_add_f32 v208, v208, v176\n\t"
    "v_add_f32 v209, v209, v177\n\t"
    "v_cvt_pk_bf16_f32 v244, v176, v177\n\t"
    "v_add_f32 v208, v208, v178\n\t"
    "v_add_f32 v209, v209, v179\n\t"
    "v_cvt_pk_bf16_f32 v245, v178, v179\n\t"
    "v_mfma_f32_16x16x32_bf16 v[168:171], a[16:19], v[64:67], v[188:191]\n\t"
    "v_exp_f32 v160, v160\n\t"
    "v_mfma_f32_16x16x32_bf16 v[168:171], a[20:23], v[68:71], v[168:171]\n\t"
    "v_exp_f32 v161, v161\n\t"
    "v_mfma_f32_16x16x32_bf16 v[168:171], a[24:27], v[72:75], v[168:171]\n\t"
    "v_exp_f32 v162, v162\n\t"
    "v_mfma_f32_16x16x32_bf16 v[168:171], a[28:31], v[76:79], v[168:171]\n\t"
    "ds_read_b128 a[8:11], v252 offset:25600\n\t"
    "v_exp_f32 v163, v163\n\t"
    "v_add_f32 v200, v200, v160\n\t"
    "v_add_f32 v201, v201, v161\n\t"
    "v_cvt_pk_bf16_f32 v214, v160, v161\n\t"
    "v_add_f32 v200, v200, v162\n\t"
    "v_add_f32 v201, v201, v163\n\t"
    "v_cvt_pk_bf16_f32 v215, v162, v163\n\t"
    "v_mfma_f32_16x16x32_bf16 v[172:175], a[28:31], v[108:111], v[192:195]\n\t"
    "v_exp_f32 v164, v164\n\t"
    "v_mfma_f32_16x16x32_bf16 v[172:175], a[24:27], v[104:107], v[172:175]\n\t"
    "v_exp_f32 v165, v165\n\t"
    "v_mfma_f32_16x16x32_bf16 v[172:175], a[20:23], v[100:103], v[172:175]\n\t"
    "v_exp_f32 v166, v166\n\t"
    "v_mfma_f32_16x16x32_bf16 v[172:175], a[16:19], v[96:99], v[172:175]\n\t"
    "ds_read_b128 a[12:15], v252 offset:26112\n\t"
    "v_subrev_u32 v253, s92, v253\n\t"
    "v_subrev_u32 v254, s92, v254\n\t"
    "v_exp_f32 v167, v167\n\t"
    "v_add_f32 v202, v202, v164\n\t"
    "v_add_f32 v203, v203, v165\n\t"
    "v_cvt_pk_bf16_f32 v222, v164, v165\n\t"
    "v_add_f32 v202, v202, v166\n\t"
    "v_add_f32 v203, v203, v167\n\t"
    "v_cvt_pk_bf16_f32 v223, v166, v167\n\t"
    "v_mfma_f32_16x16x32_bf16 v[176:179], a[16:19], v[128:131], v[196:199]\n\t"
    "v_exp_f32 v168, v168\n\t"
    "v_mfma_f32_16x16x32_bf16 v[176:179], a[20:23], v[132:135], v[176:179]\n\t"
    "v_exp_f32 v169, v169\n\t"
    "v_mfma_f32_16x16x32_bf16 v[176:179], a[24:27], v[136:139], v[176:179]\n\t"
    "v_exp_f32 v170, v170\n\t"
    "v_mfma_f32_16x16x32_bf16 v[176:179], a[28:31], v[140:143], v[176:179]\n\t"
    "v_exp_f32 v171, v171\n\t"
    "v_add_f32 v204, v204, v168\n\t"
    "v_add_f32 v205, v205, v169\n\t"
    "v_cvt_pk_bf16_f32 v230, v168, v169\n\t"
    "v_add_f32 v204, v204, v170\n\t"
    "v_add_f32 v205, v205, v171\n\t"
    "v_cvt_pk_bf16_f32 v231, v170, v171\n\t"
    "s_waitcnt lgkmcnt(0)\n\t"
    "v_mfma_f32_16x16x32_bf16 v[160:163], a[0:3], v[0:3], v[180:183]\n\t"
    "v_exp_f32 v172, v172\n\t"
    "v_mfma_f32_16x16x32_bf16 v[160:163], a[4:7], v[4:7], v[160:163]\n\t"
    "v_exp_f32 v173, v173\n\t"
    "v_mfma_f32_16x16x32_bf16 v[160:163], a[8:11], v[8:11], v[160:163]\n\t"
    "v_exp_f32 v174, v174\n\t"
    "v_mfma_f32_16x16x32_bf16 v[160:163], a[12:15], v[12:15], v[160:163]\n\t"
    "ds_read_b128 a[16:19], v252 offset:28672\n\t"
    "v_exp_f32 v175, v175\n\t"
    "v_add_f32 v206, v206, v172\n\t"
    "v_add_f32 v207, v207, v173\n\t"
    "v_cvt_pk_bf16_f32 v238, v172, v173\n\t"
    "v_add_f32 v206, v206, v174\n\t"
    "v_add_f32 v207, v207, v175\n\t"
    "v_cvt_pk_bf16_f32 v239, v174, v175\n\t"
    "v_mfma_f32_16x16x32_bf16 v[164:167], a[12:15], v[44:47], v[184:187]\n\t"
    "v_exp_f32 v176, v176\n\t"
    "v_mfma_f32_16x16x32_bf16 v[164:167], a[8:11], v[40:43], v[164:167]\n\t"
    "v_exp_f32 v177, v177\n\t"
    "v_mfma_f32_16x16x32_bf16 v[164:167], a[4:7], v[36:39], v[164:167]\n\t"
    "v_exp_f32 v178, v178\n\t"
    "v_mfma_f32_16x16x32_bf16 v[164:167], a[0:3], v[32:35], v[164:167]\n\t"
    "ds_read_b128 a[20:23], v252 offset:29184\n\t"
    "v_exp_f32 v179, v179\n\t"
    "v_add_f32 v208, v208, v176\n\t"
    "v_add_f32 v209, v209, v177\n\t"
    "v_cvt_pk_bf16_f32 v246, v176, v177\n\t"
    "v_add_f32 v208, v208, v178\n\t"
    "v_add_f32 v209, v209, v179\n\t"
    "v_cvt_pk_bf16_f32 v247, v178, v179\n\t"
    "v_mfma_f32_16x16x32_bf16 v[168:171], a[0:3], v[64:67], v[188:191]\n\t"
    "v_exp_f32 v160, v160\n\t"
    "v_mfma_f32_16x16x32_bf16 v[168:171], a[4:7], v[68:71], v[168:171]\n\t"
    "v_exp_f32 v161, v161\n\t"
    "v_mfma_f32_16x16x32_bf16 v[168:171], a[8:11], v[72:75], v[168:171]\n\t"
    "v_exp_f32 v162, v162\n\t"
    "v_mfma_f32_16x16x32_bf16 v[168:171], a[12:15], v[76:79], v[168:171]\n\t"
    "ds_read_b128 a[24:27], v252 offset:29696\n\t"
    "v_exp_f32 v163, v163\n\t"
    "v_add_f32 v200, v200, v160\n\t"
    "v_add_f32 v201, v201, v161\n\t"
    "v_cvt_pk_bf16_f32 v216, v160, v161\n\t"
    "v_add_f32 v200, v200, v162\n\t"
    "v_add_f32 v201, v201, v163\n\t"
    "v_cvt_pk_bf16_f32 v217, v162, v163\n\t"
    "v_mfma_f32_16x16x32_bf16 v[172:175], a[12:15], v[108:111], v[192:195]\n\t"
    "v_exp_f32 v164, v164\n\t"
    "v_mfma_f32_16x16x32_bf16 v[172:175], a[8:11], v[104:107], v[172:175]\n\t"
    "v_exp_f32 v165, v165\n\t"
    "v_mfma_f32_16x16x32_bf16 v[172:175], a[4:7], v[100:103], v[172:175]\n\t"
    "v_exp_f32 v166, v166\n\t"
    "v_mfma_f32_16x16x32_bf16 v[172:175], a[0:3], v[96:99], v[172:175]\n\t"
    "ds_read_b128 a[28:31], v252 offset:30208\n\t"
    "v_exp_f32 v167, v167\n\t"
    "v_add_f32 v202, v202, v164\n\t"
    "v_add_f32 v203, v203, v165\n\t"
    "v_cvt_pk_bf16_f32 v224, v164, v165\n\t"
    "v_add_f32 v202, v202, v166\n\t"
    "v_add_f32 v203, v203, v167\n\t"
    "v_cvt_pk_bf16_f32 v225, v166, v167\n\t"
    "v_mfma_f32_16x16x32_bf16 v[176:179], a[0:3], v[128:131], v[196:199]\n\t"
    "v_exp_f32 v168, v168\n\t"
    "v_mfma_f32_16x16x32_bf16 v[176:179], a[4:7], v[132:135], v[176:179]\n\t"
    "v_exp_f32 v169, v169\n\t"
    "v_mfma_f32_16x16x32_bf16 v[176:179], a[8:11], v[136:139], v[176:179]\n\t"
    "v_exp_f32 v170, v170\n\t"
    "v_mfma_f32_16x16x32_bf16 v[176:179], a[12:15], v[140:143], v[176:179]\n\t"
    "v_exp_f32 v171, v171\n\t"
    "v_add_f32 v204, v204, v168\n\t"
    "v_add_f32 v205, v205, v169\n\t"
    "v_cvt_pk_bf16_f32 v232, v168, v169\n\t"
    "v_add_f32 v204, v204, v170\n\t"
    "v_add_f32 v205, v205, v171\n\t"
    "v_cvt_pk_bf16_f32 v233, v170, v171\n\t"
    "s_waitcnt lgkmcnt(0)\n\t"
    "v_mfma_f32_16x16x32_bf16 v[160:163], a[16:19], v[0:3], v[180:183]\n\t"
    "v_exp_f32 v172, v172\n\t"
    "v_mfma_f32_16x16x32_bf16 v[160:163], a[20:23], v[4:7], v[160:163]\n\t"
    "v_exp_f32 v173, v173\n\t"
    "v_mfma_f32_16x16x32_bf16 v[160:163], a[24:27], v[8:11], v[160:163]\n\t"
    "s_waitcnt vmcnt(0) lgkmcnt(6)\n\t"
    "v_exp_f32 v174, v174\n\t"
    "v_mfma_f32_16x16x32_bf16 v[160:163], a[28:31], v[12:15], v[160:163]\n\t"
    "s_barrier\n\t"
    "v_exp_f32 v175, v175\n\t"
    "v_add_f32 v206, v206, v172\n\t"
    "v_add_f32 v207, v207, v173\n\t"
    "v_cvt_pk_bf16_f32 v240, v172, v173\n\t"
    "v_add_f32 v206, v206, v174\n\t"
    "v_add_f32 v207, v207, v175\n\t"
    "v_cvt_pk_bf16_f32 v241, v174, v175\n\t"
    "v_mfma_f32_16x16x32_bf16 v[164:167], a[28:31], v[44:47], v[184:187]\n\t"
    "v_exp_f32 v176, v176\n\t"
    "v_mfma_f32_16x16x32_bf16 v[164:167], a[24:27], v[40:43], v[164:167]\n\t"
    "s_add_u32 s53, s98, 524288\n\t"
    "v_exp_f32 v177, v177\n\t"
    "v_mfma_f32_16x16x32_bf16 v[164:167], a[20:23], v[36:39], v[164:167]\n\t"
    "s_add_u32 m0, s97, 0\n\t"
    "v_exp_f32 v178, v178\n\t"
    "v_mfma_f32_16x16x32_bf16 v[164:167], a[16:19], v[32:35], v[164:167]\n\t"
    "buffer_load_dwordx4 v255, s[24:27], s53 offen offset:0 lds\n\t"
    "ds_read_b64_tr_b16 a[0:1], v253 offset:16384\n\t"
    "ds_read_b64_tr_b16 a[2:3], v253 offset:20480\n\t"
    "ds_read_b64_tr_b16 a[4:5], v253 offset:24576\n\t"
    "ds_read_b64_tr_b16 a[6:7], v253 offset:28672\n\t"
    "v_exp_f32 v179, v179\n\t"
    "v_add_f32 v208, v208, v176\n\t"
    "v_add_f32 v209, v209, v177\n\t"
    "v_cvt_pk_bf16_f32 v248, v176, v177\n\t"
    "v_add_f32 v208, v208, v178\n\t"
    "v_add_f32 v209, v209, v179\n\t"
    "v_cvt_pk_bf16_f32 v249, v178, v179\n\t"
    "v_mfma_f32_16x16x32_bf16 v[168:171], a[16:19], v[64:67], v[188:191]\n\t"
    "v_exp_f32 v160, v160\n\t"
    "v_mfma_f32_16x16x32_bf16 v[168:171], a[20:23], v[68:71], v[168:171]\n\t"
    "v_exp_f32 v161, v161\n\t"
    "v_mfma_f32_16x16x32_bf16 v[168:171], a[24:27], v[72:75], v[168:171]\n\t"
    "s_add_u32 m0, s97, 896\n\t"
    "v_exp_f32 v162, v162\n\t"
    "v_mfma_f32_16x16x32_bf16 v[168:171], a[28:31], v[76:79], v[168:171]\n\t"
    "buffer_load_dwordx4 v255, s[24:27], s53 offen offset:128 lds\n\t"
    "ds_read_b64_tr_b16 a[12:13], v253 offset:40960\n\t"
    "ds_read_b64_tr_b16 a[14:15], v253 offset:45056\n\t"
    "ds_read_b64_tr_b16 a[8:9], v253 offset:32768\n\t"
    "ds_read_b64_tr_b16 a[10:11], v253 offset:36864\n\t"
    "v_exp_f32 v163, v163\n\t"
    "v_add_f32 v200, v200, v160\n\t"
    "v_add_f32 v201, v201, v161\n\t"
    "v_cvt_pk_bf16_f32 v218, v160, v161\n\t"
    "v_add_f32 v200, v200, v162\n\t"
    "v_add_f32 v201, v201, v163\n\t"
    "v_cvt_pk_bf16_f32 v219, v162, v163\n\t"
    "v_mfma_f32_16x16x32_bf16 v[172:175], a[28:31], v[108:111], v[192:195]\n\t"
    "v_exp_f32 v164, v164\n\t"
    "v_mfma_f32_16x16x32_bf16 v[172:175], a[24:27], v[104:107], v[172:175]\n\t"
    "s_add_u32 s53, s98, 532480\n\t"
    "v_exp_f32 v165, v165\n\t"
    "v_mfma_f32_16x16x32_bf16 v[172:175], a[20:23], v[100:103], v[172:175]\n\t"
    "s_add_u32 m0, s97, 2048\n\t"
    "v_exp_f32 v166, v166\n\t"
    "v_mfma_f32_16x16x32_bf16 v[172:175], a[16:19], v[96:99], v[172:175]\n\t"
    "buffer_load_dwordx4 v255, s[24:27], s53 offen offset:0 lds\n\t"
    "ds_read_b64_tr_b16 v[180:181], v253 offset:49152\n\t"
    "ds_read_b64_tr_b16 v[182:183], v253 offset:53248\n\t"
    "ds_read_b64_tr_b16 v[184:185], v253 offset:57344\n\t"
    "ds_read_b64_tr_b16 v[186:187], v253 offset:61440\n\t"
    "v_exp_f32 v167, v167\n\t"
    "v_add_f32 v202, v202, v164\n\t"
    "v_add_f32 v203, v203, v165\n\t"
    "v_cvt_pk_bf16_f32 v226, v164, v165\n\t"
    "v_add_f32 v202, v202, v166\n\t"
    "v_add_f32 v203, v203, v167\n\t"
    "v_cvt_pk_bf16_f32 v227, v166, v167\n\t"
    "v_mfma_f32_16x16x32_bf16 v[176:179], a[16:19], v[128:131], v[196:199]\n\t"
    "v_exp_f32 v168, v168\n\t"
    "v_mfma_f32_16x16x32_bf16 v[176:179], a[20:23], v[132:135], v[176:179]\n\t"
    "v_exp_f32 v169, v169\n\t"
    "v_mfma_f32_16x16x32_bf16 v[176:179], a[24:27], v[136:139], v[176:179]\n\t"
    "s_add_u32 m0, s97, 2944\n\t"
    "v_exp_f32 v170, v170\n\t"
    "v_mfma_f32_16x16x32_bf16 v[176:179], a[28:31], v[140:143], v[176:179]\n\t"
    "buffer_load_dwordx4 v255, s[24:27], s53 offen offset:128 lds\n\t"
    "v_exp_f32 v171, v171\n\t"
    "v_add_f32 v204, v204, v168\n\t"
    "v_add_f32 v205, v205, v169\n\t"
    "v_cvt_pk_bf16_f32 v234, v168, v169\n\t"
    "v_add_f32 v204, v204, v170\n\t"
    "v_add_f32 v205, v205, v171\n\t"
    "v_cvt_pk_bf16_f32 v235, v170, v171\n\t"
    "L_rejoinA_%=:\n\t"
    "s_waitcnt lgkmcnt(6)\n\t"
    "v_mfma_f32_16x16x32_bf16 a[96:99], a[0:3], v[24:27], a[96:99]\n\t"
    "ds_read_b64_tr_b16 a[16:17], v254 offset:16384\n\t"
    "v_add_u32 v198, 6144, v255\n\t"
    "v_mfma_f32_16x16x32_bf16 a[96:99], a[4:7], v[28:31], a[96:99]\n\t"
    "s_waitcnt lgkmcnt(1)\n\t"
    "ds_read_b64_tr_b16 a[18:19], v254 offset:20480\n\t"
    "v_mbcnt_lo_u32_b32 v197, -1, 0\n\t"
    "v_mfma_f32_16x16x32_bf16 a[96:99], a[12:15], v[20:23], a[96:99]\n\t"
    "ds_read_b64_tr_b16 a[20:21], v254 offset:24576\n\t"
    "v_mbcnt_hi_u32_b32 v197, -1, v197\n\t"
    "v_mfma_f32_16x16x32_bf16 a[96:99], a[8:11], v[16:19], a[96:99]\n\t"
    "s_add_u32 s53, s98, 589824\n\t"
    "ds_read_b64_tr_b16 a[22:23], v254 offset:28672\n\t"
    "v_lshlrev_b32 v196, 4, v197\n\t"
    "v_mfma_f32_16x16x32_bf16 a[96:99], v[180:183], v[212:215], a[96:99]\n\t"
    "s_add_u32 m0, s97, 16384\n\t"
    "ds_read_b64_tr_b16 a[28:29], v254 offset:40960\n\t"
    "v_add_f32 v200, v200, v201\n\t"
    "v_mfma_f32_16x16x32_bf16 a[96:99], v[184:187], v[216:219], a[96:99]\n\t"
    "buffer_load_dwordx4 v255, s[24:27], s53 offen offset:0 lds\n\t"
    "buffer_load_dwordx4 v198, s[24:27], s53 offen offset:2048 lds\n\t"
    "ds_read_b64_tr_b16 a[30:31], v254 offset:45056\n\t"
    "v_mfma_f32_16x16x32_bf16 a[128:131], v[184:187], v[224:227], a[128:131]\n\t"
    "ds_read_b64_tr_b16 a[24:25], v254 offset:32768\n\t"
    "v_mov_b32 v201, v200\n\t"
    "v_mfma_f32_16x16x32_bf16 a[128:131], v[180:183], v[220:223], a[128:131]\n\t"
    "s_add_u32 m0, s97, 17280\n\t"
    "ds_read_b64_tr_b16 a[26:27], v254 offset:36864\n\t"
    "v_add_f32 v202, v202, v203\n\t"
    "v_mfma_f32_16x16x32_bf16 a[128:131], a[8:11], v[48:51], a[128:131]\n\t"
    "buffer_load_dwordx4 v255, s[24:27], s53 offen offset:128 lds\n\t"
    "buffer_load_dwordx4 v198, s[24:27], s53 offen offset:2176 lds\n\t"
    "ds_read_b64_tr_b16 v[188:189], v254 offset:49152\n\t"
    "v_mfma_f32_16x16x32_bf16 a[128:131], a[12:15], v[52:55], a[128:131]\n\t"
    "s_add_u32 s53, s98, 655360\n\t"
    "ds_read_b64_tr_b16 v[190:191], v254 offset:53248\n\t"
    "v_mov_b32 v203, v202\n\t"
    "v_mfma_f32_16x16x32_bf16 a[128:131], a[4:7], v[60:63], a[128:131]\n\t"
    "s_add_u32 m0, s97, 49152\n\t"
    "ds_read_b64_tr_b16 v[192:193], v254 offset:57344\n\t"
    "v_mfma_f32_16x16x32_bf16 a[128:131], a[0:3], v[56:59], a[128:131]\n\t"
    "buffer_load_dwordx4 v255, s[24:27], s53 offen offset:0 lds\n\t"
    "buffer_load_dwordx4 v198, s[24:27], s53 offen offset:2048 lds\n\t"
    "ds_read_b64_tr_b16 v[194:195], v254 offset:61440\n\t"
    "v_permlane32_swap_b32 v200, v201\n\t"
    "v_add_f32 v200, v200, v201\n\t"
    "v_mov_b32 v201, v200\n\t"
    "v_permlane32_swap_b32 v202, v203\n\t"
    "v_add_f32 v202, v202, v203\n\t"
    "v_mov_b32 v203, v202\n\t"
    "s_waitcnt lgkmcnt(6)\n\t"
    "v_mfma_f32_16x16x32_bf16 a[132:135], a[16:19], v[56:59], a[132:135]\n\t"
    "ds_read_b64_tr_b16 a[0:1], v253 offset:16896\n\t"
    "v_mfma_f32_16x16x32_bf16 a[132:135], a[20:23], v[60:63], a[132:135]\n\t"
    "s_waitcnt lgkmcnt(1)\n\t"
    "ds_read_b64_tr_b16 a[2:3], v253 offset:20992\n\t"
    "v_mfma_f32_16x16x32_bf16 a[132:135], a[28:31], v[52:55], a[132:135]\n\t"
    "ds_read_b64_tr_b16 a[4:5], v253 offset:25088\n\t"
    "v_mfma_f32_16x16x32_bf16 a[132:135], a[24:27], v[48:51], a[132:135]\n\t"
    "ds_read_b64_tr_b16 a[6:7], v253 offset:29184\n\t"
    "v_mfma_f32_16x16x32_bf16 a[132:135], v[188:191], v[220:223], a[132:135]\n\t"
    "s_add_u32 m0, s97, 50048\n\t"
    "ds_read_b64_tr_b16 a[12:13], v253 offset:41472\n\t"
    "v_mfma_f32_16x16x32_bf16 a[132:135], v[192:195], v[224:227], a[132:135]\n\t"
    "buffer_load_dwordx4 v255, s[24:27], s53 offen offset:128 lds\n\t"
    "buffer_load_dwordx4 v198, s[24:27], s53 offen offset:2176 lds\n\t"
    "ds_read_b64_tr_b16 a[14:15], v253 offset:45568\n\t"
    "v_permlane16_swap_b32 v200, v201\n\t"
    "v_add_f32 v200, v200, v201\n\t"
    "v_rcp_f32 v201, v200\n\t"
    "v_permlane16_swap_b32 v202, v203\n\t"
    "v_add_f32 v202, v202, v203\n\t"
    "v_rcp_f32 v203, v202\n\t"
    "v_mfma_f32_16x16x32_bf16 a[100:103], v[192:195], v[216:219], a[100:103]\n\t"
    "ds_read_b64_tr_b16 a[8:9], v253 offset:33280\n\t"
    "v_accvgpr_read_b32 v160, a128\n\t"
    "v_mfma_f32_16x16x32_bf16 a[100:103], v[188:191], v[212:215], a[100:103]\n\t"
    "ds_read_b64_tr_b16 a[10:11], v253 offset:37376\n\t"
    "v_accvgpr_read_b32 v161, a129\n\t"
    "v_mfma_f32_16x16x32_bf16 a[100:103], a[24:27], v[16:19], a[100:103]\n\t"
    "ds_read_b64_tr_b16 v[180:181], v253 offset:49664\n\t"
    "v_accvgpr_read_b32 v162, a130\n\t"
    "v_mfma_f32_16x16x32_bf16 a[100:103], a[28:31], v[20:23], a[100:103]\n\t"
    "ds_read_b64_tr_b16 v[182:183], v253 offset:53760\n\t"
    "v_accvgpr_read_b32 v163, a131\n\t"
    "v_mfma_f32_16x16x32_bf16 a[100:103], a[20:23], v[28:31], a[100:103]\n\t"
    "ds_read_b64_tr_b16 v[184:185], v253 offset:57856\n\t"
    "v_accvgpr_read_b32 v164, a132\n\t"
    "v_mfma_f32_16x16x32_bf16 a[100:103], a[16:19], v[24:27], a[100:103]\n\t"
    "ds_read_b64_tr_b16 v[186:187], v253 offset:61952\n\t"
    "v_accvgpr_read_b32 v165, a133\n\t"
    "v_accvgpr_read_b32 v166, a134\n\t"
    "v_accvgpr_read_b32 v167, a135\n\t"
    "v_mul_f32 v160, v203, v160\n\t"
    "v_mul_f32 v161, v203, v161\n\t"
    "v_mul_f32 v162, v203, v162\n\t"
    "v_mul_f32 v163, v203, v163\n\t"
    "v_mul_f32 v164, v203, v164\n\t"
    "v_mul_f32 v165, v203, v165\n\t"
    "v_mul_f32 v166, v203, v166\n\t"
    "v_mul_f32 v167, v203, v167\n\t"
    "v_cvt_pk_bf16_f32 v160, v160, v161\n\t"
    "v_cvt_pk_bf16_f32 v161, v162, v163\n\t"
    "v_cvt_pk_bf16_f32 v162, v164, v165\n\t"
    "v_cvt_pk_bf16_f32 v163, v166, v167\n\t"
    "buffer_store_dwordx4 v[160:163], v196, s[36:39], s85 offen offset:0 sc1\n\t"
    "s_waitcnt lgkmcnt(6)\n\t"
    "v_mfma_f32_16x16x32_bf16 a[104:107], a[0:3], v[24:27], a[104:107]\n\t"
    "ds_read_b64_tr_b16 a[16:17], v254 offset:16896\n\t"
    "v_accvgpr_read_b32 v160, a96\n\t"
    "v_mfma_f32_16x16x32_bf16 a[104:107], a[4:7], v[28:31], a[104:107]\n\t"
    "s_waitcnt lgkmcnt(1)\n\t"
    "ds_read_b64_tr_b16 a[18:19], v254 offset:20992\n\t"
    "v_accvgpr_read_b32 v161, a97\n\t"
    "v_mfma_f32_16x16x32_bf16 a[104:107], a[12:15], v[20:23], a[104:107]\n\t"
    "ds_read_b64_tr_b16 a[20:21], v254 offset:25088\n\t"
    "v_accvgpr_read_b32 v162, a98\n\t"
    "v_mfma_f32_16x16x32_bf16 a[104:107], a[8:11], v[16:19], a[104:107]\n\t"
    "ds_read_b64_tr_b16 a[22:23], v254 offset:29184\n\t"
    "v_accvgpr_read_b32 v163, a99\n\t"
    "v_mfma_f32_16x16x32_bf16 a[104:107], v[180:183], v[212:215], a[104:107]\n\t"
    "ds_read_b64_tr_b16 a[28:29], v254 offset:41472\n\t"
    "v_accvgpr_read_b32 v164, a100\n\t"
    "v_mfma_f32_16x16x32_bf16 a[104:107], v[184:187], v[216:219], a[104:107]\n\t"
    "ds_read_b64_tr_b16 a[30:31], v254 offset:45568\n\t"
    "v_accvgpr_read_b32 v165, a101\n\t"
    "v_accvgpr_read_b32 v166, a102\n\t"
    "v_accvgpr_read_b32 v167, a103\n\t"
    "v_mul_f32 v160, v201, v160\n\t"
    "v_mul_f32 v161, v201, v161\n\t"
    "v_mul_f32 v162, v201, v162\n\t"
    "v_mul_f32 v163, v201, v163\n\t"
    "v_mul_f32 v164, v201, v164\n\t"
    "v_mul_f32 v165, v201, v165\n\t"
    "v_mul_f32 v166, v201, v166\n\t"
    "v_mul_f32 v167, v201, v167\n\t"
    "v_cvt_pk_bf16_f32 v160, v160, v161\n\t"
    "v_cvt_pk_bf16_f32 v161, v162, v163\n\t"
    "v_cvt_pk_bf16_f32 v162, v164, v165\n\t"
    "v_cvt_pk_bf16_f32 v163, v166, v167\n\t"
    "buffer_store_dwordx4 v[160:163], v196, s[36:39], s84 offen offset:0 sc1\n\t"
    "v_mfma_f32_16x16x32_bf16 a[136:139], v[184:187], v[224:227], a[136:139]\n\t"
    "ds_read_b64_tr_b16 a[24:25], v254 offset:33280\n\t"
    "v_mfma_f32_16x16x32_bf16 a[136:139], v[180:183], v[220:223], a[136:139]\n\t"
    "ds_read_b64_tr_b16 a[26:27], v254 offset:37376\n\t"
    "v_mfma_f32_16x16x32_bf16 a[136:139], a[8:11], v[48:51], a[136:139]\n\t"
    "ds_read_b64_tr_b16 v[188:189], v254 offset:49664\n\t"
    "v_mfma_f32_16x16x32_bf16 a[136:139], a[12:15], v[52:55], a[136:139]\n\t"
    "ds_read_b64_tr_b16 v[190:191], v254 offset:53760\n\t"
    "v_mfma_f32_16x16x32_bf16 a[136:139], a[4:7], v[60:63], a[136:139]\n\t"
    "ds_read_b64_tr_b16 v[192:193], v254 offset:57856\n\t"
    "v_mfma_f32_16x16x32_bf16 a[136:139], a[0:3], v[56:59], a[136:139]\n\t"
    "ds_read_b64_tr_b16 v[194:195], v254 offset:61952\n\t"
    "v_exp_f32 v172, v172\n\t"
    "v_exp_f32 v173, v173\n\t"
    "v_exp_f32 v174, v174\n\t"
    "v_exp_f32 v175, v175\n\t"
    "v_add_f32 v206, v206, v172\n\t"
    "v_add_f32 v207, v207, v173\n\t"
    "v_cvt_pk_bf16_f32 v242, v172, v173\n\t"
    "v_add_f32 v206, v206, v174\n\t"
    "v_add_f32 v207, v207, v175\n\t"
    "v_cvt_pk_bf16_f32 v243, v174, v175\n\t"
    "v_exp_f32 v176, v176\n\t"
    "v_exp_f32 v177, v177\n\t"
    "v_exp_f32 v178, v178\n\t"
    "v_exp_f32 v179, v179\n\t"
    "v_add_f32 v208, v208, v176\n\t"
    "v_add_f32 v209, v209, v177\n\t"
    "v_cvt_pk_bf16_f32 v250, v176, v177\n\t"
    "v_add_f32 v208, v208, v178\n\t"
    "v_add_f32 v209, v209, v179\n\t"
    "v_cvt_pk_bf16_f32 v251, v178, v179\n\t"
    "v_add_f32 v204, v204, v205\n\t"
    "v_mov_b32 v205, v204\n\t"
    "v_add_f32 v206, v206, v207\n\t"
    "v_mov_b32 v207, v206\n\t"
    "v_add_f32 v208, v208, v209\n\t"
    "s_waitcnt lgkmcnt(6)\n\t"
    "v_mfma_f32_16x16x32_bf16 a[140:143], a[16:19], v[56:59], a[140:143]\n\t"
    "ds_read_b64_tr_b16 a[0:1], v253 offset:17408\n\t"
    "v_mov_b32 v209, v208\n\t"
    "v_mfma_f32_16x16x32_bf16 a[140:143], a[20:23], v[60:63], a[140:143]\n\t"
    "s_waitcnt lgkmcnt(1)\n\t"
    "ds_read_b64_tr_b16 a[2:3], v253 offset:21504\n\t"
    "v_mfma_f32_16x16x32_bf16 a[140:143], a[28:31], v[52:55], a[140:143]\n\t"
    "ds_read_b64_tr_b16 a[4:5], v253 offset:25600\n\t"
    "v_mfma_f32_16x16x32_bf16 a[140:143], a[24:27], v[48:51], a[140:143]\n\t"
    "ds_read_b64_tr_b16 a[6:7], v253 offset:29696\n\t"
    "v_mfma_f32_16x16x32_bf16 a[140:143], v[188:191], v[220:223], a[140:143]\n\t"
    "ds_read_b64_tr_b16 a[12:13], v253 offset:41984\n\t"
    "v_mfma_f32_16x16x32_bf16 a[140:143], v[192:195], v[224:227], a[140:143]\n\t"
    "ds_read_b64_tr_b16 a[14:15], v253 offset:46080\n\t"
    "v_permlane32_swap_b32 v204, v205\n\t"
    "v_add_f32 v204, v204, v205\n\t"
    "v_mov_b32 v205, v204\n\t"
    "v_permlane32_swap_b32 v206, v207\n\t"
    "v_add_f32 v206, v206, v207\n\t"
    "v_mov_b32 v207, v206\n\t"
    "v_permlane32_swap_b32 v208, v209\n\t"
    "v_add_f32 v208, v208, v209\n\t"
    "v_mov_b32 v209, v208\n\t"
    "v_permlane16_swap_b32 v204, v205\n\t"
    "v_add_f32 v204, v204, v205\n\t"
    "v_rcp_f32 v205, v204\n\t"
    "v_permlane16_swap_b32 v206, v207\n\t"
    "v_add_f32 v206, v206, v207\n\t"
    "v_rcp_f32 v207, v206\n\t"
    "v_permlane16_swap_b32 v208, v209\n\t"
    "v_add_f32 v208, v208, v209\n\t"
    "v_rcp_f32 v209, v208\n\t"
    "v_max3_f32 v176, v200, v202, v204\n\t"
    "v_min3_f32 v177, v200, v202, v204\n\t"
    "v_max3_f32 v176, v176, v206, v208\n\t"
    "v_min3_f32 v177, v177, v206, v208\n\t"
    "v_cmp_nlt_f32 s[66:67], s5, v177\n\t"
    "v_cmp_ngt_f32 vcc, 0x5f800000, v176\n\t"
    "s_nop 0\n\t"
    "s_or_b64 s[66:67], s[66:67], vcc\n\t"
    "s_cbranch_scc1 L_fbA_%=\n\t"
    "L_safeA_%=:\n\t"
    "v_mfma_f32_16x16x32_bf16 a[108:111], v[192:195], v[216:219], a[108:111]\n\t"
    "ds_read_b64_tr_b16 a[8:9], v253 offset:33792\n\t"
    "v_accvgpr_read_b32 v160, a136\n\t"
    "v_mfma_f32_16x16x32_bf16 a[108:111], v[188:191], v[212:215], a[108:111]\n\t"
    "ds_read_b64_tr_b16 a[10:11], v253 offset:37888\n\t"
    "v_accvgpr_read_b32 v161, a137\n\t"
    "v_mfma_f32_16x16x32_bf16 a[108:111], a[24:27], v[16:19], a[108:111]\n\t"
    "ds_read_b64_tr_b16 v[180:181], v253 offset:50176\n\t"
    "v_accvgpr_read_b32 v162, a138\n\t"
    "v_mfma_f32_16x16x32_bf16 a[108:111], a[28:31], v[20:23], a[108:111]\n\t"
    "ds_read_b64_tr_b16 v[182:183], v253 offset:54272\n\t"
    "v_accvgpr_read_b32 v163, a139\n\t"
    "v_mfma_f32_16x16x32_bf16 a[108:111], a[20:23], v[28:31], a[108:111]\n\t"
    "ds_read_b64_tr_b16 v[184:185], v253 offset:58368\n\t"
    "v_accvgpr_read_b32 v164, a140\n\t"
    "v_mfma_f32_16x16x32_bf16 a[108:111], a[16:19], v[24:27], a[108:111]\n\t"
    "ds_read_b64_tr_b16 v[186:187], v253 offset:62464\n\t"
    "v_accvgpr_read_b32 v165, a141\n\t"
    "v_accvgpr_read_b32 v166, a142\n\t"
    "v_accvgpr_read_b32 v167, a143\n\t"
    "v_mul_f32 v160, v203, v160\n\t"
    "v_mul_f32 v161, v203, v161\n\t"
    "s_waitcnt lgkmcnt(6)\n\t"
    "v_mfma_f32_16x16x32_bf16 a[112:115], a[0:3], v[24:27], a[112:115]\n\t"
    "ds_read_b64_tr_b16 a[16:17], v254 offset:17408\n\t"
    "v_mul_f32 v162, v203, v162\n\t"
    "v_mfma_f32_16x16x32_bf16 a[112:115], a[4:7], v[28:31], a[112:115]\n\t"
    "s_waitcnt lgkmcnt(1)\n\t"
    "ds_read_b64_tr_b16 a[18:19], v254 offset:21504\n\t"
    "v_mul_f32 v163, v203, v163\n\t"
    "v_mfma_f32_16x16x32_bf16 a[112:115], a[12:15], v[20:23], a[112:115]\n\t"
    "ds_read_b64_tr_b16 a[20:21], v254 offset:25600\n\t"
    "v_mul_f32 v164, v203, v164\n\t"
    "v_mfma_f32_16x16x32_bf16 a[112:115], a[8:11], v[16:19], a[112:115]\n\t"
    "ds_read_b64_tr_b16 a[22:23], v254 offset:29696\n\t"
    "v_mul_f32 v165, v203, v165\n\t"
    "v_mfma_f32_16x16x32_bf16 a[112:115], v[180:183], v[212:215], a[112:115]\n\t"
    "ds_read_b64_tr_b16 a[28:29], v254 offset:41984\n\t"
    "v_mul_f32 v166, v203, v166\n\t"
    "v_mfma_f32_16x16x32_bf16 a[112:115], v[184:187], v[216:219], a[112:115]\n\t"
    "ds_read_b64_tr_b16 a[30:31], v254 offset:46080\n\t"
    "v_mul_f32 v167, v203, v167\n\t"
    "v_cvt_pk_bf16_f32 v104, v160, v161\n\t"
    "v_cvt_pk_bf16_f32 v105, v162, v163\n\t"
    "v_cvt_pk_bf16_f32 v106, v164, v165\n\t"
    "v_cvt_pk_bf16_f32 v107, v166, v167\n\t"
    "v_add_u32 v128, s92, v253\n\t"
    "v_add_u32 v129, s92, v254\n\t"
    "v_add_u32 v130, s93, v253\n\t"
    "v_add_u32 v131, s93, v254\n\t"
    "v_add_u32 v132, s94, v253\n\t"
    "v_add_u32 v133, s94, v254\n\t"
    "v_mul_u32_u24 v137, 128, v197\n\t"
    "v_add_u32 v137, s64, v137\n\t"
    "v_and_b32 v138, 3, v197\n\t"
    "s_mov_b32 exec_lo, 0\n\t"
    "v_lshl_add_u32 v137, v138, 2, s65\n\t"
    "s_mov_b32 exec_lo, -1\n\t"
    "v_subrev_u32 v138, s51, v196\n\t"
    "v_add_u32 v141, s90, v138\n\t"
    "v_add_u32 v142, s91, v138\n\t"
    "v_add_u32 v143, s99, v138\n\t"
    "v_bfe_u32 v138, v197, 4, 1\n\t"
    "v_lshrrev_b32 v139, 5, v197\n\t"
    "v_lshl_add_u32 v138, v138, 1, v139\n\t"
    "v_lshlrev_b32 v138, 4, v138\n\t"
    "v_and_b32 v136, 15, v197\n\t"
    "v_lshl_add_u32 v136, v136, 10, v138\n\t"
    "v_mfma_f32_16x16x32_bf16 a[144:147], v[184:187], v[224:227], a[144:147]\n\t"
    "ds_read_b64_tr_b16 a[24:25], v254 offset:33792\n\t"
    "v_accvgpr_read_b32 v168, a104\n\t"
    "v_mfma_f32_16x16x32_bf16 a[144:147], v[180:183], v[220:223], a[144:147]\n\t"
    "ds_read_b64_tr_b16 a[26:27], v254 offset:37888\n\t"
    "v_accvgpr_read_b32 v169, a105\n\t"
    "v_mfma_f32_16x16x32_bf16 a[144:147], a[8:11], v[48:51], a[144:147]\n\t"
    "ds_read_b64_tr_b16 v[188:189], v254 offset:50176\n\t"
    "v_accvgpr_read_b32 v170, a106\n\t"
    "v_mfma_f32_16x16x32_bf16 a[144:147], a[12:15], v[52:55], a[144:147]\n\t"
    "ds_read_b64_tr_b16 v[190:191], v254 offset:54272\n\t"
    "v_accvgpr_read_b32 v171, a107\n\t"
    "v_mfma_f32_16x16x32_bf16 a[144:147], a[4:7], v[60:63], a[144:147]\n\t"
    "ds_read_b64_tr_b16 v[192:193], v254 offset:58368\n\t"
    "v_accvgpr_read_b32 v172, a108\n\t"
    "v_mfma_f32_16x16x32_bf16 a[144:147], a[0:3], v[56:59], a[144:147]\n\t"
    "buffer_store_dwordx4 v[104:107], v196, s[36:39], s85 offen offset:1024 sc1\n\t"
    "ds_read_b64_tr_b16 v[194:195], v254 offset:62464\n\t"
    "ds_read_b64_tr_b16 v[0:1], v132 offset:0\n\t"
    "ds_read_b64_tr_b16 v[2:3], v132 offset:4096\n\t"
    "ds_read_b64_tr_b16 v[4:5], v132 offset:8192\n\t"
    "ds_read_b64_tr_b16 v[6:7], v132 offset:12288\n\t"
    "v_accvgpr_read_b32 v173, a109\n\t"
    "v_accvgpr_read_b32 v174, a110\n\t"
    "v_accvgpr_read_b32 v175, a111\n\t"
    "v_mul_f32 v168, v201, v168\n\t"
    "v_mul_f32 v169, v201, v169\n\t"
    "s_waitcnt lgkmcnt(10)\n\t"
    "v_mfma_f32_16x16x32_bf16 a[148:151], a[16:19], v[56:59], a[148:151]\n\t"
    "ds_read_b64_tr_b16 a[0:1], v253 offset:17920\n\t"
    "v_mul_f32 v170, v201, v170\n\t"
    "v_mfma_f32_16x16x32_bf16 a[148:151], a[20:23], v[60:63], a[148:151]\n\t"
    "s_waitcnt lgkmcnt(5)\n\t"
    "ds_read_b64_tr_b16 a[2:3], v253 offset:22016\n\t"
    "v_mul_f32 v171, v201, v171\n\t"
    "v_mfma_f32_16x16x32_bf16 a[148:151], a[28:31], v[52:55], a[148:151]\n\t"
    "ds_read_b64_tr_b16 a[4:5], v253 offset:26112\n\t"
    "v_mul_f32 v172, v201, v172\n\t"
    "v_mfma_f32_16x16x32_bf16 a[148:151], a[24:27], v[48:51], a[148:151]\n\t"
    "ds_read_b64_tr_b16 a[6:7], v253 offset:30208\n\t"
    "v_mul_f32 v173, v201, v173\n\t"
    "v_mfma_f32_16x16x32_bf16 a[148:151], v[188:191], v[220:223], a[148:151]\n\t"
    "ds_read_b64_tr_b16 a[12:13], v253 offset:42496\n\t"
    "v_mul_f32 v174, v201, v174\n\t"
    "v_mfma_f32_16x16x32_bf16 a[148:151], v[192:195], v[224:227], a[148:151]\n\t"
    "ds_read_b64_tr_b16 a[14:15], v253 offset:46592\n\t"
    "ds_read_b64_tr_b16 v[8:9], v133 offset:0\n\t"
    "ds_read_b64_tr_b16 v[10:11], v133 offset:4096\n\t"
    "ds_read_b64_tr_b16 v[12:13], v133 offset:8192\n\t"
    "ds_read_b64_tr_b16 v[14:15], v133 offset:12288\n\t"
    "v_mul_f32 v175, v201, v175\n\t"
    "v_cvt_pk_bf16_f32 v108, v168, v169\n\t"
    "v_cvt_pk_bf16_f32 v109, v170, v171\n\t"
    "v_cvt_pk_bf16_f32 v110, v172, v173\n\t"
    "v_cvt_pk_bf16_f32 v111, v174, v175\n\t"
    "v_mfma_f32_16x16x32_bf16 a[116:119], v[192:195], v[216:219], a[116:119]\n\t"
    "ds_read_b64_tr_b16 a[8:9], v253 offset:34304\n\t"
    "v_accvgpr_read_b32 v160, a144\n\t"
    "v_mfma_f32_16x16x32_bf16 a[116:119], v[188:191], v[212:215], a[116:119]\n\t"
    "ds_read_b64_tr_b16 a[10:11], v253 offset:38400\n\t"
    "v_accvgpr_read_b32 v161, a145\n\t"
    "v_mfma_f32_16x16x32_bf16 a[116:119], a[24:27], v[16:19], a[116:119]\n\t"
    "ds_read_b64_tr_b16 v[180:181], v253 offset:50688\n\t"
    "v_accvgpr_read_b32 v162, a146\n\t"
    "v_mfma_f32_16x16x32_bf16 a[116:119], a[28:31], v[20:23], a[116:119]\n\t"
    "ds_read_b64_tr_b16 v[182:183], v253 offset:54784\n\t"
    "v_accvgpr_read_b32 v163, a147\n\t"
    "v_mfma_f32_16x16x32_bf16 a[116:119], a[20:23], v[28:31], a[116:119]\n\t"
    "ds_read_b64_tr_b16 v[184:185], v253 offset:58880\n\t"
    "v_accvgpr_read_b32 v164, a148\n\t"
    "v_mfma_f32_16x16x32_bf16 a[116:119], a[16:19], v[24:27], a[116:119]\n\t"
    "buffer_store_dwordx4 v[108:111], v196, s[36:39], s84 offen offset:1024 sc1\n\t"
    "ds_read_b64_tr_b16 v[186:187], v253 offset:62976\n\t"
    "ds_read_b64_tr_b16 v[32:33], v132 offset:16384\n\t"
    "ds_read_b64_tr_b16 v[34:35], v132 offset:20480\n\t"
    "ds_read_b64_tr_b16 v[36:37], v132 offset:24576\n\t"
    "ds_read_b64_tr_b16 v[38:39], v132 offset:28672\n\t"
    "v_accvgpr_read_b32 v165, a149\n\t"
    "v_accvgpr_read_b32 v166, a150\n\t"
    "v_accvgpr_read_b32 v167, a151\n\t"
    "v_mul_f32 v160, v203, v160\n\t"
    "v_mul_f32 v161, v203, v161\n\t"
    "s_waitcnt lgkmcnt(14)\n\t"
    "v_mfma_f32_16x16x32_bf16 a[120:123], a[0:3], v[24:27], a[120:123]\n\t"
    "ds_read_b64_tr_b16 a[16:17], v254 offset:17920\n\t"
    "v_mul_f32 v162, v203, v162\n\t"
    "v_mfma_f32_16x16x32_bf16 a[120:123], a[4:7], v[28:31], a[120:123]\n\t"
    "s_waitcnt lgkmcnt(5)\n\t"
    "ds_read_b64_tr_b16 a[18:19], v254 offset:22016\n\t"
    "v_mul_f32 v163, v203, v163\n\t"
    "v_mfma_f32_16x16x32_bf16 a[120:123], a[12:15], v[20:23], a[120:123]\n\t"
    "ds_read_b64_tr_b16 a[20:21], v254 offset:26112\n\t"
    "v_mul_f32 v164, v203, v164\n\t"
    "v_mfma_f32_16x16x32_bf16 a[120:123], a[8:11], v[16:19], a[120:123]\n\t"
    "ds_read_b64_tr_b16 a[22:23], v254 offset:30208\n\t"
    "v_mul_f32 v165, v203, v165\n\t"
    "v_mfma_f32_16x16x32_bf16 a[120:123], v[180:183], v[212:215], a[120:123]\n\t"
    "ds_read_b64_tr_b16 a[28:29], v254 offset:42496\n\t"
    "v_mul_f32 v166, v203, v166\n\t"
    "v_mfma_f32_16x16x32_bf16 a[120:123], v[184:187], v[216:219], a[120:123]\n\t"
    "ds_read_b64_tr_b16 a[30:31], v254 offset:46592\n\t"
    "ds_read_b64_tr_b16 v[40:41], v133 offset:16384\n\t"
    "ds_read_b64_tr_b16 v[42:43], v133 offset:20480\n\t"
    "ds_read_b64_tr_b16 v[44:45], v133 offset:24576\n\t"
    "v_mul_f32 v167, v203, v167\n\t"
    "v_cvt_pk_bf16_f32 v96, v160, v161\n\t"
    "v_cvt_pk_bf16_f32 v97, v162, v163\n\t"
    "v_cvt_pk_bf16_f32 v98, v164, v165\n\t"
    "v_cvt_pk_bf16_f32 v99, v166, v167\n\t"
    "v_mfma_f32_16x16x32_bf16 a[152:155], v[184:187], v[224:227], a[152:155]\n\t"
    "ds_read_b64_tr_b16 a[24:25], v254 offset:34304\n\t"
    "v_accvgpr_read_b32 v168, a112\n\t"
    "v_mfma_f32_16x16x32_bf16 a[152:155], v[180:183], v[220:223], a[152:155]\n\t"
    "ds_read_b64_tr_b16 a[26:27], v254 offset:38400\n\t"
    "v_accvgpr_read_b32 v169, a113\n\t"
    "v_mfma_f32_16x16x32_bf16 a[152:155], a[8:11], v[48:51], a[152:155]\n\t"
    "ds_read_b64_tr_b16 v[188:189], v254 offset:50688\n\t"
    "v_accvgpr_read_b32 v170, a114\n\t"
    "v_mfma_f32_16x16x32_bf16 a[152:155], a[12:15], v[52:55], a[152:155]\n\t"
    "ds_read_b64_tr_b16 v[190:191], v254 offset:54784\n\t"
    "v_accvgpr_read_b32 v171, a115\n\t"
    "v_mfma_f32_16x16x32_bf16 a[152:155], a[4:7], v[60:63], a[152:155]\n\t"
    "ds_read_b64_tr_b16 v[192:193], v254 offset:58880\n\t"
    "v_accvgpr_read_b32 v172, a116\n\t"
    "v_mfma_f32_16x16x32_bf16 a[152:155], a[0:3], v[56:59], a[152:155]\n\t"
    "buffer_store_dwordx4 v[96:99], v196, s[36:39], s85 offen offset:2048 sc1\n\t"
    "ds_read_b64_tr_b16 v[194:195], v254 offset:62976\n\t"
    "ds_read_b64_tr_b16 v[46:47], v133 offset:28672\n\t"
    "ds_read_b64_tr_b16 v[64:65], v130 offset:0\n\t"
    "ds_read_b64_tr_b16 v[66:67], v130 offset:4096\n\t"
    "v_accvgpr_read_b32 v173, a117\n\t"
    "v_accvgpr_read_b32 v174, a118\n\t"
    "v_accvgpr_read_b32 v175, a119\n\t"
    "v_mul_f32 v168, v201, v168\n\t"
    "v_mul_f32 v169, v201, v169\n\t"
    "s_waitcnt lgkmcnt(12)\n\t"
    "v_mfma_f32_16x16x32_bf16 a[156:159], a[16:19], v[56:59], a[156:159]\n\t"
    "ds_read_b64_tr_b16 a[0:1], v253 offset:16384\n\t"
    "v_mul_f32 v170, v201, v170\n\t"
    "v_mfma_f32_16x16x32_bf16 a[156:159], a[20:23], v[60:63], a[156:159]\n\t"
    "s_waitcnt lgkmcnt(4)\n\t"
    "ds_read_b64_tr_b16 a[2:3], v253 offset:20480\n\t"
    "v_mul_f32 v171, v201, v171\n\t"
    "v_mfma_f32_16x16x32_bf16 a[156:159], a[28:31], v[52:55], a[156:159]\n\t"
    "ds_read_b64_tr_b16 a[4:5], v253 offset:24576\n\t"
    "v_mul_f32 v172, v201, v172\n\t"
    "v_mfma_f32_16x16x32_bf16 a[156:159], a[24:27], v[48:51], a[156:159]\n\t"
    "ds_read_b64_tr_b16 a[6:7], v253 offset:28672\n\t"
    "v_mul_f32 v173, v201, v173\n\t"
    "v_mfma_f32_16x16x32_bf16 a[156:159], v[188:191], v[220:223], a[156:159]\n\t"
    "ds_read_b64_tr_b16 a[12:13], v253 offset:40960\n\t"
    "v_mul_f32 v174, v201, v174\n\t"
    "v_mfma_f32_16x16x32_bf16 a[156:159], v[192:195], v[224:227], a[156:159]\n\t"
    "ds_read_b64_tr_b16 a[14:15], v253 offset:45056\n\t"
    "ds_read_b64_tr_b16 v[68:69], v130 offset:8192\n\t"
    "ds_read_b64_tr_b16 v[70:71], v130 offset:12288\n\t"
    "ds_read_b64_tr_b16 v[72:73], v131 offset:0\n\t"
    "v_mul_f32 v175, v201, v175\n\t"
    "v_cvt_pk_bf16_f32 v100, v168, v169\n\t"
    "v_cvt_pk_bf16_f32 v101, v170, v171\n\t"
    "v_cvt_pk_bf16_f32 v102, v172, v173\n\t"
    "v_cvt_pk_bf16_f32 v103, v174, v175\n\t"
    "v_mfma_f32_16x16x32_bf16 a[124:127], v[192:195], v[216:219], a[124:127]\n\t"
    "ds_read_b64_tr_b16 a[8:9], v253 offset:32768\n\t"
    "v_accvgpr_read_b32 v160, a152\n\t"
    "v_mfma_f32_16x16x32_bf16 a[124:127], v[188:191], v[212:215], a[124:127]\n\t"
    "ds_read_b64_tr_b16 a[10:11], v253 offset:36864\n\t"
    "v_accvgpr_read_b32 v161, a153\n\t"
    "v_mfma_f32_16x16x32_bf16 a[124:127], a[24:27], v[16:19], a[124:127]\n\t"
    "ds_read_b64_tr_b16 v[180:181], v253 offset:49152\n\t"
    "v_accvgpr_read_b32 v162, a154\n\t"
    "v_mfma_f32_16x16x32_bf16 a[124:127], a[28:31], v[20:23], a[124:127]\n\t"
    "ds_read_b64_tr_b16 v[182:183], v253 offset:53248\n\t"
    "v_accvgpr_read_b32 v163, a155\n\t"
    "v_mfma_f32_16x16x32_bf16 a[124:127], a[20:23], v[28:31], a[124:127]\n\t"
    "ds_read_b64_tr_b16 v[184:185], v253 offset:57344\n\t"
    "v_accvgpr_read_b32 v164, a156\n\t"
    "v_mfma_f32_16x16x32_bf16 a[124:127], a[16:19], v[24:27], a[124:127]\n\t"
    "buffer_store_dwordx4 v[100:103], v196, s[36:39], s84 offen offset:2048 sc1\n\t"
    "ds_read_b64_tr_b16 v[186:187], v253 offset:61440\n\t"
    "ds_read_b64_tr_b16 v[74:75], v131 offset:4096\n\t"
    "ds_read_b64_tr_b16 v[76:77], v131 offset:8192\n\t"
    "ds_read_b64_tr_b16 v[78:79], v131 offset:12288\n\t"
    "v_accvgpr_read_b32 v165, a157\n\t"
    "v_accvgpr_read_b32 v166, a158\n\t"
    "v_accvgpr_read_b32 v167, a159\n\t"
    "v_mul_f32 v160, v203, v160\n\t"
    "v_mul_f32 v161, v203, v161\n\t"
    "s_waitcnt lgkmcnt(12)\n\t"
    "v_mfma_f32_16x16x32_bf16 a[160:163], a[0:3], v[88:91], a[160:163]\n\t"
    "ds_read_b64_tr_b16 v[192:193], v254 offset:57344\n\t"
    "v_mul_f32 v162, v203, v162\n\t"
    "v_mfma_f32_16x16x32_bf16 a[160:163], a[4:7], v[92:95], a[160:163]\n\t"
    "s_waitcnt lgkmcnt(4)\n\t"
    "ds_read_b64_tr_b16 v[194:195], v254 offset:61440\n\t"
    "v_mul_f32 v163, v203, v163\n\t"
    "v_mfma_f32_16x16x32_bf16 a[160:163], a[12:15], v[84:87], a[160:163]\n\t"
    "ds_read_b64_tr_b16 v[188:189], v254 offset:49152\n\t"
    "v_mul_f32 v164, v203, v164\n\t"
    "v_mfma_f32_16x16x32_bf16 a[160:163], a[8:11], v[80:83], a[160:163]\n\t"
    "ds_read_b64_tr_b16 v[190:191], v254 offset:53248\n\t"
    "v_mul_f32 v165, v203, v165\n\t"
    "v_mfma_f32_16x16x32_bf16 a[160:163], v[180:183], v[228:231], a[160:163]\n\t"
    "ds_read_b64_tr_b16 a[24:25], v254 offset:32768\n\t"
    "v_mul_f32 v166, v203, v166\n\t"
    "v_mfma_f32_16x16x32_bf16 a[160:163], v[184:187], v[232:235], a[160:163]\n\t"
    "ds_read_b64_tr_b16 a[26:27], v254 offset:36864\n\t"
    "v_mul_f32 v167, v203, v167\n\t"
    "v_cvt_pk_bf16_f32 v104, v160, v161\n\t"
    "v_cvt_pk_bf16_f32 v105, v162, v163\n\t"
    "v_cvt_pk_bf16_f32 v106, v164, v165\n\t"
    "v_cvt_pk_bf16_f32 v107, v166, v167\n\t"
    "v_mfma_f32_16x16x32_bf16 a[192:195], v[184:187], v[240:243], a[192:195]\n\t"
    "ds_read_b64_tr_b16 a[28:29], v254 offset:40960\n\t"
    "v_accvgpr_read_b32 v168, a120\n\t"
    "v_mfma_f32_16x16x32_bf16 a[192:195], v[180:183], v[236:239], a[192:195]\n\t"
    "ds_read_b64_tr_b16 a[30:31], v254 offset:45056\n\t"
    "v_accvgpr_read_b32 v169, a121\n\t"
    "v_mfma_f32_16x16x32_bf16 a[192:195], a[8:11], v[112:115], a[192:195]\n\t"
    "ds_read_b64_tr_b16 a[20:21], v254 offset:24576\n\t"
    "v_accvgpr_read_b32 v170, a122\n\t"
    "v_mfma_f32_16x16x32_bf16 a[192:195], a[12:15], v[116:119], a[192:195]\n\t"
    "ds_read_b64_tr_b16 a[22:23], v254 offset:28672\n\t"
    "v_accvgpr_read_b32 v171, a123\n\t"
    "v_mfma_f32_16x16x32_bf16 a[192:195], a[4:7], v[124:127], a[192:195]\n\t"
    "ds_read_b64_tr_b16 a[16:17], v254 offset:16384\n\t"
    "v_accvgpr_read_b32 v172, a124\n\t"
    "v_mfma_f32_16x16x32_bf16 a[192:195], a[0:3], v[120:123], a[192:195]\n\t"
    "buffer_store_dwordx4 v[104:107], v196, s[36:39], s85 offen offset:3072 sc1\n\t"
    "ds_read_b64_tr_b16 a[18:19], v254 offset:20480\n\t"
    "v_accvgpr_read_b32 v173, a125\n\t"
    "v_accvgpr_read_b32 v174, a126\n\t"
    "v_accvgpr_read_b32 v175, a127\n\t"
    "v_mul_f32 v168, v201, v168\n\t"
    "v_mul_f32 v169, v201, v169\n\t"
    "v_mfma_f32_16x16x32_bf16 a[224:227], a[0:3], v[152:155], a[224:227]\n\t"
    "s_nop 0\n\t"
    "v_mul_f32 v170, v201, v170\n\t"
    "v_mul_f32 v171, v201, v171\n\t"
    "v_mfma_f32_16x16x32_bf16 a[224:227], a[4:7], v[156:159], a[224:227]\n\t"
    "v_mul_f32 v172, v201, v172\n\t"
    "v_mul_f32 v173, v201, v173\n\t"
    "v_mfma_f32_16x16x32_bf16 a[224:227], a[12:15], v[148:151], a[224:227]\n\t"
    "v_mul_f32 v174, v201, v174\n\t"
    "v_mul_f32 v175, v201, v175\n\t"
    "v_mfma_f32_16x16x32_bf16 a[224:227], a[8:11], v[144:147], a[224:227]\n\t"
    "v_cvt_pk_bf16_f32 v108, v168, v169\n\t"
    "v_cvt_pk_bf16_f32 v109, v170, v171\n\t"
    "v_mfma_f32_16x16x32_bf16 a[224:227], v[180:183], v[244:247], a[224:227]\n\t"
    "v_cvt_pk_bf16_f32 v110, v172, v173\n\t"
    "v_cvt_pk_bf16_f32 v111, v174, v175\n\t"
    "v_mfma_f32_16x16x32_bf16 a[224:227], v[184:187], v[248:251], a[224:227]\n\t"
    "s_waitcnt lgkmcnt(6)\n\t"
    "v_mfma_f32_16x16x32_bf16 a[228:231], v[192:195], v[248:251], a[228:231]\n\t"
    "ds_read_b64_tr_b16 a[0:1], v253 offset:16896\n\t"
    "v_mfma_f32_16x16x32_bf16 a[228:231], v[188:191], v[244:247], a[228:231]\n\t"
    "s_waitcnt lgkmcnt(1)\n\t"
    "ds_read_b64_tr_b16 a[2:3], v253 offset:20992\n\t"
    "v_mfma_f32_16x16x32_bf16 a[228:231], a[24:27], v[144:147], a[228:231]\n\t"
    "ds_read_b64_tr_b16 a[4:5], v253 offset:25088\n\t"
    "v_mfma_f32_16x16x32_bf16 a[228:231], a[28:31], v[148:151], a[228:231]\n\t"
    "ds_read_b64_tr_b16 a[6:7], v253 offset:29184\n\t"
    "v_mfma_f32_16x16x32_bf16 a[228:231], a[20:23], v[156:159], a[228:231]\n\t"
    "ds_read_b64_tr_b16 a[12:13], v253 offset:41472\n\t"
    "v_mfma_f32_16x16x32_bf16 a[228:231], a[16:19], v[152:155], a[228:231]\n\t"
    "buffer_store_dwordx4 v[108:111], v196, s[36:39], s84 offen offset:3072 sc1\n\t"
    "ds_read_b64_tr_b16 a[14:15], v253 offset:45568\n\t"
    "ds_read_b64_tr_b16 v[96:97], v128 offset:32768\n\t"
    "ds_read_b64_tr_b16 v[98:99], v128 offset:36864\n\t"
    "ds_read_b64_tr_b16 v[100:101], v128 offset:40960\n\t"
    "ds_read_b64_tr_b16 v[102:103], v128 offset:45056\n\t"
    "ds_read_b64_tr_b16 v[104:105], v129 offset:32768\n\t"
    "ds_read_b64_tr_b16 v[106:107], v129 offset:36864\n\t"
    "ds_read_b64_tr_b16 v[108:109], v129 offset:40960\n\t"
    "ds_read_b64_tr_b16 v[110:111], v129 offset:45056\n\t"
    "v_mfma_f32_16x16x32_bf16 a[196:199], a[16:19], v[120:123], a[196:199]\n\t"
    "ds_read_b64_tr_b16 a[8:9], v253 offset:33280\n\t"
    "v_accvgpr_read_b32 v160, a224\n\t"
    "v_mfma_f32_16x16x32_bf16 a[196:199], a[20:23], v[124:127], a[196:199]\n\t"
    "ds_read_b64_tr_b16 a[10:11], v253 offset:37376\n\t"
    "v_accvgpr_read_b32 v161, a225\n\t"
    "v_mfma_f32_16x16x32_bf16 a[196:199], a[28:31], v[116:119], a[196:199]\n\t"
    "ds_read_b64_tr_b16 v[180:181], v253 offset:49664\n\t"
    "v_accvgpr_read_b32 v162, a226\n\t"
    "v_mfma_f32_16x16x32_bf16 a[196:199], a[24:27], v[112:115], a[196:199]\n\t"
    "ds_read_b64_tr_b16 v[182:183], v253 offset:53760\n\t"
    "v_accvgpr_read_b32 v163, a227\n\t"
    "v_mfma_f32_16x16x32_bf16 a[196:199], v[188:191], v[236:239], a[196:199]\n\t"
    "ds_read_b64_tr_b16 v[184:185], v253 offset:57856\n\t"
    "v_accvgpr_read_b32 v164, a228\n\t"
    "v_mfma_f32_16x16x32_bf16 a[196:199], v[192:195], v[240:243], a[196:199]\n\t"
    "s_waitcnt vmcnt(8) lgkmcnt(5)\n\t"
    "s_barrier\n\t"
    "ds_read_b64_tr_b16 v[186:187], v253 offset:61952\n\t"
    "v_accvgpr_read_b32 v165, a229\n\t"
    "v_accvgpr_read_b32 v166, a230\n\t"
    "v_accvgpr_read_b32 v167, a231\n\t"
    "v_mul_f32 v160, v209, v160\n\t"
    "v_mul_f32 v161, v209, v161\n\t"
    "v_mfma_f32_16x16x32_bf16 a[164:167], v[192:195], v[232:235], a[164:167]\n\t"
    "s_nop 0\n\t"
    "v_mul_f32 v162, v209, v162\n\t"
    "v_mul_f32 v163, v209, v163\n\t"
    "v_mfma_f32_16x16x32_bf16 a[164:167], v[188:191], v[228:231], a[164:167]\n\t"
    "v_mul_f32 v164, v209, v164\n\t"
    "v_mul_f32 v165, v209, v165\n\t"
    "v_mfma_f32_16x16x32_bf16 a[164:167], a[24:27], v[80:83], a[164:167]\n\t"
    "v_mul_f32 v166, v209, v166\n\t"
    "v_mul_f32 v167, v209, v167\n\t"
    "v_mfma_f32_16x16x32_bf16 a[164:167], a[28:31], v[84:87], a[164:167]\n\t"
    "s_add_u32 s53, s98, 720896\n\t"
    "v_cvt_pk_bf16_f32 v16, v160, v161\n\t"
    "v_cvt_pk_bf16_f32 v17, v162, v163\n\t"
    "v_mfma_f32_16x16x32_bf16 a[164:167], a[20:23], v[92:95], a[164:167]\n\t"
    "s_add_u32 m0, s97, 131072\n\t"
    "v_cvt_pk_bf16_f32 v18, v164, v165\n\t"
    "v_cvt_pk_bf16_f32 v19, v166, v167\n\t"
    "v_mfma_f32_16x16x32_bf16 a[164:167], a[16:19], v[88:91], a[164:167]\n\t"
    "buffer_load_dwordx4 v255, s[24:27], s53 offen offset:0 lds\n\t"
    "buffer_load_dwordx4 v198, s[24:27], s53 offen offset:2048 lds\n\t"
    "ds_read_b64_tr_b16 a[96:97], v128 offset:0\n\t"
    "ds_read_b64_tr_b16 a[98:99], v128 offset:4096\n\t"
    "s_waitcnt lgkmcnt(15)\n\t"
    "v_mfma_f32_16x16x32_bf16 a[168:171], a[0:3], v[88:91], a[168:171]\n\t"
    "ds_read_b64_tr_b16 v[192:193], v254 offset:57856\n\t"
    "v_accvgpr_read_b32 v168, a192\n\t"
    "v_mfma_f32_16x16x32_bf16 a[168:171], a[4:7], v[92:95], a[168:171]\n\t"
    "s_waitcnt lgkmcnt(3)\n\t"
    "ds_read_b64_tr_b16 v[194:195], v254 offset:61952\n\t"
    "v_accvgpr_read_b32 v169, a193\n\t"
    "v_mfma_f32_16x16x32_bf16 a[168:171], a[12:15], v[84:87], a[168:171]\n\t"
    "ds_read_b64_tr_b16 v[188:189], v254 offset:49664\n\t"
    "v_accvgpr_read_b32 v170, a194\n\t"
    "v_mfma_f32_16x16x32_bf16 a[168:171], a[8:11], v[80:83], a[168:171]\n\t"
    "ds_read_b64_tr_b16 v[190:191], v254 offset:53760\n\t"
    "v_accvgpr_read_b32 v171, a195\n\t"
    "v_mfma_f32_16x16x32_bf16 a[168:171], v[180:183], v[228:231], a[168:171]\n\t"
    "ds_read_b64_tr_b16 a[24:25], v254 offset:33280\n\t"
    "v_accvgpr_read_b32 v172, a196\n\t"
    "v_mfma_f32_16x16x32_bf16 a[168:171], v[184:187], v[232:235], a[168:171]\n\t"
    "buffer_store_dwordx4 v[16:19], v196, s[36:39], s88 offen offset:0 sc1\n\t"
    "ds_read_b64_tr_b16 a[26:27], v254 offset:37376\n\t"
    "ds_read_b64_tr_b16 a[100:101], v128 offset:8192\n\t"
    "ds_read_b64_tr_b16 a[102:103], v128 offset:12288\n\t"
    "v_accvgpr_read_b32 v173, a197\n\t"
    "v_accvgpr_read_b32 v174, a198\n\t"
    "v_accvgpr_read_b32 v175, a199\n\t"
    "v_mul_f32 v168, v207, v168\n\t"
    "v_mul_f32 v169, v207, v169\n\t"
    "v_mfma_f32_16x16x32_bf16 a[200:203], v[184:187], v[240:243], a[200:203]\n\t"
    "ds_read_b64_tr_b16 a[28:29], v254 offset:41472\n\t"
    "v_mul_f32 v170, v207, v170\n\t"
    "v_mfma_f32_16x16x32_bf16 a[200:203], v[180:183], v[236:239], a[200:203]\n\t"
    "ds_read_b64_tr_b16 a[30:31], v254 offset:45568\n\t"
    "v_mul_f32 v171, v207, v171\n\t"
    "v_mfma_f32_16x16x32_bf16 a[200:203], a[8:11], v[112:115], a[200:203]\n\t"
    "ds_read_b64_tr_b16 a[20:21], v254 offset:25088\n\t"
    "v_mul_f32 v172, v207, v172\n\t"
    "v_mfma_f32_16x16x32_bf16 a[200:203], a[12:15], v[116:119], a[200:203]\n\t"
    "ds_read_b64_tr_b16 a[22:23], v254 offset:29184\n\t"
    "v_mul_f32 v173, v207, v173\n\t"
    "v_mfma_f32_16x16x32_bf16 a[200:203], a[4:7], v[124:127], a[200:203]\n\t"
    "s_add_u32 m0, s97, 131968\n\t"
    "ds_read_b64_tr_b16 a[16:17], v254 offset:16896\n\t"
    "v_mul_f32 v174, v207, v174\n\t"
    "v_mfma_f32_16x16x32_bf16 a[200:203], a[0:3], v[120:123], a[200:203]\n\t"
    "buffer_load_dwordx4 v255, s[24:27], s53 offen offset:128 lds\n\t"
    "buffer_load_dwordx4 v198, s[24:27], s53 offen offset:2176 lds\n\t"
    "ds_read_b64_tr_b16 a[18:19], v254 offset:20992\n\t"
    "ds_read_b64_tr_b16 a[104:105], v129 offset:0\n\t"
    "ds_read_b64_tr_b16 a[106:107], v129 offset:4096\n\t"
    "v_mul_f32 v175, v207, v175\n\t"
    "v_cvt_pk_bf16_f32 v20, v168, v169\n\t"
    "v_cvt_pk_bf16_f32 v21, v170, v171\n\t"
    "v_cvt_pk_bf16_f32 v22, v172, v173\n\t"
    "v_cvt_pk_bf16_f32 v23, v174, v175\n\t"
    "v_mfma_f32_16x16x32_bf16 a[232:235], a[0:3], v[152:155], a[232:235]\n\t"
    "s_nop 0\n\t"
    "v_accvgpr_read_b32 v160, a160\n\t"
    "v_accvgpr_read_b32 v161, a161\n\t"
    "v_mfma_f32_16x16x32_bf16 a[232:235], a[4:7], v[156:159], a[232:235]\n\t"
    "v_accvgpr_read_b32 v162, a162\n\t"
    "v_accvgpr_read_b32 v163, a163\n\t"
    "v_mfma_f32_16x16x32_bf16 a[232:235], a[12:15], v[148:151], a[232:235]\n\t"
    "v_accvgpr_read_b32 v164, a164\n\t"
    "v_accvgpr_read_b32 v165, a165\n\t"
    "v_mfma_f32_16x16x32_bf16 a[232:235], a[8:11], v[144:147], a[232:235]\n\t"
    "v_accvgpr_read_b32 v166, a166\n\t"
    "v_accvgpr_read_b32 v167, a167\n\t"
    "v_mfma_f32_16x16x32_bf16 a[232:235], v[180:183], v[244:247], a[232:235]\n\t"
    "v_mul_f32 v160, v205, v160\n\t"
    "v_mul_f32 v161, v205, v161\n\t"
    "v_mfma_f32_16x16x32_bf16 a[232:235], v[184:187], v[248:251], a[232:235]\n\t"
    "buffer_store_dwordx4 v[20:23], v196, s[36:39], s87 offen offset:0 sc1\n\t"
    "ds_read_b64_tr_b16 a[108:109], v129 offset:8192\n\t"
    "ds_read_b64_tr_b16 a[110:111], v129 offset:12288\n\t"
    "s_waitcnt lgkmcnt(12)\n\t"
    "s_waitcnt vmcnt(6)\n\t"
    "s_lshl_b32 s60, s96, 2\n\t"
    "s_add_u32 s60, s60, s40\n\t"
    "s_lshl_b32 exec_lo, 1, s60\n\t"
    "s_mov_b32 exec_hi, 0\n\t"
    "v_add_u32 v139, 1, v211\n\t"
    "buffer_store_dword v139, v137, s[12:15], 0 offen sc1\n\t"
    "s_mov_b64 exec, -1\n\t"
    "v_mfma_f32_16x16x32_bf16 a[236:239], v[192:195], v[248:251], a[236:239]\n\t"
    "ds_read_b64_tr_b16 a[0:1], v253 offset:17408\n\t"
    "v_mul_f32 v162, v205, v162\n\t"
    "v_mfma_f32_16x16x32_bf16 a[236:239], v[188:191], v[244:247], a[236:239]\n\t"
    "s_waitcnt lgkmcnt(5)\n\t"
    "ds_read_b64_tr_b16 a[2:3], v253 offset:21504\n\t"
    "v_mul_f32 v163, v205, v163\n\t"
    "v_mfma_f32_16x16x32_bf16 a[236:239], a[24:27], v[144:147], a[236:239]\n\t"
    "ds_read_b64_tr_b16 a[4:5], v253 offset:25600\n\t"
    "v_mul_f32 v164, v205, v164\n\t"
    "v_mfma_f32_16x16x32_bf16 a[236:239], a[28:31], v[148:151], a[236:239]\n\t"
    "s_add_u32 s53, s98, 786432\n\t"
    "ds_read_b64_tr_b16 a[6:7], v253 offset:29696\n\t"
    "v_mul_f32 v165, v205, v165\n\t"
    "v_mfma_f32_16x16x32_bf16 a[236:239], a[20:23], v[156:159], a[236:239]\n\t"
    "s_add_u32 m0, s97, 147456\n\t"
    "ds_read_b64_tr_b16 a[12:13], v253 offset:41984\n\t"
    "v_mul_f32 v166, v205, v166\n\t"
    "v_mfma_f32_16x16x32_bf16 a[236:239], a[16:19], v[152:155], a[236:239]\n\t"
    "buffer_load_dwordx4 v255, s[24:27], s53 offen offset:0 lds\n\t"
    "buffer_load_dwordx4 v198, s[24:27], s53 offen offset:2048 lds\n\t"
    "ds_read_b64_tr_b16 a[14:15], v253 offset:46080\n\t"
    "ds_read_b64_tr_b16 a[112:113], v128 offset:16384\n\t"
    "ds_read_b64_tr_b16 a[114:115], v128 offset:20480\n\t"
    "v_mul_f32 v167, v205, v167\n\t"
    "v_cvt_pk_bf16_f32 v24, v160, v161\n\t"
    "v_cvt_pk_bf16_f32 v25, v162, v163\n\t"
    "v_cvt_pk_bf16_f32 v26, v164, v165\n\t"
    "v_cvt_pk_bf16_f32 v27, v166, v167\n\t"
    "v_mfma_f32_16x16x32_bf16 a[204:207], a[16:19], v[120:123], a[204:207]\n\t"
    "ds_read_b64_tr_b16 a[8:9], v253 offset:33792\n\t"
    "v_accvgpr_read_b32 v168, a232\n\t"
    "v_mfma_f32_16x16x32_bf16 a[204:207], a[20:23], v[124:127], a[204:207]\n\t"
    "ds_read_b64_tr_b16 a[10:11], v253 offset:37888\n\t"
    "v_accvgpr_read_b32 v169, a233\n\t"
    "v_mfma_f32_16x16x32_bf16 a[204:207], a[28:31], v[116:119], a[204:207]\n\t"
    "ds_read_b64_tr_b16 v[180:181], v253 offset:50176\n\t"
    "v_accvgpr_read_b32 v170, a234\n\t"
    "v_mfma_f32_16x16x32_bf16 a[204:207], a[24:27], v[112:115], a[204:207]\n\t"
    "ds_read_b64_tr_b16 v[182:183], v253 offset:54272\n\t"
    "v_accvgpr_read_b32 v171, a235\n\t"
    "v_mfma_f32_16x16x32_bf16 a[204:207], v[188:191], v[236:239], a[204:207]\n\t"
    "ds_read_b64_tr_b16 v[184:185], v253 offset:58368\n\t"
    "v_accvgpr_read_b32 v172, a236\n\t"
    "v_mfma_f32_16x16x32_bf16 a[204:207], v[192:195], v[240:243], a[204:207]\n\t"
    "buffer_store_dwordx4 v[24:27], v196, s[36:39], s86 offen offset:0 sc1\n\t"
    "ds_read_b64_tr_b16 v[186:187], v253 offset:62464\n\t"
    "ds_read_b64_tr_b16 a[116:117], v128 offset:24576\n\t"
    "ds_read_b64_tr_b16 a[118:119], v128 offset:28672\n\t"
    "v_accvgpr_read_b32 v173, a237\n\t"
    "v_accvgpr_read_b32 v174, a238\n\t"
    "v_accvgpr_read_b32 v175, a239\n\t"
    "v_mul_f32 v168, v209, v168\n\t"
    "v_mul_f32 v169, v209, v169\n\t"
    "v_mfma_f32_16x16x32_bf16 a[172:175], v[192:195], v[232:235], a[172:175]\n\t"
    "s_nop 0\n\t"
    "v_mul_f32 v170, v209, v170\n\t"
    "v_mul_f32 v171, v209, v171\n\t"
    "v_mfma_f32_16x16x32_bf16 a[172:175], v[188:191], v[228:231], a[172:175]\n\t"
    "v_mul_f32 v172, v209, v172\n\t"
    "v_mul_f32 v173, v209, v173\n\t"
    "v_mfma_f32_16x16x32_bf16 a[172:175], a[24:27], v[80:83], a[172:175]\n\t"
    "v_mul_f32 v174, v209, v174\n\t"
    "v_mul_f32 v175, v209, v175\n\t"
    "v_mfma_f32_16x16x32_bf16 a[172:175], a[28:31], v[84:87], a[172:175]\n\t"
    "v_cvt_pk_bf16_f32 v28, v168, v169\n\t"
    "v_cvt_pk_bf16_f32 v29, v170, v171\n\t"
    "v_mfma_f32_16x16x32_bf16 a[172:175], a[20:23], v[92:95], a[172:175]\n\t"
    "s_add_u32 m0, s97, 148352\n\t"
    "v_cvt_pk_bf16_f32 v30, v172, v173\n\t"
    "v_cvt_pk_bf16_f32 v31, v174, v175\n\t"
    "v_mfma_f32_16x16x32_bf16 a[172:175], a[16:19], v[88:91], a[172:175]\n\t"
    "buffer_load_dwordx4 v255, s[24:27], s53 offen offset:128 lds\n\t"
    "buffer_load_dwordx4 v198, s[24:27], s53 offen offset:2176 lds\n\t"
    "ds_read_b64_tr_b16 a[120:121], v129 offset:16384\n\t"
    "ds_read_b64_tr_b16 a[122:123], v129 offset:20480\n\t"
    "s_waitcnt lgkmcnt(12)\n\t"
    "v_mfma_f32_16x16x32_bf16 a[176:179], a[0:3], v[88:91], a[176:179]\n\t"
    "ds_read_b64_tr_b16 v[192:193], v254 offset:58368\n\t"
    "v_accvgpr_read_b32 v160, a200\n\t"
    "v_mfma_f32_16x16x32_bf16 a[176:179], a[4:7], v[92:95], a[176:179]\n\t"
    "s_waitcnt lgkmcnt(5)\n\t"
    "ds_read_b64_tr_b16 v[194:195], v254 offset:62464\n\t"
    "v_accvgpr_read_b32 v161, a201\n\t"
    "v_mfma_f32_16x16x32_bf16 a[176:179], a[12:15], v[84:87], a[176:179]\n\t"
    "ds_read_b64_tr_b16 v[188:189], v254 offset:50176\n\t"
    "v_accvgpr_read_b32 v162, a202\n\t"
    "v_mfma_f32_16x16x32_bf16 a[176:179], a[8:11], v[80:83], a[176:179]\n\t"
    "ds_read_b64_tr_b16 v[190:191], v254 offset:54272\n\t"
    "v_accvgpr_read_b32 v163, a203\n\t"
    "v_mfma_f32_16x16x32_bf16 a[176:179], v[180:183], v[228:231], a[176:179]\n\t"
    "ds_read_b64_tr_b16 a[24:25], v254 offset:33792\n\t"
    "v_accvgpr_read_b32 v164, a204\n\t"
    "v_mfma_f32_16x16x32_bf16 a[176:179], v[184:187], v[232:235], a[176:179]\n\t"
    "buffer_store_dwordx4 v[28:31], v196, s[36:39], s88 offen offset:1024 sc1\n\t"
    "ds_read_b64_tr_b16 a[26:27], v254 offset:37888\n\t"
    "ds_read_b64_tr_b16 a[124:125], v129 offset:24576\n\t"
    "v_accvgpr_read_b32 v165, a205\n\t"
    "v_accvgpr_read_b32 v166, a206\n\t"
    "v_accvgpr_read_b32 v167, a207\n\t"
    "v_mul_f32 v160, v207, v160\n\t"
    "v_mul_f32 v161, v207, v161\n\t"
    "v_mfma_f32_16x16x32_bf16 a[208:211], v[184:187], v[240:243], a[208:211]\n\t"
    "ds_read_b64_tr_b16 a[28:29], v254 offset:41984\n\t"
    "v_mul_f32 v162, v207, v162\n\t"
    "v_mfma_f32_16x16x32_bf16 a[208:211], v[180:183], v[236:239], a[208:211]\n\t"
    "ds_read_b64_tr_b16 a[30:31], v254 offset:46080\n\t"
    "v_mul_f32 v163, v207, v163\n\t"
    "v_mfma_f32_16x16x32_bf16 a[208:211], a[8:11], v[112:115], a[208:211]\n\t"
    "ds_read_b64_tr_b16 a[20:21], v254 offset:25600\n\t"
    "v_mul_f32 v164, v207, v164\n\t"
    "v_mfma_f32_16x16x32_bf16 a[208:211], a[12:15], v[116:119], a[208:211]\n\t"
    "s_add_u32 s53, s98, 851968\n\t"
    "ds_read_b64_tr_b16 a[22:23], v254 offset:29696\n\t"
    "v_mul_f32 v165, v207, v165\n\t"
    "v_mfma_f32_16x16x32_bf16 a[208:211], a[4:7], v[124:127], a[208:211]\n\t"
    "s_add_u32 m0, s97, 65536\n\t"
    "ds_read_b64_tr_b16 a[16:17], v254 offset:17408\n\t"
    "v_mul_f32 v166, v207, v166\n\t"
    "v_mfma_f32_16x16x32_bf16 a[208:211], a[0:3], v[120:123], a[208:211]\n\t"
    "buffer_load_dwordx4 v255, s[24:27], s53 offen offset:0 lds\n\t"
    "buffer_load_dwordx4 v198, s[24:27], s53 offen offset:2048 lds\n\t"
    "ds_read_b64_tr_b16 a[18:19], v254 offset:21504\n\t"
    "ds_read_b64_tr_b16 a[126:127], v129 offset:28672\n\t"
    "v_mul_f32 v167, v207, v167\n\t"
    "v_cvt_pk_bf16_f32 v16, v160, v161\n\t"
    "v_cvt_pk_bf16_f32 v17, v162, v163\n\t"
    "v_cvt_pk_bf16_f32 v18, v164, v165\n\t"
    "v_cvt_pk_bf16_f32 v19, v166, v167\n\t"
    "v_mfma_f32_16x16x32_bf16 a[240:243], a[0:3], v[152:155], a[240:243]\n\t"
    "s_nop 0\n\t"
    "v_accvgpr_read_b32 v168, a168\n\t"
    "v_accvgpr_read_b32 v169, a169\n\t"
    "v_mfma_f32_16x16x32_bf16 a[240:243], a[4:7], v[156:159], a[240:243]\n\t"
    "v_accvgpr_read_b32 v170, a170\n\t"
    "v_accvgpr_read_b32 v171, a171\n\t"
    "v_mfma_f32_16x16x32_bf16 a[240:243], a[12:15], v[148:151], a[240:243]\n\t"
    "v_accvgpr_read_b32 v172, a172\n\t"
    "v_accvgpr_read_b32 v173, a173\n\t"
    "v_mfma_f32_16x16x32_bf16 a[240:243], a[8:11], v[144:147], a[240:243]\n\t"
    "v_accvgpr_read_b32 v174, a174\n\t"
    "v_accvgpr_read_b32 v175, a175\n\t"
    "v_mfma_f32_16x16x32_bf16 a[240:243], v[180:183], v[244:247], a[240:243]\n\t"
    "v_mul_f32 v168, v205, v168\n\t"
    "v_mul_f32 v169, v205, v169\n\t"
    "v_mfma_f32_16x16x32_bf16 a[240:243], v[184:187], v[248:251], a[240:243]\n\t"
    "buffer_store_dwordx4 v[16:19], v196, s[36:39], s87 offen offset:1024 sc1\n\t"
    "ds_read_b64_tr_b16 a[128:129], v128 offset:49152\n\t"
    "s_waitcnt lgkmcnt(9)\n\t"
    "v_mfma_f32_16x16x32_bf16 a[244:247], v[192:195], v[248:251], a[244:247]\n\t"
    "ds_read_b64_tr_b16 a[0:1], v253 offset:17920\n\t"
    "v_mul_f32 v170, v205, v170\n\t"
    "v_mfma_f32_16x16x32_bf16 a[244:247], v[188:191], v[244:247], a[244:247]\n\t"
    "s_waitcnt lgkmcnt(3)\n\t"
    "ds_read_b64_tr_b16 a[2:3], v253 offset:22016\n\t"
    "v_mul_f32 v171, v205, v171\n\t"
    "v_mfma_f32_16x16x32_bf16 a[244:247], a[24:27], v[144:147], a[244:247]\n\t"
    "ds_read_b64_tr_b16 a[4:5], v253 offset:26112\n\t"
    "v_mul_f32 v172, v205, v172\n\t"
    "v_mfma_f32_16x16x32_bf16 a[244:247], a[28:31], v[148:151], a[244:247]\n\t"
    "ds_read_b64_tr_b16 a[6:7], v253 offset:30208\n\t"
    "v_mul_f32 v173, v205, v173\n\t"
    "v_mfma_f32_16x16x32_bf16 a[244:247], a[20:23], v[156:159], a[244:247]\n\t"
    "s_add_u32 m0, s97, 66432\n\t"
    "ds_read_b64_tr_b16 a[12:13], v253 offset:42496\n\t"
    "v_mul_f32 v174, v205, v174\n\t"
    "v_mfma_f32_16x16x32_bf16 a[244:247], a[16:19], v[152:155], a[244:247]\n\t"
    "buffer_load_dwordx4 v255, s[24:27], s53 offen offset:128 lds\n\t"
    "buffer_load_dwordx4 v198, s[24:27], s53 offen offset:2176 lds\n\t"
    "ds_read_b64_tr_b16 a[14:15], v253 offset:46592\n\t"
    "ds_read_b64_tr_b16 a[130:131], v128 offset:53248\n\t"
    "v_mul_f32 v175, v205, v175\n\t"
    "v_cvt_pk_bf16_f32 v20, v168, v169\n\t"
    "v_cvt_pk_bf16_f32 v21, v170, v171\n\t"
    "v_cvt_pk_bf16_f32 v22, v172, v173\n\t"
    "v_cvt_pk_bf16_f32 v23, v174, v175\n\t"
    "v_mfma_f32_16x16x32_bf16 a[212:215], a[16:19], v[120:123], a[212:215]\n\t"
    "ds_read_b64_tr_b16 a[8:9], v253 offset:34304\n\t"
    "v_accvgpr_read_b32 v160, a240\n\t"
    "v_mfma_f32_16x16x32_bf16 a[212:215], a[20:23], v[124:127], a[212:215]\n\t"
    "ds_read_b64_tr_b16 a[10:11], v253 offset:38400\n\t"
    "v_accvgpr_read_b32 v161, a241\n\t"
    "v_mfma_f32_16x16x32_bf16 a[212:215], a[28:31], v[116:119], a[212:215]\n\t"
    "ds_read_b64_tr_b16 v[180:181], v253 offset:50688\n\t"
    "v_accvgpr_read_b32 v162, a242\n\t"
    "v_mfma_f32_16x16x32_bf16 a[212:215], a[24:27], v[112:115], a[212:215]\n\t"
    "ds_read_b64_tr_b16 v[182:183], v253 offset:54784\n\t"
    "v_accvgpr_read_b32 v163, a243\n\t"
    "v_mfma_f32_16x16x32_bf16 a[212:215], v[188:191], v[236:239], a[212:215]\n\t"
    "ds_read_b64_tr_b16 v[184:185], v253 offset:58880\n\t"
    "v_accvgpr_read_b32 v164, a244\n\t"
    "v_mfma_f32_16x16x32_bf16 a[212:215], v[192:195], v[240:243], a[212:215]\n\t"
    "buffer_store_dwordx4 v[20:23], v196, s[36:39], s86 offen offset:1024 sc1\n\t"
    "ds_read_b64_tr_b16 v[186:187], v253 offset:62976\n\t"
    "ds_read_b64_tr_b16 a[132:133], v128 offset:57344\n\t"
    "v_accvgpr_read_b32 v165, a245\n\t"
    "v_accvgpr_read_b32 v166, a246\n\t"
    "v_accvgpr_read_b32 v167, a247\n\t"
    "v_mul_f32 v160, v209, v160\n\t"
    "v_mul_f32 v161, v209, v161\n\t"
    "v_mfma_f32_16x16x32_bf16 a[180:183], v[192:195], v[232:235], a[180:183]\n\t"
    "s_nop 0\n\t"
    "v_mul_f32 v162, v209, v162\n\t"
    "v_mul_f32 v163, v209, v163\n\t"
    "v_mfma_f32_16x16x32_bf16 a[180:183], v[188:191], v[228:231], a[180:183]\n\t"
    "v_mul_f32 v164, v209, v164\n\t"
    "v_mul_f32 v165, v209, v165\n\t"
    "v_mfma_f32_16x16x32_bf16 a[180:183], a[24:27], v[80:83], a[180:183]\n\t"
    "v_mul_f32 v166, v209, v166\n\t"
    "v_mul_f32 v167, v209, v167\n\t"
    "v_mfma_f32_16x16x32_bf16 a[180:183], a[28:31], v[84:87], a[180:183]\n\t"
    "s_add_u32 s53, s98, 917504\n\t"
    "v_cvt_pk_bf16_f32 v24, v160, v161\n\t"
    "v_cvt_pk_bf16_f32 v25, v162, v163\n\t"
    "v_mfma_f32_16x16x32_bf16 a[180:183], a[20:23], v[92:95], a[180:183]\n\t"
    "s_add_u32 m0, s97, 32768\n\t"
    "v_cvt_pk_bf16_f32 v26, v164, v165\n\t"
    "v_cvt_pk_bf16_f32 v27, v166, v167\n\t"
    "v_mfma_f32_16x16x32_bf16 a[180:183], a[16:19], v[88:91], a[180:183]\n\t"
    "buffer_load_dwordx4 v255, s[24:27], s53 offen offset:0 lds\n\t"
    "buffer_load_dwordx4 v198, s[24:27], s53 offen offset:2048 lds\n\t"
    "ds_read_b64_tr_b16 a[134:135], v128 offset:61440\n\t"
    "s_waitcnt lgkmcnt(9)\n\t"
    "v_mfma_f32_16x16x32_bf16 a[184:187], a[0:3], v[88:91], a[184:187]\n\t"
    "ds_read_b64_tr_b16 v[192:193], v254 offset:58880\n\t"
    "v_accvgpr_read_b32 v168, a208\n\t"
    "v_mfma_f32_16x16x32_bf16 a[184:187], a[4:7], v[92:95], a[184:187]\n\t"
    "s_waitcnt lgkmcnt(3)\n\t"
    "ds_read_b64_tr_b16 v[194:195], v254 offset:62976\n\t"
    "v_accvgpr_read_b32 v169, a209\n\t"
    "v_mfma_f32_16x16x32_bf16 a[184:187], a[12:15], v[84:87], a[184:187]\n\t"
    "ds_read_b64_tr_b16 v[188:189], v254 offset:50688\n\t"
    "v_accvgpr_read_b32 v170, a210\n\t"
    "v_mfma_f32_16x16x32_bf16 a[184:187], a[8:11], v[80:83], a[184:187]\n\t"
    "ds_read_b64_tr_b16 v[190:191], v254 offset:54784\n\t"
    "v_accvgpr_read_b32 v171, a211\n\t"
    "v_mfma_f32_16x16x32_bf16 a[184:187], v[180:183], v[228:231], a[184:187]\n\t"
    "ds_read_b64_tr_b16 a[24:25], v254 offset:34304\n\t"
    "v_accvgpr_read_b32 v172, a212\n\t"
    "v_mfma_f32_16x16x32_bf16 a[184:187], v[184:187], v[232:235], a[184:187]\n\t"
    "buffer_store_dwordx4 v[24:27], v196, s[36:39], s88 offen offset:2048 sc1\n\t"
    "ds_read_b64_tr_b16 a[26:27], v254 offset:38400\n\t"
    "ds_read_b64_tr_b16 a[136:137], v129 offset:49152\n\t"
    "v_accvgpr_read_b32 v173, a213\n\t"
    "v_accvgpr_read_b32 v174, a214\n\t"
    "v_accvgpr_read_b32 v175, a215\n\t"
    "v_mul_f32 v168, v207, v168\n\t"
    "v_mul_f32 v169, v207, v169\n\t"
    "v_mfma_f32_16x16x32_bf16 a[216:219], v[184:187], v[240:243], a[216:219]\n\t"
    "ds_read_b64_tr_b16 a[28:29], v254 offset:42496\n\t"
    "v_mul_f32 v170, v207, v170\n\t"
    "v_mfma_f32_16x16x32_bf16 a[216:219], v[180:183], v[236:239], a[216:219]\n\t"
    "ds_read_b64_tr_b16 a[30:31], v254 offset:46592\n\t"
    "v_mul_f32 v171, v207, v171\n\t"
    "v_mfma_f32_16x16x32_bf16 a[216:219], a[8:11], v[112:115], a[216:219]\n\t"
    "ds_read_b64_tr_b16 a[20:21], v254 offset:26112\n\t"
    "v_mul_f32 v172, v207, v172\n\t"
    "v_mfma_f32_16x16x32_bf16 a[216:219], a[12:15], v[116:119], a[216:219]\n\t"
    "ds_read_b64_tr_b16 a[22:23], v254 offset:30208\n\t"
    "v_mul_f32 v173, v207, v173\n\t"
    "v_mfma_f32_16x16x32_bf16 a[216:219], a[4:7], v[124:127], a[216:219]\n\t"
    "s_add_u32 m0, s97, 33664\n\t"
    "ds_read_b64_tr_b16 a[16:17], v254 offset:17920\n\t"
    "v_mul_f32 v174, v207, v174\n\t"
    "v_mfma_f32_16x16x32_bf16 a[216:219], a[0:3], v[120:123], a[216:219]\n\t"
    "buffer_load_dwordx4 v255, s[24:27], s53 offen offset:128 lds\n\t"
    "buffer_load_dwordx4 v198, s[24:27], s53 offen offset:2176 lds\n\t"
    "ds_read_b64_tr_b16 a[18:19], v254 offset:22016\n\t"
    "ds_read_b64_tr_b16 a[138:139], v129 offset:53248\n\t"
    "v_mul_f32 v175, v207, v175\n\t"
    "v_cvt_pk_bf16_f32 v28, v168, v169\n\t"
    "v_cvt_pk_bf16_f32 v29, v170, v171\n\t"
    "v_cvt_pk_bf16_f32 v30, v172, v173\n\t"
    "v_cvt_pk_bf16_f32 v31, v174, v175\n\t"
    "v_mfma_f32_16x16x32_bf16 a[248:251], a[0:3], v[152:155], a[248:251]\n\t"
    "s_nop 0\n\t"
    "v_accvgpr_read_b32 v160, a176\n\t"
    "v_accvgpr_read_b32 v161, a177\n\t"
    "v_mfma_f32_16x16x32_bf16 a[248:251], a[4:7], v[156:159], a[248:251]\n\t"
    "v_accvgpr_read_b32 v162, a178\n\t"
    "v_accvgpr_read_b32 v163, a179\n\t"
    "v_mfma_f32_16x16x32_bf16 a[248:251], a[12:15], v[148:151], a[248:251]\n\t"
    "v_accvgpr_read_b32 v164, a180\n\t"
    "v_accvgpr_read_b32 v165, a181\n\t"
    "v_mfma_f32_16x16x32_bf16 a[248:251], a[8:11], v[144:147], a[248:251]\n\t"
    "v_accvgpr_read_b32 v166, a182\n\t"
    "v_accvgpr_read_b32 v167, a183\n\t"
    "v_mfma_f32_16x16x32_bf16 a[248:251], v[180:183], v[244:247], a[248:251]\n\t"
    "v_mul_f32 v160, v205, v160\n\t"
    "v_mul_f32 v161, v205, v161\n\t"
    "v_mfma_f32_16x16x32_bf16 a[248:251], v[184:187], v[248:251], a[248:251]\n\t"
    "buffer_store_dwordx4 v[28:31], v196, s[36:39], s87 offen offset:2048 sc1\n\t"
    "ds_read_b64_tr_b16 a[140:141], v129 offset:57344\n\t"
    "s_waitcnt lgkmcnt(9)\n\t"
    "s_mov_b32 exec_hi, 0\n\t"
    "buffer_load_dword v138, v137, s[12:15], 0 offen sc1\n\t"
    "s_mov_b64 exec, -1\n\t"
    "v_mfma_f32_16x16x32_bf16 a[252:255], v[192:195], v[248:251], a[252:255]\n\t"
    "s_nop 0\n\t"
    "v_mul_f32 v162, v205, v162\n\t"
    "v_mul_f32 v163, v205, v163\n\t"
    "v_mfma_f32_16x16x32_bf16 a[252:255], v[188:191], v[244:247], a[252:255]\n\t"
    "s_waitcnt lgkmcnt(2)\n\t"
    "v_mul_f32 v164, v205, v164\n\t"
    "v_mul_f32 v165, v205, v165\n\t"
    "v_mfma_f32_16x16x32_bf16 a[252:255], a[24:27], v[144:147], a[252:255]\n\t"
    "v_mul_f32 v166, v205, v166\n\t"
    "v_mul_f32 v167, v205, v167\n\t"
    "v_mfma_f32_16x16x32_bf16 a[252:255], a[28:31], v[148:151], a[252:255]\n\t"
    "v_cvt_pk_bf16_f32 v16, v160, v161\n\t"
    "v_cvt_pk_bf16_f32 v17, v162, v163\n\t"
    "v_mfma_f32_16x16x32_bf16 a[252:255], a[20:23], v[156:159], a[252:255]\n\t"
    "v_cvt_pk_bf16_f32 v18, v164, v165\n\t"
    "v_cvt_pk_bf16_f32 v19, v166, v167\n\t"
    "v_mfma_f32_16x16x32_bf16 a[252:255], a[16:19], v[152:155], a[252:255]\n\t"
    "ds_read_b64_tr_b16 a[142:143], v129 offset:61440\n\t"
    "v_mfma_f32_16x16x32_bf16 a[220:223], a[16:19], v[120:123], a[220:223]\n\t"
    "s_nop 0\n\t"
    "v_accvgpr_read_b32 v168, a248\n\t"
    "v_accvgpr_read_b32 v169, a249\n\t"
    "v_mfma_f32_16x16x32_bf16 a[220:223], a[20:23], v[124:127], a[220:223]\n\t"
    "v_accvgpr_read_b32 v170, a250\n\t"
    "v_accvgpr_read_b32 v171, a251\n\t"
    "v_mfma_f32_16x16x32_bf16 a[220:223], a[28:31], v[116:119], a[220:223]\n\t"
    "v_accvgpr_read_b32 v172, a252\n\t"
    "v_accvgpr_read_b32 v173, a253\n\t"
    "v_mfma_f32_16x16x32_bf16 a[220:223], a[24:27], v[112:115], a[220:223]\n\t"
    "v_accvgpr_read_b32 v174, a254\n\t"
    "v_accvgpr_read_b32 v175, a255\n\t"
    "v_mfma_f32_16x16x32_bf16 a[220:223], v[188:191], v[236:239], a[220:223]\n\t"
    "v_mul_f32 v168, v209, v168\n\t"
    "v_mul_f32 v169, v209, v169\n\t"
    "v_mfma_f32_16x16x32_bf16 a[220:223], v[192:195], v[240:243], a[220:223]\n\t"
    "buffer_store_dwordx4 v[16:19], v196, s[36:39], s86 offen offset:2048 sc1\n\t"
    "v_mfma_f32_16x16x32_bf16 a[188:191], v[192:195], v[232:235], a[188:191]\n\t"
    "s_nop 0\n\t"
    "v_mul_f32 v170, v209, v170\n\t"
    "v_mul_f32 v171, v209, v171\n\t"
    "v_mfma_f32_16x16x32_bf16 a[188:191], v[188:191], v[228:231], a[188:191]\n\t"
    "v_mul_f32 v172, v209, v172\n\t"
    "v_mul_f32 v173, v209, v173\n\t"
    "v_mfma_f32_16x16x32_bf16 a[188:191], a[24:27], v[80:83], a[188:191]\n\t"
    "v_mul_f32 v174, v209, v174\n\t"
    "v_mul_f32 v175, v209, v175\n\t"
    "v_mfma_f32_16x16x32_bf16 a[188:191], a[28:31], v[84:87], a[188:191]\n\t"
    "v_cvt_pk_bf16_f32 v20, v168, v169\n\t"
    "v_cvt_pk_bf16_f32 v21, v170, v171\n\t"
    "v_mfma_f32_16x16x32_bf16 a[188:191], a[20:23], v[92:95], a[188:191]\n\t"
    "v_cvt_pk_bf16_f32 v22, v172, v173\n\t"
    "v_cvt_pk_bf16_f32 v23, v174, v175\n\t"
    "v_mfma_f32_16x16x32_bf16 a[188:191], a[16:19], v[88:91], a[188:191]\n\t"
    "buffer_store_dwordx4 v[20:23], v196, s[36:39], s88 offen offset:3072 sc1\n\t"
    "v_accvgpr_read_b32 v160, a216\n\t"
    "v_accvgpr_read_b32 v161, a217\n\t"
    "v_accvgpr_read_b32 v162, a218\n\t"
    "v_accvgpr_read_b32 v163, a219\n\t"
    "v_accvgpr_read_b32 v164, a220\n\t"
    "v_accvgpr_read_b32 v165, a221\n\t"
    "v_accvgpr_read_b32 v166, a222\n\t"
    "v_accvgpr_read_b32 v167, a223\n\t"
    "v_mul_f32 v160, v207, v160\n\t"
    "v_mul_f32 v161, v207, v161\n\t"
    "v_mul_f32 v162, v207, v162\n\t"
    "v_mul_f32 v163, v207, v163\n\t"
    "v_mul_f32 v164, v207, v164\n\t"
    "v_mul_f32 v165, v207, v165\n\t"
    "v_mul_f32 v166, v207, v166\n\t"
    "v_mul_f32 v167, v207, v167\n\t"
    "v_cvt_pk_bf16_f32 v24, v160, v161\n\t"
    "v_cvt_pk_bf16_f32 v25, v162, v163\n\t"
    "v_cvt_pk_bf16_f32 v26, v164, v165\n\t"
    "v_cvt_pk_bf16_f32 v27, v166, v167\n\t"
    "buffer_store_dwordx4 v[24:27], v196, s[36:39], s87 offen offset:3072 sc1\n\t"
    "v_accvgpr_read_b32 v168, a184\n\t"
    "v_accvgpr_read_b32 v169, a185\n\t"
    "v_accvgpr_read_b32 v170, a186\n\t"
    "v_accvgpr_read_b32 v171, a187\n\t"
    "v_accvgpr_read_b32 v172, a188\n\t"
    "v_accvgpr_read_b32 v173, a189\n\t"
    "v_accvgpr_read_b32 v174, a190\n\t"
    "v_accvgpr_read_b32 v175, a191\n\t"
    "v_mul_f32 v168, v205, v168\n\t"
    "v_mul_f32 v169, v205, v169\n\t"
    "v_mul_f32 v170, v205, v170\n\t"
    "v_mul_f32 v171, v205, v171\n\t"
    "v_mul_f32 v172, v205, v172\n\t"
    "v_mul_f32 v173, v205, v173\n\t"
    "v_mul_f32 v174, v205, v174\n\t"
    "v_mul_f32 v175, v205, v175\n\t"
    "v_cvt_pk_bf16_f32 v28, v168, v169\n\t"
    "v_cvt_pk_bf16_f32 v29, v170, v171\n\t"
    "v_cvt_pk_bf16_f32 v30, v172, v173\n\t"
    "v_cvt_pk_bf16_f32 v31, v174, v175\n\t"
    "buffer_store_dwordx4 v[28:31], v196, s[36:39], s86 offen offset:3072 sc1\n\t"
    "s_waitcnt vmcnt(4) lgkmcnt(0)\n\t"
    "s_barrier\n\t"
    "s_mov_b32 exec_hi, 0\n\t"
    "L_chk1aA_%=:\n\t"
    "v_sub_u32 v138, v138, v211\n\t"
    "v_cmp_gt_u32 vcc, 1, v138\n\t"
    "s_cbranch_vccnz L_spin1aA_%=\n\t"
    "L_got1aA_%=:\n\t"
    "s_mov_b64 exec, -1\n\t"
    "s_add_u32 m0, s97, 81920\n\t"
    "s_add_u32 s53, s98, 983040\n\t"
    "buffer_load_dwordx4 v255, s[24:27], s53 offen offset:0 lds\n\t"
    "buffer_load_dwordx4 v198, s[24:27], s53 offen offset:2048 lds\n\t"
    "s_add_u32 m0, s97, 82816\n\t"
    "s_nop 0\n\t"
    "buffer_load_dwordx4 v255, s[24:27], s53 offen offset:128 lds\n\t"
    "buffer_load_dwordx4 v198, s[24:27], s53 offen offset:2176 lds\n\t"
    "s_add_u32 m0, s51, 0\n\t"
    "s_add_u32 s48, s48, 28672\n\t"
    "buffer_load_dwordx4 v196, s[20:23], s48 offen offset:0 sc1 lds\n\t"
    "buffer_load_dwordx4 v196, s[20:23], s48 offen offset:1024 sc1 lds\n\t"
    "buffer_load_dwordx4 v196, s[20:23], s48 offen offset:2048 sc1 lds\n\t"
    "buffer_load_dwordx4 v196, s[20:23], s48 offen offset:3072 sc1 lds\n\t"
    "s_add_u32 m0, s51, 4096\n\t"
    "s_add_u32 s48, s48, 4096\n\t"
    "buffer_load_dwordx4 v196, s[20:23], s48 offen offset:0 sc1 lds\n\t"
    "buffer_load_dwordx4 v196, s[20:23], s48 offen offset:1024 sc1 lds\n\t"
    "buffer_load_dwordx4 v196, s[20:23], s48 offen offset:2048 sc1 lds\n\t"
    "buffer_load_dwordx4 v196, s[20:23], s48 offen offset:3072 sc1 lds\n\t"
    "ds_read_b64_tr_b16 a[144:145], v132 offset:0\n\t"
    "ds_read_b64_tr_b16 a[146:147], v132 offset:4096\n\t"
    "ds_read_b64_tr_b16 a[148:149], v132 offset:8192\n\t"
    "ds_read_b64_tr_b16 a[150:151], v132 offset:12288\n\t"
    "ds_read_b64_tr_b16 a[152:153], v133 offset:0\n\t"
    "ds_read_b64_tr_b16 a[154:155], v133 offset:4096\n\t"
    "ds_read_b64_tr_b16 a[156:157], v133 offset:8192\n\t"
    "ds_read_b64_tr_b16 a[158:159], v133 offset:12288\n\t"
    "ds_read_b64_tr_b16 a[160:161], v132 offset:16384\n\t"
    "ds_read_b64_tr_b16 a[162:163], v132 offset:20480\n\t"
    "ds_read_b64_tr_b16 a[164:165], v132 offset:24576\n\t"
    "ds_read_b64_tr_b16 a[166:167], v132 offset:28672\n\t"
    "ds_read_b64_tr_b16 a[168:169], v133 offset:16384\n\t"
    "ds_read_b64_tr_b16 a[170:171], v133 offset:20480\n\t"
    "ds_read_b64_tr_b16 a[172:173], v133 offset:24576\n\t"
    "ds_read_b64_tr_b16 a[174:175], v133 offset:28672\n\t"
    "ds_read_b64_tr_b16 a[176:177], v130 offset:0\n\t"
    "ds_read_b64_tr_b16 a[178:179], v130 offset:4096\n\t"
    "ds_read_b64_tr_b16 a[180:181], v130 offset:8192\n\t"
    "ds_read_b64_tr_b16 a[182:183], v130 offset:12288\n\t"
    "ds_read_b64_tr_b16 a[184:185], v131 offset:0\n\t"
    "ds_read_b64_tr_b16 a[186:187], v131 offset:4096\n\t"
    "ds_read_b64_tr_b16 a[188:189], v131 offset:8192\n\t"
    "ds_read_b64_tr_b16 a[190:191], v131 offset:12288\n\t"
    "ds_read_b64_tr_b16 a[192:193], v128 offset:32768\n\t"
    "ds_read_b64_tr_b16 a[194:195], v128 offset:36864\n\t"
    "ds_read_b64_tr_b16 a[196:197], v128 offset:40960\n\t"
    "ds_read_b64_tr_b16 a[198:199], v128 offset:45056\n\t"
    "ds_read_b64_tr_b16 a[200:201], v129 offset:32768\n\t"
    "ds_read_b64_tr_b16 a[202:203], v129 offset:36864\n\t"
    "ds_read_b64_tr_b16 a[204:205], v129 offset:40960\n\t"
    "ds_read_b64_tr_b16 a[206:207], v129 offset:45056\n\t"
    "s_add_u32 m0, s51, 98304\n\t"
    "s_add_u32 s48, s48, 28672\n\t"
    "buffer_load_dwordx4 v196, s[20:23], s48 offen offset:0 sc1 lds\n\t"
    "buffer_load_dwordx4 v196, s[20:23], s48 offen offset:1024 sc1 lds\n\t"
    "buffer_load_dwordx4 v196, s[20:23], s48 offen offset:2048 sc1 lds\n\t"
    "buffer_load_dwordx4 v196, s[20:23], s48 offen offset:3072 sc1 lds\n\t"
    "s_add_u32 m0, s51, 102400\n\t"
    "s_add_u32 s48, s48, 4096\n\t"
    "buffer_load_dwordx4 v196, s[20:23], s48 offen offset:0 sc1 lds\n\t"
    "buffer_load_dwordx4 v196, s[20:23], s48 offen offset:1024 sc1 lds\n\t"
    "buffer_load_dwordx4 v196, s[20:23], s48 offen offset:2048 sc1 lds\n\t"
    "buffer_load_dwordx4 v196, s[20:23], s48 offen offset:3072 sc1 lds\n\t"
    "s_waitcnt vmcnt(8) lgkmcnt(0)\n\t"
    "s_barrier\n\t"
    "L_tail_%=:\n\t"
    "s_cmp_eq_u32 s45, 1\n\t"
    "s_cbranch_scc1 L_e1_%=\n\t"
    "ds_read_b128 a[0:3], v141 offset:0\n\t"
    "ds_read_b128 a[4:7], v141 offset:1024\n\t"
    "ds_read_b128 a[8:11], v141 offset:2048\n\t"
    "ds_read_b128 a[12:15], v141 offset:3072\n\t"
    "ds_read_b128 a[16:19], v141 offset:4096\n\t"
    "ds_read_b128 a[20:23], v141 offset:5120\n\t"
    "ds_read_b128 a[24:27], v141 offset:6144\n\t"
    "ds_read_b128 a[28:31], v141 offset:7168\n\t"
    "ds_read_b64_tr_b16 a[208:209], v130 offset:16384\n\t"
    "ds_read_b64_tr_b16 a[210:211], v130 offset:20480\n\t"
    "ds_read_b64_tr_b16 a[212:213], v130 offset:24576\n\t"
    "ds_read_b64_tr_b16 a[214:215], v130 offset:28672\n\t"
    "ds_read_b64_tr_b16 a[216:217], v131 offset:16384\n\t"
    "ds_read_b64_tr_b16 a[218:219], v131 offset:20480\n\t"
    "ds_read_b64_tr_b16 a[220:221], v131 offset:24576\n\t"
    "ds_read_b64_tr_b16 a[222:223], v131 offset:28672\n\t"
    "s_waitcnt lgkmcnt(8)\n\t"
    "v_mfma_f32_16x16x32_bf16 v[144:147], a[32:35], a[0:3], 0\n\t"
    "ds_read_b128 a[224:227], v141 offset:8192\n\t"
    "v_mfma_f32_16x16x32_bf16 v[144:147], a[36:39], a[4:7], v[144:147]\n\t"
    "ds_read_b128 a[228:231], v141 offset:9216\n\t"
    "v_mfma_f32_16x16x32_bf16 v[144:147], a[48:51], a[8:11], v[144:147]\n\t"
    "ds_read_b128 a[232:235], v141 offset:10240\n\t"
    "v_mfma_f32_16x16x32_bf16 v[144:147], a[52:55], a[12:15], v[144:147]\n\t"
    "ds_read_b128 a[236:239], v141 offset:11264\n\t"
    "v_mfma_f32_16x16x32_bf16 v[144:147], a[64:67], a[16:19], v[144:147]\n\t"
    "ds_read_b128 a[240:243], v141 offset:12288\n\t"
    "v_mfma_f32_16x16x32_bf16 v[144:147], a[68:71], a[20:23], v[144:147]\n\t"
    "ds_read_b128 a[244:247], v141 offset:13312\n\t"
    "v_mfma_f32_16x16x32_bf16 v[144:147], a[80:83], a[24:27], v[144:147]\n\t"
    "ds_read_b128 a[248:251], v141 offset:14336\n\t"
    "v_mfma_f32_16x16x32_bf16 v[144:147], a[84:87], a[28:31], v[144:147]\n\t"
    "ds_read_b128 a[252:255], v141 offset:15360\n\t"
    "s_waitcnt vmcnt(20)\n\t"
    "s_lshl_b32 s60, s96, 2\n\t"
    "s_add_u32 s60, s60, s40\n\t"
    "s_lshl_b32 exec_lo, 1, s60\n\t"
    "s_mov_b32 exec_hi, 0\n\t"
    "v_add_u32 v139, 2, v211\n\t"
    "buffer_store_dword v139, v137, s[12:15], 0 offen sc1\n\t"
    "s_mov_b64 exec, -1\n\t"
    "v_mfma_f32_16x16x32_bf16 v[148:151], a[92:95], a[28:31], 0\n\t"
    "s_add_u32 m0, s99, 0\n\t"
    "v_mfma_f32_16x16x32_bf16 v[148:151], a[88:91], a[24:27], v[148:151]\n\t"
    "s_add_u32 s48, s48, 28672\n\t"
    "v_mfma_f32_16x16x32_bf16 v[148:151], a[76:79], a[20:23], v[148:151]\n\t"
    "buffer_load_dwordx4 v196, s[20:23], s48 offen offset:0 sc1 lds\n\t"
    "v_mfma_f32_16x16x32_bf16 v[148:151], a[72:75], a[16:19], v[148:151]\n\t"
    "v_mfma_f32_16x16x32_bf16 v[148:151], a[60:63], a[12:15], v[148:151]\n\t"
    "buffer_load_dwordx4 v196, s[20:23], s48 offen offset:1024 sc1 lds\n\t"
    "v_mfma_f32_16x16x32_bf16 v[148:151], a[56:59], a[8:11], v[148:151]\n\t"
    "v_mfma_f32_16x16x32_bf16 v[148:151], a[44:47], a[4:7], v[148:151]\n\t"
    "buffer_load_dwordx4 v196, s[20:23], s48 offen offset:2048 sc1 lds\n\t"
    "v_mfma_f32_16x16x32_bf16 v[148:151], a[40:43], a[0:3], v[148:151]\n\t"
    "s_waitcnt lgkmcnt(0)\n\t"
    "v_mfma_f32_16x16x32_bf16 v[148:151], v[8:11], a[224:227], v[148:151]\n\t"
    "ds_read_b128 a[0:3], v141 offset:16384\n\t"
    "v_mfma_f32_16x16x32_bf16 v[148:151], v[12:15], a[228:231], v[148:151]\n\t"
    "ds_read_b128 a[4:7], v141 offset:17408\n\t"
    "v_mfma_f32_16x16x32_bf16 v[148:151], v[40:43], a[232:235], v[148:151]\n\t"
    "ds_read_b128 a[8:11], v141 offset:18432\n\t"
    "v_mfma_f32_16x16x32_bf16 v[148:151], v[44:47], a[236:239], v[148:151]\n\t"
    "ds_read_b128 a[12:15], v141 offset:19456\n\t"
    "v_mfma_f32_16x16x32_bf16 v[148:151], v[72:75], a[240:243], v[148:151]\n\t"
    "ds_read_b128 a[16:19], v141 offset:20480\n\t"
    "v_mfma_f32_16x16x32_bf16 v[148:151], v[76:79], a[244:247], v[148:151]\n\t"
    "ds_read_b128 a[20:23], v141 offset:21504\n\t"
    "v_mfma_f32_16x16x32_bf16 v[148:151], v[104:107], a[248:251], v[148:151]\n\t"
    "ds_read_b128 a[24:27], v141 offset:22528\n\t"
    "v_mfma_f32_16x16x32_bf16 v[148:151], v[108:111], a[252:255], v[148:151]\n\t"
    "ds_read_b128 a[28:31], v141 offset:23552\n\t"
    "v_mfma_f32_16x16x32_bf16 v[144:147], v[100:103], a[252:255], v[144:147]\n\t"
    "v_mfma_f32_16x16x32_bf16 v[144:147], v[96:99], a[248:251], v[144:147]\n\t"
    "buffer_load_dwordx4 v196, s[20:23], s48 offen offset:3072 sc1 lds\n\t"
    "v_mfma_f32_16x16x32_bf16 v[144:147], v[68:71], a[244:247], v[144:147]\n\t"
    "s_add_u32 m0, s99, 4096\n\t"
    "v_mfma_f32_16x16x32_bf16 v[144:147], v[64:67], a[240:243], v[144:147]\n\t"
    "v_mfma_f32_16x16x32_bf16 v[144:147], v[36:39], a[236:239], v[144:147]\n\t"
    "s_add_u32 s48, s48, 4096\n\t"
    "v_mfma_f32_16x16x32_bf16 v[144:147], v[32:35], a[232:235], v[144:147]\n\t"
    "buffer_load_dwordx4 v196, s[20:23], s48 offen offset:0 sc1 lds\n\t"
    "v_mfma_f32_16x16x32_bf16 v[144:147], v[4:7], a[228:231], v[144:147]\n\t"
    "buffer_load_dwordx4 v196, s[20:23], s48 offen offset:1024 sc1 lds\n\t"
    "v_mfma_f32_16x16x32_bf16 v[144:147], v[0:3], a[224:227], v[144:147]\n\t"
    "s_waitcnt lgkmcnt(0)\n\t"
    "v_mfma_f32_16x16x32_bf16 v[144:147], a[96:99], a[0:3], v[144:147]\n\t"
    "ds_read_b128 a[224:227], v141 offset:24576\n\t"
    "v_mfma_f32_16x16x32_bf16 v[144:147], a[100:103], a[4:7], v[144:147]\n\t"
    "ds_read_b128 a[228:231], v141 offset:25600\n\t"
    "v_mfma_f32_16x16x32_bf16 v[144:147], a[112:115], a[8:11], v[144:147]\n\t"
    "ds_read_b128 a[232:235], v141 offset:26624\n\t"
    "v_mfma_f32_16x16x32_bf16 v[144:147], a[116:119], a[12:15], v[144:147]\n\t"
    "ds_read_b128 a[236:239], v141 offset:27648\n\t"
    "v_mfma_f32_16x16x32_bf16 v[144:147], a[128:131], a[16:19], v[144:147]\n\t"
    "ds_read_b128 a[240:243], v141 offset:28672\n\t"
    "v_mfma_f32_16x16x32_bf16 v[144:147], a[132:135], a[20:23], v[144:147]\n\t"
    "ds_read_b128 a[244:247], v141 offset:29696\n\t"
    "v_mfma_f32_16x16x32_bf16 v[144:147], a[144:147], a[24:27], v[144:147]\n\t"
    "ds_read_b128 a[248:251], v141 offset:30720\n\t"
    "v_mfma_f32_16x16x32_bf16 v[144:147], a[148:151], a[28:31], v[144:147]\n\t"
    "ds_read_b128 a[252:255], v141 offset:31744\n\t"
    "v_mfma_f32_16x16x32_bf16 v[148:151], a[156:159], a[28:31], v[148:151]\n\t"
    "v_mfma_f32_16x16x32_bf16 v[148:151], a[152:155], a[24:27], v[148:151]\n\t"
    "buffer_load_dwordx4 v196, s[20:23], s48 offen offset:2048 sc1 lds\n\t"
    "v_mfma_f32_16x16x32_bf16 v[148:151], a[140:143], a[20:23], v[148:151]\n\t"
    "v_mfma_f32_16x16x32_bf16 v[148:151], a[136:139], a[16:19], v[148:151]\n\t"
    "buffer_load_dwordx4 v196, s[20:23], s48 offen offset:3072 sc1 lds\n\t"
    "v_mfma_f32_16x16x32_bf16 v[148:151], a[124:127], a[12:15], v[148:151]\n\t"
    "v_mfma_f32_16x16x32_bf16 v[148:151], a[120:123], a[8:11], v[148:151]\n\t"
    "v_mfma_f32_16x16x32_bf16 v[148:151], a[108:111], a[4:7], v[148:151]\n\t"
    "s_waitcnt vmcnt(9) lgkmcnt(0)\n\t"
    "v_mfma_f32_16x16x32_bf16 v[148:151], a[104:107], a[0:3], v[148:151]\n\t"
    "s_barrier\n\t"
    "v_mfma_f32_16x16x32_bf16 v[148:151], a[168:171], a[224:227], v[148:151]\n\t"
    "ds_read_b128 a[0:3], v142 offset:0\n\t"
    "v_mfma_f32_16x16x32_bf16 v[148:151], a[172:175], a[228:231], v[148:151]\n\t"
    "ds_read_b128 a[4:7], v142 offset:1024\n\t"
    "v_mfma_f32_16x16x32_bf16 v[148:151], a[184:187], a[232:235], v[148:151]\n\t"
    "ds_read_b128 a[8:11], v142 offset:2048\n\t"
    "v_mfma_f32_16x16x32_bf16 v[148:151], a[188:191], a[236:239], v[148:151]\n\t"
    "ds_read_b128 a[12:15], v142 offset:3072\n\t"
    "v_mfma_f32_16x16x32_bf16 v[148:151], a[200:203], a[240:243], v[148:151]\n\t"
    "ds_read_b128 a[16:19], v142 offset:4096\n\t"
    "v_mfma_f32_16x16x32_bf16 v[148:151], a[204:207], a[244:247], v[148:151]\n\t"
    "ds_read_b128 a[20:23], v142 offset:5120\n\t"
    "v_mfma_f32_16x16x32_bf16 v[148:151], a[216:219], a[248:251], v[148:151]\n\t"
    "ds_read_b128 a[24:27], v142 offset:6144\n\t"
    "v_mfma_f32_16x16x32_bf16 v[148:151], a[220:223], a[252:255], v[148:151]\n\t"
    "ds_read_b128 a[28:31], v142 offset:7168\n\t"
    "v_mfma_f32_16x16x32_bf16 v[144:147], a[212:215], a[252:255], v[144:147]\n\t"
    "v_mfma_f32_16x16x32_bf16 v[144:147], a[208:211], a[248:251], v[144:147]\n\t"
    "v_mfma_f32_16x16x32_bf16 v[144:147], a[196:199], a[244:247], v[144:147]\n\t"
    "v_mfma_f32_16x16x32_bf16 v[144:147], a[192:195], a[240:243], v[144:147]\n\t"
    "v_mfma_f32_16x16x32_bf16 v[144:147], a[180:183], a[236:239], v[144:147]\n\t"
    "v_mfma_f32_16x16x32_bf16 v[144:147], a[176:179], a[232:235], v[144:147]\n\t"
    "v_mfma_f32_16x16x32_bf16 v[144:147], a[164:167], a[228:231], v[144:147]\n\t"
    "v_mfma_f32_16x16x32_bf16 v[144:147], a[160:163], a[224:227], v[144:147]\n\t"
    "s_waitcnt lgkmcnt(0)\n\t"
    "v_mfma_f32_16x16x32_bf16 v[152:155], a[32:35], a[0:3], 0\n\t"
    "s_lshl_b32 s9, s47, 2\n\t"
    "ds_read_b128 a[224:227], v142 offset:8192\n\t"
    "v_cvt_pk_bf16_f32 v194, v148, v149\n\t"
    "v_mfma_f32_16x16x32_bf16 v[152:155], a[36:39], a[4:7], v[152:155]\n\t"
    "s_add_u32 s9, s9, s95\n\t"
    "ds_read_b128 a[228:231], v142 offset:9216\n\t"
    "v_cvt_pk_bf16_f32 v195, v150, v151\n\t"
    "v_mfma_f32_16x16x32_bf16 v[152:155], a[48:51], a[8:11], v[152:155]\n\t"
    "s_mul_i32 s9, s9, 4096\n\t"
    "ds_read_b128 a[232:235], v142 offset:10240\n\t"
    "v_mfma_f32_16x16x32_bf16 v[152:155], a[52:55], a[12:15], v[152:155]\n\t"
    "s_add_u32 s47, s47, 1\n\t"
    "ds_read_b128 a[236:239], v142 offset:11264\n\t"
    "v_mfma_f32_16x16x32_bf16 v[152:155], a[64:67], a[16:19], v[152:155]\n\t"
    "s_cmp_eq_u32 s47, s46\n\t"
    "ds_read_b128 a[240:243], v142 offset:12288\n\t"
    "v_cvt_pk_bf16_f32 v192, v144, v145\n\t"
    "v_mfma_f32_16x16x32_bf16 v[152:155], a[68:71], a[20:23], v[152:155]\n\t"
    "s_cmov_b32 s47, 0\n\t"
    "ds_read_b128 a[244:247], v142 offset:13312\n\t"
    "v_cvt_pk_bf16_f32 v193, v146, v147\n\t"
    "v_mfma_f32_16x16x32_bf16 v[152:155], a[80:83], a[24:27], v[152:155]\n\t"
    "ds_read_b128 a[248:251], v142 offset:14336\n\t"
    "v_mfma_f32_16x16x32_bf16 v[152:155], a[84:87], a[28:31], v[152:155]\n\t"
    "buffer_store_dwordx4 v[192:195], v196, s[68:71], s9 offen sc1\n\t"
    "s_mov_b32 exec_hi, 0\n\t"
    "buffer_load_dword v138, v137, s[12:15], 0 offen sc1\n\t"
    "s_mov_b64 exec, -1\n\t"
    "v_mfma_f32_16x16x32_bf16 v[156:159], a[92:95], a[28:31], 0\n\t"
    "s_add_u32 m0, s90, 0\n\t"
    "ds_read_b128 a[252:255], v142 offset:15360\n\t"
    "v_mfma_f32_16x16x32_bf16 v[156:159], a[88:91], a[24:27], v[156:159]\n\t"
    "s_add_u32 s48, s48, 28672\n\t"
    "v_mfma_f32_16x16x32_bf16 v[156:159], a[76:79], a[20:23], v[156:159]\n\t"
    "buffer_load_dwordx4 v196, s[20:23], s48 offen offset:0 sc1 lds\n\t"
    "v_mfma_f32_16x16x32_bf16 v[156:159], a[72:75], a[16:19], v[156:159]\n\t"
    "v_mfma_f32_16x16x32_bf16 v[156:159], a[60:63], a[12:15], v[156:159]\n\t"
    "buffer_load_dwordx4 v196, s[20:23], s48 offen offset:1024 sc1 lds\n\t"
    "v_mfma_f32_16x16x32_bf16 v[156:159], a[56:59], a[8:11], v[156:159]\n\t"
    "v_mfma_f32_16x16x32_bf16 v[156:159], a[44:47], a[4:7], v[156:159]\n\t"
    "buffer_load_dwordx4 v196, s[20:23], s48 offen offset:2048 sc1 lds\n\t"
    "v_mfma_f32_16x16x32_bf16 v[156:159], a[40:43], a[0:3], v[156:159]\n\t"
    "s_waitcnt lgkmcnt(0)\n\t"
    "v_mfma_f32_16x16x32_bf16 v[156:159], v[8:11], a[224:227], v[156:159]\n\t"
    "ds_read_b128 a[0:3], v142 offset:16384\n\t"
    "v_mfma_f32_16x16x32_bf16 v[156:159], v[12:15], a[228:231], v[156:159]\n\t"
    "ds_read_b128 a[4:7], v142 offset:17408\n\t"
    "v_mfma_f32_16x16x32_bf16 v[156:159], v[40:43], a[232:235], v[156:159]\n\t"
    "ds_read_b128 a[8:11], v142 offset:18432\n\t"
    "v_mfma_f32_16x16x32_bf16 v[156:159], v[44:47], a[236:239], v[156:159]\n\t"
    "ds_read_b128 a[12:15], v142 offset:19456\n\t"
    "v_mfma_f32_16x16x32_bf16 v[156:159], v[72:75], a[240:243], v[156:159]\n\t"
    "ds_read_b128 a[16:19], v142 offset:20480\n\t"
    "v_mfma_f32_16x16x32_bf16 v[156:159], v[76:79], a[244:247], v[156:159]\n\t"
    "ds_read_b128 a[20:23], v142 offset:21504\n\t"
    "v_mfma_f32_16x16x32_bf16 v[156:159], v[104:107], a[248:251], v[156:159]\n\t"
    "ds_read_b128 a[24:27], v142 offset:22528\n\t"
    "v_mfma_f32_16x16x32_bf16 v[156:159], v[108:111], a[252:255], v[156:159]\n\t"
    "ds_read_b128 a[28:31], v142 offset:23552\n\t"
    "v_mfma_f32_16x16x32_bf16 v[152:155], v[100:103], a[252:255], v[152:155]\n\t"
    "v_mfma_f32_16x16x32_bf16 v[152:155], v[96:99], a[248:251], v[152:155]\n\t"
    "buffer_load_dwordx4 v196, s[20:23], s48 offen offset:3072 sc1 lds\n\t"
    "v_mfma_f32_16x16x32_bf16 v[152:155], v[68:71], a[244:247], v[152:155]\n\t"
    "s_add_u32 m0, s90, 4096\n\t"
    "v_mfma_f32_16x16x32_bf16 v[152:155], v[64:67], a[240:243], v[152:155]\n\t"
    "v_mfma_f32_16x16x32_bf16 v[152:155], v[36:39], a[236:239], v[152:155]\n\t"
    "s_add_u32 s48, s48, 4096\n\t"
    "v_mfma_f32_16x16x32_bf16 v[152:155], v[32:35], a[232:235], v[152:155]\n\t"
    "buffer_load_dwordx4 v196, s[20:23], s48 offen offset:0 sc1 lds\n\t"
    "v_mfma_f32_16x16x32_bf16 v[152:155], v[4:7], a[228:231], v[152:155]\n\t"
    "buffer_load_dwordx4 v196, s[20:23], s48 offen offset:1024 sc1 lds\n\t"
    "v_mfma_f32_16x16x32_bf16 v[152:155], v[0:3], a[224:227], v[152:155]\n\t"
    "s_waitcnt lgkmcnt(0)\n\t"
    "v_mfma_f32_16x16x32_bf16 v[152:155], a[96:99], a[0:3], v[152:155]\n\t"
    "ds_read_b128 a[224:227], v142 offset:24576\n\t"
    "v_mfma_f32_16x16x32_bf16 v[152:155], a[100:103], a[4:7], v[152:155]\n\t"
    "ds_read_b128 a[228:231], v142 offset:25600\n\t"
    "v_mfma_f32_16x16x32_bf16 v[152:155], a[112:115], a[8:11], v[152:155]\n\t"
    "ds_read_b128 a[232:235], v142 offset:26624\n\t"
    "v_mfma_f32_16x16x32_bf16 v[152:155], a[116:119], a[12:15], v[152:155]\n\t"
    "ds_read_b128 a[236:239], v142 offset:27648\n\t"
    "v_mfma_f32_16x16x32_bf16 v[152:155], a[128:131], a[16:19], v[152:155]\n\t"
    "ds_read_b128 a[240:243], v142 offset:28672\n\t"
    "v_mfma_f32_16x16x32_bf16 v[152:155], a[132:135], a[20:23], v[152:155]\n\t"
    "ds_read_b128 a[244:247], v142 offset:29696\n\t"
    "v_mfma_f32_16x16x32_bf16 v[152:155], a[144:147], a[24:27], v[152:155]\n\t"
    "ds_read_b128 a[248:251], v142 offset:30720\n\t"
    "v_mfma_f32_16x16x32_bf16 v[152:155], a[148:151], a[28:31], v[152:155]\n\t"
    "ds_read_b128 a[252:255], v142 offset:31744\n\t"
    "v_mfma_f32_16x16x32_bf16 v[156:159], a[156:159], a[28:31], v[156:159]\n\t"
    "v_mfma_f32_16x16x32_bf16 v[156:159], a[152:155], a[24:27], v[156:159]\n\t"
    "buffer_load_dwordx4 v196, s[20:23], s48 offen offset:2048 sc1 lds\n\t"
    "v_mfma_f32_16x16x32_bf16 v[156:159], a[140:143], a[20:23], v[156:159]\n\t"
    "v_mfma_f32_16x16x32_bf16 v[156:159], a[136:139], a[16:19], v[156:159]\n\t"
    "buffer_load_dwordx4 v196, s[20:23], s48 offen offset:3072 sc1 lds\n\t"
    "v_mfma_f32_16x16x32_bf16 v[156:159], a[124:127], a[12:15], v[156:159]\n\t"
    "v_mfma_f32_16x16x32_bf16 v[156:159], a[120:123], a[8:11], v[156:159]\n\t"
    "v_mfma_f32_16x16x32_bf16 v[156:159], a[108:111], a[4:7], v[156:159]\n\t"
    "s_waitcnt vmcnt(10) lgkmcnt(0)\n\t"
    "v_mfma_f32_16x16x32_bf16 v[156:159], a[104:107], a[0:3], v[156:159]\n\t"
    "s_barrier\n\t"
    "v_mfma_f32_16x16x32_bf16 v[156:159], a[168:171], a[224:227], v[156:159]\n\t"
    "ds_read_b128 a[0:3], v143 offset:0\n\t"
    "v_mfma_f32_16x16x32_bf16 v[156:159], a[172:175], a[228:231], v[156:159]\n\t"
    "ds_read_b128 a[4:7], v143 offset:1024\n\t"
    "v_mfma_f32_16x16x32_bf16 v[156:159], a[184:187], a[232:235], v[156:159]\n\t"
    "ds_read_b128 a[8:11], v143 offset:2048\n\t"
    "v_mfma_f32_16x16x32_bf16 v[156:159], a[188:191], a[236:239], v[156:159]\n\t"
    "ds_read_b128 a[12:15], v143 offset:3072\n\t"
    "v_mfma_f32_16x16x32_bf16 v[156:159], a[200:203], a[240:243], v[156:159]\n\t"
    "ds_read_b128 a[16:19], v143 offset:4096\n\t"
    "v_mfma_f32_16x16x32_bf16 v[156:159], a[204:207], a[244:247], v[156:159]\n\t"
    "ds_read_b128 a[20:23], v143 offset:5120\n\t"
    "v_mfma_f32_16x16x32_bf16 v[156:159], a[216:219], a[248:251], v[156:159]\n\t"
    "ds_read_b128 a[24:27], v143 offset:6144\n\t"
    "v_mfma_f32_16x16x32_bf16 v[156:159], a[220:223], a[252:255], v[156:159]\n\t"
    "ds_read_b128 a[28:31], v143 offset:7168\n\t"
    "v_mfma_f32_16x16x32_bf16 v[152:155], a[212:215], a[252:255], v[152:155]\n\t"
    "v_mfma_f32_16x16x32_bf16 v[152:155], a[208:211], a[248:251], v[152:155]\n\t"
    "v_mfma_f32_16x16x32_bf16 v[152:155], a[196:199], a[244:247], v[152:155]\n\t"
    "v_mfma_f32_16x16x32_bf16 v[152:155], a[192:195], a[240:243], v[152:155]\n\t"
    "v_mfma_f32_16x16x32_bf16 v[152:155], a[180:183], a[236:239], v[152:155]\n\t"
    "v_mfma_f32_16x16x32_bf16 v[152:155], a[176:179], a[232:235], v[152:155]\n\t"
    "v_mfma_f32_16x16x32_bf16 v[152:155], a[164:167], a[228:231], v[152:155]\n\t"
    "v_mfma_f32_16x16x32_bf16 v[152:155], a[160:163], a[224:227], v[152:155]\n\t"
    "s_waitcnt lgkmcnt(0)\n\t"
    "s_waitcnt vmcnt(8)\n\t"
    "s_mov_b32 exec_hi, 0\n\t"
    "L_chk1b0_%=:\n\t"
    "v_sub_u32 v138, v138, v211\n\t"
    "v_cmp_gt_u32 vcc, 2, v138\n\t"
    "s_cbranch_vccnz L_spin1b0_%=\n\t"
    "L_got1b0_%=:\n\t"
    "s_mov_b64 exec, -1\n\t"
    "v_mfma_f32_16x16x32_bf16 v[144:147], a[32:35], a[0:3], 0\n\t"
    "s_lshl_b32 s9, s47, 2\n\t"
    "ds_read_b128 a[224:227], v143 offset:8192\n\t"
    "v_cvt_pk_bf16_f32 v194, v156, v157\n\t"
    "v_mfma_f32_16x16x32_bf16 v[144:147], a[36:39], a[4:7], v[144:147]\n\t"
    "s_add_u32 s9, s9, s95\n\t"
    "ds_read_b128 a[228:231], v143 offset:9216\n\t"
    "v_cvt_pk_bf16_f32 v195, v158, v159\n\t"
    "v_mfma_f32_16x16x32_bf16 v[144:147], a[48:51], a[8:11], v[144:147]\n\t"
    "s_mul_i32 s9, s9, 4096\n\t"
    "ds_read_b128 a[232:235], v143 offset:10240\n\t"
    "v_mfma_f32_16x16x32_bf16 v[144:147], a[52:55], a[12:15], v[144:147]\n\t"
    "s_add_u32 s47, s47, 1\n\t"
    "ds_read_b128 a[236:239], v143 offset:11264\n\t"
    "v_mfma_f32_16x16x32_bf16 v[144:147], a[64:67], a[16:19], v[144:147]\n\t"
    "s_cmp_eq_u32 s47, s46\n\t"
    "ds_read_b128 a[240:243], v143 offset:12288\n\t"
    "v_cvt_pk_bf16_f32 v192, v152, v153\n\t"
    "v_mfma_f32_16x16x32_bf16 v[144:147], a[68:71], a[20:23], v[144:147]\n\t"
    "s_cmov_b32 s47, 0\n\t"
    "ds_read_b128 a[244:247], v143 offset:13312\n\t"
    "v_cvt_pk_bf16_f32 v193, v154, v155\n\t"
    "v_mfma_f32_16x16x32_bf16 v[144:147], a[80:83], a[24:27], v[144:147]\n\t"
    "ds_read_b128 a[248:251], v143 offset:14336\n\t"
    "v_mfma_f32_16x16x32_bf16 v[144:147], a[84:87], a[28:31], v[144:147]\n\t"
    "buffer_store_dwordx4 v[192:195], v196, s[68:71], s9 offen sc1\n\t"
    "v_mfma_f32_16x16x32_bf16 v[148:151], a[92:95], a[28:31], 0\n\t"
    "s_add_u32 m0, s91, 0\n\t"
    "ds_read_b128 a[252:255], v143 offset:15360\n\t"
    "v_mfma_f32_16x16x32_bf16 v[148:151], a[88:91], a[24:27], v[148:151]\n\t"
    "s_add_u32 s48, s48, 28672\n\t"
    "v_mfma_f32_16x16x32_bf16 v[148:151], a[76:79], a[20:23], v[148:151]\n\t"
    "buffer_load_dwordx4 v196, s[20:23], s48 offen offset:0 sc1 lds\n\t"
    "v_mfma_f32_16x16x32_bf16 v[148:151], a[72:75], a[16:19], v[148:151]\n\t"
    "v_mfma_f32_16x16x32_bf16 v[148:151], a[60:63], a[12:15], v[148:151]\n\t"
    "buffer_load_dwordx4 v196, s[20:23], s48 offen offset:1024 sc1 lds\n\t"
    "v_mfma_f32_16x16x32_bf16 v[148:151], a[56:59], a[8:11], v[148:151]\n\t"
    "v_mfma_f32_16x16x32_bf16 v[148:151], a[44:47], a[4:7], v[148:151]\n\t"
    "buffer_load_dwordx4 v196, s[20:23], s48 offen offset:2048 sc1 lds\n\t"
    "v_mfma_f32_16x16x32_bf16 v[148:151], a[40:43], a[0:3], v[148:151]\n\t"
    "s_waitcnt lgkmcnt(0)\n\t"
    "v_mfma_f32_16x16x32_bf16 v[148:151], v[8:11], a[224:227], v[148:151]\n\t"
    "ds_read_b128 a[0:3], v143 offset:16384\n\t"
    "v_mfma_f32_16x16x32_bf16 v[148:151], v[12:15], a[228:231], v[148:151]\n\t"
    "ds_read_b128 a[4:7], v143 offset:17408\n\t"
    "v_mfma_f32_16x16x32_bf16 v[148:151], v[40:43], a[232:235], v[148:151]\n\t"
    "ds_read_b128 a[8:11], v143 offset:18432\n\t"
    "v_mfma_f32_16x16x32_bf16 v[148:151], v[44:47], a[236:239], v[148:151]\n\t"
    "ds_read_b128 a[12:15], v143 offset:19456\n\t"
    "v_mfma_f32_16x16x32_bf16 v[148:151], v[72:75], a[240:243], v[148:151]\n\t"
    "ds_read_b128 a[16:19], v143 offset:20480\n\t"
    "v_mfma_f32_16x16x32_bf16 v[148:151], v[76:79], a[244:247], v[148:151]\n\t"
    "ds_read_b128 a[20:23], v143 offset:21504\n\t"
    "v_mfma_f32_16x16x32_bf16 v[148:151], v[104:107], a[248:251], v[148:151]\n\t"
    "ds_read_b128 a[24:27], v143 offset:22528\n\t"
    "v_mfma_f32_16x16x32_bf16 v[148:151], v[108:111], a[252:255], v[148:151]\n\t"
    "ds_read_b128 a[28:31], v143 offset:23552\n\t"
    "v_mfma_f32_16x16x32_bf16 v[144:147], v[100:103], a[252:255], v[144:147]\n\t"
    "v_mfma_f32_16x16x32_bf16 v[144:147], v[96:99], a[248:251], v[144:147]\n\t"
    "buffer_load_dwordx4 v196, s[20:23], s48 offen offset:3072 sc1 lds\n\t"
    "v_mfma_f32_16x16x32_bf16 v[144:147], v[68:71], a[244:247], v[144:147]\n\t"
    "s_add_u32 m0, s91, 4096\n\t"
    "v_mfma_f32_16x16x32_bf16 v[144:147], v[64:67], a[240:243], v[144:147]\n\t"
    "v_mfma_f32_16x16x32_bf16 v[144:147], v[36:39], a[236:239], v[144:147]\n\t"
    "s_add_u32 s48, s48, 4096\n\t"
    "v_mfma_f32_16x16x32_bf16 v[144:147], v[32:35], a[232:235], v[144:147]\n\t"
    "buffer_load_dwordx4 v196, s[20:23], s48 offen offset:0 sc1 lds\n\t"
    "v_mfma_f32_16x16x32_bf16 v[144:147], v[4:7], a[228:231], v[144:147]\n\t"
    "buffer_load_dwordx4 v196, s[20:23], s48 offen offset:1024 sc1 lds\n\t"
    "v_mfma_f32_16x16x32_bf16 v[144:147], v[0:3], a[224:227], v[144:147]\n\t"
    "s_waitcnt lgkmcnt(0)\n\t"
    "v_mfma_f32_16x16x32_bf16 v[144:147], a[96:99], a[0:3], v[144:147]\n\t"
    "ds_read_b128 a[224:227], v143 offset:24576\n\t"
    "v_mfma_f32_16x16x32_bf16 v[144:147], a[100:103], a[4:7], v[144:147]\n\t"
    "ds_read_b128 a[228:231], v143 offset:25600\n\t"
    "v_mfma_f32_16x16x32_bf16 v[144:147], a[112:115], a[8:11], v[144:147]\n\t"
    "ds_read_b128 a[232:235], v143 offset:26624\n\t"
    "v_mfma_f32_16x16x32_bf16 v[144:147], a[116:119], a[12:15], v[144:147]\n\t"
    "ds_read_b128 a[236:239], v143 offset:27648\n\t"
    "v_mfma_f32_16x16x32_bf16 v[144:147], a[128:131], a[16:19], v[144:147]\n\t"
    "ds_read_b128 a[240:243], v143 offset:28672\n\t"
    "v_mfma_f32_16x16x32_bf16 v[144:147], a[132:135], a[20:23], v[144:147]\n\t"
    "ds_read_b128 a[244:247], v143 offset:29696\n\t"
    "v_mfma_f32_16x16x32_bf16 v[144:147], a[144:147], a[24:27], v[144:147]\n\t"
    "ds_read_b128 a[248:251], v143 offset:30720\n\t"
    "v_mfma_f32_16x16x32_bf16 v[144:147], a[148:151], a[28:31], v[144:147]\n\t"
    "ds_read_b128 a[252:255], v143 offset:31744\n\t"
    "v_mfma_f32_16x16x32_bf16 v[148:151], a[156:159], a[28:31], v[148:151]\n\t"
    "v_mfma_f32_16x16x32_bf16 v[148:151], a[152:155], a[24:27], v[148:151]\n\t"
    "buffer_load_dwordx4 v196, s[20:23], s48 offen offset:2048 sc1 lds\n\t"
    "v_mfma_f32_16x16x32_bf16 v[148:151], a[140:143], a[20:23], v[148:151]\n\t"
    "v_mfma_f32_16x16x32_bf16 v[148:151], a[136:139], a[16:19], v[148:151]\n\t"
    "buffer_load_dwordx4 v196, s[20:23], s48 offen offset:3072 sc1 lds\n\t"
    "v_mfma_f32_16x16x32_bf16 v[148:151], a[124:127], a[12:15], v[148:151]\n\t"
    "v_mfma_f32_16x16x32_bf16 v[148:151], a[120:123], a[8:11], v[148:151]\n\t"
    "v_mfma_f32_16x16x32_bf16 v[148:151], a[108:111], a[4:7], v[148:151]\n\t"
    "s_waitcnt vmcnt(9) lgkmcnt(0)\n\t"
    "v_mfma_f32_16x16x32_bf16 v[148:151], a[104:107], a[0:3], v[148:151]\n\t"
    "s_barrier\n\t"
    "v_mfma_f32_16x16x32_bf16 v[148:151], a[168:171], a[224:227], v[148:151]\n\t"
    "ds_read_b128 a[0:3], v141 offset:0\n\t"
    "v_mfma_f32_16x16x32_bf16 v[148:151], a[172:175], a[228:231], v[148:151]\n\t"
    "ds_read_b128 a[4:7], v141 offset:1024\n\t"
    "v_mfma_f32_16x16x32_bf16 v[148:151], a[184:187], a[232:235], v[148:151]\n\t"
    "ds_read_b128 a[8:11], v141 offset:2048\n\t"
    "v_mfma_f32_16x16x32_bf16 v[148:151], a[188:191], a[236:239], v[148:151]\n\t"
    "ds_read_b128 a[12:15], v141 offset:3072\n\t"
    "v_mfma_f32_16x16x32_bf16 v[148:151], a[200:203], a[240:243], v[148:151]\n\t"
    "ds_read_b128 a[16:19], v141 offset:4096\n\t"
    "v_mfma_f32_16x16x32_bf16 v[148:151], a[204:207], a[244:247], v[148:151]\n\t"
    "ds_read_b128 a[20:23], v141 offset:5120\n\t"
    "v_mfma_f32_16x16x32_bf16 v[148:151], a[216:219], a[248:251], v[148:151]\n\t"
    "ds_read_b128 a[24:27], v141 offset:6144\n\t"
    "v_mfma_f32_16x16x32_bf16 v[148:151], a[220:223], a[252:255], v[148:151]\n\t"
    "ds_read_b128 a[28:31], v141 offset:7168\n\t"
    "v_mfma_f32_16x16x32_bf16 v[144:147], a[212:215], a[252:255], v[144:147]\n\t"
    "v_mfma_f32_16x16x32_bf16 v[144:147], a[208:211], a[248:251], v[144:147]\n\t"
    "v_mfma_f32_16x16x32_bf16 v[144:147], a[196:199], a[244:247], v[144:147]\n\t"
    "v_mfma_f32_16x16x32_bf16 v[144:147], a[192:195], a[240:243], v[144:147]\n\t"
    "v_mfma_f32_16x16x32_bf16 v[144:147], a[180:183], a[236:239], v[144:147]\n\t"
    "v_mfma_f32_16x16x32_bf16 v[144:147], a[176:179], a[232:235], v[144:147]\n\t"
    "v_mfma_f32_16x16x32_bf16 v[144:147], a[164:167], a[228:231], v[144:147]\n\t"
    "v_mfma_f32_16x16x32_bf16 v[144:147], a[160:163], a[224:227], v[144:147]\n\t"
    "s_waitcnt lgkmcnt(0)\n\t"
    "L_rest_%=:\n\t"
    "v_mfma_f32_16x16x32_bf16 v[152:155], a[32:35], a[0:3], 0\n\t"
    "s_lshl_b32 s9, s47, 2\n\t"
    "ds_read_b128 a[224:227], v141 offset:8192\n\t"
    "v_cvt_pk_bf16_f32 v194, v148, v149\n\t"
    "v_mfma_f32_16x16x32_bf16 v[152:155], a[36:39], a[4:7], v[152:155]\n\t"
    "s_add_u32 s9, s9, s95\n\t"
    "ds_read_b128 a[228:231], v141 offset:9216\n\t"
    "v_cvt_pk_bf16_f32 v195, v150, v151\n\t"
    "v_mfma_f32_16x16x32_bf16 v[152:155], a[48:51], a[8:11], v[152:155]\n\t"
    "s_mul_i32 s9, s9, 4096\n\t"
    "ds_read_b128 a[232:235], v141 offset:10240\n\t"
    "v_mfma_f32_16x16x32_bf16 v[152:155], a[52:55], a[12:15], v[152:155]\n\t"
    "s_add_u32 s47, s47, 1\n\t"
    "ds_read_b128 a[236:239], v141 offset:11264\n\t"
    "v_mfma_f32_16x16x32_bf16 v[152:155], a[64:67], a[16:19], v[152:155]\n\t"
    "s_cmp_eq_u32 s47, s46\n\t"
    "ds_read_b128 a[240:243], v141 offset:12288\n\t"
    "v_cvt_pk_bf16_f32 v192, v144, v145\n\t"
    "v_mfma_f32_16x16x32_bf16 v[152:155], a[68:71], a[20:23], v[152:155]\n\t"
    "s_cmov_b32 s47, 0\n\t"
    "ds_read_b128 a[244:247], v141 offset:13312\n\t"
    "v_cvt_pk_bf16_f32 v193, v146, v147\n\t"
    "v_mfma_f32_16x16x32_bf16 v[152:155], a[80:83], a[24:27], v[152:155]\n\t"
    "ds_read_b128 a[248:251], v141 offset:14336\n\t"
    "v_mfma_f32_16x16x32_bf16 v[152:155], a[84:87], a[28:31], v[152:155]\n\t"
    "buffer_store_dwordx4 v[192:195], v196, s[68:71], s9 offen sc1\n\t"
    "v_mfma_f32_16x16x32_bf16 v[156:159], a[92:95], a[28:31], 0\n\t"
    "s_add_u32 m0, s99, 0\n\t"
    "ds_read_b128 a[252:255], v141 offset:15360\n\t"
    "v_mfma_f32_16x16x32_bf16 v[156:159], a[88:91], a[24:27], v[156:159]\n\t"
    "s_add_u32 s48, s48, 28672\n\t"
    "v_mfma_f32_16x16x32_bf16 v[156:159], a[76:79], a[20:23], v[156:159]\n\t"
    "buffer_load_dwordx4 v196, s[20:23], s48 offen offset:0 sc1 lds\n\t"
    "v_mfma_f32_16x16x32_bf16 v[156:159], a[72:75], a[16:19], v[156:159]\n\t"
    "v_mfma_f32_16x16x32_bf16 v[156:159], a[60:63], a[12:15], v[156:159]\n\t"
    "buffer_load_dwordx4 v196, s[20:23], s48 offen offset:1024 sc1 lds\n\t"
    "v_mfma_f32_16x16x32_bf16 v[156:159], a[56:59], a[8:11], v[156:159]\n\t"
    "v_mfma_f32_16x16x32_bf16 v[156:159], a[44:47], a[4:7], v[156:159]\n\t"
    "buffer_load_dwordx4 v196, s[20:23], s48 offen offset:2048 sc1 lds\n\t"
    "v_mfma_f32_16x16x32_bf16 v[156:159], a[40:43], a[0:3], v[156:159]\n\t"
    "s_waitcnt lgkmcnt(0)\n\t"
    "v_mfma_f32_16x16x32_bf16 v[156:159], v[8:11], a[224:227], v[156:159]\n\t"
    "ds_read_b128 a[0:3], v141 offset:16384\n\t"
    "v_mfma_f32_16x16x32_bf16 v[156:159], v[12:15], a[228:231], v[156:159]\n\t"
    "ds_read_b128 a[4:7], v141 offset:17408\n\t"
    "v_mfma_f32_16x16x32_bf16 v[156:159], v[40:43], a[232:235], v[156:159]\n\t"
    "ds_read_b128 a[8:11], v141 offset:18432\n\t"
    "v_mfma_f32_16x16x32_bf16 v[156:159], v[44:47], a[236:239], v[156:159]\n\t"
    "ds_read_b128 a[12:15], v141 offset:19456\n\t"
    "v_mfma_f32_16x16x32_bf16 v[156:159], v[72:75], a[240:243], v[156:159]\n\t"
    "ds_read_b128 a[16:19], v141 offset:20480\n\t"
    "v_mfma_f32_16x16x32_bf16 v[156:159], v[76:79], a[244:247], v[156:159]\n\t"
    "ds_read_b128 a[20:23], v141 offset:21504\n\t"
    "v_mfma_f32_16x16x32_bf16 v[156:159], v[104:107], a[248:251], v[156:159]\n\t"
    "ds_read_b128 a[24:27], v141 offset:22528\n\t"
    "v_mfma_f32_16x16x32_bf16 v[156:159], v[108:111], a[252:255], v[156:159]\n\t"
    "ds_read_b128 a[28:31], v141 offset:23552\n\t"
    "v_mfma_f32_16x16x32_bf16 v[152:155], v[100:103], a[252:255], v[152:155]\n\t"
    "v_mfma_f32_16x16x32_bf16 v[152:155], v[96:99], a[248:251], v[152:155]\n\t"
    "buffer_load_dwordx4 v196, s[20:23], s48 offen offset:3072 sc1 lds\n\t"
    "v_mfma_f32_16x16x32_bf16 v[152:155], v[68:71], a[244:247], v[152:155]\n\t"
    "s_add_u32 m0, s99, 4096\n\t"
    "v_mfma_f32_16x16x32_bf16 v[152:155], v[64:67], a[240:243], v[152:155]\n\t"
    "v_mfma_f32_16x16x32_bf16 v[152:155], v[36:39], a[236:239], v[152:155]\n\t"
    "s_add_u32 s48, s48, 4096\n\t"
    "v_mfma_f32_16x16x32_bf16 v[152:155], v[32:35], a[232:235], v[152:155]\n\t"
    "buffer_load_dwordx4 v196, s[20:23], s48 offen offset:0 sc1 lds\n\t"
    "v_mfma_f32_16x16x32_bf16 v[152:155], v[4:7], a[228:231], v[152:155]\n\t"
    "buffer_load_dwordx4 v196, s[20:23], s48 offen offset:1024 sc1 lds\n\t"
    "v_mfma_f32_16x16x32_bf16 v[152:155], v[0:3], a[224:227], v[152:155]\n\t"
    "s_waitcnt lgkmcnt(0)\n\t"
    "v_mfma_f32_16x16x32_bf16 v[152:155], a[96:99], a[0:3], v[152:155]\n\t"
    "ds_read_b128 a[224:227], v141 offset:24576\n\t"
    "v_mfma_f32_16x16x32_bf16 v[152:155], a[100:103], a[4:7], v[152:155]\n\t"
    "ds_read_b128 a[228:231], v141 offset:25600\n\t"
    "v_mfma_f32_16x16x32_bf16 v[152:155], a[112:115], a[8:11], v[152:155]\n\t"
    "ds_read_b128 a[232:235], v141 offset:26624\n\t"
    "v_mfma_f32_16x16x32_bf16 v[152:155], a[116:119], a[12:15], v[152:155]\n\t"
    "ds_read_b128 a[236:239], v141 offset:27648\n\t"
    "v_mfma_f32_16x16x32_bf16 v[152:155], a[128:131], a[16:19], v[152:155]\n\t"
    "ds_read_b128 a[240:243], v141 offset:28672\n\t"
    "v_mfma_f32_16x16x32_bf16 v[152:155], a[132:135], a[20:23], v[152:155]\n\t"
    "ds_read_b128 a[244:247], v141 offset:29696\n\t"
    "v_mfma_f32_16x16x32_bf16 v[152:155], a[144:147], a[24:27], v[152:155]\n\t"
    "ds_read_b128 a[248:251], v141 offset:30720\n\t"
    "v_mfma_f32_16x16x32_bf16 v[152:155], a[148:151], a[28:31], v[152:155]\n\t"
    "ds_read_b128 a[252:255], v141 offset:31744\n\t"
    "v_mfma_f32_16x16x32_bf16 v[156:159], a[156:159], a[28:31], v[156:159]\n\t"
    "v_mfma_f32_16x16x32_bf16 v[156:159], a[152:155], a[24:27], v[156:159]\n\t"
    "buffer_load_dwordx4 v196, s[20:23], s48 offen offset:2048 sc1 lds\n\t"
    "v_mfma_f32_16x16x32_bf16 v[156:159], a[140:143], a[20:23], v[156:159]\n\t"
    "v_mfma_f32_16x16x32_bf16 v[156:159], a[136:139], a[16:19], v[156:159]\n\t"
    "buffer_load_dwordx4 v196, s[20:23], s48 offen offset:3072 sc1 lds\n\t"
    "v_mfma_f32_16x16x32_bf16 v[156:159], a[124:127], a[12:15], v[156:159]\n\t"
    "v_mfma_f32_16x16x32_bf16 v[156:159], a[120:123], a[8:11], v[156:159]\n\t"
    "v_mfma_f32_16x16x32_bf16 v[156:159], a[108:111], a[4:7], v[156:159]\n\t"
    "s_waitcnt vmcnt(9) lgkmcnt(0)\n\t"
    "v_mfma_f32_16x16x32_bf16 v[156:159], a[104:107], a[0:3], v[156:159]\n\t"
    "s_barrier\n\t"
    "v_mfma_f32_16x16x32_bf16 v[156:159], a[168:171], a[224:227], v[156:159]\n\t"
    "ds_read_b128 a[0:3], v142 offset:0\n\t"
    "v_mfma_f32_16x16x32_bf16 v[156:159], a[172:175], a[228:231], v[156:159]\n\t"
    "ds_read_b128 a[4:7], v142 offset:1024\n\t"
    "v_mfma_f32_16x16x32_bf16 v[156:159], a[184:187], a[232:235], v[156:159]\n\t"
    "ds_read_b128 a[8:11], v142 offset:2048\n\t"
    "v_mfma_f32_16x16x32_bf16 v[156:159], a[188:191], a[236:239], v[156:159]\n\t"
    "ds_read_b128 a[12:15], v142 offset:3072\n\t"
    "v_mfma_f32_16x16x32_bf16 v[156:159], a[200:203], a[240:243], v[156:159]\n\t"
    "ds_read_b128 a[16:19], v142 offset:4096\n\t"
    "v_mfma_f32_16x16x32_bf16 v[156:159], a[204:207], a[244:247], v[156:159]\n\t"
    "ds_read_b128 a[20:23], v142 offset:5120\n\t"
    "v_mfma_f32_16x16x32_bf16 v[156:159], a[216:219], a[248:251], v[156:159]\n\t"
    "ds_read_b128 a[24:27], v142 offset:6144\n\t"
    "v_mfma_f32_16x16x32_bf16 v[156:159], a[220:223], a[252:255], v[156:159]\n\t"
    "ds_read_b128 a[28:31], v142 offset:7168\n\t"
    "v_mfma_f32_16x16x32_bf16 v[152:155], a[212:215], a[252:255], v[152:155]\n\t"
    "v_mfma_f32_16x16x32_bf16 v[152:155], a[208:211], a[248:251], v[152:155]\n\t"
    "v_mfma_f32_16x16x32_bf16 v[152:155], a[196:199], a[244:247], v[152:155]\n\t"
    "v_mfma_f32_16x16x32_bf16 v[152:155], a[192:195], a[240:243], v[152:155]\n\t"
    "v_mfma_f32_16x16x32_bf16 v[152:155], a[180:183], a[236:239], v[152:155]\n\t"
    "v_mfma_f32_16x16x32_bf16 v[152:155], a[176:179], a[232:235], v[152:155]\n\t"
    "v_mfma_f32_16x16x32_bf16 v[152:155], a[164:167], a[228:231], v[152:155]\n\t"
    "v_mfma_f32_16x16x32_bf16 v[152:155], a[160:163], a[224:227], v[152:155]\n\t"
    "s_waitcnt lgkmcnt(0)\n\t"
    "v_mfma_f32_16x16x32_bf16 v[144:147], a[32:35], a[0:3], 0\n\t"
    "s_lshl_b32 s9, s47, 2\n\t"
    "ds_read_b128 a[224:227], v142 offset:8192\n\t"
    "v_cvt_pk_bf16_f32 v194, v156, v157\n\t"
    "v_mfma_f32_16x16x32_bf16 v[144:147], a[36:39], a[4:7], v[144:147]\n\t"
    "s_add_u32 s9, s9, s95\n\t"
    "ds_read_b128 a[228:231], v142 offset:9216\n\t"
    "v_cvt_pk_bf16_f32 v195, v158, v159\n\t"
    "v_mfma_f32_16x16x32_bf16 v[144:147], a[48:51], a[8:11], v[144:147]\n\t"
    "s_mul_i32 s9, s9, 4096\n\t"
    "ds_read_b128 a[232:235], v142 offset:10240\n\t"
    "v_mfma_f32_16x16x32_bf16 v[144:147], a[52:55], a[12:15], v[144:147]\n\t"
    "s_add_u32 s47, s47, 1\n\t"
    "ds_read_b128 a[236:239], v142 offset:11264\n\t"
    "v_mfma_f32_16x16x32_bf16 v[144:147], a[64:67], a[16:19], v[144:147]\n\t"
    "s_cmp_eq_u32 s47, s46\n\t"
    "ds_read_b128 a[240:243], v142 offset:12288\n\t"
    "v_cvt_pk_bf16_f32 v192, v152, v153\n\t"
    "v_mfma_f32_16x16x32_bf16 v[144:147], a[68:71], a[20:23], v[144:147]\n\t"
    "s_cmov_b32 s47, 0\n\t"
    "ds_read_b128 a[244:247], v142 offset:13312\n\t"
    "v_cvt_pk_bf16_f32 v193, v154, v155\n\t"
    "v_mfma_f32_16x16x32_bf16 v[144:147], a[80:83], a[24:27], v[144:147]\n\t"
    "ds_read_b128 a[248:251], v142 offset:14336\n\t"
    "v_mfma_f32_16x16x32_bf16 v[144:147], a[84:87], a[28:31], v[144:147]\n\t"
    "buffer_store_dwordx4 v[192:195], v196, s[68:71], s9 offen sc1\n\t"
    "v_mfma_f32_16x16x32_bf16 v[148:151], a[92:95], a[28:31], 0\n\t"
    "s_add_u32 m0, s90, 0\n\t"
    "ds_read_b128 a[252:255], v142 offset:15360\n\t"
    "v_mfma_f32_16x16x32_bf16 v[148:151], a[88:91], a[24:27], v[148:151]\n\t"
    "s_add_u32 s48, s48, 28672\n\t"
    "v_mfma_f32_16x16x32_bf16 v[148:151], a[76:79], a[20:23], v[148:151]\n\t"
    "buffer_load_dwordx4 v196, s[20:23], s48 offen offset:0 sc1 lds\n\t"
    "v_mfma_f32_16x16x32_bf16 v[148:151], a[72:75], a[16:19], v[148:151]\n\t"
    "v_mfma_f32_16x16x32_bf16 v[148:151], a[60:63], a[12:15], v[148:151]\n\t"
    "buffer_load_dwordx4 v196, s[20:23], s48 offen offset:1024 sc1 lds\n\t"
    "v_mfma_f32_16x16x32_bf16 v[148:151], a[56:59], a[8:11], v[148:151]\n\t"
    "v_mfma_f32_16x16x32_bf16 v[148:151], a[44:47], a[4:7], v[148:151]\n\t"
    "buffer_load_dwordx4 v196, s[20:23], s48 offen offset:2048 sc1 lds\n\t"
    "v_mfma_f32_16x16x32_bf16 v[148:151], a[40:43], a[0:3], v[148:151]\n\t"
    "s_waitcnt lgkmcnt(0)\n\t"
    "v_mfma_f32_16x16x32_bf16 v[148:151], v[8:11], a[224:227], v[148:151]\n\t"
    "ds_read_b128 a[0:3], v142 offset:16384\n\t"
    "v_mfma_f32_16x16x32_bf16 v[148:151], v[12:15], a[228:231], v[148:151]\n\t"
    "ds_read_b128 a[4:7], v142 offset:17408\n\t"
    "v_mfma_f32_16x16x32_bf16 v[148:151], v[40:43], a[232:235], v[148:151]\n\t"
    "ds_read_b128 a[8:11], v142 offset:18432\n\t"
    "v_mfma_f32_16x16x32_bf16 v[148:151], v[44:47], a[236:239], v[148:151]\n\t"
    "ds_read_b128 a[12:15], v142 offset:19456\n\t"
    "v_mfma_f32_16x16x32_bf16 v[148:151], v[72:75], a[240:243], v[148:151]\n\t"
    "ds_read_b128 a[16:19], v142 offset:20480\n\t"
    "v_mfma_f32_16x16x32_bf16 v[148:151], v[76:79], a[244:247], v[148:151]\n\t"
    "ds_read_b128 a[20:23], v142 offset:21504\n\t"
    "v_mfma_f32_16x16x32_bf16 v[148:151], v[104:107], a[248:251], v[148:151]\n\t"
    "ds_read_b128 a[24:27], v142 offset:22528\n\t"
    "v_mfma_f32_16x16x32_bf16 v[148:151], v[108:111], a[252:255], v[148:151]\n\t"
    "ds_read_b128 a[28:31], v142 offset:23552\n\t"
    "v_mfma_f32_16x16x32_bf16 v[144:147], v[100:103], a[252:255], v[144:147]\n\t"
    "v_mfma_f32_16x16x32_bf16 v[144:147], v[96:99], a[248:251], v[144:147]\n\t"
    "buffer_load_dwordx4 v196, s[20:23], s48 offen offset:3072 sc1 lds\n\t"
    "v_mfma_f32_16x16x32_bf16 v[144:147], v[68:71], a[244:247], v[144:147]\n\t"
    "s_add_u32 m0, s90, 4096\n\t"
    "v_mfma_f32_16x16x32_bf16 v[144:147], v[64:67], a[240:243], v[144:147]\n\t"
    "v_mfma_f32_16x16x32_bf16 v[144:147], v[36:39], a[236:239], v[144:147]\n\t"
    "s_add_u32 s48, s48, 4096\n\t"
    "v_mfma_f32_16x16x32_bf16 v[144:147], v[32:35], a[232:235], v[144:147]\n\t"
    "buffer_load_dwordx4 v196, s[20:23], s48 offen offset:0 sc1 lds\n\t"
    "v_mfma_f32_16x16x32_bf16 v[144:147], v[4:7], a[228:231], v[144:147]\n\t"
    "buffer_load_dwordx4 v196, s[20:23], s48 offen offset:1024 sc1 lds\n\t"
    "v_mfma_f32_16x16x32_bf16 v[144:147], v[0:3], a[224:227], v[144:147]\n\t"
    "s_waitcnt lgkmcnt(0)\n\t"
    "v_mfma_f32_16x16x32_bf16 v[144:147], a[96:99], a[0:3], v[144:147]\n\t"
    "ds_read_b128 a[224:227], v142 offset:24576\n\t"
    "v_mfma_f32_16x16x32_bf16 v[144:147], a[100:103], a[4:7], v[144:147]\n\t"
    "ds_read_b128 a[228:231], v142 offset:25600\n\t"
    "v_mfma_f32_16x16x32_bf16 v[144:147], a[112:115], a[8:11], v[144:147]\n\t"
    "ds_read_b128 a[232:235], v142 offset:26624\n\t"
    "v_mfma_f32_16x16x32_bf16 v[144:147], a[116:119], a[12:15], v[144:147]\n\t"
    "ds_read_b128 a[236:239], v142 offset:27648\n\t"
    "v_mfma_f32_16x16x32_bf16 v[144:147], a[128:131], a[16:19], v[144:147]\n\t"
    "ds_read_b128 a[240:243], v142 offset:28672\n\t"
    "v_mfma_f32_16x16x32_bf16 v[144:147], a[132:135], a[20:23], v[144:147]\n\t"
    "ds_read_b128 a[244:247], v142 offset:29696\n\t"
    "v_mfma_f32_16x16x32_bf16 v[144:147], a[144:147], a[24:27], v[144:147]\n\t"
    "ds_read_b128 a[248:251], v142 offset:30720\n\t"
    "v_mfma_f32_16x16x32_bf16 v[144:147], a[148:151], a[28:31], v[144:147]\n\t"
    "ds_read_b128 a[252:255], v142 offset:31744\n\t"
    "v_mfma_f32_16x16x32_bf16 v[148:151], a[156:159], a[28:31], v[148:151]\n\t"
    "v_mfma_f32_16x16x32_bf16 v[148:151], a[152:155], a[24:27], v[148:151]\n\t"
    "buffer_load_dwordx4 v196, s[20:23], s48 offen offset:2048 sc1 lds\n\t"
    "v_mfma_f32_16x16x32_bf16 v[148:151], a[140:143], a[20:23], v[148:151]\n\t"
    "v_mfma_f32_16x16x32_bf16 v[148:151], a[136:139], a[16:19], v[148:151]\n\t"
    "buffer_load_dwordx4 v196, s[20:23], s48 offen offset:3072 sc1 lds\n\t"
    "v_mfma_f32_16x16x32_bf16 v[148:151], a[124:127], a[12:15], v[148:151]\n\t"
    "v_mfma_f32_16x16x32_bf16 v[148:151], a[120:123], a[8:11], v[148:151]\n\t"
    "v_mfma_f32_16x16x32_bf16 v[148:151], a[108:111], a[4:7], v[148:151]\n\t"
    "s_waitcnt vmcnt(9) lgkmcnt(0)\n\t"
    "v_mfma_f32_16x16x32_bf16 v[148:151], a[104:107], a[0:3], v[148:151]\n\t"
    "s_barrier\n\t"
    "v_mfma_f32_16x16x32_bf16 v[148:151], a[168:171], a[224:227], v[148:151]\n\t"
    "ds_read_b128 a[0:3], v143 offset:0\n\t"
    "v_mfma_f32_16x16x32_bf16 v[148:151], a[172:175], a[228:231], v[148:151]\n\t"
    "ds_read_b128 a[4:7], v143 offset:1024\n\t"
    "v_mfma_f32_16x16x32_bf16 v[148:151], a[184:187], a[232:235], v[148:151]\n\t"
    "ds_read_b128 a[8:11], v143 offset:2048\n\t"
    "v_mfma_f32_16x16x32_bf16 v[148:151], a[188:191], a[236:239], v[148:151]\n\t"
    "ds_read_b128 a[12:15], v143 offset:3072\n\t"
    "v_mfma_f32_16x16x32_bf16 v[148:151], a[200:203], a[240:243], v[148:151]\n\t"
    "ds_read_b128 a[16:19], v143 offset:4096\n\t"
    "v_mfma_f32_16x16x32_bf16 v[148:151], a[204:207], a[244:247], v[148:151]\n\t"
    "ds_read_b128 a[20:23], v143 offset:5120\n\t"
    "v_mfma_f32_16x16x32_bf16 v[148:151], a[216:219], a[248:251], v[148:151]\n\t"
    "ds_read_b128 a[24:27], v143 offset:6144\n\t"
    "v_mfma_f32_16x16x32_bf16 v[148:151], a[220:223], a[252:255], v[148:151]\n\t"
    "ds_read_b128 a[28:31], v143 offset:7168\n\t"
    "v_mfma_f32_16x16x32_bf16 v[144:147], a[212:215], a[252:255], v[144:147]\n\t"
    "v_mfma_f32_16x16x32_bf16 v[144:147], a[208:211], a[248:251], v[144:147]\n\t"
    "v_mfma_f32_16x16x32_bf16 v[144:147], a[196:199], a[244:247], v[144:147]\n\t"
    "v_mfma_f32_16x16x32_bf16 v[144:147], a[192:195], a[240:243], v[144:147]\n\t"
    "v_mfma_f32_16x16x32_bf16 v[144:147], a[180:183], a[236:239], v[144:147]\n\t"
    "v_mfma_f32_16x16x32_bf16 v[144:147], a[176:179], a[232:235], v[144:147]\n\t"
    "v_mfma_f32_16x16x32_bf16 v[144:147], a[164:167], a[228:231], v[144:147]\n\t"
    "v_mfma_f32_16x16x32_bf16 v[144:147], a[160:163], a[224:227], v[144:147]\n\t"
    "s_waitcnt lgkmcnt(0)\n\t"
    "v_mfma_f32_16x16x32_bf16 v[152:155], a[32:35], a[0:3], 0\n\t"
    "s_lshl_b32 s9, s47, 2\n\t"
    "ds_read_b128 a[224:227], v143 offset:8192\n\t"
    "v_cvt_pk_bf16_f32 v194, v148, v149\n\t"
    "v_mfma_f32_16x16x32_bf16 v[152:155], a[36:39], a[4:7], v[152:155]\n\t"
    "s_add_u32 s9, s9, s95\n\t"
    "ds_read_b128 a[228:231], v143 offset:9216\n\t"
    "v_cvt_pk_bf16_f32 v195, v150, v151\n\t"
    "v_mfma_f32_16x16x32_bf16 v[152:155], a[48:51], a[8:11], v[152:155]\n\t"
    "s_mul_i32 s9, s9, 4096\n\t"
    "ds_read_b128 a[232:235], v143 offset:10240\n\t"
    "v_mfma_f32_16x16x32_bf16 v[152:155], a[52:55], a[12:15], v[152:155]\n\t"
    "s_add_u32 s47, s47, 1\n\t"
    "ds_read_b128 a[236:239], v143 offset:11264\n\t"
    "v_mfma_f32_16x16x32_bf16 v[152:155], a[64:67], a[16:19], v[152:155]\n\t"
    "s_cmp_eq_u32 s47, s46\n\t"
    "ds_read_b128 a[240:243], v143 offset:12288\n\t"
    "v_cvt_pk_bf16_f32 v192, v144, v145\n\t"
    "v_mfma_f32_16x16x32_bf16 v[152:155], a[68:71], a[20:23], v[152:155]\n\t"
    "s_cmov_b32 s47, 0\n\t"
    "ds_read_b128 a[244:247], v143 offset:13312\n\t"
    "v_cvt_pk_bf16_f32 v193, v146, v147\n\t"
    "v_mfma_f32_16x16x32_bf16 v[152:155], a[80:83], a[24:27], v[152:155]\n\t"
    "ds_read_b128 a[248:251], v143 offset:14336\n\t"
    "v_mfma_f32_16x16x32_bf16 v[152:155], a[84:87], a[28:31], v[152:155]\n\t"
    "buffer_store_dwordx4 v[192:195], v196, s[68:71], s9 offen sc1\n\t"
    "v_mfma_f32_16x16x32_bf16 v[156:159], a[92:95], a[28:31], 0\n\t"
    "s_add_u32 m0, s91, 0\n\t"
    "ds_read_b128 a[252:255], v143 offset:15360\n\t"
    "v_mfma_f32_16x16x32_bf16 v[156:159], a[88:91], a[24:27], v[156:159]\n\t"
    "s_add_u32 s48, s48, 28672\n\t"
    "v_mfma_f32_16x16x32_bf16 v[156:159], a[76:79], a[20:23], v[156:159]\n\t"
    "buffer_load_dwordx4 v196, s[20:23], s48 offen offset:0 sc1 lds\n\t"
    "v_mfma_f32_16x16x32_bf16 v[156:159], a[72:75], a[16:19], v[156:159]\n\t"
    "v_mfma_f32_16x16x32_bf16 v[156:159], a[60:63], a[12:15], v[156:159]\n\t"
    "buffer_load_dwordx4 v196, s[20:23], s48 offen offset:1024 sc1 lds\n\t"
    "v_mfma_f32_16x16x32_bf16 v[156:159], a[56:59], a[8:11], v[156:159]\n\t"
    "v_mfma_f32_16x16x32_bf16 v[156:159], a[44:47], a[4:7], v[156:159]\n\t"
    "buffer_load_dwordx4 v196, s[20:23], s48 offen offset:2048 sc1 lds\n\t"
    "v_mfma_f32_16x16x32_bf16 v[156:159], a[40:43], a[0:3], v[156:159]\n\t"
    "s_waitcnt lgkmcnt(0)\n\t"
    "v_mfma_f32_16x16x32_bf16 v[156:159], v[8:11], a[224:227], v[156:159]\n\t"
    "ds_read_b128 a[0:3], v143 offset:16384\n\t"
    "v_mfma_f32_16x16x32_bf16 v[156:159], v[12:15], a[228:231], v[156:159]\n\t"
    "ds_read_b128 a[4:7], v143 offset:17408\n\t"
    "v_mfma_f32_16x16x32_bf16 v[156:159], v[40:43], a[232:235], v[156:159]\n\t"
    "ds_read_b128 a[8:11], v143 offset:18432\n\t"
    "v_mfma_f32_16x16x32_bf16 v[156:159], v[44:47], a[236:239], v[156:159]\n\t"
    "ds_read_b128 a[12:15], v143 offset:19456\n\t"
    "v_mfma_f32_16x16x32_bf16 v[156:159], v[72:75], a[240:243], v[156:159]\n\t"
    "ds_read_b128 a[16:19], v143 offset:20480\n\t"
    "v_mfma_f32_16x16x32_bf16 v[156:159], v[76:79], a[244:247], v[156:159]\n\t"
    "ds_read_b128 a[20:23], v143 offset:21504\n\t"
    "v_mfma_f32_16x16x32_bf16 v[156:159], v[104:107], a[248:251], v[156:159]\n\t"
    "ds_read_b128 a[24:27], v143 offset:22528\n\t"
    "v_mfma_f32_16x16x32_bf16 v[156:159], v[108:111], a[252:255], v[156:159]\n\t"
    "ds_read_b128 a[28:31], v143 offset:23552\n\t"
    "v_mfma_f32_16x16x32_bf16 v[152:155], v[100:103], a[252:255], v[152:155]\n\t"
    "v_mfma_f32_16x16x32_bf16 v[152:155], v[96:99], a[248:251], v[152:155]\n\t"
    "buffer_load_dwordx4 v196, s[20:23], s48 offen offset:3072 sc1 lds\n\t"
    "v_mfma_f32_16x16x32_bf16 v[152:155], v[68:71], a[244:247], v[152:155]\n\t"
    "s_add_u32 m0, s91, 4096\n\t"
    "v_mfma_f32_16x16x32_bf16 v[152:155], v[64:67], a[240:243], v[152:155]\n\t"
    "v_mfma_f32_16x16x32_bf16 v[152:155], v[36:39], a[236:239], v[152:155]\n\t"
    "s_add_u32 s48, s48, 4096\n\t"
    "v_mfma_f32_16x16x32_bf16 v[152:155], v[32:35], a[232:235], v[152:155]\n\t"
    "buffer_load_dwordx4 v196, s[20:23], s48 offen offset:0 sc1 lds\n\t"
    "v_mfma_f32_16x16x32_bf16 v[152:155], v[4:7], a[228:231], v[152:155]\n\t"
    "buffer_load_dwordx4 v196, s[20:23], s48 offen offset:1024 sc1 lds\n\t"
    "v_mfma_f32_16x16x32_bf16 v[152:155], v[0:3], a[224:227], v[152:155]\n\t"
    "s_waitcnt lgkmcnt(0)\n\t"
    "v_mfma_f32_16x16x32_bf16 v[152:155], a[96:99], a[0:3], v[152:155]\n\t"
    "ds_read_b128 a[224:227], v143 offset:24576\n\t"
    "v_mfma_f32_16x16x32_bf16 v[152:155], a[100:103], a[4:7], v[152:155]\n\t"
    "ds_read_b128 a[228:231], v143 offset:25600\n\t"
    "v_mfma_f32_16x16x32_bf16 v[152:155], a[112:115], a[8:11], v[152:155]\n\t"
    "ds_read_b128 a[232:235], v143 offset:26624\n\t"
    "v_mfma_f32_16x16x32_bf16 v[152:155], a[116:119], a[12:15], v[152:155]\n\t"
    "ds_read_b128 a[236:239], v143 offset:27648\n\t"
    "v_mfma_f32_16x16x32_bf16 v[152:155], a[128:131], a[16:19], v[152:155]\n\t"
    "ds_read_b128 a[240:243], v143 offset:28672\n\t"
    "v_mfma_f32_16x16x32_bf16 v[152:155], a[132:135], a[20:23], v[152:155]\n\t"
    "ds_read_b128 a[244:247], v143 offset:29696\n\t"
    "v_mfma_f32_16x16x32_bf16 v[152:155], a[144:147], a[24:27], v[152:155]\n\t"
    "ds_read_b128 a[248:251], v143 offset:30720\n\t"
    "v_mfma_f32_16x16x32_bf16 v[152:155], a[148:151], a[28:31], v[152:155]\n\t"
    "ds_read_b128 a[252:255], v143 offset:31744\n\t"
    "v_mfma_f32_16x16x32_bf16 v[156:159], a[156:159], a[28:31], v[156:159]\n\t"
    "v_mfma_f32_16x16x32_bf16 v[156:159], a[152:155], a[24:27], v[156:159]\n\t"
    "buffer_load_dwordx4 v196, s[20:23], s48 offen offset:2048 sc1 lds\n\t"
    "v_mfma_f32_16x16x32_bf16 v[156:159], a[140:143], a[20:23], v[156:159]\n\t"
    "v_mfma_f32_16x16x32_bf16 v[156:159], a[136:139], a[16:19], v[156:159]\n\t"
    "buffer_load_dwordx4 v196, s[20:23], s48 offen offset:3072 sc1 lds\n\t"
    "v_mfma_f32_16x16x32_bf16 v[156:159], a[124:127], a[12:15], v[156:159]\n\t"
    "v_mfma_f32_16x16x32_bf16 v[156:159], a[120:123], a[8:11], v[156:159]\n\t"
    "v_mfma_f32_16x16x32_bf16 v[156:159], a[108:111], a[4:7], v[156:159]\n\t"
    "s_waitcnt vmcnt(9) lgkmcnt(0)\n\t"
    "v_mfma_f32_16x16x32_bf16 v[156:159], a[104:107], a[0:3], v[156:159]\n\t"
    "s_barrier\n\t"
    "v_mfma_f32_16x16x32_bf16 v[156:159], a[168:171], a[224:227], v[156:159]\n\t"
    "ds_read_b128 a[0:3], v141 offset:0\n\t"
    "v_mfma_f32_16x16x32_bf16 v[156:159], a[172:175], a[228:231], v[156:159]\n\t"
    "ds_read_b128 a[4:7], v141 offset:1024\n\t"
    "v_mfma_f32_16x16x32_bf16 v[156:159], a[184:187], a[232:235], v[156:159]\n\t"
    "ds_read_b128 a[8:11], v141 offset:2048\n\t"
    "v_mfma_f32_16x16x32_bf16 v[156:159], a[188:191], a[236:239], v[156:159]\n\t"
    "ds_read_b128 a[12:15], v141 offset:3072\n\t"
    "v_mfma_f32_16x16x32_bf16 v[156:159], a[200:203], a[240:243], v[156:159]\n\t"
    "ds_read_b128 a[16:19], v141 offset:4096\n\t"
    "v_mfma_f32_16x16x32_bf16 v[156:159], a[204:207], a[244:247], v[156:159]\n\t"
    "ds_read_b128 a[20:23], v141 offset:5120\n\t"
    "v_mfma_f32_16x16x32_bf16 v[156:159], a[216:219], a[248:251], v[156:159]\n\t"
    "ds_read_b128 a[24:27], v141 offset:6144\n\t"
    "v_mfma_f32_16x16x32_bf16 v[156:159], a[220:223], a[252:255], v[156:159]\n\t"
    "ds_read_b128 a[28:31], v141 offset:7168\n\t"
    "v_mfma_f32_16x16x32_bf16 v[152:155], a[212:215], a[252:255], v[152:155]\n\t"
    "v_mfma_f32_16x16x32_bf16 v[152:155], a[208:211], a[248:251], v[152:155]\n\t"
    "v_mfma_f32_16x16x32_bf16 v[152:155], a[196:199], a[244:247], v[152:155]\n\t"
    "v_mfma_f32_16x16x32_bf16 v[152:155], a[192:195], a[240:243], v[152:155]\n\t"
    "v_mfma_f32_16x16x32_bf16 v[152:155], a[180:183], a[236:239], v[152:155]\n\t"
    "v_mfma_f32_16x16x32_bf16 v[152:155], a[176:179], a[232:235], v[152:155]\n\t"
    "v_mfma_f32_16x16x32_bf16 v[152:155], a[164:167], a[228:231], v[152:155]\n\t"
    "v_mfma_f32_16x16x32_bf16 v[152:155], a[160:163], a[224:227], v[152:155]\n\t"
    "s_waitcnt lgkmcnt(0)\n\t"
    "s_lshl_b32 s60, s96, 2\n\t"
    "s_add_u32 s60, s60, s40\n\t"
    "s_lshl_b32 exec_lo, 1, s60\n\t"
    "s_mov_b32 exec_hi, 0\n\t"
    "v_add_u32 v139, 3, v211\n\t"
    "buffer_store_dword v139, v137, s[12:15], 0 offen sc1\n\t"
    "s_mov_b64 exec, -1\n\t"
    "v_mfma_f32_16x16x32_bf16 v[144:147], a[32:35], a[0:3], 0\n\t"
    "s_lshl_b32 s9, s47, 2\n\t"
    "ds_read_b128 a[224:227], v141 offset:8192\n\t"
    "v_cvt_pk_bf16_f32 v194, v156, v157\n\t"
    "v_mfma_f32_16x16x32_bf16 v[144:147], a[36:39], a[4:7], v[144:147]\n\t"
    "s_add_u32 s9, s9, s95\n\t"
    "ds_read_b128 a[228:231], v141 offset:9216\n\t"
    "v_cvt_pk_bf16_f32 v195, v158, v159\n\t"
    "v_mfma_f32_16x16x32_bf16 v[144:147], a[48:51], a[8:11], v[144:147]\n\t"
    "s_mul_i32 s9, s9, 4096\n\t"
    "ds_read_b128 a[232:235], v141 offset:10240\n\t"
    "v_mfma_f32_16x16x32_bf16 v[144:147], a[52:55], a[12:15], v[144:147]\n\t"
    "s_add_u32 s47, s47, 1\n\t"
    "ds_read_b128 a[236:239], v141 offset:11264\n\t"
    "v_mfma_f32_16x16x32_bf16 v[144:147], a[64:67], a[16:19], v[144:147]\n\t"
    "s_cmp_eq_u32 s47, s46\n\t"
    "ds_read_b128 a[240:243], v141 offset:12288\n\t"
    "v_cvt_pk_bf16_f32 v192, v152, v153\n\t"
    "v_mfma_f32_16x16x32_bf16 v[144:147], a[68:71], a[20:23], v[144:147]\n\t"
    "s_cmov_b32 s47, 0\n\t"
    "ds_read_b128 a[244:247], v141 offset:13312\n\t"
    "v_cvt_pk_bf16_f32 v193, v154, v155\n\t"
    "v_mfma_f32_16x16x32_bf16 v[144:147], a[80:83], a[24:27], v[144:147]\n\t"
    "ds_read_b128 a[248:251], v141 offset:14336\n\t"
    "v_mfma_f32_16x16x32_bf16 v[144:147], a[84:87], a[28:31], v[144:147]\n\t"
    "buffer_store_dwordx4 v[192:195], v196, s[68:71], s9 offen sc1\n\t"
    "v_mfma_f32_16x16x32_bf16 v[148:151], a[92:95], a[28:31], 0\n\t"
    "s_add_u32 m0, s99, 0\n\t"
    "ds_read_b128 a[252:255], v141 offset:15360\n\t"
    "v_mfma_f32_16x16x32_bf16 v[148:151], a[88:91], a[24:27], v[148:151]\n\t"
    "s_add_u32 s48, s48, 28672\n\t"
    "v_mfma_f32_16x16x32_bf16 v[148:151], a[76:79], a[20:23], v[148:151]\n\t"
    "buffer_load_dwordx4 v196, s[20:23], s48 offen offset:0 sc1 lds\n\t"
    "v_mfma_f32_16x16x32_bf16 v[148:151], a[72:75], a[16:19], v[148:151]\n\t"
    "v_mfma_f32_16x16x32_bf16 v[148:151], a[60:63], a[12:15], v[148:151]\n\t"
    "buffer_load_dwordx4 v196, s[20:23], s48 offen offset:1024 sc1 lds\n\t"
    "v_mfma_f32_16x16x32_bf16 v[148:151], a[56:59], a[8:11], v[148:151]\n\t"
    "v_mfma_f32_16x16x32_bf16 v[148:151], a[44:47], a[4:7], v[148:151]\n\t"
    "buffer_load_dwordx4 v196, s[20:23], s48 offen offset:2048 sc1 lds\n\t"
    "v_mfma_f32_16x16x32_bf16 v[148:151], a[40:43], a[0:3], v[148:151]\n\t"
    "s_waitcnt lgkmcnt(0)\n\t"
    "v_mfma_f32_16x16x32_bf16 v[148:151], v[8:11], a[224:227], v[148:151]\n\t"
    "ds_read_b128 a[0:3], v141 offset:16384\n\t"
    "v_mfma_f32_16x16x32_bf16 v[148:151], v[12:15], a[228:231], v[148:151]\n\t"
    "ds_read_b128 a[4:7], v141 offset:17408\n\t"
    "v_mfma_f32_16x16x32_bf16 v[148:151], v[40:43], a[232:235], v[148:151]\n\t"
    "ds_read_b128 a[8:11], v141 offset:18432\n\t"
    "v_mfma_f32_16x16x32_bf16 v[148:151], v[44:47], a[236:239], v[148:151]\n\t"
    "ds_read_b128 a[12:15], v141 offset:19456\n\t"
    "v_mfma_f32_16x16x32_bf16 v[148:151], v[72:75], a[240:243], v[148:151]\n\t"
    "ds_read_b128 a[16:19], v141 offset:20480\n\t"
    "v_mfma_f32_16x16x32_bf16 v[148:151], v[76:79], a[244:247], v[148:151]\n\t"
    "ds_read_b128 a[20:23], v141 offset:21504\n\t"
    "v_mfma_f32_16x16x32_bf16 v[148:151], v[104:107], a[248:251], v[148:151]\n\t"
    "ds_read_b128 a[24:27], v141 offset:22528\n\t"
    "v_mfma_f32_16x16x32_bf16 v[148:151], v[108:111], a[252:255], v[148:151]\n\t"
    "ds_read_b128 a[28:31], v141 offset:23552\n\t"
    "v_mfma_f32_16x16x32_bf16 v[144:147], v[100:103], a[252:255], v[144:147]\n\t"
    "v_mfma_f32_16x16x32_bf16 v[144:147], v[96:99], a[248:251], v[144:147]\n\t"
    "buffer_load_dwordx4 v196, s[20:23], s48 offen offset:3072 sc1 lds\n\t"
    "v_mfma_f32_16x16x32_bf16 v[144:147], v[68:71], a[244:247], v[144:147]\n\t"
    "s_add_u32 m0, s99, 4096\n\t"
    "v_mfma_f32_16x16x32_bf16 v[144:147], v[64:67], a[240:243], v[144:147]\n\t"
    "v_mfma_f32_16x16x32_bf16 v[144:147], v[36:39], a[236:239], v[144:147]\n\t"
    "s_add_u32 s48, s48, 4096\n\t"
    "v_mfma_f32_16x16x32_bf16 v[144:147], v[32:35], a[232:235], v[144:147]\n\t"
    "buffer_load_dwordx4 v196, s[20:23], s48 offen offset:0 sc1 lds\n\t"
    "v_mfma_f32_16x16x32_bf16 v[144:147], v[4:7], a[228:231], v[144:147]\n\t"
    "buffer_load_dwordx4 v196, s[20:23], s48 offen offset:1024 sc1 lds\n\t"
    "v_mfma_f32_16x16x32_bf16 v[144:147], v[0:3], a[224:227], v[144:147]\n\t"
    "s_waitcnt lgkmcnt(0)\n\t"
    "v_mfma_f32_16x16x32_bf16 v[144:147], a[96:99], a[0:3], v[144:147]\n\t"
    "ds_read_b128 a[224:227], v141 offset:24576\n\t"
    "v_mfma_f32_16x16x32_bf16 v[144:147], a[100:103], a[4:7], v[144:147]\n\t"
    "ds_read_b128 a[228:231], v141 offset:25600\n\t"
    "v_mfma_f32_16x16x32_bf16 v[144:147], a[112:115], a[8:11], v[144:147]\n\t"
    "ds_read_b128 a[232:235], v141 offset:26624\n\t"
    "v_mfma_f32_16x16x32_bf16 v[144:147], a[116:119], a[12:15], v[144:147]\n\t"
    "ds_read_b128 a[236:239], v141 offset:27648\n\t"
    "v_mfma_f32_16x16x32_bf16 v[144:147], a[128:131], a[16:19], v[144:147]\n\t"
    "ds_read_b128 a[240:243], v141 offset:28672\n\t"
    "v_mfma_f32_16x16x32_bf16 v[144:147], a[132:135], a[20:23], v[144:147]\n\t"
    "ds_read_b128 a[244:247], v141 offset:29696\n\t"
    "v_mfma_f32_16x16x32_bf16 v[144:147], a[144:147], a[24:27], v[144:147]\n\t"
    "ds_read_b128 a[248:251], v141 offset:30720\n\t"
    "v_mfma_f32_16x16x32_bf16 v[144:147], a[148:151], a[28:31], v[144:147]\n\t"
    "ds_read_b128 a[252:255], v141 offset:31744\n\t"
    "v_mfma_f32_16x16x32_bf16 v[148:151], a[156:159], a[28:31], v[148:151]\n\t"
    "v_mfma_f32_16x16x32_bf16 v[148:151], a[152:155], a[24:27], v[148:151]\n\t"
    "buffer_load_dwordx4 v196, s[20:23], s48 offen offset:2048 sc1 lds\n\t"
    "v_mfma_f32_16x16x32_bf16 v[148:151], a[140:143], a[20:23], v[148:151]\n\t"
    "v_mfma_f32_16x16x32_bf16 v[148:151], a[136:139], a[16:19], v[148:151]\n\t"
    "buffer_load_dwordx4 v196, s[20:23], s48 offen offset:3072 sc1 lds\n\t"
    "v_mfma_f32_16x16x32_bf16 v[148:151], a[124:127], a[12:15], v[148:151]\n\t"
    "v_mfma_f32_16x16x32_bf16 v[148:151], a[120:123], a[8:11], v[148:151]\n\t"
    "v_mfma_f32_16x16x32_bf16 v[148:151], a[108:111], a[4:7], v[148:151]\n\t"
    "s_waitcnt vmcnt(10) lgkmcnt(0)\n\t"
    "v_mfma_f32_16x16x32_bf16 v[148:151], a[104:107], a[0:3], v[148:151]\n\t"
    "s_barrier\n\t"
    "v_mfma_f32_16x16x32_bf16 v[148:151], a[168:171], a[224:227], v[148:151]\n\t"
    "ds_read_b128 a[0:3], v142 offset:0\n\t"
    "v_mfma_f32_16x16x32_bf16 v[148:151], a[172:175], a[228:231], v[148:151]\n\t"
    "ds_read_b128 a[4:7], v142 offset:1024\n\t"
    "v_mfma_f32_16x16x32_bf16 v[148:151], a[184:187], a[232:235], v[148:151]\n\t"
    "ds_read_b128 a[8:11], v142 offset:2048\n\t"
    "v_mfma_f32_16x16x32_bf16 v[148:151], a[188:191], a[236:239], v[148:151]\n\t"
    "ds_read_b128 a[12:15], v142 offset:3072\n\t"
    "v_mfma_f32_16x16x32_bf16 v[148:151], a[200:203], a[240:243], v[148:151]\n\t"
    "ds_read_b128 a[16:19], v142 offset:4096\n\t"
    "v_mfma_f32_16x16x32_bf16 v[148:151], a[204:207], a[244:247], v[148:151]\n\t"
    "ds_read_b128 a[20:23], v142 offset:5120\n\t"
    "v_mfma_f32_16x16x32_bf16 v[148:151], a[216:219], a[248:251], v[148:151]\n\t"
    "ds_read_b128 a[24:27], v142 offset:6144\n\t"
    "v_mfma_f32_16x16x32_bf16 v[148:151], a[220:223], a[252:255], v[148:151]\n\t"
    "ds_read_b128 a[28:31], v142 offset:7168\n\t"
    "v_mfma_f32_16x16x32_bf16 v[144:147], a[212:215], a[252:255], v[144:147]\n\t"
    "v_mfma_f32_16x16x32_bf16 v[144:147], a[208:211], a[248:251], v[144:147]\n\t"
    "v_mfma_f32_16x16x32_bf16 v[144:147], a[196:199], a[244:247], v[144:147]\n\t"
    "v_mfma_f32_16x16x32_bf16 v[144:147], a[192:195], a[240:243], v[144:147]\n\t"
    "v_mfma_f32_16x16x32_bf16 v[144:147], a[180:183], a[236:239], v[144:147]\n\t"
    "v_mfma_f32_16x16x32_bf16 v[144:147], a[176:179], a[232:235], v[144:147]\n\t"
    "v_mfma_f32_16x16x32_bf16 v[144:147], a[164:167], a[228:231], v[144:147]\n\t"
    "v_mfma_f32_16x16x32_bf16 v[144:147], a[160:163], a[224:227], v[144:147]\n\t"
    "s_waitcnt lgkmcnt(0)\n\t"
    "v_mfma_f32_16x16x32_bf16 v[160:163], a[32:35], a[0:3], 0\n\t"
    "s_lshl_b32 s9, s47, 2\n\t"
    "ds_read_b128 a[224:227], v142 offset:8192\n\t"
    "v_cvt_pk_bf16_f32 v194, v148, v149\n\t"
    "v_mfma_f32_16x16x32_bf16 v[160:163], a[36:39], a[4:7], v[160:163]\n\t"
    "s_add_u32 s9, s9, s95\n\t"
    "ds_read_b128 a[228:231], v142 offset:9216\n\t"
    "v_cvt_pk_bf16_f32 v195, v150, v151\n\t"
    "v_mfma_f32_16x16x32_bf16 v[160:163], a[48:51], a[8:11], v[160:163]\n\t"
    "s_mul_i32 s9, s9, 4096\n\t"
    "ds_read_b128 a[232:235], v142 offset:10240\n\t"
    "v_mfma_f32_16x16x32_bf16 v[160:163], a[52:55], a[12:15], v[160:163]\n\t"
    "s_add_u32 s47, s47, 1\n\t"
    "ds_read_b128 a[236:239], v142 offset:11264\n\t"
    "v_mfma_f32_16x16x32_bf16 v[160:163], a[64:67], a[16:19], v[160:163]\n\t"
    "s_cmp_eq_u32 s47, s46\n\t"
    "ds_read_b128 a[240:243], v142 offset:12288\n\t"
    "v_cvt_pk_bf16_f32 v192, v144, v145\n\t"
    "v_mfma_f32_16x16x32_bf16 v[160:163], a[68:71], a[20:23], v[160:163]\n\t"
    "s_cmov_b32 s47, 0\n\t"
    "ds_read_b128 a[244:247], v142 offset:13312\n\t"
    "v_cvt_pk_bf16_f32 v193, v146, v147\n\t"
    "v_mfma_f32_16x16x32_bf16 v[160:163], a[80:83], a[24:27], v[160:163]\n\t"
    "ds_read_b128 a[248:251], v142 offset:14336\n\t"
    "v_mfma_f32_16x16x32_bf16 v[160:163], a[84:87], a[28:31], v[160:163]\n\t"
    "buffer_store_dwordx4 v[192:195], v196, s[68:71], s9 offen sc1\n\t"
    "v_mfma_f32_16x16x32_bf16 v[164:167], a[92:95], a[28:31], 0\n\t"
    "s_add_u32 m0, s90, 0\n\t"
    "ds_read_b128 a[252:255], v142 offset:15360\n\t"
    "v_mfma_f32_16x16x32_bf16 v[164:167], a[88:91], a[24:27], v[164:167]\n\t"
    "s_add_u32 s48, s48, 28672\n\t"
    "v_mfma_f32_16x16x32_bf16 v[164:167], a[76:79], a[20:23], v[164:167]\n\t"
    "buffer_load_dwordx4 v196, s[20:23], s48 offen offset:0 sc1 lds\n\t"
    "v_mfma_f32_16x16x32_bf16 v[164:167], a[72:75], a[16:19], v[164:167]\n\t"
    "v_mfma_f32_16x16x32_bf16 v[164:167], a[60:63], a[12:15], v[164:167]\n\t"
    "buffer_load_dwordx4 v196, s[20:23], s48 offen offset:1024 sc1 lds\n\t"
    "v_mfma_f32_16x16x32_bf16 v[164:167], a[56:59], a[8:11], v[164:167]\n\t"
    "v_mfma_f32_16x16x32_bf16 v[164:167], a[44:47], a[4:7], v[164:167]\n\t"
    "buffer_load_dwordx4 v196, s[20:23], s48 offen offset:2048 sc1 lds\n\t"
    "v_mfma_f32_16x16x32_bf16 v[164:167], a[40:43], a[0:3], v[164:167]\n\t"
    "s_waitcnt lgkmcnt(0)\n\t"
    "v_mfma_f32_16x16x32_bf16 v[164:167], v[8:11], a[224:227], v[164:167]\n\t"
    "ds_read_b128 a[0:3], v142 offset:16384\n\t"
    "v_mfma_f32_16x16x32_bf16 v[164:167], v[12:15], a[228:231], v[164:167]\n\t"
    "ds_read_b128 a[4:7], v142 offset:17408\n\t"
    "v_mfma_f32_16x16x32_bf16 v[164:167], v[40:43], a[232:235], v[164:167]\n\t"
    "ds_read_b128 a[8:11], v142 offset:18432\n\t"
    "v_mfma_f32_16x16x32_bf16 v[164:167], v[44:47], a[236:239], v[164:167]\n\t"
    "ds_read_b128 a[12:15], v142 offset:19456\n\t"
    "v_mfma_f32_16x16x32_bf16 v[164:167], v[72:75], a[240:243], v[164:167]\n\t"
    "ds_read_b128 a[16:19], v142 offset:20480\n\t"
    "v_mfma_f32_16x16x32_bf16 v[164:167], v[76:79], a[244:247], v[164:167]\n\t"
    "ds_read_b128 a[20:23], v142 offset:21504\n\t"
    "v_mfma_f32_16x16x32_bf16 v[164:167], v[104:107], a[248:251], v[164:167]\n\t"
    "ds_read_b128 a[24:27], v142 offset:22528\n\t"
    "v_mfma_f32_16x16x32_bf16 v[164:167], v[108:111], a[252:255], v[164:167]\n\t"
    "ds_read_b128 a[28:31], v142 offset:23552\n\t"
    "s_waitcnt vmcnt(12)\n\t"
    "s_lshl_b32 exec_hi, 1, s95\n\t"
    "s_mov_b32 exec_lo, 0\n\t"
    "v_add_u32 v139, 1, v211\n\t"
    "buffer_store_dword v139, v137, s[12:15], 0 offen sc1\n\t"
    "s_mov_b64 exec, -1\n\t"
    "v_mfma_f32_16x16x32_bf16 v[160:163], v[100:103], a[252:255], v[160:163]\n\t"
    "v_mfma_f32_16x16x32_bf16 v[160:163], v[96:99], a[248:251], v[160:163]\n\t"
    "buffer_load_dwordx4 v196, s[20:23], s48 offen offset:3072 sc1 lds\n\t"
    "v_mfma_f32_16x16x32_bf16 v[160:163], v[68:71], a[244:247], v[160:163]\n\t"
    "s_add_u32 m0, s90, 4096\n\t"
    "v_mfma_f32_16x16x32_bf16 v[160:163], v[64:67], a[240:243], v[160:163]\n\t"
    "v_mfma_f32_16x16x32_bf16 v[160:163], v[36:39], a[236:239], v[160:163]\n\t"
    "s_add_u32 s48, s48, 4096\n\t"
    "v_mfma_f32_16x16x32_bf16 v[160:163], v[32:35], a[232:235], v[160:163]\n\t"
    "buffer_load_dwordx4 v196, s[20:23], s48 offen offset:0 sc1 lds\n\t"
    "v_mfma_f32_16x16x32_bf16 v[160:163], v[4:7], a[228:231], v[160:163]\n\t"
    "buffer_load_dwordx4 v196, s[20:23], s48 offen offset:1024 sc1 lds\n\t"
    "v_mfma_f32_16x16x32_bf16 v[160:163], v[0:3], a[224:227], v[160:163]\n\t"
    "s_waitcnt lgkmcnt(0)\n\t"
    "v_mfma_f32_16x16x32_bf16 v[160:163], a[96:99], a[0:3], v[160:163]\n\t"
    "s_add_u32 s60, s95, 3\n\t"
    "ds_read_b128 a[224:227], v142 offset:24576\n\t"
    "v_mfma_f32_16x16x32_bf16 v[160:163], a[100:103], a[4:7], v[160:163]\n\t"
    "s_and_b32 s60, s60, 3\n\t"
    "ds_read_b128 a[228:231], v142 offset:25600\n\t"
    "v_mfma_f32_16x16x32_bf16 v[160:163], a[112:115], a[8:11], v[160:163]\n\t"
    "s_mul_i32 s60, s60, 4096\n\t"
    "ds_read_b128 a[232:235], v142 offset:26624\n\t"
    "v_mfma_f32_16x16x32_bf16 v[160:163], a[116:119], a[12:15], v[160:163]\n\t"
    "s_add_u32 s84, s49, s60\n\t"
    "ds_read_b128 a[236:239], v142 offset:27648\n\t"
    "v_mfma_f32_16x16x32_bf16 v[160:163], a[128:131], a[16:19], v[160:163]\n\t"
    "s_add_u32 s85, s50, s60\n\t"
    "ds_read_b128 a[240:243], v142 offset:28672\n\t"
    "v_mfma_f32_16x16x32_bf16 v[160:163], a[132:135], a[20:23], v[160:163]\n\t"
    "s_add_u32 s86, s59, s60\n\t"
    "ds_read_b128 a[244:247], v142 offset:29696\n\t"
    "v_mfma_f32_16x16x32_bf16 v[160:163], a[144:147], a[24:27], v[160:163]\n\t"
    "s_add_u32 s60, s95, 2\n\t"
    "ds_read_b128 a[248:251], v142 offset:30720\n\t"
    "v_mfma_f32_16x16x32_bf16 v[160:163], a[148:151], a[28:31], v[160:163]\n\t"
    "s_and_b32 s60, s60, 3\n\t"
    "ds_read_b128 a[252:255], v142 offset:31744\n\t"
    "v_mfma_f32_16x16x32_bf16 v[164:167], a[156:159], a[28:31], v[164:167]\n\t"
    "s_mul_i32 s60, s60, 4096\n\t"
    "v_mfma_f32_16x16x32_bf16 v[164:167], a[152:155], a[24:27], v[164:167]\n\t"
    "buffer_load_dwordx4 v196, s[20:23], s48 offen offset:2048 sc1 lds\n\t"
    "v_mfma_f32_16x16x32_bf16 v[164:167], a[140:143], a[20:23], v[164:167]\n\t"
    "s_add_u32 s87, s49, s60\n\t"
    "v_mfma_f32_16x16x32_bf16 v[164:167], a[136:139], a[16:19], v[164:167]\n\t"
    "buffer_load_dwordx4 v196, s[20:23], s48 offen offset:3072 sc1 lds\n\t"
    "v_mfma_f32_16x16x32_bf16 v[164:167], a[124:127], a[12:15], v[164:167]\n\t"
    "s_add_u32 s88, s50, s60\n\t"
    "v_mfma_f32_16x16x32_bf16 v[164:167], a[120:123], a[8:11], v[164:167]\n\t"
    "s_add_u32 s89, s59, s60\n\t"
    "v_mfma_f32_16x16x32_bf16 v[164:167], a[108:111], a[4:7], v[164:167]\n\t"
    "s_waitcnt vmcnt(10) lgkmcnt(0)\n\t"
    "v_mfma_f32_16x16x32_bf16 v[164:167], a[104:107], a[0:3], v[164:167]\n\t"
    "s_barrier\n\t"
    "v_mfma_f32_16x16x32_bf16 v[164:167], a[168:171], a[224:227], v[164:167]\n\t"
    "s_add_u32 s60, s95, 1\n\t"
    "ds_read_b128 a[0:3], v143 offset:0\n\t"
    "v_mfma_f32_16x16x32_bf16 v[164:167], a[172:175], a[228:231], v[164:167]\n\t"
    "s_and_b32 s60, s60, 3\n\t"
    "ds_read_b128 a[4:7], v143 offset:1024\n\t"
    "v_mfma_f32_16x16x32_bf16 v[164:167], a[184:187], a[232:235], v[164:167]\n\t"
    "s_mul_i32 s60, s60, 4096\n\t"
    "ds_read_b128 a[8:11], v143 offset:2048\n\t"
    "v_mfma_f32_16x16x32_bf16 v[164:167], a[188:191], a[236:239], v[164:167]\n\t"
    "s_add_u32 s90, s49, s60\n\t"
    "ds_read_b128 a[12:15], v143 offset:3072\n\t"
    "v_mfma_f32_16x16x32_bf16 v[164:167], a[200:203], a[240:243], v[164:167]\n\t"
    "s_add_u32 s91, s50, s60\n\t"
    "ds_read_b128 a[16:19], v143 offset:4096\n\t"
    "v_mfma_f32_16x16x32_bf16 v[164:167], a[204:207], a[244:247], v[164:167]\n\t"
    "s_add_u32 s92, s59, s60\n\t"
    "ds_read_b128 a[20:23], v143 offset:5120\n\t"
    "v_mfma_f32_16x16x32_bf16 v[164:167], a[216:219], a[248:251], v[164:167]\n\t"
    "ds_read_b128 a[24:27], v143 offset:6144\n\t"
    "v_mfma_f32_16x16x32_bf16 v[164:167], a[220:223], a[252:255], v[164:167]\n\t"
    "ds_read_b128 a[28:31], v143 offset:7168\n\t"
    "v_mfma_f32_16x16x32_bf16 v[160:163], a[212:215], a[252:255], v[160:163]\n\t"
    "v_mfma_f32_16x16x32_bf16 v[160:163], a[208:211], a[248:251], v[160:163]\n\t"
    "v_mfma_f32_16x16x32_bf16 v[160:163], a[196:199], a[244:247], v[160:163]\n\t"
    "v_mfma_f32_16x16x32_bf16 v[160:163], a[192:195], a[240:243], v[160:163]\n\t"
    "v_mfma_f32_16x16x32_bf16 v[160:163], a[180:183], a[236:239], v[160:163]\n\t"
    "v_mfma_f32_16x16x32_bf16 v[160:163], a[176:179], a[232:235], v[160:163]\n\t"
    "v_mfma_f32_16x16x32_bf16 v[160:163], a[164:167], a[228:231], v[160:163]\n\t"
    "v_mfma_f32_16x16x32_bf16 v[160:163], a[160:163], a[224:227], v[160:163]\n\t"
    "s_waitcnt lgkmcnt(0)\n\t"
    "s_mov_b32 exec_lo, 0\n\t"
    "s_mov_b32 exec_hi, 15\n\t"
    "buffer_load_dword v138, v137, s[12:15], 0 offen sc1\n\t"
    "s_mov_b64 exec, -1\n\t"
    "v_mfma_f32_16x16x32_bf16 v[168:171], a[32:35], a[0:3], 0\n\t"
    "s_lshl_b32 s9, s47, 2\n\t"
    "ds_read_b128 a[224:227], v143 offset:8192\n\t"
    "v_cvt_pk_bf16_f32 v194, v164, v165\n\t"
    "v_mfma_f32_16x16x32_bf16 v[168:171], a[36:39], a[4:7], v[168:171]\n\t"
    "s_add_u32 s9, s9, s95\n\t"
    "ds_read_b128 a[228:231], v143 offset:9216\n\t"
    "v_cvt_pk_bf16_f32 v195, v166, v167\n\t"
    "v_mfma_f32_16x16x32_bf16 v[168:171], a[48:51], a[8:11], v[168:171]\n\t"
    "s_mul_i32 s9, s9, 4096\n\t"
    "ds_read_b128 a[232:235], v143 offset:10240\n\t"
    "v_mfma_f32_16x16x32_bf16 v[168:171], a[52:55], a[12:15], v[168:171]\n\t"
    "s_add_u32 s47, s47, 1\n\t"
    "ds_read_b128 a[236:239], v143 offset:11264\n\t"
    "v_mfma_f32_16x16x32_bf16 v[168:171], a[64:67], a[16:19], v[168:171]\n\t"
    "s_cmp_eq_u32 s47, s46\n\t"
    "ds_read_b128 a[240:243], v143 offset:12288\n\t"
    "v_cvt_pk_bf16_f32 v192, v160, v161\n\t"
    "v_mfma_f32_16x16x32_bf16 v[168:171], a[68:71], a[20:23], v[168:171]\n\t"
    "s_cmov_b32 s47, 0\n\t"
    "ds_read_b128 a[244:247], v143 offset:13312\n\t"
    "v_cvt_pk_bf16_f32 v193, v162, v163\n\t"
    "v_mfma_f32_16x16x32_bf16 v[168:171], a[80:83], a[24:27], v[168:171]\n\t"
    "ds_read_b128 a[248:251], v143 offset:14336\n\t"
    "v_mfma_f32_16x16x32_bf16 v[168:171], a[84:87], a[28:31], v[168:171]\n\t"
    "buffer_store_dwordx4 v[192:195], v196, s[68:71], s9 offen sc1\n\t"
    "v_mfma_f32_16x16x32_bf16 v[172:175], a[92:95], a[28:31], 0\n\t"
    "ds_read_b128 a[252:255], v143 offset:15360\n\t"
    "v_mfma_f32_16x16x32_bf16 v[172:175], a[88:91], a[24:27], v[172:175]\n\t"
    "s_add_u32 s47, s95, 1\n\t"
    "v_mfma_f32_16x16x32_bf16 v[172:175], a[76:79], a[20:23], v[172:175]\n\t"
    "s_and_b32 s47, s47, 3\n\t"
    "v_mfma_f32_16x16x32_bf16 v[172:175], a[72:75], a[16:19], v[172:175]\n\t"
    "s_add_u32 s47, s47, 32\n\t"
    "v_mfma_f32_16x16x32_bf16 v[172:175], a[60:63], a[12:15], v[172:175]\n\t"
    "v_mfma_f32_16x16x32_bf16 v[172:175], a[56:59], a[8:11], v[172:175]\n\t"
    "v_mfma_f32_16x16x32_bf16 v[172:175], a[44:47], a[4:7], v[172:175]\n\t"
    "v_mfma_f32_16x16x32_bf16 v[172:175], a[40:43], a[0:3], v[172:175]\n\t"
    "s_waitcnt lgkmcnt(0)\n\t"
    "v_mfma_f32_16x16x32_bf16 v[172:175], v[8:11], a[224:227], v[172:175]\n\t"
    "ds_read_b128 a[0:3], v143 offset:16384\n\t"
    "v_mfma_f32_16x16x32_bf16 v[172:175], v[12:15], a[228:231], v[172:175]\n\t"
    "ds_read_b128 a[4:7], v143 offset:17408\n\t"
    "v_mfma_f32_16x16x32_bf16 v[172:175], v[40:43], a[232:235], v[172:175]\n\t"
    "ds_read_b128 a[8:11], v143 offset:18432\n\t"
    "v_mfma_f32_16x16x32_bf16 v[172:175], v[44:47], a[236:239], v[172:175]\n\t"
    "ds_read_b128 a[12:15], v143 offset:19456\n\t"
    "v_mfma_f32_16x16x32_bf16 v[172:175], v[72:75], a[240:243], v[172:175]\n\t"
    "ds_read_b128 a[16:19], v143 offset:20480\n\t"
    "v_mfma_f32_16x16x32_bf16 v[172:175], v[76:79], a[244:247], v[172:175]\n\t"
    "ds_read_b128 a[20:23], v143 offset:21504\n\t"
    "v_mfma_f32_16x16x32_bf16 v[172:175], v[104:107], a[248:251], v[172:175]\n\t"
    "ds_read_b128 a[24:27], v143 offset:22528\n\t"
    "v_mfma_f32_16x16x32_bf16 v[172:175], v[108:111], a[252:255], v[172:175]\n\t"
    "ds_read_b128 a[28:31], v143 offset:23552\n\t"
    "v_mfma_f32_16x16x32_bf16 v[168:171], v[100:103], a[252:255], v[168:171]\n\t"
    "v_mfma_f32_16x16x32_bf16 v[168:171], v[96:99], a[248:251], v[168:171]\n\t"
    "v_mfma_f32_16x16x32_bf16 v[168:171], v[68:71], a[244:247], v[168:171]\n\t"
    "v_mfma_f32_16x16x32_bf16 v[168:171], v[64:67], a[240:243], v[168:171]\n\t"
    "v_mfma_f32_16x16x32_bf16 v[168:171], v[36:39], a[236:239], v[168:171]\n\t"
    "v_mfma_f32_16x16x32_bf16 v[168:171], v[32:35], a[232:235], v[168:171]\n\t"
    "v_mfma_f32_16x16x32_bf16 v[168:171], v[4:7], a[228:231], v[168:171]\n\t"
    "v_mfma_f32_16x16x32_bf16 v[168:171], v[0:3], a[224:227], v[168:171]\n\t"
    "s_waitcnt lgkmcnt(0)\n\t"
    "v_mfma_f32_16x16x32_bf16 v[168:171], a[96:99], a[0:3], v[168:171]\n\t"
    "ds_read_b128 a[224:227], v143 offset:24576\n\t"
    "v_mfma_f32_16x16x32_bf16 v[168:171], a[100:103], a[4:7], v[168:171]\n\t"
    "ds_read_b128 a[228:231], v143 offset:25600\n\t"
    "v_mfma_f32_16x16x32_bf16 v[168:171], a[112:115], a[8:11], v[168:171]\n\t"
    "ds_read_b128 a[232:235], v143 offset:26624\n\t"
    "v_mfma_f32_16x16x32_bf16 v[168:171], a[116:119], a[12:15], v[168:171]\n\t"
    "ds_read_b128 a[236:239], v143 offset:27648\n\t"
    "v_mfma_f32_16x16x32_bf16 v[168:171], a[128:131], a[16:19], v[168:171]\n\t"
    "ds_read_b128 a[240:243], v143 offset:28672\n\t"
    "v_mfma_f32_16x16x32_bf16 v[168:171], a[132:135], a[20:23], v[168:171]\n\t"
    "ds_read_b128 a[244:247], v143 offset:29696\n\t"
    "v_mfma_f32_16x16x32_bf16 v[168:171], a[144:147], a[24:27], v[168:171]\n\t"
    "ds_read_b128 a[248:251], v143 offset:30720\n\t"
    "v_mfma_f32_16x16x32_bf16 v[168:171], a[148:151], a[28:31], v[168:171]\n\t"
    "ds_read_b128 a[252:255], v143 offset:31744\n\t"
    "s_waitcnt vmcnt(1)\n\t"
    "s_mov_b32 exec_lo, 0\n\t"
    "s_mov_b32 exec_hi, 15\n\t"
    "L_chk2a_%=:\n\t"
    "v_sub_u32 v138, v138, v211\n\t"
    "v_cmp_gt_u32 vcc, 1, v138\n\t"
    "s_cbranch_vccnz L_spin2a_%=\n\t"
    "L_got2a_%=:\n\t"
    "s_mov_b64 exec, -1\n\t"
    "s_mov_b32 exec_lo, 0\n\t"
    "s_mov_b32 exec_hi, 15\n\t"
    "buffer_load_dword v138, v137, s[12:15], 0 offen sc1\n\t"
    "s_mov_b64 exec, -1\n\t"
    "v_mfma_f32_16x16x32_bf16 v[172:175], a[156:159], a[28:31], v[172:175]\n\t"
    "buffer_load_dwordx4 v[16:19], v196, s[68:71], s84 offen offset:0 sc1\n\t"
    "v_mfma_f32_16x16x32_bf16 v[172:175], a[152:155], a[24:27], v[172:175]\n\t"
    "buffer_load_dwordx4 v[24:27], v196, s[68:71], s85 offen offset:0 sc1\n\t"
    "v_mfma_f32_16x16x32_bf16 v[172:175], a[140:143], a[20:23], v[172:175]\n\t"
    "buffer_load_dwordx4 v[48:51], v196, s[68:71], s86 offen offset:0 sc1\n\t"
    "v_mfma_f32_16x16x32_bf16 v[172:175], a[136:139], a[16:19], v[172:175]\n\t"
    "buffer_load_dwordx4 v[56:59], v196, s[68:71], s87 offen offset:0 sc1\n\t"
    "v_mfma_f32_16x16x32_bf16 v[172:175], a[124:127], a[12:15], v[172:175]\n\t"
    "buffer_load_dwordx4 v[80:83], v196, s[68:71], s88 offen offset:0 sc1\n\t"
    "v_mfma_f32_16x16x32_bf16 v[172:175], a[120:123], a[8:11], v[172:175]\n\t"
    "buffer_load_dwordx4 v[88:91], v196, s[68:71], s89 offen offset:0 sc1\n\t"
    "v_mfma_f32_16x16x32_bf16 v[172:175], a[108:111], a[4:7], v[172:175]\n\t"
    "s_waitcnt vmcnt(9) lgkmcnt(0)\n\t"
    "v_mfma_f32_16x16x32_bf16 v[172:175], a[104:107], a[0:3], v[172:175]\n\t"
    "s_barrier\n\t"
    "v_mfma_f32_16x16x32_bf16 v[172:175], a[168:171], a[224:227], v[172:175]\n\t"
    "ds_read_b128 a[0:3], v141 offset:0\n\t"
    "v_mfma_f32_16x16x32_bf16 v[172:175], a[172:175], a[228:231], v[172:175]\n\t"
    "ds_read_b128 a[4:7], v141 offset:1024\n\t"
    "v_mfma_f32_16x16x32_bf16 v[172:175], a[184:187], a[232:235], v[172:175]\n\t"
    "ds_read_b128 a[8:11], v141 offset:2048\n\t"
    "v_mfma_f32_16x16x32_bf16 v[172:175], a[188:191], a[236:239], v[172:175]\n\t"
    "ds_read_b128 a[12:15], v141 offset:3072\n\t"
    "v_mfma_f32_16x16x32_bf16 v[172:175], a[200:203], a[240:243], v[172:175]\n\t"
    "ds_read_b128 a[16:19], v141 offset:4096\n\t"
    "v_mfma_f32_16x16x32_bf16 v[172:175], a[204:207], a[244:247], v[172:175]\n\t"
    "ds_read_b128 a[20:23], v141 offset:5120\n\t"
    "v_mfma_f32_16x16x32_bf16 v[172:175], a[216:219], a[248:251], v[172:175]\n\t"
    "ds_read_b128 a[24:27], v141 offset:6144\n\t"
    "v_mfma_f32_16x16x32_bf16 v[172:175], a[220:223], a[252:255], v[172:175]\n\t"
    "ds_read_b128 a[28:31], v141 offset:7168\n\t"
    "v_mfma_f32_16x16x32_bf16 v[168:171], a[212:215], a[252:255], v[168:171]\n\t"
    "v_mfma_f32_16x16x32_bf16 v[168:171], a[208:211], a[248:251], v[168:171]\n\t"
    "v_mfma_f32_16x16x32_bf16 v[168:171], a[196:199], a[244:247], v[168:171]\n\t"
    "v_mfma_f32_16x16x32_bf16 v[168:171], a[192:195], a[240:243], v[168:171]\n\t"
    "v_mfma_f32_16x16x32_bf16 v[168:171], a[180:183], a[236:239], v[168:171]\n\t"
    "v_mfma_f32_16x16x32_bf16 v[168:171], a[176:179], a[232:235], v[168:171]\n\t"
    "v_mfma_f32_16x16x32_bf16 v[168:171], a[164:167], a[228:231], v[168:171]\n\t"
    "v_mfma_f32_16x16x32_bf16 v[168:171], a[160:163], a[224:227], v[168:171]\n\t"
    "s_waitcnt lgkmcnt(0)\n\t"
    "v_mfma_f32_16x16x32_bf16 v[176:179], a[32:35], a[0:3], 0\n\t"
    "ds_read_b128 a[224:227], v141 offset:8192\n\t"
    "v_mfma_f32_16x16x32_bf16 v[176:179], a[36:39], a[4:7], v[176:179]\n\t"
    "ds_read_b128 a[228:231], v141 offset:9216\n\t"
    "v_mfma_f32_16x16x32_bf16 v[176:179], a[48:51], a[8:11], v[176:179]\n\t"
    "ds_read_b128 a[232:235], v141 offset:10240\n\t"
    "v_mfma_f32_16x16x32_bf16 v[176:179], a[52:55], a[12:15], v[176:179]\n\t"
    "ds_read_b128 a[236:239], v141 offset:11264\n\t"
    "v_mfma_f32_16x16x32_bf16 v[176:179], a[64:67], a[16:19], v[176:179]\n\t"
    "ds_read_b128 a[240:243], v141 offset:12288\n\t"
    "v_mfma_f32_16x16x32_bf16 v[176:179], a[68:71], a[20:23], v[176:179]\n\t"
    "ds_read_b128 a[244:247], v141 offset:13312\n\t"
    "v_mfma_f32_16x16x32_bf16 v[176:179], a[80:83], a[24:27], v[176:179]\n\t"
    "ds_read_b128 a[248:251], v141 offset:14336\n\t"
    "v_mfma_f32_16x16x32_bf16 v[176:179], a[84:87], a[28:31], v[176:179]\n\t"
    "ds_read_b128 a[252:255], v141 offset:15360\n\t"
    "s_waitcnt vmcnt(6)\n\t"
    "s_lshl_b32 exec_hi, 1, s95\n\t"
    "s_mov_b32 exec_lo, 0\n\t"
    "v_add_u32 v139, 2, v211\n\t"
    "buffer_store_dword v139, v137, s[12:15], 0 offen sc1\n\t"
    "s_mov_b64 exec, -1\n\t"
    "v_sub_u32 v138, v138, v211\n\t"
    "s_nop 0\n\t"
    "v_readlane_b32 s9, v138, s47\n\t"
    "v_mfma_f32_16x16x32_bf16 v[180:183], a[92:95], a[28:31], 0\n\t"
    "s_cmp_lt_u32 s9, 2\n\t"
    "v_mfma_f32_16x16x32_bf16 v[180:183], a[88:91], a[24:27], v[180:183]\n\t"
    "s_cbranch_scc1 L_nf_%=\n\t"
    "buffer_load_dwordx4 v[112:115], v196, s[68:71], s90 offen offset:0 sc1\n\t"
    "buffer_load_dwordx4 v[120:123], v196, s[68:71], s91 offen offset:0 sc1\n\t"
    "buffer_load_dwordx4 v[212:215], v196, s[68:71], s92 offen offset:0 sc1\n\t"
    "L_nf_%=:\n\t"
    "v_mfma_f32_16x16x32_bf16 v[180:183], a[76:79], a[20:23], v[180:183]\n\t"
    "v_mfma_f32_16x16x32_bf16 v[180:183], a[72:75], a[16:19], v[180:183]\n\t"
    "v_mfma_f32_16x16x32_bf16 v[180:183], a[60:63], a[12:15], v[180:183]\n\t"
    "v_mfma_f32_16x16x32_bf16 v[180:183], a[56:59], a[8:11], v[180:183]\n\t"
    "v_mfma_f32_16x16x32_bf16 v[180:183], a[44:47], a[4:7], v[180:183]\n\t"
    "v_mfma_f32_16x16x32_bf16 v[180:183], a[40:43], a[0:3], v[180:183]\n\t"
    "s_waitcnt lgkmcnt(0)\n\t"
    "v_mfma_f32_16x16x32_bf16 v[180:183], v[8:11], a[224:227], v[180:183]\n\t"
    "ds_read_b128 a[0:3], v141 offset:16384\n\t"
    "v_mfma_f32_16x16x32_bf16 v[180:183], v[12:15], a[228:231], v[180:183]\n\t"
    "ds_read_b128 a[4:7], v141 offset:17408\n\t"
    "v_mfma_f32_16x16x32_bf16 v[180:183], v[40:43], a[232:235], v[180:183]\n\t"
    "ds_read_b128 a[8:11], v141 offset:18432\n\t"
    "v_mfma_f32_16x16x32_bf16 v[180:183], v[44:47], a[236:239], v[180:183]\n\t"
    "ds_read_b128 a[12:15], v141 offset:19456\n\t"
    "v_mfma_f32_16x16x32_bf16 v[180:183], v[72:75], a[240:243], v[180:183]\n\t"
    "ds_read_b128 a[16:19], v141 offset:20480\n\t"
    "v_mfma_f32_16x16x32_bf16 v[180:183], v[76:79], a[244:247], v[180:183]\n\t"
    "ds_read_b128 a[20:23], v141 offset:21504\n\t"
    "v_mfma_f32_16x16x32_bf16 v[180:183], v[104:107], a[248:251], v[180:183]\n\t"
    "ds_read_b128 a[24:27], v141 offset:22528\n\t"
    "v_mfma_f32_16x16x32_bf16 v[180:183], v[108:111], a[252:255], v[180:183]\n\t"
    "ds_read_b128 a[28:31], v141 offset:23552\n\t"
    "v_mfma_f32_16x16x32_bf16 v[176:179], v[100:103], a[252:255], v[176:179]\n\t"
    "v_mfma_f32_16x16x32_bf16 v[176:179], v[96:99], a[248:251], v[176:179]\n\t"
    "v_mfma_f32_16x16x32_bf16 v[176:179], v[68:71], a[244:247], v[176:179]\n\t"
    "v_mfma_f32_16x16x32_bf16 v[176:179], v[64:67], a[240:243], v[176:179]\n\t"
    "v_mfma_f32_16x16x32_bf16 v[176:179], v[36:39], a[236:239], v[176:179]\n\t"
    "v_mfma_f32_16x16x32_bf16 v[176:179], v[32:35], a[232:235], v[176:179]\n\t"
    "v_mfma_f32_16x16x32_bf16 v[176:179], v[4:7], a[228:231], v[176:179]\n\t"
    "v_mfma_f32_16x16x32_bf16 v[176:179], v[0:3], a[224:227], v[176:179]\n\t"
    "s_waitcnt lgkmcnt(0)\n\t"
    "v_mfma_f32_16x16x32_bf16 v[176:179], a[96:99], a[0:3], v[176:179]\n\t"
    "ds_read_b128 a[224:227], v141 offset:24576\n\t"
    "v_mfma_f32_16x16x32_bf16 v[176:179], a[100:103], a[4:7], v[176:179]\n\t"
    "ds_read_b128 a[228:231], v141 offset:25600\n\t"
    "v_mfma_f32_16x16x32_bf16 v[176:179], a[112:115], a[8:11], v[176:179]\n\t"
    "ds_read_b128 a[232:235], v141 offset:26624\n\t"
    "v_mfma_f32_16x16x32_bf16 v[176:179], a[116:119], a[12:15], v[176:179]\n\t"
    "ds_read_b128 a[236:239], v141 offset:27648\n\t"
    "v_mfma_f32_16x16x32_bf16 v[176:179], a[128:131], a[16:19], v[176:179]\n\t"
    "ds_read_b128 a[240:243], v141 offset:28672\n\t"
    "v_mfma_f32_16x16x32_bf16 v[176:179], a[132:135], a[20:23], v[176:179]\n\t"
    "ds_read_b128 a[244:247], v141 offset:29696\n\t"
    "v_mfma_f32_16x16x32_bf16 v[176:179], a[144:147], a[24:27], v[176:179]\n\t"
    "ds_read_b128 a[248:251], v141 offset:30720\n\t"
    "v_mfma_f32_16x16x32_bf16 v[176:179], a[148:151], a[28:31], v[176:179]\n\t"
    "ds_read_b128 a[252:255], v141 offset:31744\n\t"
    "s_mov_b32 exec_lo, 0\n\t"
    "s_mov_b32 exec_hi, 15\n\t"
    "buffer_load_dword v138, v137, s[12:15], 0 offen sc1\n\t"
    "s_mov_b64 exec, -1\n\t"
    "v_mfma_f32_16x16x32_bf16 v[180:183], a[156:159], a[28:31], v[180:183]\n\t"
    "v_mfma_f32_16x16x32_bf16 v[180:183], a[152:155], a[24:27], v[180:183]\n\t"
    "v_mfma_f32_16x16x32_bf16 v[180:183], a[140:143], a[20:23], v[180:183]\n\t"
    "v_mfma_f32_16x16x32_bf16 v[180:183], a[136:139], a[16:19], v[180:183]\n\t"
    "v_mfma_f32_16x16x32_bf16 v[180:183], a[124:127], a[12:15], v[180:183]\n\t"
    "v_mfma_f32_16x16x32_bf16 v[180:183], a[120:123], a[8:11], v[180:183]\n\t"
    "v_mfma_f32_16x16x32_bf16 v[180:183], a[108:111], a[4:7], v[180:183]\n\t"
    "v_mfma_f32_16x16x32_bf16 v[180:183], a[104:107], a[0:3], v[180:183]\n\t"
    "s_waitcnt lgkmcnt(0)\n\t"
    "v_mfma_f32_16x16x32_bf16 v[180:183], a[168:171], a[224:227], v[180:183]\n\t"
    "v_mfma_f32_16x16x32_bf16 v[180:183], a[172:175], a[228:231], v[180:183]\n\t"
    "v_mfma_f32_16x16x32_bf16 v[180:183], a[184:187], a[232:235], v[180:183]\n\t"
    "v_mfma_f32_16x16x32_bf16 v[180:183], a[188:191], a[236:239], v[180:183]\n\t"
    "v_mfma_f32_16x16x32_bf16 v[180:183], a[200:203], a[240:243], v[180:183]\n\t"
    "s_waitcnt vmcnt(2)\n\t"
    "v_lshlrev_b32 v184, 16, v24\n\t"
    "v_and_b32 v185, 0xffff0000, v24\n\t"
    "v_mfma_f32_16x16x32_bf16 v[180:183], a[204:207], a[244:247], v[180:183]\n\t"
    "v_lshlrev_b32 v186, 16, v25\n\t"
    "v_and_b32 v187, 0xffff0000, v25\n\t"
    "v_mfma_f32_16x16x32_bf16 v[180:183], a[216:219], a[248:251], v[180:183]\n\t"
    "v_lshlrev_b32 v188, 16, v26\n\t"
    "v_and_b32 v189, 0xffff0000, v26\n\t"
    "v_mfma_f32_16x16x32_bf16 v[180:183], a[220:223], a[252:255], v[180:183]\n\t"
    "v_lshlrev_b32 v190, 16, v27\n\t"
    "v_and_b32 v191, 0xffff0000, v27\n\t"
    "v_mfma_f32_16x16x32_bf16 v[176:179], a[212:215], a[252:255], v[176:179]\n\t"
    "v_add_f32 v168, v168, v184\n\t"
    "v_add_f32 v169, v169, v185\n\t"
    "v_mfma_f32_16x16x32_bf16 v[176:179], a[208:211], a[248:251], v[176:179]\n\t"
    "v_add_f32 v170, v170, v186\n\t"
    "v_add_f32 v171, v171, v187\n\t"
    "v_mfma_f32_16x16x32_bf16 v[176:179], a[196:199], a[244:247], v[176:179]\n\t"
    "v_add_f32 v172, v172, v188\n\t"
    "v_add_f32 v173, v173, v189\n\t"
    "v_mfma_f32_16x16x32_bf16 v[176:179], a[192:195], a[240:243], v[176:179]\n\t"
    "v_add_f32 v174, v174, v190\n\t"
    "v_add_f32 v175, v175, v191\n\t"
    "v_mfma_f32_16x16x32_bf16 v[176:179], a[180:183], a[236:239], v[176:179]\n\t"
    "v_lshlrev_b32 v184, 16, v80\n\t"
    "v_and_b32 v185, 0xffff0000, v80\n\t"
    "v_mfma_f32_16x16x32_bf16 v[176:179], a[176:179], a[232:235], v[176:179]\n\t"
    "v_lshlrev_b32 v186, 16, v81\n\t"
    "v_and_b32 v187, 0xffff0000, v81\n\t"
    "v_mfma_f32_16x16x32_bf16 v[176:179], a[164:167], a[228:231], v[176:179]\n\t"
    "v_lshlrev_b32 v188, 16, v82\n\t"
    "v_and_b32 v189, 0xffff0000, v82\n\t"
    "v_mfma_f32_16x16x32_bf16 v[176:179], a[160:163], a[224:227], v[176:179]\n\t"
    "v_lshlrev_b32 v190, 16, v83\n\t"
    "v_and_b32 v191, 0xffff0000, v83\n\t"
    "v_add_f32 v168, v168, v184\n\t"
    "v_add_f32 v169, v169, v185\n\t"
    "v_add_f32 v170, v170, v186\n\t"
    "v_add_f32 v171, v171, v187\n\t"
    "v_add_f32 v172, v172, v188\n\t"
    "v_add_f32 v173, v173, v189\n\t"
    "v_add_f32 v174, v174, v190\n\t"
    "v_add_f32 v175, v175, v191\n\t"
    "v_lshlrev_b32 v184, 16, v16\n\t"
    "v_and_b32 v185, 0xffff0000, v16\n\t"
    "v_lshlrev_b32 v186, 16, v17\n\t"
    "v_and_b32 v187, 0xffff0000, v17\n\t"
    "v_lshlrev_b32 v188, 16, v18\n\t"
    "v_and_b32 v189, 0xffff0000, v18\n\t"
    "v_lshlrev_b32 v190, 16, v19\n\t"
    "v_and_b32 v191, 0xffff0000, v19\n\t"
    "v_add_f32 v160, v160, v184\n\t"
    "v_add_f32 v161, v161, v185\n\t"
    "v_add_f32 v162, v162, v186\n\t"
    "v_add_f32 v163, v163, v187\n\t"
    "v_add_f32 v164, v164, v188\n\t"
    "v_add_f32 v165, v165, v189\n\t"
    "v_add_f32 v166, v166, v190\n\t"
    "v_add_f32 v167, v167, v191\n\t"
    "s_cmp_lt_u32 s9, 2\n\t"
    "s_cbranch_scc0 L_have_%=\n\t"
    "s_waitcnt vmcnt(0)\n\t"
    "s_mov_b32 exec_lo, 0\n\t"
    "s_mov_b32 exec_hi, 15\n\t"
    "L_chk2b_%=:\n\t"
    "v_sub_u32 v138, v138, v211\n\t"
    "v_cmp_gt_u32 vcc, 2, v138\n\t"
    "s_cbranch_vccnz L_spin2b_%=\n\t"
    "L_got2b_%=:\n\t"
    "s_mov_b64 exec, -1\n\t"
    "buffer_load_dwordx4 v[112:115], v196, s[68:71], s90 offen offset:0 sc1\n\t"
    "buffer_load_dwordx4 v[120:123], v196, s[68:71], s91 offen offset:0 sc1\n\t"
    "buffer_load_dwordx4 v[212:215], v196, s[68:71], s92 offen offset:0 sc1\n\t"
    "L_have_%=:\n\t"
    "v_lshlrev_b32 v184, 16, v56\n\t"
    "v_and_b32 v185, 0xffff0000, v56\n\t"
    "v_lshlrev_b32 v186, 16, v57\n\t"
    "v_and_b32 v187, 0xffff0000, v57\n\t"
    "v_lshlrev_b32 v188, 16, v58\n\t"
    "v_and_b32 v189, 0xffff0000, v58\n\t"
    "v_lshlrev_b32 v190, 16, v59\n\t"
    "v_and_b32 v191, 0xffff0000, v59\n\t"
    "v_add_f32 v160, v160, v184\n\t"
    "v_add_f32 v161, v161, v185\n\t"
    "v_add_f32 v162, v162, v186\n\t"
    "v_add_f32 v163, v163, v187\n\t"
    "v_add_f32 v164, v164, v188\n\t"
    "v_add_f32 v165, v165, v189\n\t"
    "v_add_f32 v166, v166, v190\n\t"
    "v_add_f32 v167, v167, v191\n\t"
    "v_lshlrev_b32 v184, 16, v48\n\t"
    "v_and_b32 v185, 0xffff0000, v48\n\t"
    "v_lshlrev_b32 v186, 16, v49\n\t"
    "v_and_b32 v187, 0xffff0000, v49\n\t"
    "v_lshlrev_b32 v188, 16, v50\n\t"
    "v_and_b32 v189, 0xffff0000, v50\n\t"
    "v_lshlrev_b32 v190, 16, v51\n\t"
    "v_and_b32 v191, 0xffff0000, v51\n\t"
    "v_add_f32 v176, v176, v184\n\t"
    "v_add_f32 v177, v177, v185\n\t"
    "v_add_f32 v178, v178, v186\n\t"
    "v_add_f32 v179, v179, v187\n\t"
    "v_add_f32 v180, v180, v188\n\t"
    "v_add_f32 v181, v181, v189\n\t"
    "v_add_f32 v182, v182, v190\n\t"
    "v_add_f32 v183, v183, v191\n\t"
    "v_lshlrev_b32 v184, 16, v88\n\t"
    "v_and_b32 v185, 0xffff0000, v88\n\t"
    "v_lshlrev_b32 v186, 16, v89\n\t"
    "v_and_b32 v187, 0xffff0000, v89\n\t"
    "v_lshlrev_b32 v188, 16, v90\n\t"
    "v_and_b32 v189, 0xffff0000, v90\n\t"
    "v_lshlrev_b32 v190, 16, v91\n\t"
    "v_and_b32 v191, 0xffff0000, v91\n\t"
    "v_add_f32 v176, v176, v184\n\t"
    "v_add_f32 v177, v177, v185\n\t"
    "v_add_f32 v178, v178, v186\n\t"
    "v_add_f32 v179, v179, v187\n\t"
    "v_add_f32 v180, v180, v188\n\t"
    "v_add_f32 v181, v181, v189\n\t"
    "v_add_f32 v182, v182, v190\n\t"
    "v_add_f32 v183, v183, v191\n\t"
    "s_waitcnt vmcnt(0)\n\t"
    "v_lshlrev_b32 v184, 16, v120\n\t"
    "v_and_b32 v185, 0xffff0000, v120\n\t"
    "v_lshlrev_b32 v186, 16, v121\n\t"
    "v_and_b32 v187, 0xffff0000, v121\n\t"
    "v_lshlrev_b32 v188, 16, v122\n\t"
    "v_and_b32 v189, 0xffff0000, v122\n\t"
    "v_lshlrev_b32 v190, 16, v123\n\t"
    "v_and_b32 v191, 0xffff0000, v123\n\t"
    "v_add_f32 v168, v168, v184\n\t"
    "v_add_f32 v169, v169, v185\n\t"
    "v_add_f32 v170, v170, v186\n\t"
    "v_add_f32 v171, v171, v187\n\t"
    "v_add_f32 v172, v172, v188\n\t"
    "v_add_f32 v173, v173, v189\n\t"
    "v_add_f32 v174, v174, v190\n\t"
    "v_add_f32 v175, v175, v191\n\t"
    "v_cvt_pk_bf16_f32 v168, v168, v169\n\t"
    "v_cvt_pk_bf16_f32 v169, v170, v171\n\t"
    "v_cvt_pk_bf16_f32 v170, v172, v173\n\t"
    "v_cvt_pk_bf16_f32 v171, v174, v175\n\t"
    "s_lshr_b32 s53, s50, 0\n\t"
    "s_nop 0\n\t"
    "v_permlane16_swap_b32 v168, v170\n\t"
    "v_permlane16_swap_b32 v169, v171\n\t"
    "buffer_store_dwordx4 v[168:171], v136, s[72:75], s53 offen sc1\n\t"
    "v_lshlrev_b32 v184, 16, v212\n\t"
    "v_and_b32 v185, 0xffff0000, v212\n\t"
    "v_lshlrev_b32 v186, 16, v213\n\t"
    "v_and_b32 v187, 0xffff0000, v213\n\t"
    "v_lshlrev_b32 v188, 16, v214\n\t"
    "v_and_b32 v189, 0xffff0000, v214\n\t"
    "v_lshlrev_b32 v190, 16, v215\n\t"
    "v_and_b32 v191, 0xffff0000, v215\n\t"
    "v_add_f32 v176, v176, v184\n\t"
    "v_add_f32 v177, v177, v185\n\t"
    "v_add_f32 v178, v178, v186\n\t"
    "v_add_f32 v179, v179, v187\n\t"
    "v_add_f32 v180, v180, v188\n\t"
    "v_add_f32 v181, v181, v189\n\t"
    "v_add_f32 v182, v182, v190\n\t"
    "v_add_f32 v183, v183, v191\n\t"
    "v_cvt_pk_bf16_f32 v176, v176, v177\n\t"
    "v_cvt_pk_bf16_f32 v177, v178, v179\n\t"
    "v_cvt_pk_bf16_f32 v178, v180, v181\n\t"
    "v_cvt_pk_bf16_f32 v179, v182, v183\n\t"
    "s_lshr_b32 s53, s59, 0\n\t"
    "s_nop 0\n\t"
    "v_permlane16_swap_b32 v176, v178\n\t"
    "v_permlane16_swap_b32 v177, v179\n\t"
    "buffer_store_dwordx4 v[176:179], v136, s[72:75], s53 offen sc1\n\t"
    "s_cmp_lt_u32 s95, s8\n\t"
    "s_cbranch_scc0 L_two_%=\n\t"
    "v_lshlrev_b32 v184, 16, v112\n\t"
    "v_and_b32 v185, 0xffff0000, v112\n\t"
    "v_lshlrev_b32 v186, 16, v113\n\t"
    "v_and_b32 v187, 0xffff0000, v113\n\t"
    "v_lshlrev_b32 v188, 16, v114\n\t"
    "v_and_b32 v189, 0xffff0000, v114\n\t"
    "v_lshlrev_b32 v190, 16, v115\n\t"
    "v_and_b32 v191, 0xffff0000, v115\n\t"
    "v_add_f32 v160, v160, v184\n\t"
    "v_add_f32 v161, v161, v185\n\t"
    "v_add_f32 v162, v162, v186\n\t"
    "v_add_f32 v163, v163, v187\n\t"
    "v_add_f32 v164, v164, v188\n\t"
    "v_add_f32 v165, v165, v189\n\t"
    "v_add_f32 v166, v166, v190\n\t"
    "v_add_f32 v167, v167, v191\n\t"
    "v_cvt_pk_bf16_f32 v160, v160, v161\n\t"
    "v_cvt_pk_bf16_f32 v161, v162, v163\n\t"
    "v_cvt_pk_bf16_f32 v162, v164, v165\n\t"
    "v_cvt_pk_bf16_f32 v163, v166, v167\n\t"
    "s_lshr_b32 s53, s49, 0\n\t"
    "s_nop 0\n\t"
    "v_permlane16_swap_b32 v160, v162\n\t"
    "v_permlane16_swap_b32 v161, v163\n\t"
    "buffer_store_dwordx4 v[160:163], v136, s[72:75], s53 offen sc1\n\t"
    "L_two_%=:\n\t"
    "s_cmp_lt_u32 s9, 2\n\t"
    "s_cbranch_scc1 L_seen_%=\n\t"
    "s_mov_b32 exec_lo, 0\n\t"
    "s_mov_b32 exec_hi, 15\n\t"
    "L_chk2c_%=:\n\t"
    "v_sub_u32 v138, v138, v211\n\t"
    "v_cmp_gt_u32 vcc, 2, v138\n\t"
    "s_cbranch_vccnz L_spin2c_%=\n\t"
    "L_got2c_%=:\n\t"
    "s_mov_b64 exec, -1\n\t"
    "L_seen_%=:\n\t"
    "s_lshl_b32 exec_hi, 1, s95\n\t"
    "s_mov_b32 exec_lo, 0\n\t"
    "v_add_u32 v139, 3, v211\n\t"
    "buffer_store_dword v139, v137, s[12:15], 0 offen sc1\n\t"
    "s_mov_b64 exec, -1\n\t"
    "s_endpgm\n\t"
    "L_e1_%=:\n\t"
    "ds_read_b128 a[0:3], v142 offset:0\n\t"
    "ds_read_b128 a[4:7], v142 offset:1024\n\t"
    "ds_read_b128 a[8:11], v142 offset:2048\n\t"
    "ds_read_b128 a[12:15], v142 offset:3072\n\t"
    "ds_read_b128 a[16:19], v142 offset:4096\n\t"
    "ds_read_b128 a[20:23], v142 offset:5120\n\t"
    "ds_read_b128 a[24:27], v142 offset:6144\n\t"
    "ds_read_b128 a[28:31], v142 offset:7168\n\t"
    "ds_read_b64_tr_b16 a[208:209], v130 offset:16384\n\t"
    "ds_read_b64_tr_b16 a[210:211], v130 offset:20480\n\t"
    "ds_read_b64_tr_b16 a[212:213], v130 offset:24576\n\t"
    "ds_read_b64_tr_b16 a[214:215], v130 offset:28672\n\t"
    "ds_read_b64_tr_b16 a[216:217], v131 offset:16384\n\t"
    "ds_read_b64_tr_b16 a[218:219], v131 offset:20480\n\t"
    "ds_read_b64_tr_b16 a[220:221], v131 offset:24576\n\t"
    "ds_read_b64_tr_b16 a[222:223], v131 offset:28672\n\t"
    "s_waitcnt lgkmcnt(8)\n\t"
    "v_mfma_f32_16x16x32_bf16 v[152:155], a[32:35], a[0:3], 0\n\t"
    "ds_read_b128 a[224:227], v142 offset:8192\n\t"
    "v_mfma_f32_16x16x32_bf16 v[152:155], a[36:39], a[4:7], v[152:155]\n\t"
    "ds_read_b128 a[228:231], v142 offset:9216\n\t"
    "v_mfma_f32_16x16x32_bf16 v[152:155], a[48:51], a[8:11], v[152:155]\n\t"
    "ds_read_b128 a[232:235], v142 offset:10240\n\t"
    "v_mfma_f32_16x16x32_bf16 v[152:155], a[52:55], a[12:15], v[152:155]\n\t"
    "ds_read_b128 a[236:239], v142 offset:11264\n\t"
    "v_mfma_f32_16x16x32_bf16 v[152:155], a[64:67], a[16:19], v[152:155]\n\t"
    "ds_read_b128 a[240:243], v142 offset:12288\n\t"
    "v_mfma_f32_16x16x32_bf16 v[152:155], a[68:71], a[20:23], v[152:155]\n\t"
    "ds_read_b128 a[244:247], v142 offset:13312\n\t"
    "v_mfma_f32_16x16x32_bf16 v[152:155], a[80:83], a[24:27], v[152:155]\n\t"
    "ds_read_b128 a[248:251], v142 offset:14336\n\t"
    "v_mfma_f32_16x16x32_bf16 v[152:155], a[84:87], a[28:31], v[152:155]\n\t"
    "ds_read_b128 a[252:255], v142 offset:15360\n\t"
    "s_waitcnt vmcnt(20)\n\t"
    "s_lshl_b32 s60, s96, 2\n\t"
    "s_add_u32 s60, s60, s40\n\t"
    "s_lshl_b32 exec_lo, 1, s60\n\t"
    "s_mov_b32 exec_hi, 0\n\t"
    "v_add_u32 v139, 2, v211\n\t"
    "buffer_store_dword v139, v137, s[12:15], 0 offen sc1\n\t"
    "s_mov_b64 exec, -1\n\t"
    "v_mfma_f32_16x16x32_bf16 v[156:159], a[92:95], a[28:31], 0\n\t"
    "s_add_u32 m0, s90, 0\n\t"
    "v_mfma_f32_16x16x32_bf16 v[156:159], a[88:91], a[24:27], v[156:159]\n\t"
    "s_add_u32 s48, s48, 28672\n\t"
    "v_mfma_f32_16x16x32_bf16 v[156:159], a[76:79], a[20:23], v[156:159]\n\t"
    "buffer_load_dwordx4 v196, s[20:23], s48 offen offset:0 sc1 lds\n\t"
    "v_mfma_f32_16x16x32_bf16 v[156:159], a[72:75], a[16:19], v[156:159]\n\t"
    "v_mfma_f32_16x16x32_bf16 v[156:159], a[60:63], a[12:15], v[156:159]\n\t"
    "buffer_load_dwordx4 v196, s[20:23], s48 offen offset:1024 sc1 lds\n\t"
    "v_mfma_f32_16x16x32_bf16 v[156:159], a[56:59], a[8:11], v[156:159]\n\t"
    "v_mfma_f32_16x16x32_bf16 v[156:159], a[44:47], a[4:7], v[156:159]\n\t"
    "buffer_load_dwordx4 v196, s[20:23], s48 offen offset:2048 sc1 lds\n\t"
    "v_mfma_f32_16x16x32_bf16 v[156:159], a[40:43], a[0:3], v[156:159]\n\t"
    "s_waitcnt lgkmcnt(0)\n\t"
    "v_mfma_f32_16x16x32_bf16 v[156:159], v[8:11], a[224:227], v[156:159]\n\t"
    "ds_read_b128 a[0:3], v142 offset:16384\n\t"
    "v_mfma_f32_16x16x32_bf16 v[156:159], v[12:15], a[228:231], v[156:159]\n\t"
    "ds_read_b128 a[4:7], v142 offset:17408\n\t"
    "v_mfma_f32_16x16x32_bf16 v[156:159], v[40:43], a[232:235], v[156:159]\n\t"
    "ds_read_b128 a[8:11], v142 offset:18432\n\t"
    "v_mfma_f32_16x16x32_bf16 v[156:159], v[44:47], a[236:239], v[156:159]\n\t"
    "ds_read_b128 a[12:15], v142 offset:19456\n\t"
    "v_mfma_f32_16x16x32_bf16 v[156:159], v[72:75], a[240:243], v[156:159]\n\t"
    "ds_read_b128 a[16:19], v142 offset:20480\n\t"
    "v_mfma_f32_16x16x32_bf16 v[156:159], v[76:79], a[244:247], v[156:159]\n\t"
    "ds_read_b128 a[20:23], v142 offset:21504\n\t"
    "v_mfma_f32_16x16x32_bf16 v[156:159], v[104:107], a[248:251], v[156:159]\n\t"
    "ds_read_b128 a[24:27], v142 offset:22528\n\t"
    "v_mfma_f32_16x16x32_bf16 v[156:159], v[108:111], a[252:255], v[156:159]\n\t"
    "ds_read_b128 a[28:31], v142 offset:23552\n\t"
    "v_mfma_f32_16x16x32_bf16 v[152:155], v[100:103], a[252:255], v[152:155]\n\t"
    "v_mfma_f32_16x16x32_bf16 v[152:155], v[96:99], a[248:251], v[152:155]\n\t"
    "buffer_load_dwordx4 v196, s[20:23], s48 offen offset:3072 sc1 lds\n\t"
    "v_mfma_f32_16x16x32_bf16 v[152:155], v[68:71], a[244:247], v[152:155]\n\t"
    "s_add_u32 m0, s90, 4096\n\t"
    "v_mfma_f32_16x16x32_bf16 v[152:155], v[64:67], a[240:243], v[152:155]\n\t"
    "v_mfma_f32_16x16x32_bf16 v[152:155], v[36:39], a[236:239], v[152:155]\n\t"
    "s_add_u32 s48, s48, 4096\n\t"
    "v_mfma_f32_16x16x32_bf16 v[152:155], v[32:35], a[232:235], v[152:155]\n\t"
    "buffer_load_dwordx4 v196, s[20:23], s48 offen offset:0 sc1 lds\n\t"
    "v_mfma_f32_16x16x32_bf16 v[152:155], v[4:7], a[228:231], v[152:155]\n\t"
    "buffer_load_dwordx4 v196, s[20:23], s48 offen offset:1024 sc1 lds\n\t"
    "v_mfma_f32_16x16x32_bf16 v[152:155], v[0:3], a[224:227], v[152:155]\n\t"
    "s_waitcnt lgkmcnt(0)\n\t"
    "v_mfma_f32_16x16x32_bf16 v[152:155], a[96:99], a[0:3], v[152:155]\n\t"
    "ds_read_b128 a[224:227], v142 offset:24576\n\t"
    "v_mfma_f32_16x16x32_bf16 v[152:155], a[100:103], a[4:7], v[152:155]\n\t"
    "ds_read_b128 a[228:231], v142 offset:25600\n\t"
    "v_mfma_f32_16x16x32_bf16 v[152:155], a[112:115], a[8:11], v[152:155]\n\t"
    "ds_read_b128 a[232:235], v142 offset:26624\n\t"
    "v_mfma_f32_16x16x32_bf16 v[152:155], a[116:119], a[12:15], v[152:155]\n\t"
    "ds_read_b128 a[236:239], v142 offset:27648\n\t"
    "v_mfma_f32_16x16x32_bf16 v[152:155], a[128:131], a[16:19], v[152:155]\n\t"
    "ds_read_b128 a[240:243], v142 offset:28672\n\t"
    "v_mfma_f32_16x16x32_bf16 v[152:155], a[132:135], a[20:23], v[152:155]\n\t"
    "ds_read_b128 a[244:247], v142 offset:29696\n\t"
    "v_mfma_f32_16x16x32_bf16 v[152:155], a[144:147], a[24:27], v[152:155]\n\t"
    "ds_read_b128 a[248:251], v142 offset:30720\n\t"
    "v_mfma_f32_16x16x32_bf16 v[152:155], a[148:151], a[28:31], v[152:155]\n\t"
    "ds_read_b128 a[252:255], v142 offset:31744\n\t"
    "v_mfma_f32_16x16x32_bf16 v[156:159], a[156:159], a[28:31], v[156:159]\n\t"
    "v_mfma_f32_16x16x32_bf16 v[156:159], a[152:155], a[24:27], v[156:159]\n\t"
    "buffer_load_dwordx4 v196, s[20:23], s48 offen offset:2048 sc1 lds\n\t"
    "v_mfma_f32_16x16x32_bf16 v[156:159], a[140:143], a[20:23], v[156:159]\n\t"
    "v_mfma_f32_16x16x32_bf16 v[156:159], a[136:139], a[16:19], v[156:159]\n\t"
    "buffer_load_dwordx4 v196, s[20:23], s48 offen offset:3072 sc1 lds\n\t"
    "v_mfma_f32_16x16x32_bf16 v[156:159], a[124:127], a[12:15], v[156:159]\n\t"
    "v_mfma_f32_16x16x32_bf16 v[156:159], a[120:123], a[8:11], v[156:159]\n\t"
    "v_mfma_f32_16x16x32_bf16 v[156:159], a[108:111], a[4:7], v[156:159]\n\t"
    "s_waitcnt vmcnt(9) lgkmcnt(0)\n\t"
    "v_mfma_f32_16x16x32_bf16 v[156:159], a[104:107], a[0:3], v[156:159]\n\t"
    "s_barrier\n\t"
    "v_mfma_f32_16x16x32_bf16 v[156:159], a[168:171], a[224:227], v[156:159]\n\t"
    "ds_read_b128 a[0:3], v143 offset:0\n\t"
    "v_mfma_f32_16x16x32_bf16 v[156:159], a[172:175], a[228:231], v[156:159]\n\t"
    "ds_read_b128 a[4:7], v143 offset:1024\n\t"
    "v_mfma_f32_16x16x32_bf16 v[156:159], a[184:187], a[232:235], v[156:159]\n\t"
    "ds_read_b128 a[8:11], v143 offset:2048\n\t"
    "v_mfma_f32_16x16x32_bf16 v[156:159], a[188:191], a[236:239], v[156:159]\n\t"
    "ds_read_b128 a[12:15], v143 offset:3072\n\t"
    "v_mfma_f32_16x16x32_bf16 v[156:159], a[200:203], a[240:243], v[156:159]\n\t"
    "ds_read_b128 a[16:19], v143 offset:4096\n\t"
    "v_mfma_f32_16x16x32_bf16 v[156:159], a[204:207], a[244:247], v[156:159]\n\t"
    "ds_read_b128 a[20:23], v143 offset:5120\n\t"
    "v_mfma_f32_16x16x32_bf16 v[156:159], a[216:219], a[248:251], v[156:159]\n\t"
    "ds_read_b128 a[24:27], v143 offset:6144\n\t"
    "v_mfma_f32_16x16x32_bf16 v[156:159], a[220:223], a[252:255], v[156:159]\n\t"
    "ds_read_b128 a[28:31], v143 offset:7168\n\t"
    "v_mfma_f32_16x16x32_bf16 v[152:155], a[212:215], a[252:255], v[152:155]\n\t"
    "v_mfma_f32_16x16x32_bf16 v[152:155], a[208:211], a[248:251], v[152:155]\n\t"
    "v_mfma_f32_16x16x32_bf16 v[152:155], a[196:199], a[244:247], v[152:155]\n\t"
    "v_mfma_f32_16x16x32_bf16 v[152:155], a[192:195], a[240:243], v[152:155]\n\t"
    "v_mfma_f32_16x16x32_bf16 v[152:155], a[180:183], a[236:239], v[152:155]\n\t"
    "v_mfma_f32_16x16x32_bf16 v[152:155], a[176:179], a[232:235], v[152:155]\n\t"
    "v_mfma_f32_16x16x32_bf16 v[152:155], a[164:167], a[228:231], v[152:155]\n\t"
    "v_mfma_f32_16x16x32_bf16 v[152:155], a[160:163], a[224:227], v[152:155]\n\t"
    "s_waitcnt lgkmcnt(0)\n\t"
    "v_mfma_f32_16x16x32_bf16 v[144:147], a[32:35], a[0:3], 0\n\t"
    "s_lshl_b32 s9, s47, 2\n\t"
    "ds_read_b128 a[224:227], v143 offset:8192\n\t"
    "v_cvt_pk_bf16_f32 v194, v156, v157\n\t"
    "v_mfma_f32_16x16x32_bf16 v[144:147], a[36:39], a[4:7], v[144:147]\n\t"
    "s_add_u32 s9, s9, s95\n\t"
    "ds_read_b128 a[228:231], v143 offset:9216\n\t"
    "v_cvt_pk_bf16_f32 v195, v158, v159\n\t"
    "v_mfma_f32_16x16x32_bf16 v[144:147], a[48:51], a[8:11], v[144:147]\n\t"
    "s_mul_i32 s9, s9, 4096\n\t"
    "ds_read_b128 a[232:235], v143 offset:10240\n\t"
    "v_mfma_f32_16x16x32_bf16 v[144:147], a[52:55], a[12:15], v[144:147]\n\t"
    "s_add_u32 s47, s47, 1\n\t"
    "ds_read_b128 a[236:239], v143 offset:11264\n\t"
    "v_mfma_f32_16x16x32_bf16 v[144:147], a[64:67], a[16:19], v[144:147]\n\t"
    "s_cmp_eq_u32 s47, s46\n\t"
    "ds_read_b128 a[240:243], v143 offset:12288\n\t"
    "v_cvt_pk_bf16_f32 v192, v152, v153\n\t"
    "v_mfma_f32_16x16x32_bf16 v[144:147], a[68:71], a[20:23], v[144:147]\n\t"
    "s_cmov_b32 s47, 0\n\t"
    "ds_read_b128 a[244:247], v143 offset:13312\n\t"
    "v_cvt_pk_bf16_f32 v193, v154, v155\n\t"
    "v_mfma_f32_16x16x32_bf16 v[144:147], a[80:83], a[24:27], v[144:147]\n\t"
    "ds_read_b128 a[248:251], v143 offset:14336\n\t"
    "v_mfma_f32_16x16x32_bf16 v[144:147], a[84:87], a[28:31], v[144:147]\n\t"
    "buffer_store_dwordx4 v[192:195], v196, s[68:71], s9 offen sc1\n\t"
    "s_mov_b32 exec_hi, 0\n\t"
    "buffer_load_dword v138, v137, s[12:15], 0 offen sc1\n\t"
    "s_mov_b64 exec, -1\n\t"
    "v_mfma_f32_16x16x32_bf16 v[148:151], a[92:95], a[28:31], 0\n\t"
    "s_add_u32 m0, s91, 0\n\t"
    "ds_read_b128 a[252:255], v143 offset:15360\n\t"
    "v_mfma_f32_16x16x32_bf16 v[148:151], a[88:91], a[24:27], v[148:151]\n\t"
    "s_add_u32 s48, s48, 28672\n\t"
    "v_mfma_f32_16x16x32_bf16 v[148:151], a[76:79], a[20:23], v[148:151]\n\t"
    "buffer_load_dwordx4 v196, s[20:23], s48 offen offset:0 sc1 lds\n\t"
    "v_mfma_f32_16x16x32_bf16 v[148:151], a[72:75], a[16:19], v[148:151]\n\t"
    "v_mfma_f32_16x16x32_bf16 v[148:151], a[60:63], a[12:15], v[148:151]\n\t"
    "buffer_load_dwordx4 v196, s[20:23], s48 offen offset:1024 sc1 lds\n\t"
    "v_mfma_f32_16x16x32_bf16 v[148:151], a[56:59], a[8:11], v[148:151]\n\t"
    "v_mfma_f32_16x16x32_bf16 v[148:151], a[44:47], a[4:7], v[148:151]\n\t"
    "buffer_load_dwordx4 v196, s[20:23], s48 offen offset:2048 sc1 lds\n\t"
    "v_mfma_f32_16x16x32_bf16 v[148:151], a[40:43], a[0:3], v[148:151]\n\t"
    "s_waitcnt lgkmcnt(0)\n\t"
    "v_mfma_f32_16x16x32_bf16 v[148:151], v[8:11], a[224:227], v[148:151]\n\t"
    "ds_read_b128 a[0:3], v143 offset:16384\n\t"
    "v_mfma_f32_16x16x32_bf16 v[148:151], v[12:15], a[228:231], v[148:151]\n\t"
    "ds_read_b128 a[4:7], v143 offset:17408\n\t"
    "v_mfma_f32_16x16x32_bf16 v[148:151], v[40:43], a[232:235], v[148:151]\n\t"
    "ds_read_b128 a[8:11], v143 offset:18432\n\t"
    "v_mfma_f32_16x16x32_bf16 v[148:151], v[44:47], a[236:239], v[148:151]\n\t"
    "ds_read_b128 a[12:15], v143 offset:19456\n\t"
    "v_mfma_f32_16x16x32_bf16 v[148:151], v[72:75], a[240:243], v[148:151]\n\t"
    "ds_read_b128 a[16:19], v143 offset:20480\n\t"
    "v_mfma_f32_16x16x32_bf16 v[148:151], v[76:79], a[244:247], v[148:151]\n\t"
    "ds_read_b128 a[20:23], v143 offset:21504\n\t"
    "v_mfma_f32_16x16x32_bf16 v[148:151], v[104:107], a[248:251], v[148:151]\n\t"
    "ds_read_b128 a[24:27], v143 offset:22528\n\t"
    "v_mfma_f32_16x16x32_bf16 v[148:151], v[108:111], a[252:255], v[148:151]\n\t"
    "ds_read_b128 a[28:31], v143 offset:23552\n\t"
    "v_mfma_f32_16x16x32_bf16 v[144:147], v[100:103], a[252:255], v[144:147]\n\t"
    "v_mfma_f32_16x16x32_bf16 v[144:147], v[96:99], a[248:251], v[144:147]\n\t"
    "buffer_load_dwordx4 v196, s[20:23], s48 offen offset:3072 sc1 lds\n\t"
    "v_mfma_f32_16x16x32_bf16 v[144:147], v[68:71], a[244:247], v[144:147]\n\t"
    "s_add_u32 m0, s91, 4096\n\t"
    "v_mfma_f32_16x16x32_bf16 v[144:147], v[64:67], a[240:243], v[144:147]\n\t"
    "v_mfma_f32_16x16x32_bf16 v[144:147], v[36:39], a[236:239], v[144:147]\n\t"
    "s_add_u32 s48, s48, 4096\n\t"
    "v_mfma_f32_16x16x32_bf16 v[144:147], v[32:35], a[232:235], v[144:147]\n\t"
    "buffer_load_dwordx4 v196, s[20:23], s48 offen offset:0 sc1 lds\n\t"
    "v_mfma_f32_16x16x32_bf16 v[144:147], v[4:7], a[228:231], v[144:147]\n\t"
    "buffer_load_dwordx4 v196, s[20:23], s48 offen offset:1024 sc1 lds\n\t"
    "v_mfma_f32_16x16x32_bf16 v[144:147], v[0:3], a[224:227], v[144:147]\n\t"
    "s_waitcnt lgkmcnt(0)\n\t"
    "v_mfma_f32_16x16x32_bf16 v[144:147], a[96:99], a[0:3], v[144:147]\n\t"
    "ds_read_b128 a[224:227], v143 offset:24576\n\t"
    "v_mfma_f32_16x16x32_bf16 v[144:147], a[100:103], a[4:7], v[144:147]\n\t"
    "ds_read_b128 a[228:231], v143 offset:25600\n\t"
    "v_mfma_f32_16x16x32_bf16 v[144:147], a[112:115], a[8:11], v[144:147]\n\t"
    "ds_read_b128 a[232:235], v143 offset:26624\n\t"
    "v_mfma_f32_16x16x32_bf16 v[144:147], a[116:119], a[12:15], v[144:147]\n\t"
    "ds_read_b128 a[236:239], v143 offset:27648\n\t"
    "v_mfma_f32_16x16x32_bf16 v[144:147], a[128:131], a[16:19], v[144:147]\n\t"
    "ds_read_b128 a[240:243], v143 offset:28672\n\t"
    "v_mfma_f32_16x16x32_bf16 v[144:147], a[132:135], a[20:23], v[144:147]\n\t"
    "ds_read_b128 a[244:247], v143 offset:29696\n\t"
    "v_mfma_f32_16x16x32_bf16 v[144:147], a[144:147], a[24:27], v[144:147]\n\t"
    "ds_read_b128 a[248:251], v143 offset:30720\n\t"
    "v_mfma_f32_16x16x32_bf16 v[144:147], a[148:151], a[28:31], v[144:147]\n\t"
    "ds_read_b128 a[252:255], v143 offset:31744\n\t"
    "v_mfma_f32_16x16x32_bf16 v[148:151], a[156:159], a[28:31], v[148:151]\n\t"
    "v_mfma_f32_16x16x32_bf16 v[148:151], a[152:155], a[24:27], v[148:151]\n\t"
    "buffer_load_dwordx4 v196, s[20:23], s48 offen offset:2048 sc1 lds\n\t"
    "v_mfma_f32_16x16x32_bf16 v[148:151], a[140:143], a[20:23], v[148:151]\n\t"
    "v_mfma_f32_16x16x32_bf16 v[148:151], a[136:139], a[16:19], v[148:151]\n\t"
    "buffer_load_dwordx4 v196, s[20:23], s48 offen offset:3072 sc1 lds\n\t"
    "v_mfma_f32_16x16x32_bf16 v[148:151], a[124:127], a[12:15], v[148:151]\n\t"
    "v_mfma_f32_16x16x32_bf16 v[148:151], a[120:123], a[8:11], v[148:151]\n\t"
    "v_mfma_f32_16x16x32_bf16 v[148:151], a[108:111], a[4:7], v[148:151]\n\t"
    "s_waitcnt vmcnt(10) lgkmcnt(0)\n\t"
    "v_mfma_f32_16x16x32_bf16 v[148:151], a[104:107], a[0:3], v[148:151]\n\t"
    "s_barrier\n\t"
    "v_mfma_f32_16x16x32_bf16 v[148:151], a[168:171], a[224:227], v[148:151]\n\t"
    "ds_read_b128 a[0:3], v141 offset:0\n\t"
    "v_mfma_f32_16x16x32_bf16 v[148:151], a[172:175], a[228:231], v[148:151]\n\t"
    "ds_read_b128 a[4:7], v141 offset:1024\n\t"
    "v_mfma_f32_16x16x32_bf16 v[148:151], a[184:187], a[232:235], v[148:151]\n\t"
    "ds_read_b128 a[8:11], v141 offset:2048\n\t"
    "v_mfma_f32_16x16x32_bf16 v[148:151], a[188:191], a[236:239], v[148:151]\n\t"
    "ds_read_b128 a[12:15], v141 offset:3072\n\t"
    "v_mfma_f32_16x16x32_bf16 v[148:151], a[200:203], a[240:243], v[148:151]\n\t"
    "ds_read_b128 a[16:19], v141 offset:4096\n\t"
    "v_mfma_f32_16x16x32_bf16 v[148:151], a[204:207], a[244:247], v[148:151]\n\t"
    "ds_read_b128 a[20:23], v141 offset:5120\n\t"
    "v_mfma_f32_16x16x32_bf16 v[148:151], a[216:219], a[248:251], v[148:151]\n\t"
    "ds_read_b128 a[24:27], v141 offset:6144\n\t"
    "v_mfma_f32_16x16x32_bf16 v[148:151], a[220:223], a[252:255], v[148:151]\n\t"
    "ds_read_b128 a[28:31], v141 offset:7168\n\t"
    "v_mfma_f32_16x16x32_bf16 v[144:147], a[212:215], a[252:255], v[144:147]\n\t"
    "v_mfma_f32_16x16x32_bf16 v[144:147], a[208:211], a[248:251], v[144:147]\n\t"
    "v_mfma_f32_16x16x32_bf16 v[144:147], a[196:199], a[244:247], v[144:147]\n\t"
    "v_mfma_f32_16x16x32_bf16 v[144:147], a[192:195], a[240:243], v[144:147]\n\t"
    "v_mfma_f32_16x16x32_bf16 v[144:147], a[180:183], a[236:239], v[144:147]\n\t"
    "v_mfma_f32_16x16x32_bf16 v[144:147], a[176:179], a[232:235], v[144:147]\n\t"
    "v_mfma_f32_16x16x32_bf16 v[144:147], a[164:167], a[228:231], v[144:147]\n\t"
    "v_mfma_f32_16x16x32_bf16 v[144:147], a[160:163], a[224:227], v[144:147]\n\t"
    "s_waitcnt lgkmcnt(0)\n\t"
    "s_waitcnt vmcnt(8)\n\t"
    "s_mov_b32 exec_hi, 0\n\t"
    "L_chk1b1_%=:\n\t"
    "v_sub_u32 v138, v138, v211\n\t"
    "v_cmp_gt_u32 vcc, 2, v138\n\t"
    "s_cbranch_vccnz L_spin1b1_%=\n\t"
    "L_got1b1_%=:\n\t"
    "s_mov_b64 exec, -1\n\t"
    "s_branch L_rest_%=\n\t"
    "L_spin1aA_%=:\n\t"
    "s_sub_u32 s57, s57, 1\n\t"
    "s_cbranch_scc1 L_got1aA_%=\n\t"
    "s_sleep 1\n\t"
    "buffer_load_dword v138, v137, s[12:15], 0 offen sc1\n\t"
    "s_waitcnt vmcnt(0)\n\t"
    "s_branch L_chk1aA_%=\n\t"
    "L_spin1aB_%=:\n\t"
    "s_sub_u32 s57, s57, 1\n\t"
    "s_cbranch_scc1 L_got1aB_%=\n\t"
    "s_sleep 1\n\t"
    "buffer_load_dword v138, v137, s[12:15], 0 offen sc1\n\t"
    "s_waitcnt vmcnt(0)\n\t"
    "s_branch L_chk1aB_%=\n\t"
    "L_spin1b0_%=:\n\t"
    "s_sub_u32 s57, s57, 1\n\t"
    "s_cbranch_scc1 L_got1b0_%=\n\t"
    "s_sleep 1\n\t"
    "buffer_load_dword v138, v137, s[12:15], 0 offen sc1\n\t"
    "s_waitcnt vmcnt(0)\n\t"
    "s_branch L_chk1b0_%=\n\t"
    "L_spin1b1_%=:\n\t"
    "s_sub_u32 s57, s57, 1\n\t"
    "s_cbranch_scc1 L_got1b1_%=\n\t"
    "s_sleep 1\n\t"
    "buffer_load_dword v138, v137, s[12:15], 0 offen sc1\n\t"
    "s_waitcnt vmcnt(0)\n\t"
    "s_branch L_chk1b1_%=\n\t"
    "L_spin2a_%=:\n\t"
    "s_sub_u32 s57, s57, 1\n\t"
    "s_cbranch_scc1 L_got2a_%=\n\t"
    "s_sleep 1\n\t"
    "buffer_load_dword v138, v137, s[12:15], 0 offen sc1\n\t"
    "s_waitcnt vmcnt(0)\n\t"
    "s_branch L_chk2a_%=\n\t"
    "L_spin2b_%=:\n\t"
    "s_sub_u32 s57, s57, 1\n\t"
    "s_cbranch_scc1 L_got2b_%=\n\t"
    "s_sleep 1\n\t"
    "buffer_load_dword v138, v137, s[12:15], 0 offen sc1\n\t"
    "s_waitcnt vmcnt(0)\n\t"
    "s_branch L_chk2b_%=\n\t"
    "L_spin2c_%=:\n\t"
    "s_sub_u32 s57, s57, 1\n\t"
    "s_cbranch_scc1 L_got2c_%=\n\t"
    "s_sleep 1\n\t"
    "buffer_load_dword v138, v137, s[12:15], 0 offen sc1\n\t"
    "s_waitcnt vmcnt(0)\n\t"
    "s_branch L_chk2c_%=\n\t"
    "L_again_%=:\n\t"
    "s_waitcnt vmcnt(0) lgkmcnt(0)\n\t"
    "s_nop 7\n\t"
    "s_barrier\n\t"
    "s_mov_b32 s48, 0\n\t"
    "s_branch L_start_%=\n\t"
    ".p2align 6\n\t"
    "L_w3_%=:\n\t"
    "s_waitcnt vmcnt(16) lgkmcnt(0)\n\t"
    "s_barrier\n\t"
    "ds_read_b128 v[184:187], v184\n\t"
    "ds_read_b64_tr_b16 a[0:1], v244 offset:32768\n\t"
    "ds_read_b64_tr_b16 a[2:3], v244 offset:36864\n\t"
    "ds_read_b64_tr_b16 a[4:5], v244 offset:40960\n\t"
    "ds_read_b64_tr_b16 a[6:7], v244 offset:45056\n\t"
    "ds_read_b64_tr_b16 a[8:9], v244 offset:49152\n\t"
    "ds_read_b64_tr_b16 a[10:11], v244 offset:53248\n\t"
    "ds_read_b64_tr_b16 a[12:13], v244 offset:57344\n\t"
    "ds_read_b64_tr_b16 a[14:15], v244 offset:61440\n\t"
    "ds_read_b64_tr_b16 a[28:29], v245 offset:57344\n\t"
    "ds_read_b64_tr_b16 a[30:31], v245 offset:61440\n\t"
    "ds_read_b64_tr_b16 a[24:25], v245 offset:49152\n\t"
    "ds_read_b64_tr_b16 a[26:27], v245 offset:53248\n\t"
    "ds_read_b64_tr_b16 a[20:21], v245 offset:40960\n\t"
    "ds_read_b64_tr_b16 a[22:23], v245 offset:45056\n\t"
    "ds_read_b64_tr_b16 a[16:17], v245 offset:32768\n\t"
    "ds_read_b64_tr_b16 a[18:19], v245 offset:36864\n\t"
    "s_waitcnt vmcnt(12)\n\t"
    "ds_read_b128 v[212:215], v246 offset:0\n\t"
    "ds_read_b128 v[216:219], v246 offset:512\n\t"
    "ds_read_b128 v[220:223], v246 offset:1024\n\t"
    "ds_read_b128 v[224:227], v246 offset:1536\n\t"
    "s_waitcnt lgkmcnt(0)\n\t"
    "v_mfma_f32_16x16x32_bf16 v[0:3], a[0:3], v[212:215], 0\n\t"
    "ds_read_b64_tr_b16 a[32:33], v244 offset:33280\n\t"
    "ds_read_b64_tr_b16 a[34:35], v244 offset:37376\n\t"
    "v_mfma_f32_16x16x32_bf16 v[0:3], a[4:7], v[216:219], v[0:3]\n\t"
    "ds_read_b64_tr_b16 a[36:37], v244 offset:41472\n\t"
    "ds_read_b64_tr_b16 a[38:39], v244 offset:45568\n\t"
    "v_mfma_f32_16x16x32_bf16 v[0:3], a[8:11], v[220:223], v[0:3]\n\t"
    "s_waitcnt lgkmcnt(8)\n\t"
    "ds_read_b64_tr_b16 a[40:41], v244 offset:49664\n\t"
    "ds_read_b64_tr_b16 a[42:43], v244 offset:53760\n\t"
    "v_mfma_f32_16x16x32_bf16 v[0:3], a[12:15], v[224:227], v[0:3]\n\t"
    "ds_read_b64_tr_b16 a[44:45], v244 offset:57856\n\t"
    "ds_read_b64_tr_b16 a[46:47], v244 offset:61952\n\t"
    "v_mfma_f32_16x16x32_bf16 v[4:7], a[28:31], v[224:227], 0\n\t"
    "ds_read_b64_tr_b16 a[60:61], v245 offset:57856\n\t"
    "ds_read_b64_tr_b16 a[62:63], v245 offset:61952\n\t"
    "v_mfma_f32_16x16x32_bf16 v[4:7], a[24:27], v[220:223], v[4:7]\n\t"
    "ds_read_b64_tr_b16 a[56:57], v245 offset:49664\n\t"
    "ds_read_b64_tr_b16 a[58:59], v245 offset:53760\n\t"
    "v_mfma_f32_16x16x32_bf16 v[4:7], a[20:23], v[216:219], v[4:7]\n\t"
    "s_waitcnt lgkmcnt(4)\n\t"
    "ds_read_b64_tr_b16 a[52:53], v245 offset:41472\n\t"
    "ds_read_b64_tr_b16 a[54:55], v245 offset:45568\n\t"
    "v_mfma_f32_16x16x32_bf16 v[4:7], a[16:19], v[212:215], v[4:7]\n\t"
    "ds_read_b64_tr_b16 a[48:49], v245 offset:33280\n\t"
    "ds_read_b64_tr_b16 a[50:51], v245 offset:37376\n\t"
    "v_mfma_f32_16x16x32_bf16 v[8:11], a[32:35], v[212:215], 0\n\t"
    "s_waitcnt vmcnt(6)\n\t"
    "s_barrier\n\t"
    "ds_read_b64_tr_b16 a[64:65], v244 offset:33792\n\t"
    "ds_read_b64_tr_b16 a[66:67], v244 offset:37888\n\t"
    "v_mfma_f32_16x16x32_bf16 v[8:11], a[36:39], v[216:219], v[8:11]\n\t"
    "ds_read_b64_tr_b16 a[68:69], v244 offset:41984\n\t"
    "ds_read_b64_tr_b16 a[70:71], v244 offset:46080\n\t"
    "v_mfma_f32_16x16x32_bf16 v[8:11], a[40:43], v[220:223], v[8:11]\n\t"
    "s_waitcnt lgkmcnt(4)\n\t"
    "ds_read_b64_tr_b16 a[72:73], v244 offset:50176\n\t"
    "ds_read_b64_tr_b16 a[74:75], v244 offset:54272\n\t"
    "v_mfma_f32_16x16x32_bf16 v[8:11], a[44:47], v[224:227], v[8:11]\n\t"
    "s_add_u32 s63, s92, 0\n\t"
    "ds_read_b64_tr_b16 a[76:77], v244 offset:58368\n\t"
    "ds_read_b64_tr_b16 a[78:79], v244 offset:62464\n\t"
    "v_mfma_f32_16x16x32_bf16 v[12:15], a[60:63], v[224:227], 0\n\t"
    "s_add_u32 m0, s52, 8192\n\t"
    "ds_read_b64_tr_b16 a[92:93], v245 offset:58368\n\t"
    "ds_read_b64_tr_b16 a[94:95], v245 offset:62464\n\t"
    "v_mfma_f32_16x16x32_bf16 v[12:15], a[56:59], v[220:223], v[12:15]\n\t"
    "buffer_load_dwordx4 v255, s[20:23], s63 offen offset:0 lds\n\t"
    "buffer_load_dwordx4 v201, s[20:23], s63 offen offset:2048 lds\n\t"
    "ds_read_b64_tr_b16 a[88:89], v245 offset:50176\n\t"
    "ds_read_b64_tr_b16 a[90:91], v245 offset:54272\n\t"
    "v_mfma_f32_16x16x32_bf16 v[12:15], a[52:55], v[216:219], v[12:15]\n\t"
    "s_waitcnt lgkmcnt(4)\n\t"
    "ds_read_b64_tr_b16 a[84:85], v245 offset:41984\n\t"
    "ds_read_b64_tr_b16 a[86:87], v245 offset:46080\n\t"
    "v_mfma_f32_16x16x32_bf16 v[12:15], a[48:51], v[212:215], v[12:15]\n\t"
    "ds_read_b64_tr_b16 a[80:81], v245 offset:33792\n\t"
    "ds_read_b64_tr_b16 a[82:83], v245 offset:37888\n\t"
    "v_mfma_f32_16x16x32_bf16 v[16:19], a[64:67], v[212:215], 0\n\t"
    "ds_read_b64_tr_b16 a[96:97], v244 offset:34304\n\t"
    "ds_read_b64_tr_b16 a[98:99], v244 offset:38400\n\t"
    "v_mfma_f32_16x16x32_bf16 v[16:19], a[68:71], v[216:219], v[16:19]\n\t"
    "ds_read_b64_tr_b16 a[100:101], v244 offset:42496\n\t"
    "ds_read_b64_tr_b16 a[102:103], v244 offset:46592\n\t"
    "v_mfma_f32_16x16x32_bf16 v[16:19], a[72:75], v[220:223], v[16:19]\n\t"
    "s_waitcnt lgkmcnt(4)\n\t"
    "ds_read_b64_tr_b16 a[104:105], v244 offset:50688\n\t"
    "ds_read_b64_tr_b16 a[106:107], v244 offset:54784\n\t"
    "v_mfma_f32_16x16x32_bf16 v[16:19], a[76:79], v[224:227], v[16:19]\n\t"
    "ds_read_b64_tr_b16 a[108:109], v244 offset:58880\n\t"
    "ds_read_b64_tr_b16 a[110:111], v244 offset:62976\n\t"
    "v_mfma_f32_16x16x32_bf16 v[20:23], a[92:95], v[224:227], 0\n\t"
    "s_add_u32 m0, s52, 9088\n\t"
    "ds_read_b64_tr_b16 a[124:125], v245 offset:58880\n\t"
    "ds_read_b64_tr_b16 a[126:127], v245 offset:62976\n\t"
    "v_mfma_f32_16x16x32_bf16 v[20:23], a[88:91], v[220:223], v[20:23]\n\t"
    "buffer_load_dwordx4 v255, s[20:23], s63 offen offset:128 lds\n\t"
    "buffer_load_dwordx4 v201, s[20:23], s63 offen offset:2176 lds\n\t"
    "ds_read_b64_tr_b16 a[120:121], v245 offset:50688\n\t"
    "ds_read_b64_tr_b16 a[122:123], v245 offset:54784\n\t"
    "v_mfma_f32_16x16x32_bf16 v[20:23], a[84:87], v[216:219], v[20:23]\n\t"
    "s_waitcnt lgkmcnt(4)\n\t"
    "ds_read_b64_tr_b16 a[116:117], v245 offset:42496\n\t"
    "ds_read_b64_tr_b16 a[118:119], v245 offset:46592\n\t"
    "v_mfma_f32_16x16x32_bf16 v[20:23], a[80:83], v[212:215], v[20:23]\n\t"
    "s_waitcnt vmcnt(4)\n\t"
    "ds_read_b64_tr_b16 a[112:113], v245 offset:34304\n\t"
    "ds_read_b64_tr_b16 a[114:115], v245 offset:38400\n\t"
    "v_mfma_f32_16x16x32_bf16 v[24:27], a[96:99], v[212:215], 0\n\t"
    "ds_read_b128 v[228:231], v246 offset:4096\n\t"
    "ds_read_b128 v[232:235], v246 offset:4608\n\t"
    "v_mfma_f32_16x16x32_bf16 v[24:27], a[100:103], v[216:219], v[24:27]\n\t"
    "ds_read_b128 v[236:239], v246 offset:5120\n\t"
    "ds_read_b128 v[240:243], v246 offset:5632\n\t"
    "v_mfma_f32_16x16x32_bf16 v[24:27], a[104:107], v[220:223], v[24:27]\n\t"
    "s_waitcnt lgkmcnt(4)\n\t"
    "v_add3_u32 v184, v184, v185, v186\n\t"
    "v_add_u32 v184, v184, v187\n\t"
    "v_mfma_f32_16x16x32_bf16 v[24:27], a[108:111], v[224:227], v[24:27]\n\t"
    "s_mov_b32 s53, 1048576\n\t"
    "v_and_b32 v184, 6, v184\n\t"
    "v_cmp_ne_u32 vcc, 0, v184\n\t"
    "v_mfma_f32_16x16x32_bf16 v[28:31], a[124:127], v[224:227], 0\n\t"
    "s_add_u32 m0, s51, 0\n\t"
    "s_cbranch_vccnz L_again_%=\n\t"
    "v_mfma_f32_16x16x32_bf16 v[28:31], a[120:123], v[220:223], v[28:31]\n\t"
    "buffer_load_dwordx4 v248, s[24:27], s53 offen offset:0 lds\n\t"
    "buffer_load_dwordx4 v200, s[24:27], s53 offen offset:2048 lds\n\t"
    "v_mfma_f32_16x16x32_bf16 v[28:31], a[116:119], v[216:219], v[28:31]\n\t"
    "s_mov_b32 s53, 1081344\n\t"
    "s_add_u32 m0, s51, 4096\n\t"
    "v_mfma_f32_16x16x32_bf16 v[28:31], a[112:115], v[212:215], v[28:31]\n\t"
    "buffer_load_dwordx4 v248, s[24:27], s53 offen offset:0 lds\n\t"
    "buffer_load_dwordx4 v200, s[24:27], s53 offen offset:2048 lds\n\t"
    "s_waitcnt lgkmcnt(0)\n\t"
    "v_mfma_f32_16x16x32_bf16 v[32:35], a[0:3], v[228:231], 0\n\t"
    "s_add_u32 s63, s93, 0\n\t"
    "v_mfma_f32_16x16x32_bf16 v[32:35], a[4:7], v[232:235], v[32:35]\n\t"
    "s_add_u32 m0, s52, 12288\n\t"
    "v_mfma_f32_16x16x32_bf16 v[32:35], a[8:11], v[236:239], v[32:35]\n\t"
    "buffer_load_dwordx4 v255, s[20:23], s63 offen offset:0 lds\n\t"
    "v_mfma_f32_16x16x32_bf16 v[32:35], a[12:15], v[240:243], v[32:35]\n\t"
    "buffer_load_dwordx4 v201, s[20:23], s63 offen offset:2048 lds\n\t"
    "v_mfma_f32_16x16x32_bf16 v[36:39], a[28:31], v[240:243], 0\n\t"
    "v_mfma_f32_16x16x32_bf16 v[36:39], a[24:27], v[236:239], v[36:39]\n\t"
    "s_add_u32 m0, s52, 13184\n\t"
    "v_mfma_f32_16x16x32_bf16 v[36:39], a[20:23], v[232:235], v[36:39]\n\t"
    "buffer_load_dwordx4 v255, s[20:23], s63 offen offset:128 lds\n\t"
    "v_mfma_f32_16x16x32_bf16 v[36:39], a[16:19], v[228:231], v[36:39]\n\t"
    "buffer_load_dwordx4 v201, s[20:23], s63 offen offset:2176 lds\n\t"
    "v_mfma_f32_16x16x32_bf16 v[40:43], a[32:35], v[228:231], 0\n\t"
    "s_mov_b32 s53, 1048704\n\t"
    "v_mfma_f32_16x16x32_bf16 v[40:43], a[36:39], v[232:235], v[40:43]\n\t"
    "s_add_u32 m0, s51, 1024\n\t"
    "v_mfma_f32_16x16x32_bf16 v[40:43], a[40:43], v[236:239], v[40:43]\n\t"
    "buffer_load_dwordx4 v248, s[24:27], s53 offen offset:0 lds\n\t"
    "v_mfma_f32_16x16x32_bf16 v[40:43], a[44:47], v[240:243], v[40:43]\n\t"
    "buffer_load_dwordx4 v200, s[24:27], s53 offen offset:2048 lds\n\t"
    "v_mfma_f32_16x16x32_bf16 v[44:47], a[60:63], v[240:243], 0\n\t"
    "v_mfma_f32_16x16x32_bf16 v[44:47], a[56:59], v[236:239], v[44:47]\n\t"
    "v_mfma_f32_16x16x32_bf16 v[44:47], a[52:55], v[232:235], v[44:47]\n\t"
    "s_waitcnt vmcnt(10)\n\t"
    "ds_read_b128 v[212:215], v246 offset:8192\n\t"
    "v_mfma_f32_16x16x32_bf16 v[44:47], a[48:51], v[228:231], v[44:47]\n\t"
    "ds_read_b128 v[216:219], v246 offset:8704\n\t"
    "v_mfma_f32_16x16x32_bf16 v[48:51], a[64:67], v[228:231], 0\n\t"
    "ds_read_b128 v[220:223], v246 offset:9216\n\t"
    "v_mfma_f32_16x16x32_bf16 v[48:51], a[68:71], v[232:235], v[48:51]\n\t"
    "ds_read_b128 v[224:227], v246 offset:9728\n\t"
    "v_mfma_f32_16x16x32_bf16 v[48:51], a[72:75], v[236:239], v[48:51]\n\t"
    "s_mov_b32 s53, 1081472\n\t"
    "v_mfma_f32_16x16x32_bf16 v[48:51], a[76:79], v[240:243], v[48:51]\n\t"
    "s_add_u32 m0, s51, 5120\n\t"
    "v_mfma_f32_16x16x32_bf16 v[52:55], a[92:95], v[240:243], 0\n\t"
    "buffer_load_dwordx4 v248, s[24:27], s53 offen offset:0 lds\n\t"
    "v_mfma_f32_16x16x32_bf16 v[52:55], a[88:91], v[236:239], v[52:55]\n\t"
    "buffer_load_dwordx4 v200, s[24:27], s53 offen offset:2048 lds\n\t"
    "v_mfma_f32_16x16x32_bf16 v[52:55], a[84:87], v[232:235], v[52:55]\n\t"
    "s_add_u32 s63, s90, 256\n\t"
    "v_mfma_f32_16x16x32_bf16 v[52:55], a[80:83], v[228:231], v[52:55]\n\t"
    "s_add_u32 m0, s52, 0\n\t"
    "v_mfma_f32_16x16x32_bf16 v[56:59], a[96:99], v[228:231], 0\n\t"
    "buffer_load_dwordx4 v255, s[20:23], s63 offen offset:0 lds\n\t"
    "v_mfma_f32_16x16x32_bf16 v[56:59], a[100:103], v[232:235], v[56:59]\n\t"
    "buffer_load_dwordx4 v201, s[20:23], s63 offen offset:2048 lds\n\t"
    "v_mfma_f32_16x16x32_bf16 v[56:59], a[104:107], v[236:239], v[56:59]\n\t"
    "v_mfma_f32_16x16x32_bf16 v[56:59], a[108:111], v[240:243], v[56:59]\n\t"
    "s_add_u32 m0, s52, 896\n\t"
    "v_mfma_f32_16x16x32_bf16 v[60:63], a[124:127], v[240:243], 0\n\t"
    "buffer_load_dwordx4 v255, s[20:23], s63 offen offset:128 lds\n\t"
    "v_mfma_f32_16x16x32_bf16 v[60:63], a[120:123], v[236:239], v[60:63]\n\t"
    "buffer_load_dwordx4 v201, s[20:23], s63 offen offset:2176 lds\n\t"
    "v_mfma_f32_16x16x32_bf16 v[60:63], a[116:119], v[232:235], v[60:63]\n\t"
    "s_waitcnt lgkmcnt(0)\n\t"
    "v_mfma_f32_16x16x32_bf16 v[60:63], a[112:115], v[228:231], v[60:63]\n\t"
    "s_waitcnt vmcnt(12)\n\t"
    "s_barrier\n\t"
    "v_mfma_f32_16x16x32_bf16 v[64:67], a[0:3], v[212:215], 0\n\t"
    "s_add_u32 s63, s91, 256\n\t"
    "ds_read_b64_tr_b16 a[128:129], v244 offset:0\n\t"
    "ds_read_b64_tr_b16 a[130:131], v244 offset:4096\n\t"
    "v_mfma_f32_16x16x32_bf16 v[64:67], a[4:7], v[216:219], v[64:67]\n\t"
    "s_add_u32 m0, s52, 4096\n\t"
    "ds_read_b64_tr_b16 a[132:133], v244 offset:8192\n\t"
    "ds_read_b64_tr_b16 a[134:135], v244 offset:12288\n\t"
    "v_mfma_f32_16x16x32_bf16 v[64:67], a[8:11], v[220:223], v[64:67]\n\t"
    "buffer_load_dwordx4 v255, s[20:23], s63 offen offset:0 lds\n\t"
    "v_mfma_f32_16x16x32_bf16 v[64:67], a[12:15], v[224:227], v[64:67]\n\t"
    "buffer_load_dwordx4 v201, s[20:23], s63 offen offset:2048 lds\n\t"
    "v_mfma_f32_16x16x32_bf16 v[68:71], a[28:31], v[224:227], 0\n\t"
    "s_mov_b32 s31, 0x20000\n\t"
    "ds_read_b64_tr_b16 a[136:137], v244 offset:16384\n\t"
    "ds_read_b64_tr_b16 a[138:139], v244 offset:20480\n\t"
    "v_mfma_f32_16x16x32_bf16 v[68:71], a[24:27], v[220:223], v[68:71]\n\t"
    "s_add_u32 m0, s52, 4992\n\t"
    "ds_read_b64_tr_b16 a[140:141], v244 offset:24576\n\t"
    "ds_read_b64_tr_b16 a[142:143], v244 offset:28672\n\t"
    "v_mfma_f32_16x16x32_bf16 v[68:71], a[20:23], v[216:219], v[68:71]\n\t"
    "buffer_load_dwordx4 v255, s[20:23], s63 offen offset:128 lds\n\t"
    "v_mfma_f32_16x16x32_bf16 v[68:71], a[16:19], v[212:215], v[68:71]\n\t"
    "buffer_load_dwordx4 v201, s[20:23], s63 offen offset:2176 lds\n\t"
    "v_mfma_f32_16x16x32_bf16 v[72:75], a[32:35], v[212:215], 0\n\t"
    "s_add_u32 s63, s92, 256\n\t"
    "ds_read_b64_tr_b16 a[156:157], v245 offset:24576\n\t"
    "ds_read_b64_tr_b16 a[158:159], v245 offset:28672\n\t"
    "v_mfma_f32_16x16x32_bf16 v[72:75], a[36:39], v[216:219], v[72:75]\n\t"
    "s_add_u32 m0, s52, 8192\n\t"
    "ds_read_b64_tr_b16 a[152:153], v245 offset:16384\n\t"
    "ds_read_b64_tr_b16 a[154:155], v245 offset:20480\n\t"
    "v_mfma_f32_16x16x32_bf16 v[72:75], a[40:43], v[220:223], v[72:75]\n\t"
    "buffer_load_dwordx4 v255, s[20:23], s63 offen offset:0 lds\n\t"
    "v_mfma_f32_16x16x32_bf16 v[72:75], a[44:47], v[224:227], v[72:75]\n\t"
    "buffer_load_dwordx4 v201, s[20:23], s63 offen offset:2048 lds\n\t"
    "v_mfma_f32_16x16x32_bf16 v[76:79], a[60:63], v[224:227], 0\n\t"
    "s_mov_b32 s19, 0x20000\n\t"
    "ds_read_b64_tr_b16 a[148:149], v245 offset:8192\n\t"
    "ds_read_b64_tr_b16 a[150:151], v245 offset:12288\n\t"
    "v_mfma_f32_16x16x32_bf16 v[76:79], a[56:59], v[220:223], v[76:79]\n\t"
    "s_mov_b32 s39, 0x20000\n\t"
    "ds_read_b64_tr_b16 a[144:145], v245 offset:0\n\t"
    "ds_read_b64_tr_b16 a[146:147], v245 offset:4096\n\t"
    "v_mfma_f32_16x16x32_bf16 v[76:79], a[52:55], v[216:219], v[76:79]\n\t"
    "s_waitcnt vmcnt(14)\n\t"
    "ds_read_b128 v[228:231], v246 offset:12288\n\t"
    "ds_read_b64_tr_b16 a[160:161], v244 offset:512\n\t"
    "v_mfma_f32_16x16x32_bf16 v[76:79], a[48:51], v[212:215], v[76:79]\n\t"
    "s_mov_b32 s15, 0x20000\n\t"
    "ds_read_b128 v[232:235], v246 offset:12800\n\t"
    "s_mov_b32 exec_lo, 16711680\n\t"
    "s_mov_b32 exec_hi, 0\n\t"
    "buffer_load_dword v176, v204, s[24:27], 0 offen\n\t"
    "buffer_load_dword v177, v205, s[20:23], 0 offen\n\t"
    "s_mov_b64 exec, -1\n\t"
    "v_mfma_f32_16x16x32_bf16 v[80:83], a[64:67], v[212:215], 0\n\t"
    "s_lshl_b32 s97, s40, 12\n\t"
    "ds_read_b128 v[236:239], v246 offset:13312\n\t"
    "ds_read_b64_tr_b16 a[162:163], v244 offset:4608\n\t"
    "v_mfma_f32_16x16x32_bf16 v[80:83], a[68:71], v[216:219], v[80:83]\n\t"
    "s_lshl_b32 s98, s40, 14\n\t"
    "ds_read_b128 v[240:243], v246 offset:13824\n\t"
    "ds_read_b64_tr_b16 a[164:165], v244 offset:8704\n\t"
    "v_mfma_f32_16x16x32_bf16 v[80:83], a[72:75], v[220:223], v[80:83]\n\t"
    "s_lshl_b32 s60, s41, 5\n\t"
    "ds_read_b64_tr_b16 a[166:167], v244 offset:12800\n\t"
    "ds_read_b64_tr_b16 a[168:169], v244 offset:16896\n\t"
    "v_mfma_f32_16x16x32_bf16 v[80:83], a[76:79], v[224:227], v[80:83]\n\t"
    "s_add_u32 m0, s52, 9088\n\t"
    "ds_read_b64_tr_b16 a[170:171], v244 offset:20992\n\t"
    "v_mfma_f32_16x16x32_bf16 v[84:87], a[92:95], v[224:227], 0\n\t"
    "buffer_load_dwordx4 v255, s[20:23], s63 offen offset:128 lds\n\t"
    "v_mfma_f32_16x16x32_bf16 v[84:87], a[88:91], v[220:223], v[84:87]\n\t"
    "buffer_load_dwordx4 v201, s[20:23], s63 offen offset:2176 lds\n\t"
    "v_mfma_f32_16x16x32_bf16 v[84:87], a[84:87], v[216:219], v[84:87]\n\t"
    "s_add_u32 s60, s60, s42\n\t"
    "ds_read_b64_tr_b16 a[172:173], v244 offset:25088\n\t"
    "v_mfma_f32_16x16x32_bf16 v[84:87], a[80:83], v[212:215], v[84:87]\n\t"
    "s_lshl_b32 s60, s60, 18\n\t"
    "ds_read_b64_tr_b16 a[174:175], v244 offset:29184\n\t"
    "v_mfma_f32_16x16x32_bf16 v[88:91], a[96:99], v[212:215], 0\n\t"
    "s_add_u32 s28, s70, s60\n\t"
    "s_addc_u32 s29, s71, 0\n\t"
    "ds_read_b64_tr_b16 a[188:189], v245 offset:25088\n\t"
    "v_mfma_f32_16x16x32_bf16 v[88:91], a[100:103], v[216:219], v[88:91]\n\t"
    "s_mov_b32 s30, 0x40000\n\t"
    "ds_read_b64_tr_b16 a[190:191], v245 offset:29184\n\t"
    "v_mfma_f32_16x16x32_bf16 v[88:91], a[104:107], v[220:223], v[88:91]\n\t"
    "s_add_u32 s16, s72, s60\n\t"
    "s_addc_u32 s17, s73, 0\n\t"
    "ds_read_b64_tr_b16 a[184:185], v245 offset:16896\n\t"
    "v_mfma_f32_16x16x32_bf16 v[88:91], a[108:111], v[224:227], v[88:91]\n\t"
    "s_mov_b32 s18, 0x40000\n\t"
    "ds_read_b64_tr_b16 a[186:187], v245 offset:20992\n\t"
    "v_mfma_f32_16x16x32_bf16 v[92:95], a[124:127], v[224:227], 0\n\t"
    "s_add_u32 s12, s80, 50331648\n\t"
    "s_addc_u32 s13, s81, 0\n\t"
    "ds_read_b64_tr_b16 a[180:181], v245 offset:8704\n\t"
    "v_mfma_f32_16x16x32_bf16 v[92:95], a[120:123], v[220:223], v[92:95]\n\t"
    "s_mov_b32 s14, 2097152\n\t"
    "ds_read_b64_tr_b16 a[182:183], v245 offset:12800\n\t"
    "v_mfma_f32_16x16x32_bf16 v[92:95], a[116:119], v[216:219], v[92:95]\n\t"
    "s_waitcnt lgkmcnt(12)\n\t"
    "ds_read_b64_tr_b16 a[176:177], v245 offset:512\n\t"
    "v_mfma_f32_16x16x32_bf16 v[92:95], a[112:115], v[212:215], v[92:95]\n\t"
    "ds_read_b64_tr_b16 a[178:179], v245 offset:4608\n\t"
    "s_waitcnt vmcnt(14)\n\t"
    "s_barrier\n\t"
    "v_mfma_f32_16x16x32_bf16 v[96:99], a[0:3], v[228:231], 0\n\t"
    "s_mov_b32 s53, 2097152\n\t"
    "ds_read_b64_tr_b16 a[192:193], v244 offset:1024\n\t"
    "ds_read_b64_tr_b16 a[194:195], v244 offset:5120\n\t"
    "v_mfma_f32_16x16x32_bf16 v[96:99], a[4:7], v[232:235], v[96:99]\n\t"
    "s_add_u32 m0, s51, 65536\n\t"
    "ds_read_b64_tr_b16 a[196:197], v244 offset:9216\n\t"
    "v_mfma_f32_16x16x32_bf16 v[96:99], a[8:11], v[236:239], v[96:99]\n\t"
    "buffer_load_dwordx4 v248, s[24:27], s53 offen offset:0 lds\n\t"
    "v_mfma_f32_16x16x32_bf16 v[96:99], a[12:15], v[240:243], v[96:99]\n\t"
    "buffer_load_dwordx4 v200, s[24:27], s53 offen offset:2048 lds\n\t"
    "v_mfma_f32_16x16x32_bf16 v[100:103], a[28:31], v[240:243], 0\n\t"
    "s_mov_b32 s53, 2129920\n\t"
    "ds_read_b64_tr_b16 a[198:199], v244 offset:13312\n\t"
    "v_mfma_f32_16x16x32_bf16 v[100:103], a[24:27], v[236:239], v[100:103]\n\t"
    "s_add_u32 m0, s51, 69632\n\t"
    "ds_read_b64_tr_b16 a[200:201], v244 offset:17408\n\t"
    "v_mfma_f32_16x16x32_bf16 v[100:103], a[20:23], v[232:235], v[100:103]\n\t"
    "buffer_load_dwordx4 v248, s[24:27], s53 offen offset:0 lds\n\t"
    "v_mfma_f32_16x16x32_bf16 v[100:103], a[16:19], v[228:231], v[100:103]\n\t"
    "buffer_load_dwordx4 v200, s[24:27], s53 offen offset:2048 lds\n\t"
    "v_mfma_f32_16x16x32_bf16 v[104:107], a[32:35], v[228:231], 0\n\t"
    "s_add_u32 s63, s93, 256\n\t"
    "ds_read_b64_tr_b16 a[202:203], v244 offset:21504\n\t"
    "v_mfma_f32_16x16x32_bf16 v[104:107], a[36:39], v[232:235], v[104:107]\n\t"
    "s_add_u32 m0, s52, 12288\n\t"
    "ds_read_b64_tr_b16 a[204:205], v244 offset:25600\n\t"
    "v_mfma_f32_16x16x32_bf16 v[104:107], a[40:43], v[236:239], v[104:107]\n\t"
    "buffer_load_dwordx4 v255, s[20:23], s63 offen offset:0 lds\n\t"
    "v_mfma_f32_16x16x32_bf16 v[104:107], a[44:47], v[240:243], v[104:107]\n\t"
    "buffer_load_dwordx4 v201, s[20:23], s63 offen offset:2048 lds\n\t"
    "v_mfma_f32_16x16x32_bf16 v[108:111], a[60:63], v[240:243], 0\n\t"
    "s_lshl_b32 s64, s41, 2\n\t"
    "ds_read_b64_tr_b16 a[206:207], v244 offset:29696\n\t"
    "ds_read_b64_tr_b16 a[220:221], v245 offset:25600\n\t"
    "v_mfma_f32_16x16x32_bf16 v[108:111], a[56:59], v[236:239], v[108:111]\n\t"
    "s_add_u32 s64, s64, s95\n\t"
    "ds_read_b64_tr_b16 a[222:223], v245 offset:29696\n\t"
    "v_mfma_f32_16x16x32_bf16 v[108:111], a[52:55], v[232:235], v[108:111]\n\t"
    "s_waitcnt vmcnt(16)\n\t"
    "ds_read_b128 v[212:215], v246 offset:0\n\t"
    "v_mfma_f32_16x16x32_bf16 v[108:111], a[48:51], v[228:231], v[108:111]\n\t"
    "s_mul_i32 s64, s64, 4096\n\t"
    "ds_read_b128 v[216:219], v246 offset:512\n\t"
    "v_mfma_f32_16x16x32_bf16 v[112:115], a[64:67], v[228:231], 0\n\t"
    "s_lshl_b32 s65, s41, 3\n\t"
    "ds_read_b128 v[220:223], v246 offset:1024\n\t"
    "v_mfma_f32_16x16x32_bf16 v[112:115], a[68:71], v[232:235], v[112:115]\n\t"
    "s_add_u32 s65, s65, s96\n\t"
    "ds_read_b128 v[224:227], v246 offset:1536\n\t"
    "v_mfma_f32_16x16x32_bf16 v[112:115], a[72:75], v[236:239], v[112:115]\n\t"
    "s_lshl_b32 s65, s65, 2\n\t"
    "ds_read_b64_tr_b16 a[216:217], v245 offset:17408\n\t"
    "ds_read_b64_tr_b16 a[218:219], v245 offset:21504\n\t"
    "v_mfma_f32_16x16x32_bf16 v[112:115], a[76:79], v[240:243], v[112:115]\n\t"
    "s_add_u32 m0, s52, 13184\n\t"
    "ds_read_b64_tr_b16 a[212:213], v245 offset:9216\n\t"
    "v_mfma_f32_16x16x32_bf16 v[116:119], a[92:95], v[240:243], 0\n\t"
    "buffer_load_dwordx4 v255, s[20:23], s63 offen offset:128 lds\n\t"
    "v_mfma_f32_16x16x32_bf16 v[116:119], a[88:91], v[236:239], v[116:119]\n\t"
    "buffer_load_dwordx4 v201, s[20:23], s63 offen offset:2176 lds\n\t"
    "v_mfma_f32_16x16x32_bf16 v[116:119], a[84:87], v[232:235], v[116:119]\n\t"
    "s_add_u32 s65, s65, s40\n\t"
    "ds_read_b64_tr_b16 a[214:215], v245 offset:13312\n\t"
    "v_mfma_f32_16x16x32_bf16 v[116:119], a[80:83], v[228:231], v[116:119]\n\t"
    "s_lshl_b32 s65, s65, 7\n\t"
    "ds_read_b64_tr_b16 a[208:209], v245 offset:1024\n\t"
    "v_mfma_f32_16x16x32_bf16 v[120:123], a[96:99], v[228:231], 0\n\t"
    "s_add_u32 s65, s65, 1048576\n\t"
    "ds_read_b64_tr_b16 a[210:211], v245 offset:5120\n\t"
    "v_mfma_f32_16x16x32_bf16 v[120:123], a[100:103], v[232:235], v[120:123]\n\t"
    "s_lshl2_add_u32 s60, s41, s95\n\t"
    "ds_read_b64_tr_b16 a[224:225], v244 offset:1536\n\t"
    "v_mfma_f32_16x16x32_bf16 v[120:123], a[104:107], v[236:239], v[120:123]\n\t"
    "s_lshr_b32 s61, s40, 1\n\t"
    "ds_read_b64_tr_b16 a[226:227], v244 offset:5632\n\t"
    "ds_read_b64_tr_b16 a[228:229], v244 offset:9728\n\t"
    "v_mfma_f32_16x16x32_bf16 v[120:123], a[108:111], v[240:243], v[120:123]\n\t"
    "s_lshl1_add_u32 s60, s60, s61\n\t"
    "ds_read_b64_tr_b16 a[230:231], v244 offset:13824\n\t"
    "v_mfma_f32_16x16x32_bf16 v[124:127], a[124:127], v[240:243], 0\n\t"
    "s_mul_i32 s60, s60, 10\n\t"
    "ds_read_b64_tr_b16 a[232:233], v244 offset:17920\n\t"
    "v_mfma_f32_16x16x32_bf16 v[124:127], a[120:123], v[236:239], v[124:127]\n\t"
    "s_and_b32 s61, s40, 1\n\t"
    "ds_read_b64_tr_b16 a[234:235], v244 offset:22016\n\t"
    "v_mfma_f32_16x16x32_bf16 v[124:127], a[116:119], v[232:235], v[124:127]\n\t"
    "s_waitcnt lgkmcnt(12)\n\t"
    "ds_read_b64_tr_b16 a[236:237], v244 offset:26112\n\t"
    "v_mfma_f32_16x16x32_bf16 v[124:127], a[112:115], v[228:231], v[124:127]\n\t"
    "s_waitcnt lgkmcnt(15)\n\t"
    "ds_read_b64_tr_b16 a[238:239], v244 offset:30208\n\t"
    "v_mfma_f32_16x16x32_bf16 v[0:3], a[128:131], v[212:215], v[0:3]\n\t"
    "s_mov_b32 s53, 2097280\n\t"
    "ds_read_b64_tr_b16 a[252:253], v245 offset:26112\n\t"
    "v_mfma_f32_16x16x32_bf16 v[0:3], a[132:135], v[216:219], v[0:3]\n\t"
    "s_add_u32 m0, s51, 66560\n\t"
    "ds_read_b64_tr_b16 a[254:255], v245 offset:30208\n\t"
    "v_mfma_f32_16x16x32_bf16 v[0:3], a[136:139], v[220:223], v[0:3]\n\t"
    "buffer_load_dwordx4 v248, s[24:27], s53 offen offset:0 lds\n\t"
    "v_mfma_f32_16x16x32_bf16 v[0:3], a[140:143], v[224:227], v[0:3]\n\t"
    "buffer_load_dwordx4 v200, s[24:27], s53 offen offset:2048 lds\n\t"
    "v_mfma_f32_16x16x32_bf16 v[4:7], a[156:159], v[224:227], v[4:7]\n\t"
    "s_mov_b32 s53, 2130048\n\t"
    "ds_read_b64_tr_b16 a[248:249], v245 offset:17920\n\t"
    "v_mfma_f32_16x16x32_bf16 v[4:7], a[152:155], v[220:223], v[4:7]\n\t"
    "s_add_u32 m0, s51, 70656\n\t"
    "ds_read_b64_tr_b16 a[250:251], v245 offset:22016\n\t"
    "v_mfma_f32_16x16x32_bf16 v[4:7], a[148:151], v[216:219], v[4:7]\n\t"
    "buffer_load_dwordx4 v248, s[24:27], s53 offen offset:0 lds\n\t"
    "v_mfma_f32_16x16x32_bf16 v[4:7], a[144:147], v[212:215], v[4:7]\n\t"
    "buffer_load_dwordx4 v200, s[24:27], s53 offen offset:2048 lds\n\t"
    "v_mfma_f32_16x16x32_bf16 v[8:11], a[160:163], v[212:215], v[8:11]\n\t"
    "s_add_u32 s63, s90, 512\n\t"
    "ds_read_b64_tr_b16 a[244:245], v245 offset:9728\n\t"
    "v_mfma_f32_16x16x32_bf16 v[8:11], a[164:167], v[216:219], v[8:11]\n\t"
    "s_add_u32 m0, s52, 0\n\t"
    "ds_read_b64_tr_b16 a[246:247], v245 offset:13824\n\t"
    "v_mfma_f32_16x16x32_bf16 v[8:11], a[168:171], v[220:223], v[8:11]\n\t"
    "buffer_load_dwordx4 v255, s[20:23], s63 offen offset:0 lds\n\t"
    "v_mfma_f32_16x16x32_bf16 v[8:11], a[172:175], v[224:227], v[8:11]\n\t"
    "buffer_load_dwordx4 v201, s[20:23], s63 offen offset:2048 lds\n\t"
    "v_mfma_f32_16x16x32_bf16 v[12:15], a[188:191], v[224:227], v[12:15]\n\t"
    "s_add_u32 s60, s60, s61\n\t"
    "ds_read_b64_tr_b16 a[240:241], v245 offset:1536\n\t"
    "v_mfma_f32_16x16x32_bf16 v[12:15], a[184:187], v[220:223], v[12:15]\n\t"
    "s_waitcnt lgkmcnt(7)\n\t"
    "ds_read_b64_tr_b16 a[242:243], v245 offset:5632\n\t"
    "v_mfma_f32_16x16x32_bf16 v[12:15], a[180:183], v[216:219], v[12:15]\n\t"
    "s_waitcnt vmcnt(20)\n\t"
    "ds_read_b128 v[228:231], v246 offset:4096\n\t"
    "v_mfma_f32_16x16x32_bf16 v[12:15], a[176:179], v[212:215], v[12:15]\n\t"
    "s_lshl3_add_u32 s60, s60, s96\n\t"
    "ds_read_b128 v[232:235], v246 offset:4608\n\t"
    "v_mfma_f32_16x16x32_bf16 v[16:19], a[192:195], v[212:215], v[16:19]\n\t"
    "s_lshl_b32 s60, s60, 12\n\t"
    "ds_read_b128 v[236:239], v246 offset:5120\n\t"
    "v_mfma_f32_16x16x32_bf16 v[16:19], a[196:199], v[216:219], v[16:19]\n\t"
    "s_add_u32 s36, s80, s60\n\t"
    "s_addc_u32 s37, s81, 0\n\t"
    "ds_read_b128 v[240:243], v246 offset:5632\n\t"
    "v_mfma_f32_16x16x32_bf16 v[16:19], a[200:203], v[220:223], v[16:19]\n\t"
    "s_mov_b32 s38, 266240\n\t"
    "v_mfma_f32_16x16x32_bf16 v[16:19], a[204:207], v[224:227], v[16:19]\n\t"
    "s_add_u32 m0, s52, 896\n\t"
    "v_mfma_f32_16x16x32_bf16 v[20:23], a[220:223], v[224:227], v[20:23]\n\t"
    "buffer_load_dwordx4 v255, s[20:23], s63 offen offset:128 lds\n\t"
    "v_mfma_f32_16x16x32_bf16 v[20:23], a[216:219], v[220:223], v[20:23]\n\t"
    "buffer_load_dwordx4 v201, s[20:23], s63 offen offset:2176 lds\n\t"
    "v_mfma_f32_16x16x32_bf16 v[20:23], a[212:215], v[216:219], v[20:23]\n\t"
    "s_mov_b32 s84, 0\n\t"
    "v_mfma_f32_16x16x32_bf16 v[20:23], a[208:211], v[212:215], v[20:23]\n\t"
    "s_mov_b32 s85, 65536\n\t"
    "v_mfma_f32_16x16x32_bf16 v[24:27], a[224:227], v[212:215], v[24:27]\n\t"
    "s_mov_b32 s86, 131072\n\t"
    "v_mfma_f32_16x16x32_bf16 v[24:27], a[228:231], v[216:219], v[24:27]\n\t"
    "s_mov_b32 s87, 196608\n\t"
    "v_mfma_f32_16x16x32_bf16 v[24:27], a[232:235], v[220:223], v[24:27]\n\t"
    "s_waitcnt lgkmcnt(4)\n\t"
    "v_mfma_f32_16x16x32_bf16 v[24:27], a[236:239], v[224:227], v[24:27]\n\t"
    "s_mov_b32 s88, 262144\n\t"
    "v_mfma_f32_16x16x32_bf16 v[28:31], a[252:255], v[224:227], v[28:31]\n\t"
    "s_mov_b32 s89, 0x3e0293ee\n\t"
    "v_mfma_f32_16x16x32_bf16 v[28:31], a[248:251], v[220:223], v[28:31]\n\t"
    "s_mov_b32 s58, 0\n\t"
    "v_mfma_f32_16x16x32_bf16 v[28:31], a[244:247], v[216:219], v[28:31]\n\t"
    "s_waitcnt lgkmcnt(0)\n\t"
    "v_mfma_f32_16x16x32_bf16 v[28:31], a[240:243], v[212:215], v[28:31]\n\t"
    "v_mfma_f32_16x16x32_bf16 v[32:35], a[128:131], v[228:231], v[32:35]\n\t"
    "s_add_u32 s63, s91, 512\n\t"
    "v_mfma_f32_16x16x32_bf16 v[32:35], a[132:135], v[232:235], v[32:35]\n\t"
    "s_add_u32 m0, s52, 4096\n\t"
    "v_mfma_f32_16x16x32_bf16 v[32:35], a[136:139], v[236:239], v[32:35]\n\t"
    "buffer_load_dwordx4 v255, s[20:23], s63 offen offset:0 lds\n\t"
    "v_mfma_f32_16x16x32_bf16 v[32:35], a[140:143], v[240:243], v[32:35]\n\t"
    "buffer_load_dwordx4 v201, s[20:23], s63 offen offset:2048 lds\n\t"
    "v_mfma_f32_16x16x32_bf16 v[36:39], a[156:159], v[240:243], v[36:39]\n\t"
    "v_mfma_f32_16x16x32_bf16 v[36:39], a[152:155], v[236:239], v[36:39]\n\t"
    "s_add_u32 m0, s52, 4992\n\t"
    "v_mfma_f32_16x16x32_bf16 v[36:39], a[148:151], v[232:235], v[36:39]\n\t"
    "buffer_load_dwordx4 v255, s[20:23], s63 offen offset:128 lds\n\t"
    "v_mfma_f32_16x16x32_bf16 v[36:39], a[144:147], v[228:231], v[36:39]\n\t"
    "buffer_load_dwordx4 v201, s[20:23], s63 offen offset:2176 lds\n\t"
    "v_mfma_f32_16x16x32_bf16 v[40:43], a[160:163], v[228:231], v[40:43]\n\t"
    "v_mfma_f32_16x16x32_bf16 v[40:43], a[164:167], v[232:235], v[40:43]\n\t"
    "v_mfma_f32_16x16x32_bf16 v[40:43], a[168:171], v[236:239], v[40:43]\n\t"
    "buffer_load_dwordx4 v[160:163], v250, s[28:31], 0 offen offset:0\n\t"
    "v_mfma_f32_16x16x32_bf16 v[40:43], a[172:175], v[240:243], v[40:43]\n\t"
    "buffer_load_dwordx4 v[164:167], v250, s[28:31], 0 offen offset:64\n\t"
    "v_mfma_f32_16x16x32_bf16 v[44:47], a[188:191], v[240:243], v[44:47]\n\t"
    "v_mfma_f32_16x16x32_bf16 v[44:47], a[184:187], v[236:239], v[44:47]\n\t"
    "v_mfma_f32_16x16x32_bf16 v[44:47], a[180:183], v[232:235], v[44:47]\n\t"
    "s_waitcnt vmcnt(22)\n\t"
    "ds_read_b128 v[212:215], v246 offset:8192\n\t"
    "v_mfma_f32_16x16x32_bf16 v[44:47], a[176:179], v[228:231], v[44:47]\n\t"
    "ds_read_b128 v[216:219], v246 offset:8704\n\t"
    "v_mfma_f32_16x16x32_bf16 v[48:51], a[192:195], v[228:231], v[48:51]\n\t"
    "ds_read_b128 v[220:223], v246 offset:9216\n\t"
    "v_mfma_f32_16x16x32_bf16 v[48:51], a[196:199], v[232:235], v[48:51]\n\t"
    "ds_read_b128 v[224:227], v246 offset:9728\n\t"
    "v_mfma_f32_16x16x32_bf16 v[48:51], a[200:203], v[236:239], v[48:51]\n\t"
    "v_mfma_f32_16x16x32_bf16 v[48:51], a[204:207], v[240:243], v[48:51]\n\t"
    "v_mfma_f32_16x16x32_bf16 v[52:55], a[220:223], v[240:243], v[52:55]\n\t"
    "buffer_load_dwordx4 v[168:171], v250, s[28:31], 0 offen offset:128\n\t"
    "v_mfma_f32_16x16x32_bf16 v[52:55], a[216:219], v[236:239], v[52:55]\n\t"
    "buffer_load_dwordx4 v[172:175], v250, s[28:31], 0 offen offset:192\n\t"
    "v_mfma_f32_16x16x32_bf16 v[52:55], a[212:215], v[232:235], v[52:55]\n\t"
    "v_mfma_f32_16x16x32_bf16 v[52:55], a[208:211], v[228:231], v[52:55]\n\t"
    "v_mul_u32_u24 v202, 128, v249\n\t"
    "v_add_u32 v202, s64, v202\n\t"
    "v_and_b32 v203, 3, v249\n\t"
    "s_mov_b32 exec_lo, 0\n\t"
    "v_lshl_add_u32 v202, v203, 2, s65\n\t"
    "s_mov_b32 exec_lo, -1\n\t"
    "v_mfma_f32_16x16x32_bf16 v[56:59], a[224:227], v[228:231], v[56:59]\n\t"
    "v_mfma_f32_16x16x32_bf16 v[56:59], a[228:231], v[232:235], v[56:59]\n\t"
    "buffer_load_dword v211, v202, s[12:15], 0 offen sc1\n\t"
    "v_mfma_f32_16x16x32_bf16 v[56:59], a[232:235], v[236:239], v[56:59]\n\t"
    "v_mfma_f32_16x16x32_bf16 v[56:59], a[236:239], v[240:243], v[56:59]\n\t"
    "v_mfma_f32_16x16x32_bf16 v[60:63], a[252:255], v[240:243], v[60:63]\n\t"
    "v_mfma_f32_16x16x32_bf16 v[60:63], a[248:251], v[236:239], v[60:63]\n\t"
    "v_mfma_f32_16x16x32_bf16 v[60:63], a[244:247], v[232:235], v[60:63]\n\t"
    "s_waitcnt lgkmcnt(0)\n\t"
    "v_mfma_f32_16x16x32_bf16 v[60:63], a[240:243], v[228:231], v[60:63]\n\t"
    "s_waitcnt vmcnt(13)\n\t"
    "s_barrier\n\t"
    "v_mfma_f32_16x16x32_bf16 v[64:67], a[128:131], v[212:215], v[64:67]\n\t"
    "s_add_u32 s63, s92, 512\n\t"
    "ds_read_b64_tr_b16 a[0:1], v253 offset:0\n\t"
    "ds_read_b64_tr_b16 a[2:3], v253 offset:4096\n\t"
    "v_mfma_f32_16x16x32_bf16 v[64:67], a[132:135], v[216:219], v[64:67]\n\t"
    "s_add_u32 m0, s52, 8192\n\t"
    "ds_read_b64_tr_b16 a[4:5], v253 offset:8192\n\t"
    "v_mfma_f32_16x16x32_bf16 v[64:67], a[136:139], v[220:223], v[64:67]\n\t"
    "buffer_load_dwordx4 v255, s[20:23], s63 offen offset:0 lds\n\t"
    "v_mfma_f32_16x16x32_bf16 v[64:67], a[140:143], v[224:227], v[64:67]\n\t"
    "buffer_load_dwordx4 v201, s[20:23], s63 offen offset:2048 lds\n\t"
    "v_mfma_f32_16x16x32_bf16 v[68:71], a[156:159], v[224:227], v[68:71]\n\t"
    "ds_read_b64_tr_b16 a[6:7], v253 offset:12288\n\t"
    "v_mfma_f32_16x16x32_bf16 v[68:71], a[152:155], v[220:223], v[68:71]\n\t"
    "s_add_u32 m0, s52, 9088\n\t"
    "ds_read_b64_tr_b16 a[8:9], v253 offset:16384\n\t"
    "ds_read_b64_tr_b16 a[10:11], v253 offset:20480\n\t"
    "v_mfma_f32_16x16x32_bf16 v[68:71], a[148:151], v[216:219], v[68:71]\n\t"
    "buffer_load_dwordx4 v255, s[20:23], s63 offen offset:128 lds\n\t"
    "v_mfma_f32_16x16x32_bf16 v[68:71], a[144:147], v[212:215], v[68:71]\n\t"
    "buffer_load_dwordx4 v201, s[20:23], s63 offen offset:2176 lds\n\t"
    "v_mfma_f32_16x16x32_bf16 v[72:75], a[160:163], v[212:215], v[72:75]\n\t"
    "ds_read_b64_tr_b16 a[12:13], v253 offset:24576\n\t"
    "v_mfma_f32_16x16x32_bf16 v[72:75], a[164:167], v[216:219], v[72:75]\n\t"
    "ds_read_b64_tr_b16 a[14:15], v253 offset:28672\n\t"
    "v_mfma_f32_16x16x32_bf16 v[72:75], a[168:171], v[220:223], v[72:75]\n\t"
    "ds_read_b64_tr_b16 a[28:29], v254 offset:24576\n\t"
    "ds_read_b64_tr_b16 a[30:31], v254 offset:28672\n\t"
    "v_mfma_f32_16x16x32_bf16 v[72:75], a[172:175], v[224:227], v[72:75]\n\t"
    "ds_read_b64_tr_b16 a[24:25], v254 offset:16384\n\t"
    "v_mfma_f32_16x16x32_bf16 v[76:79], a[188:191], v[224:227], v[76:79]\n\t"
    "ds_read_b64_tr_b16 a[26:27], v254 offset:20480\n\t"
    "v_mfma_f32_16x16x32_bf16 v[76:79], a[184:187], v[220:223], v[76:79]\n\t"
    "ds_read_b64_tr_b16 a[20:21], v254 offset:8192\n\t"
    "ds_read_b64_tr_b16 a[22:23], v254 offset:12288\n\t"
    "v_mfma_f32_16x16x32_bf16 v[76:79], a[180:183], v[216:219], v[76:79]\n\t"
    "s_waitcnt vmcnt(21)\n\t"
    "ds_read_b128 v[228:231], v246 offset:12288\n\t"
    "v_mfma_f32_16x16x32_bf16 v[76:79], a[176:179], v[212:215], v[76:79]\n\t"
    "ds_read_b128 v[232:235], v246 offset:12800\n\t"
    "s_mov_b32 exec_lo, 4278190080\n\t"
    "s_mov_b32 exec_hi, 0\n\t"
    "buffer_load_dword v178, v204, s[24:27], 0 offen\n\t"
    "buffer_load_dword v179, v205, s[20:23], 0 offen\n\t"
    "s_mov_b64 exec, -1\n\t"
    "v_mfma_f32_16x16x32_bf16 v[80:83], a[192:195], v[212:215], v[80:83]\n\t"
    "ds_read_b128 v[236:239], v246 offset:13312\n\t"
    "v_mfma_f32_16x16x32_bf16 v[80:83], a[196:199], v[216:219], v[80:83]\n\t"
    "ds_read_b128 v[240:243], v246 offset:13824\n\t"
    "ds_read_b64_tr_b16 a[16:17], v254 offset:0\n\t"
    "v_mfma_f32_16x16x32_bf16 v[80:83], a[200:203], v[220:223], v[80:83]\n\t"
    "ds_read_b64_tr_b16 a[18:19], v254 offset:4096\n\t"
    "v_mfma_f32_16x16x32_bf16 v[80:83], a[204:207], v[224:227], v[80:83]\n\t"
    "ds_read_b64_tr_b16 a[32:33], v253 offset:512\n\t"
    "v_mfma_f32_16x16x32_bf16 v[84:87], a[220:223], v[224:227], v[84:87]\n\t"
    "ds_read_b64_tr_b16 a[34:35], v253 offset:4608\n\t"
    "ds_read_b64_tr_b16 a[36:37], v253 offset:8704\n\t"
    "v_mfma_f32_16x16x32_bf16 v[84:87], a[216:219], v[220:223], v[84:87]\n\t"
    "ds_read_b64_tr_b16 a[38:39], v253 offset:12800\n\t"
    "v_mfma_f32_16x16x32_bf16 v[84:87], a[212:215], v[216:219], v[84:87]\n\t"
    "ds_read_b64_tr_b16 a[40:41], v253 offset:16896\n\t"
    "v_mfma_f32_16x16x32_bf16 v[84:87], a[208:211], v[212:215], v[84:87]\n\t"
    "ds_read_b64_tr_b16 a[42:43], v253 offset:20992\n\t"
    "ds_read_b64_tr_b16 a[44:45], v253 offset:25088\n\t"
    "v_mfma_f32_16x16x32_bf16 v[88:91], a[224:227], v[212:215], v[88:91]\n\t"
    "ds_read_b64_tr_b16 a[46:47], v253 offset:29184\n\t"
    "v_mfma_f32_16x16x32_bf16 v[88:91], a[228:231], v[216:219], v[88:91]\n\t"
    "ds_read_b64_tr_b16 a[60:61], v254 offset:25088\n\t"
    "v_mfma_f32_16x16x32_bf16 v[88:91], a[232:235], v[220:223], v[88:91]\n\t"
    "ds_read_b64_tr_b16 a[62:63], v254 offset:29184\n\t"
    "ds_read_b64_tr_b16 a[56:57], v254 offset:16896\n\t"
    "v_mfma_f32_16x16x32_bf16 v[88:91], a[236:239], v[224:227], v[88:91]\n\t"
    "ds_read_b64_tr_b16 a[58:59], v254 offset:20992\n\t"
    "v_mfma_f32_16x16x32_bf16 v[92:95], a[252:255], v[224:227], v[92:95]\n\t"
    "ds_read_b64_tr_b16 a[52:53], v254 offset:8704\n\t"
    "v_mfma_f32_16x16x32_bf16 v[92:95], a[248:251], v[220:223], v[92:95]\n\t"
    "ds_read_b64_tr_b16 a[54:55], v254 offset:12800\n\t"
    "v_mfma_f32_16x16x32_bf16 v[92:95], a[244:247], v[216:219], v[92:95]\n\t"
    "s_waitcnt lgkmcnt(15)\n\t"
    "ds_read_b64_tr_b16 a[48:49], v254 offset:512\n\t"
    "v_mfma_f32_16x16x32_bf16 v[92:95], a[240:243], v[212:215], v[92:95]\n\t"
    "ds_read_b64_tr_b16 a[50:51], v254 offset:4608\n\t"
    "v_mfma_f32_16x16x32_bf16 v[96:99], a[128:131], v[228:231], v[96:99]\n\t"
    "s_mov_b32 s53, 3145728\n\t"
    "ds_read_b64_tr_b16 a[64:65], v253 offset:1024\n\t"
    "ds_read_b64_tr_b16 a[66:67], v253 offset:5120\n\t"
    "v_mfma_f32_16x16x32_bf16 v[96:99], a[132:135], v[232:235], v[96:99]\n\t"
    "s_add_u32 m0, s51, 32768\n\t"
    "ds_read_b64_tr_b16 a[68:69], v253 offset:9216\n\t"
    "v_mfma_f32_16x16x32_bf16 v[96:99], a[136:139], v[236:239], v[96:99]\n\t"
    "buffer_load_dwordx4 v248, s[24:27], s53 offen offset:0 lds\n\t"
    "v_mfma_f32_16x16x32_bf16 v[96:99], a[140:143], v[240:243], v[96:99]\n\t"
    "buffer_load_dwordx4 v200, s[24:27], s53 offen offset:2048 lds\n\t"
    "v_mfma_f32_16x16x32_bf16 v[100:103], a[156:159], v[240:243], v[100:103]\n\t"
    "s_mov_b32 s53, 3178496\n\t"
    "ds_read_b64_tr_b16 a[70:71], v253 offset:13312\n\t"
    "v_mfma_f32_16x16x32_bf16 v[100:103], a[152:155], v[236:239], v[100:103]\n\t"
    "s_add_u32 m0, s51, 36864\n\t"
    "ds_read_b64_tr_b16 a[72:73], v253 offset:17408\n\t"
    "v_mfma_f32_16x16x32_bf16 v[100:103], a[148:151], v[232:235], v[100:103]\n\t"
    "buffer_load_dwordx4 v248, s[24:27], s53 offen offset:0 lds\n\t"
    "v_mfma_f32_16x16x32_bf16 v[100:103], a[144:147], v[228:231], v[100:103]\n\t"
    "buffer_load_dwordx4 v200, s[24:27], s53 offen offset:2048 lds\n\t"
    "v_mfma_f32_16x16x32_bf16 v[104:107], a[160:163], v[228:231], v[104:107]\n\t"
    "s_add_u32 s63, s93, 512\n\t"
    "ds_read_b64_tr_b16 a[74:75], v253 offset:21504\n\t"
    "v_mfma_f32_16x16x32_bf16 v[104:107], a[164:167], v[232:235], v[104:107]\n\t"
    "s_add_u32 m0, s52, 12288\n\t"
    "ds_read_b64_tr_b16 a[76:77], v253 offset:25600\n\t"
    "v_mfma_f32_16x16x32_bf16 v[104:107], a[168:171], v[236:239], v[104:107]\n\t"
    "buffer_load_dwordx4 v255, s[20:23], s63 offen offset:0 lds\n\t"
    "v_mfma_f32_16x16x32_bf16 v[104:107], a[172:175], v[240:243], v[104:107]\n\t"
    "buffer_load_dwordx4 v201, s[20:23], s63 offen offset:2048 lds\n\t"
    "v_mfma_f32_16x16x32_bf16 v[108:111], a[188:191], v[240:243], v[108:111]\n\t"
    "ds_read_b64_tr_b16 a[78:79], v253 offset:29696\n\t"
    "ds_read_b64_tr_b16 a[92:93], v254 offset:25600\n\t"
    "v_mfma_f32_16x16x32_bf16 v[108:111], a[184:187], v[236:239], v[108:111]\n\t"
    "ds_read_b64_tr_b16 a[94:95], v254 offset:29696\n\t"
    "v_mfma_f32_16x16x32_bf16 v[108:111], a[180:183], v[232:235], v[108:111]\n\t"
    "s_waitcnt vmcnt(21)\n\t"
    "ds_read_b128 v[212:215], v246 offset:0\n\t"
    "v_mfma_f32_16x16x32_bf16 v[108:111], a[176:179], v[228:231], v[108:111]\n\t"
    "ds_read_b128 v[216:219], v246 offset:512\n\t"
    "v_mfma_f32_16x16x32_bf16 v[112:115], a[192:195], v[228:231], v[112:115]\n\t"
    "ds_read_b128 v[220:223], v246 offset:1024\n\t"
    "v_mfma_f32_16x16x32_bf16 v[112:115], a[196:199], v[232:235], v[112:115]\n\t"
    "ds_read_b128 v[224:227], v246 offset:1536\n\t"
    "v_mfma_f32_16x16x32_bf16 v[112:115], a[200:203], v[236:239], v[112:115]\n\t"
    "ds_read_b64_tr_b16 a[88:89], v254 offset:17408\n\t"
    "ds_read_b64_tr_b16 a[90:91], v254 offset:21504\n\t"
    "v_mfma_f32_16x16x32_bf16 v[112:115], a[204:207], v[240:243], v[112:115]\n\t"
    "s_add_u32 m0, s52, 13184\n\t"
    "ds_read_b64_tr_b16 a[84:85], v254 offset:9216\n\t"
    "v_mfma_f32_16x16x32_bf16 v[116:119], a[220:223], v[240:243], v[116:119]\n\t"
    "buffer_load_dwordx4 v255, s[20:23], s63 offen offset:128 lds\n\t"
    "v_mfma_f32_16x16x32_bf16 v[116:119], a[216:219], v[236:239], v[116:119]\n\t"
    "buffer_load_dwordx4 v201, s[20:23], s63 offen offset:2176 lds\n\t"
    "v_mfma_f32_16x16x32_bf16 v[116:119], a[212:215], v[232:235], v[116:119]\n\t"
    "ds_read_b64_tr_b16 a[86:87], v254 offset:13312\n\t"
    "v_mfma_f32_16x16x32_bf16 v[116:119], a[208:211], v[228:231], v[116:119]\n\t"
    "ds_read_b64_tr_b16 a[80:81], v254 offset:1024\n\t"
    "v_mfma_f32_16x16x32_bf16 v[120:123], a[224:227], v[228:231], v[120:123]\n\t"
    "ds_read_b64_tr_b16 a[82:83], v254 offset:5120\n\t"
    "v_mfma_f32_16x16x32_bf16 v[120:123], a[228:231], v[232:235], v[120:123]\n\t"
    "ds_read_b64_tr_b16 a[96:97], v253 offset:1536\n\t"
    "v_mfma_f32_16x16x32_bf16 v[120:123], a[232:235], v[236:239], v[120:123]\n\t"
    "ds_read_b64_tr_b16 a[98:99], v253 offset:5632\n\t"
    "ds_read_b64_tr_b16 a[100:101], v253 offset:9728\n\t"
    "v_mfma_f32_16x16x32_bf16 v[120:123], a[236:239], v[240:243], v[120:123]\n\t"
    "ds_read_b64_tr_b16 a[102:103], v253 offset:13824\n\t"
    "v_mfma_f32_16x16x32_bf16 v[124:127], a[252:255], v[240:243], v[124:127]\n\t"
    "ds_read_b64_tr_b16 a[104:105], v253 offset:17920\n\t"
    "v_mfma_f32_16x16x32_bf16 v[124:127], a[248:251], v[236:239], v[124:127]\n\t"
    "ds_read_b64_tr_b16 a[106:107], v253 offset:22016\n\t"
    "v_mfma_f32_16x16x32_bf16 v[124:127], a[244:247], v[232:235], v[124:127]\n\t"
    "s_waitcnt lgkmcnt(12)\n\t"
    "ds_read_b64_tr_b16 a[108:109], v253 offset:26112\n\t"
    "v_mfma_f32_16x16x32_bf16 v[124:127], a[240:243], v[228:231], v[124:127]\n\t"
    "s_waitcnt lgkmcnt(15)\n\t"
    "ds_read_b64_tr_b16 a[110:111], v253 offset:30208\n\t"
    "v_mfma_f32_16x16x32_bf16 v[0:3], a[0:3], v[212:215], v[0:3]\n\t"
    "s_mov_b32 s53, 3145856\n\t"
    "ds_read_b64_tr_b16 a[124:125], v254 offset:26112\n\t"
    "ds_read_b64_tr_b16 a[126:127], v254 offset:30208\n\t"
    "v_mfma_f32_16x16x32_bf16 v[0:3], a[4:7], v[216:219], v[0:3]\n\t"
    "s_add_u32 m0, s51, 33792\n\t"
    "ds_read_b64_tr_b16 a[120:121], v254 offset:17920\n\t"
    "ds_read_b64_tr_b16 a[122:123], v254 offset:22016\n\t"
    "v_mfma_f32_16x16x32_bf16 v[0:3], a[8:11], v[220:223], v[0:3]\n\t"
    "buffer_load_dwordx4 v248, s[24:27], s53 offen offset:0 lds\n\t"
    "v_mfma_f32_16x16x32_bf16 v[0:3], a[12:15], v[224:227], v[0:3]\n\t"
    "buffer_load_dwordx4 v200, s[24:27], s53 offen offset:2048 lds\n\t"
    "v_mfma_f32_16x16x32_bf16 v[4:7], a[28:31], v[224:227], v[4:7]\n\t"
    "s_mov_b32 s53, 3178624\n\t"
    "ds_read_b64_tr_b16 a[116:117], v254 offset:9728\n\t"
    "ds_read_b64_tr_b16 a[118:119], v254 offset:13824\n\t"
    "v_mfma_f32_16x16x32_bf16 v[4:7], a[24:27], v[220:223], v[4:7]\n\t"
    "s_add_u32 m0, s51, 37888\n\t"
    "ds_read_b64_tr_b16 a[112:113], v254 offset:1536\n\t"
    "ds_read_b64_tr_b16 a[114:115], v254 offset:5632\n\t"
    "v_mfma_f32_16x16x32_bf16 v[4:7], a[20:23], v[216:219], v[4:7]\n\t"
    "buffer_load_dwordx4 v248, s[24:27], s53 offen offset:0 lds\n\t"
    "v_mfma_f32_16x16x32_bf16 v[4:7], a[16:19], v[212:215], v[4:7]\n\t"
    "buffer_load_dwordx4 v200, s[24:27], s53 offen offset:2048 lds\n\t"
    "v_mfma_f32_16x16x32_bf16 v[8:11], a[32:35], v[212:215], v[8:11]\n\t"
    "s_add_u32 s63, s90, 768\n\t"
    "v_mov_b32 v180, 0xc1a00000\n\t"
    "v_mov_b32 v181, 0xc1a00000\n\t"
    "v_mfma_f32_16x16x32_bf16 v[8:11], a[36:39], v[216:219], v[8:11]\n\t"
    "s_add_u32 m0, s52, 0\n\t"
    "v_mov_b32 v182, 0xc1a00000\n\t"
    "v_mov_b32 v183, 0xc1a00000\n\t"
    "v_mfma_f32_16x16x32_bf16 v[8:11], a[40:43], v[220:223], v[8:11]\n\t"
    "buffer_load_dwordx4 v255, s[20:23], s63 offen offset:0 lds\n\t"
    "v_mfma_f32_16x16x32_bf16 v[8:11], a[44:47], v[224:227], v[8:11]\n\t"
    "buffer_load_dwordx4 v201, s[20:23], s63 offen offset:2048 lds\n\t"
    "v_mfma_f32_16x16x32_bf16 v[12:15], a[60:63], v[224:227], v[12:15]\n\t"
    "v_mov_b32 v184, 0xc1a00000\n\t"
    "v_mov_b32 v185, 0xc1a00000\n\t"
    "v_mfma_f32_16x16x32_bf16 v[12:15], a[56:59], v[220:223], v[12:15]\n\t"
    "s_waitcnt lgkmcnt(8)\n\t"
    "v_mov_b32 v186, 0xc1a00000\n\t"
    "v_mov_b32 v187, 0xc1a00000\n\t"
    "v_mfma_f32_16x16x32_bf16 v[12:15], a[52:55], v[216:219], v[12:15]\n\t"
    "s_waitcnt vmcnt(25)\n\t"
    "ds_read_b128 v[228:231], v246 offset:4096\n\t"
    "v_mov_b32 v188, 0xc1a00000\n\t"
    "v_mfma_f32_16x16x32_bf16 v[12:15], a[48:51], v[212:215], v[12:15]\n\t"
    "ds_read_b128 v[232:235], v246 offset:4608\n\t"
    "v_mov_b32 v189, 0xc1a00000\n\t"
    "v_mfma_f32_16x16x32_bf16 v[16:19], a[64:67], v[212:215], v[16:19]\n\t"
    "ds_read_b128 v[236:239], v246 offset:5120\n\t"
    "v_mov_b32 v190, 0xc1a00000\n\t"
    "v_mfma_f32_16x16x32_bf16 v[16:19], a[68:71], v[216:219], v[16:19]\n\t"
    "ds_read_b128 v[240:243], v246 offset:5632\n\t"
    "v_mov_b32 v191, 0xc1a00000\n\t"
    "v_mfma_f32_16x16x32_bf16 v[16:19], a[72:75], v[220:223], v[16:19]\n\t"
    "v_mov_b32 v192, 0xc1a00000\n\t"
    "v_mov_b32 v193, 0xc1a00000\n\t"
    "v_mfma_f32_16x16x32_bf16 v[16:19], a[76:79], v[224:227], v[16:19]\n\t"
    "s_add_u32 m0, s52, 896\n\t"
    "v_mov_b32 v194, 0xc1a00000\n\t"
    "v_mov_b32 v195, 0xc1a00000\n\t"
    "v_mfma_f32_16x16x32_bf16 v[20:23], a[92:95], v[224:227], v[20:23]\n\t"
    "buffer_load_dwordx4 v255, s[20:23], s63 offen offset:128 lds\n\t"
    "v_mfma_f32_16x16x32_bf16 v[20:23], a[88:91], v[220:223], v[20:23]\n\t"
    "buffer_load_dwordx4 v201, s[20:23], s63 offen offset:2176 lds\n\t"
    "v_mfma_f32_16x16x32_bf16 v[20:23], a[84:87], v[216:219], v[20:23]\n\t"
    "v_mfma_f32_16x16x32_bf16 v[20:23], a[80:83], v[212:215], v[20:23]\n\t"
    "v_mfma_f32_16x16x32_bf16 v[24:27], a[96:99], v[212:215], v[24:27]\n\t"
    "v_mfma_f32_16x16x32_bf16 v[24:27], a[100:103], v[216:219], v[24:27]\n\t"
    "v_mfma_f32_16x16x32_bf16 v[24:27], a[104:107], v[220:223], v[24:27]\n\t"
    "s_waitcnt lgkmcnt(4)\n\t"
    "v_mfma_f32_16x16x32_bf16 v[24:27], a[108:111], v[224:227], v[24:27]\n\t"
    "v_mfma_f32_16x16x32_bf16 v[28:31], a[124:127], v[224:227], v[28:31]\n\t"
    "v_mfma_f32_16x16x32_bf16 v[28:31], a[120:123], v[220:223], v[28:31]\n\t"
    "v_mfma_f32_16x16x32_bf16 v[28:31], a[116:119], v[216:219], v[28:31]\n\t"
    "s_waitcnt lgkmcnt(0)\n\t"
    "v_mfma_f32_16x16x32_bf16 v[28:31], a[112:115], v[212:215], v[28:31]\n\t"
    "v_mfma_f32_16x16x32_bf16 v[32:35], a[0:3], v[228:231], v[32:35]\n\t"
    "s_add_u32 s63, s91, 768\n\t"
    "v_mfma_f32_16x16x32_bf16 v[32:35], a[4:7], v[232:235], v[32:35]\n\t"
    "s_add_u32 m0, s52, 4096\n\t"
    "v_mfma_f32_16x16x32_bf16 v[32:35], a[8:11], v[236:239], v[32:35]\n\t"
    "buffer_load_dwordx4 v255, s[20:23], s63 offen offset:0 lds\n\t"
    "v_mfma_f32_16x16x32_bf16 v[32:35], a[12:15], v[240:243], v[32:35]\n\t"
    "buffer_load_dwordx4 v201, s[20:23], s63 offen offset:2048 lds\n\t"
    "v_mfma_f32_16x16x32_bf16 v[36:39], a[28:31], v[240:243], v[36:39]\n\t"
    "v_mfma_f32_16x16x32_bf16 v[36:39], a[24:27], v[236:239], v[36:39]\n\t"
    "s_add_u32 m0, s52, 4992\n\t"
    "v_mfma_f32_16x16x32_bf16 v[36:39], a[20:23], v[232:235], v[36:39]\n\t"
    "buffer_load_dwordx4 v255, s[20:23], s63 offen offset:128 lds\n\t"
    "v_mfma_f32_16x16x32_bf16 v[36:39], a[16:19], v[228:231], v[36:39]\n\t"
    "buffer_load_dwordx4 v201, s[20:23], s63 offen offset:2176 lds\n\t"
    "v_mfma_f32_16x16x32_bf16 v[40:43], a[32:35], v[228:231], v[40:43]\n\t"
    "v_mfma_f32_16x16x32_bf16 v[40:43], a[36:39], v[232:235], v[40:43]\n\t"
    "v_mfma_f32_16x16x32_bf16 v[40:43], a[40:43], v[236:239], v[40:43]\n\t"
    "v_mfma_f32_16x16x32_bf16 v[40:43], a[44:47], v[240:243], v[40:43]\n\t"
    "v_mfma_f32_16x16x32_bf16 v[44:47], a[60:63], v[240:243], v[44:47]\n\t"
    "s_waitcnt vmcnt(27)\n\t"
    "v_xor_b32 v160, 0x80008000, v160\n\t"
    "v_xor_b32 v161, 0x80008000, v161\n\t"
    "v_mfma_f32_16x16x32_bf16 v[44:47], a[56:59], v[236:239], v[44:47]\n\t"
    "v_xor_b32 v162, 0x80008000, v162\n\t"
    "v_xor_b32 v163, 0x80008000, v163\n\t"
    "v_mfma_f32_16x16x32_bf16 v[44:47], a[52:55], v[232:235], v[44:47]\n\t"
    "s_waitcnt vmcnt(22)\n\t"
    "ds_read_b128 v[212:215], v246 offset:8192\n\t"
    "v_xor_b32 v164, 0x80008000, v164\n\t"
    "v_mfma_f32_16x16x32_bf16 v[44:47], a[48:51], v[228:231], v[44:47]\n\t"
    "ds_read_b128 v[216:219], v246 offset:8704\n\t"
    "v_xor_b32 v165, 0x80008000, v165\n\t"
    "v_mfma_f32_16x16x32_bf16 v[48:51], a[64:67], v[228:231], v[48:51]\n\t"
    "ds_read_b128 v[220:223], v246 offset:9216\n\t"
    "v_xor_b32 v166, 0x80008000, v166\n\t"
    "v_mfma_f32_16x16x32_bf16 v[48:51], a[68:71], v[232:235], v[48:51]\n\t"
    "ds_read_b128 v[224:227], v246 offset:9728\n\t"
    "v_xor_b32 v167, 0x80008000, v167\n\t"
    "v_mfma_f32_16x16x32_bf16 v[48:51], a[72:75], v[236:239], v[48:51]\n\t"
    "v_xor_b32 v168, 0x80008000, v168\n\t"
    "v_xor_b32 v169, 0x80008000, v169\n\t"
    "v_mfma_f32_16x16x32_bf16 v[48:51], a[76:79], v[240:243], v[48:51]\n\t"
    "v_xor_b32 v170, 0x80008000, v170\n\t"
    "v_xor_b32 v171, 0x80008000, v171\n\t"
    "v_mfma_f32_16x16x32_bf16 v[52:55], a[92:95], v[240:243], v[52:55]\n\t"
    "v_xor_b32 v172, 0x80008000, v172\n\t"
    "v_xor_b32 v173, 0x80008000, v173\n\t"
    "v_mfma_f32_16x16x32_bf16 v[52:55], a[88:91], v[236:239], v[52:55]\n\t"
    "v_xor_b32 v174, 0x80008000, v174\n\t"
    "v_xor_b32 v175, 0x80008000, v175\n\t"
    "v_mfma_f32_16x16x32_bf16 v[52:55], a[84:87], v[232:235], v[52:55]\n\t"
    "v_mfma_f32_16x16x32_bf16 v[52:55], a[80:83], v[228:231], v[52:55]\n\t"
    "v_mfma_f32_16x16x32_bf16 v[56:59], a[96:99], v[228:231], v[56:59]\n\t"
    "v_mfma_f32_16x16x32_bf16 v[56:59], a[100:103], v[232:235], v[56:59]\n\t"
    "v_mfma_f32_16x16x32_bf16 v[56:59], a[104:107], v[236:239], v[56:59]\n\t"
    "v_mfma_f32_16x16x32_bf16 v[56:59], a[108:111], v[240:243], v[56:59]\n\t"
    "v_mfma_f32_16x16x32_bf16 v[60:63], a[124:127], v[240:243], v[60:63]\n\t"
    "v_mfma_f32_16x16x32_bf16 v[60:63], a[120:123], v[236:239], v[60:63]\n\t"
    "v_mfma_f32_16x16x32_bf16 v[60:63], a[116:119], v[232:235], v[60:63]\n\t"
    "s_waitcnt lgkmcnt(0)\n\t"
    "v_mfma_f32_16x16x32_bf16 v[60:63], a[112:115], v[228:231], v[60:63]\n\t"
    "s_waitcnt vmcnt(8)\n\t"
    "s_barrier\n\t"
    "v_mfma_f32_16x16x32_bf16 v[64:67], a[0:3], v[212:215], v[64:67]\n\t"
    "s_add_u32 s63, s92, 768\n\t"
    "ds_read_b64_tr_b16 a[128:129], v244 offset:32768\n\t"
    "ds_read_b64_tr_b16 a[130:131], v244 offset:36864\n\t"
    "v_mfma_f32_16x16x32_bf16 v[64:67], a[4:7], v[216:219], v[64:67]\n\t"
    "s_add_u32 m0, s52, 8192\n\t"
    "ds_read_b64_tr_b16 a[132:133], v244 offset:40960\n\t"
    "v_mfma_f32_16x16x32_bf16 v[64:67], a[8:11], v[220:223], v[64:67]\n\t"
    "buffer_load_dwordx4 v255, s[20:23], s63 offen offset:0 lds\n\t"
    "v_mfma_f32_16x16x32_bf16 v[64:67], a[12:15], v[224:227], v[64:67]\n\t"
    "buffer_load_dwordx4 v201, s[20:23], s63 offen offset:2048 lds\n\t"
    "v_mfma_f32_16x16x32_bf16 v[68:71], a[28:31], v[224:227], v[68:71]\n\t"
    "ds_read_b64_tr_b16 a[134:135], v244 offset:45056\n\t"
    "ds_read_b64_tr_b16 a[136:137], v244 offset:49152\n\t"
    "v_mfma_f32_16x16x32_bf16 v[68:71], a[24:27], v[220:223], v[68:71]\n\t"
    "s_add_u32 m0, s52, 9088\n\t"
    "ds_read_b64_tr_b16 a[138:139], v244 offset:53248\n\t"
    "v_mfma_f32_16x16x32_bf16 v[68:71], a[20:23], v[216:219], v[68:71]\n\t"
    "buffer_load_dwordx4 v255, s[20:23], s63 offen offset:128 lds\n\t"
    "v_mfma_f32_16x16x32_bf16 v[68:71], a[16:19], v[212:215], v[68:71]\n\t"
    "buffer_load_dwordx4 v201, s[20:23], s63 offen offset:2176 lds\n\t"
    "v_mfma_f32_16x16x32_bf16 v[72:75], a[32:35], v[212:215], v[72:75]\n\t"
    "ds_read_b64_tr_b16 a[140:141], v244 offset:57344\n\t"
    "ds_read_b64_tr_b16 a[142:143], v244 offset:61440\n\t"
    "v_mfma_f32_16x16x32_bf16 v[72:75], a[36:39], v[216:219], v[72:75]\n\t"
    "s_mov_b32 s53, 0\n\t"
    "ds_read_b64_tr_b16 a[156:157], v245 offset:57344\n\t"
    "v_mfma_f32_16x16x32_bf16 v[72:75], a[40:43], v[220:223], v[72:75]\n\t"
    "s_mov_b32 m0, 0\n\t"
    "ds_read_b64_tr_b16 a[158:159], v245 offset:61440\n\t"
    "ds_read_b64_tr_b16 a[152:153], v245 offset:49152\n\t"
    "v_mfma_f32_16x16x32_bf16 v[72:75], a[44:47], v[224:227], v[72:75]\n\t"
    "buffer_load_dwordx4 v247, s[28:31], s53 offen offset:0 nt lds\n\t"
    "buffer_load_dwordx4 v247, s[28:31], s53 offen offset:2048 nt lds\n\t"
    "v_mfma_f32_16x16x32_bf16 v[76:79], a[60:63], v[224:227], v[76:79]\n\t"
    "ds_read_b64_tr_b16 a[154:155], v245 offset:53248\n\t"
    "v_mfma_f32_16x16x32_bf16 v[76:79], a[56:59], v[220:223], v[76:79]\n\t"
    "ds_read_b64_tr_b16 a[148:149], v245 offset:40960\n\t"
    "ds_read_b64_tr_b16 a[150:151], v245 offset:45056\n\t"
    "v_mfma_f32_16x16x32_bf16 v[76:79], a[52:55], v[216:219], v[76:79]\n\t"
    "s_waitcnt vmcnt(18)\n\t"
    "ds_read_b128 v[228:231], v246 offset:12288\n\t"
    "v_mfma_f32_16x16x32_bf16 v[76:79], a[48:51], v[212:215], v[76:79]\n\t"
    "ds_read_b128 v[232:235], v246 offset:12800\n\t"
    "ds_read_b64_tr_b16 a[144:145], v245 offset:32768\n\t"
    "v_mfma_f32_16x16x32_bf16 v[80:83], a[64:67], v[212:215], v[80:83]\n\t"
    "ds_read_b128 v[236:239], v246 offset:13312\n\t"
    "v_mfma_f32_16x16x32_bf16 v[80:83], a[68:71], v[216:219], v[80:83]\n\t"
    "ds_read_b128 v[240:243], v246 offset:13824\n\t"
    "ds_read_b64_tr_b16 a[146:147], v245 offset:36864\n\t"
    "v_mfma_f32_16x16x32_bf16 v[80:83], a[72:75], v[220:223], v[80:83]\n\t"
    "s_mov_b32 m0, 896\n\t"
    "ds_read_b64_tr_b16 a[160:161], v244 offset:33280\n\t"
    "v_mfma_f32_16x16x32_bf16 v[80:83], a[76:79], v[224:227], v[80:83]\n\t"
    "buffer_load_dwordx4 v247, s[28:31], s53 offen offset:128 nt lds\n\t"
    "buffer_load_dwordx4 v247, s[28:31], s53 offen offset:2176 nt lds\n\t"
    "v_mfma_f32_16x16x32_bf16 v[84:87], a[92:95], v[224:227], v[84:87]\n\t"
    "ds_read_b64_tr_b16 a[162:163], v244 offset:37376\n\t"
    "ds_read_b64_tr_b16 a[164:165], v244 offset:41472\n\t"
    "v_mfma_f32_16x16x32_bf16 v[84:87], a[88:91], v[220:223], v[84:87]\n\t"
    "s_mov_b32 s53, 4096\n\t"
    "ds_read_b64_tr_b16 a[166:167], v244 offset:45568\n\t"
    "v_mfma_f32_16x16x32_bf16 v[84:87], a[84:87], v[216:219], v[84:87]\n\t"
    "s_mov_b32 m0, 4096\n\t"
    "ds_read_b64_tr_b16 a[168:169], v244 offset:49664\n\t"
    "ds_read_b64_tr_b16 a[170:171], v244 offset:53760\n\t"
    "v_mfma_f32_16x16x32_bf16 v[84:87], a[80:83], v[212:215], v[84:87]\n\t"
    "buffer_load_dwordx4 v247, s[28:31], s53 offen offset:0 nt lds\n\t"
    "buffer_load_dwordx4 v247, s[28:31], s53 offen offset:2048 nt lds\n\t"
    "v_mfma_f32_16x16x32_bf16 v[88:91], a[96:99], v[212:215], v[88:91]\n\t"
    "ds_read_b64_tr_b16 a[172:173], v244 offset:57856\n\t"
    "v_mfma_f32_16x16x32_bf16 v[88:91], a[100:103], v[216:219], v[88:91]\n\t"
    "ds_read_b64_tr_b16 a[174:175], v244 offset:61952\n\t"
    "ds_read_b64_tr_b16 a[188:189], v245 offset:57856\n\t"
    "v_mfma_f32_16x16x32_bf16 v[88:91], a[104:107], v[220:223], v[88:91]\n\t"
    "s_mov_b32 m0, 4992\n\t"
    "ds_read_b64_tr_b16 a[190:191], v245 offset:61952\n\t"
    "v_mfma_f32_16x16x32_bf16 v[88:91], a[108:111], v[224:227], v[88:91]\n\t"
    "buffer_load_dwordx4 v247, s[28:31], s53 offen offset:128 nt lds\n\t"
    "buffer_load_dwordx4 v247, s[28:31], s53 offen offset:2176 nt lds\n\t"
    "v_mfma_f32_16x16x32_bf16 v[92:95], a[124:127], v[224:227], v[92:95]\n\t"
    "ds_read_b64_tr_b16 a[184:185], v245 offset:49664\n\t"
    "ds_read_b64_tr_b16 a[186:187], v245 offset:53760\n\t"
    "v_mfma_f32_16x16x32_bf16 v[92:95], a[120:123], v[220:223], v[92:95]\n\t"
    "ds_read_b64_tr_b16 a[180:181], v245 offset:41472\n\t"
    "v_mfma_f32_16x16x32_bf16 v[92:95], a[116:119], v[216:219], v[92:95]\n\t"
    "s_waitcnt lgkmcnt(14)\n\t"
    "ds_read_b64_tr_b16 a[182:183], v245 offset:45568\n\t"
    "ds_read_b64_tr_b16 a[176:177], v245 offset:33280\n\t"
    "v_mfma_f32_16x16x32_bf16 v[92:95], a[112:115], v[212:215], v[92:95]\n\t"
    "ds_read_b64_tr_b16 a[178:179], v245 offset:37376\n\t"
    "v_mfma_f32_16x16x32_bf16 v[96:99], a[0:3], v[228:231], v[96:99]\n\t"
    "s_add_u32 s63, s93, 768\n\t"
    "ds_read_b64_tr_b16 a[192:193], v244 offset:33792\n\t"
    "ds_read_b64_tr_b16 a[194:195], v244 offset:37888\n\t"
    "v_mfma_f32_16x16x32_bf16 v[96:99], a[4:7], v[232:235], v[96:99]\n\t"
    "s_add_u32 m0, s52, 12288\n\t"
    "ds_read_b64_tr_b16 a[196:197], v244 offset:41984\n\t"
    "v_mfma_f32_16x16x32_bf16 v[96:99], a[8:11], v[236:239], v[96:99]\n\t"
    "buffer_load_dwordx4 v255, s[20:23], s63 offen offset:0 lds\n\t"
    "v_mfma_f32_16x16x32_bf16 v[96:99], a[12:15], v[240:243], v[96:99]\n\t"
    "buffer_load_dwordx4 v201, s[20:23], s63 offen offset:2048 lds\n\t"
    "v_mfma_f32_16x16x32_bf16 v[100:103], a[28:31], v[240:243], v[100:103]\n\t"
    "ds_read_b64_tr_b16 a[198:199], v244 offset:46080\n\t"
    "v_mfma_f32_16x16x32_bf16 v[100:103], a[24:27], v[236:239], v[100:103]\n\t"
    "s_add_u32 m0, s52, 13184\n\t"
    "ds_read_b64_tr_b16 a[200:201], v244 offset:50176\n\t"
    "v_mfma_f32_16x16x32_bf16 v[100:103], a[20:23], v[232:235], v[100:103]\n\t"
    "buffer_load_dwordx4 v255, s[20:23], s63 offen offset:128 lds\n\t"
    "v_mfma_f32_16x16x32_bf16 v[100:103], a[16:19], v[228:231], v[100:103]\n\t"
    "buffer_load_dwordx4 v201, s[20:23], s63 offen offset:2176 lds\n\t"
    "v_mfma_f32_16x16x32_bf16 v[104:107], a[32:35], v[228:231], v[104:107]\n\t"
    "ds_read_b64_tr_b16 a[202:203], v244 offset:54272\n\t"
    "v_mfma_f32_16x16x32_bf16 v[104:107], a[36:39], v[232:235], v[104:107]\n\t"
    "s_mov_b32 s53, 8192\n\t"
    "ds_read_b64_tr_b16 a[204:205], v244 offset:58368\n\t"
    "v_mfma_f32_16x16x32_bf16 v[104:107], a[40:43], v[236:239], v[104:107]\n\t"
    "s_mov_b32 m0, 8192\n\t"
    "ds_read_b64_tr_b16 a[206:207], v244 offset:62464\n\t"
    "ds_read_b64_tr_b16 a[220:221], v245 offset:58368\n\t"
    "v_mfma_f32_16x16x32_bf16 v[104:107], a[44:47], v[240:243], v[104:107]\n\t"
    "buffer_load_dwordx4 v247, s[28:31], s53 offen offset:0 nt lds\n\t"
    "buffer_load_dwordx4 v247, s[28:31], s53 offen offset:2048 nt lds\n\t"
    "v_mfma_f32_16x16x32_bf16 v[108:111], a[60:63], v[240:243], v[108:111]\n\t"
    "ds_read_b64_tr_b16 a[222:223], v245 offset:62464\n\t"
    "v_mfma_f32_16x16x32_bf16 v[108:111], a[56:59], v[236:239], v[108:111]\n\t"
    "ds_read_b64_tr_b16 a[216:217], v245 offset:50176\n\t"
    "v_mfma_f32_16x16x32_bf16 v[108:111], a[52:55], v[232:235], v[108:111]\n\t"
    "s_waitcnt vmcnt(22)\n\t"
    "ds_read_b128 v[212:215], v246 offset:0\n\t"
    "v_mfma_f32_16x16x32_bf16 v[108:111], a[48:51], v[228:231], v[108:111]\n\t"
    "ds_read_b128 v[216:219], v246 offset:512\n\t"
    "v_mfma_f32_16x16x32_bf16 v[112:115], a[64:67], v[228:231], v[112:115]\n\t"
    "ds_read_b128 v[220:223], v246 offset:1024\n\t"
    "v_mfma_f32_16x16x32_bf16 v[112:115], a[68:71], v[232:235], v[112:115]\n\t"
    "ds_read_b128 v[224:227], v246 offset:1536\n\t"
    "ds_read_b64_tr_b16 a[218:219], v245 offset:54272\n\t"
    "v_mfma_f32_16x16x32_bf16 v[112:115], a[72:75], v[236:239], v[112:115]\n\t"
    "s_mov_b32 m0, 9088\n\t"
    "ds_read_b64_tr_b16 a[212:213], v245 offset:41984\n\t"
    "v_mfma_f32_16x16x32_bf16 v[112:115], a[76:79], v[240:243], v[112:115]\n\t"
    "buffer_load_dwordx4 v247, s[28:31], s53 offen offset:128 nt lds\n\t"
    "buffer_load_dwordx4 v247, s[28:31], s53 offen offset:2176 nt lds\n\t"
    "v_mfma_f32_16x16x32_bf16 v[116:119], a[92:95], v[240:243], v[116:119]\n\t"
    "ds_read_b64_tr_b16 a[214:215], v245 offset:46080\n\t"
    "v_mfma_f32_16x16x32_bf16 v[116:119], a[88:91], v[236:239], v[116:119]\n\t"
    "s_mov_b32 s53, 12288\n\t"
    "ds_read_b64_tr_b16 a[208:209], v245 offset:33792\n\t"
    "v_mfma_f32_16x16x32_bf16 v[116:119], a[84:87], v[232:235], v[116:119]\n\t"
    "s_mov_b32 m0, 12288\n\t"
    "ds_read_b64_tr_b16 a[210:211], v245 offset:37888\n\t"
    "v_mfma_f32_16x16x32_bf16 v[116:119], a[80:83], v[228:231], v[116:119]\n\t"
    "buffer_load_dwordx4 v247, s[28:31], s53 offen offset:0 nt lds\n\t"
    "buffer_load_dwordx4 v247, s[28:31], s53 offen offset:2048 nt lds\n\t"
    "v_mfma_f32_16x16x32_bf16 v[120:123], a[96:99], v[228:231], v[120:123]\n\t"
    "ds_read_b64_tr_b16 a[224:225], v244 offset:34304\n\t"
    "v_mfma_f32_16x16x32_bf16 v[120:123], a[100:103], v[232:235], v[120:123]\n\t"
    "ds_read_b64_tr_b16 a[226:227], v244 offset:38400\n\t"
    "ds_read_b64_tr_b16 a[228:229], v244 offset:42496\n\t"
    "v_mfma_f32_16x16x32_bf16 v[120:123], a[104:107], v[236:239], v[120:123]\n\t"
    "s_mov_b32 m0, 13184\n\t"
    "ds_read_b64_tr_b16 a[230:231], v244 offset:46592\n\t"
    "v_mfma_f32_16x16x32_bf16 v[120:123], a[108:111], v[240:243], v[120:123]\n\t"
    "buffer_load_dwordx4 v247, s[28:31], s53 offen offset:128 nt lds\n\t"
    "buffer_load_dwordx4 v247, s[28:31], s53 offen offset:2176 nt lds\n\t"
    "v_mfma_f32_16x16x32_bf16 v[124:127], a[124:127], v[240:243], v[124:127]\n\t"
    "ds_read_b64_tr_b16 a[232:233], v244 offset:50688\n\t"
    "v_mfma_f32_16x16x32_bf16 v[124:127], a[120:123], v[236:239], v[124:127]\n\t"
    "ds_read_b64_tr_b16 a[234:235], v244 offset:54784\n\t"
    "v_mfma_f32_16x16x32_bf16 v[124:127], a[116:119], v[232:235], v[124:127]\n\t"
    "s_waitcnt lgkmcnt(11)\n\t"
    "ds_read_b64_tr_b16 a[236:237], v244 offset:58880\n\t"
    "v_mfma_f32_16x16x32_bf16 v[124:127], a[112:115], v[228:231], v[124:127]\n\t"
    "s_waitcnt lgkmcnt(15)\n\t"
    "ds_read_b64_tr_b16 a[238:239], v244 offset:62976\n\t"
    "v_mfma_f32_16x16x32_bf16 v[0:3], a[128:131], v[212:215], v[0:3]\n\t"
    "s_mov_b32 s71, 0x20000\n\t"
    "ds_read_b64_tr_b16 a[252:253], v245 offset:58880\n\t"
    "v_accvgpr_write_b32 a32, v160\n\t"
    "v_mfma_f32_16x16x32_bf16 v[0:3], a[132:135], v[216:219], v[0:3]\n\t"
    "s_mov_b32 s53, 16384\n\t"
    "ds_read_b64_tr_b16 a[254:255], v245 offset:62976\n\t"
    "v_accvgpr_write_b32 a33, v161\n\t"
    "v_mfma_f32_16x16x32_bf16 v[0:3], a[136:139], v[220:223], v[0:3]\n\t"
    "s_mov_b32 m0, 16384\n\t"
    "ds_read_b64_tr_b16 a[248:249], v245 offset:50688\n\t"
    "v_accvgpr_write_b32 a34, v162\n\t"
    "v_mfma_f32_16x16x32_bf16 v[0:3], a[140:143], v[224:227], v[0:3]\n\t"
    "buffer_load_dwordx4 v247, s[28:31], s53 offen offset:0 nt lds\n\t"
    "buffer_load_dwordx4 v247, s[28:31], s53 offen offset:2048 nt lds\n\t"
    "v_mfma_f32_16x16x32_bf16 v[4:7], a[156:159], v[224:227], v[4:7]\n\t"
    "s_mov_b32 s75, 0x20000\n\t"
    "ds_read_b64_tr_b16 a[250:251], v245 offset:54784\n\t"
    "v_accvgpr_write_b32 a35, v163\n\t"
    "v_mfma_f32_16x16x32_bf16 v[4:7], a[152:155], v[220:223], v[4:7]\n\t"
    "s_mov_b32 s5, 0x1f800000\n\t"
    "ds_read_b64_tr_b16 a[244:245], v245 offset:42496\n\t"
    "v_accvgpr_write_b32 a36, v164\n\t"
    "v_mfma_f32_16x16x32_bf16 v[4:7], a[148:151], v[216:219], v[4:7]\n\t"
    "s_mov_b32 m0, 17280\n\t"
    "ds_read_b64_tr_b16 a[246:247], v245 offset:46592\n\t"
    "v_accvgpr_write_b32 a37, v165\n\t"
    "v_mfma_f32_16x16x32_bf16 v[4:7], a[144:147], v[212:215], v[4:7]\n\t"
    "buffer_load_dwordx4 v247, s[28:31], s53 offen offset:128 nt lds\n\t"
    "buffer_load_dwordx4 v247, s[28:31], s53 offen offset:2176 nt lds\n\t"
    "v_mfma_f32_16x16x32_bf16 v[8:11], a[160:163], v[212:215], v[8:11]\n\t"
    "s_lshr_b32 s45, s96, 2\n\t"
    "ds_read_b64_tr_b16 a[240:241], v245 offset:34304\n\t"
    "v_accvgpr_write_b32 a38, v166\n\t"
    "v_mfma_f32_16x16x32_bf16 v[8:11], a[164:167], v[216:219], v[8:11]\n\t"
    "s_mov_b32 s53, 20480\n\t"
    "ds_read_b64_tr_b16 a[242:243], v245 offset:38400\n\t"
    "v_accvgpr_write_b32 a39, v167\n\t"
    "v_mfma_f32_16x16x32_bf16 v[8:11], a[168:171], v[220:223], v[8:11]\n\t"
    "s_mov_b32 m0, 20480\n\t"
    "v_accvgpr_write_b32 a40, v168\n\t"
    "v_accvgpr_write_b32 a41, v169\n\t"
    "v_mfma_f32_16x16x32_bf16 v[8:11], a[172:175], v[224:227], v[8:11]\n\t"
    "buffer_load_dwordx4 v247, s[28:31], s53 offen offset:0 nt lds\n\t"
    "buffer_load_dwordx4 v247, s[28:31], s53 offen offset:2048 nt lds\n\t"
    "v_mfma_f32_16x16x32_bf16 v[12:15], a[188:191], v[224:227], v[12:15]\n\t"
    "s_sub_u32 s46, 10, s45\n\t"
    "v_accvgpr_write_b32 a42, v170\n\t"
    "v_accvgpr_write_b32 a43, v171\n\t"
    "v_mfma_f32_16x16x32_bf16 v[12:15], a[184:187], v[220:223], v[12:15]\n\t"
    "s_waitcnt lgkmcnt(8)\n\t"
    "v_accvgpr_write_b32 a44, v172\n\t"
    "v_accvgpr_write_b32 a45, v173\n\t"
    "v_mfma_f32_16x16x32_bf16 v[12:15], a[180:183], v[216:219], v[12:15]\n\t"
    "s_waitcnt vmcnt(30)\n\t"
    "s_mov_b32 m0, 21376\n\t"
    "ds_read_b128 v[228:231], v246 offset:4096\n\t"
    "v_mfma_f32_16x16x32_bf16 v[12:15], a[176:179], v[212:215], v[12:15]\n\t"
    "buffer_load_dwordx4 v247, s[28:31], s53 offen offset:128 nt lds\n\t"
    "buffer_load_dwordx4 v247, s[28:31], s53 offen offset:2176 nt lds\n\t"
    "ds_read_b128 v[232:235], v246 offset:4608\n\t"
    "v_mfma_f32_16x16x32_bf16 v[16:19], a[192:195], v[212:215], v[16:19]\n\t"
    "s_mul_i32 s60, s45, 0x110\n\t"
    "ds_read_b128 v[236:239], v246 offset:5120\n\t"
    "v_accvgpr_write_b32 a46, v174\n\t"
    "v_mfma_f32_16x16x32_bf16 v[16:19], a[196:199], v[216:219], v[16:19]\n\t"
    "s_sub_u32 s60, 0x863, s60\n\t"
    "ds_read_b128 v[240:243], v246 offset:5632\n\t"
    "v_accvgpr_write_b32 a47, v175\n\t"
    "v_mfma_f32_16x16x32_bf16 v[16:19], a[200:203], v[220:223], v[16:19]\n\t"
    "s_sub_u32 s8, 2, s45\n\t"
    "v_accvgpr_write_b32 a48, 0\n\t"
    "v_accvgpr_write_b32 a49, 0\n\t"
    "v_mfma_f32_16x16x32_bf16 v[16:19], a[204:207], v[224:227], v[16:19]\n\t"
    "s_lshl_b32 s61, s95, 2\n\t"
    "v_accvgpr_write_b32 a50, 0\n\t"
    "v_accvgpr_write_b32 a51, 0\n\t"
    "v_mfma_f32_16x16x32_bf16 v[20:23], a[220:223], v[224:227], v[20:23]\n\t"
    "s_lshr_b32 s60, s60, s61\n\t"
    "v_mov_b32 v207, 0\n\t"
    "v_mov_b32 v206, 0\n\t"
    "v_mfma_f32_16x16x32_bf16 v[20:23], a[216:219], v[220:223], v[20:23]\n\t"
    "s_mov_b32 s53, 24576\n\t"
    "v_mov_b32 v205, 0\n\t"
    "v_mov_b32 v204, 0\n\t"
    "v_mfma_f32_16x16x32_bf16 v[20:23], a[212:215], v[216:219], v[20:23]\n\t"
    "s_mov_b32 m0, 24576\n\t"
    "v_mov_b32 v203, 0\n\t"
    "v_mov_b32 v202, 0\n\t"
    "v_mfma_f32_16x16x32_bf16 v[20:23], a[208:211], v[212:215], v[20:23]\n\t"
    "buffer_load_dwordx4 v247, s[28:31], s53 offen offset:0 nt lds\n\t"
    "buffer_load_dwordx4 v247, s[28:31], s53 offen offset:2048 nt lds\n\t"
    "v_mfma_f32_16x16x32_bf16 v[24:27], a[224:227], v[212:215], v[24:27]\n\t"
    "s_and_b32 s47, s60, 15\n\t"
    "v_mov_b32 v201, 0\n\t"
    "v_mov_b32 v200, 0\n\t"
    "v_mfma_f32_16x16x32_bf16 v[24:27], a[228:231], v[216:219], v[24:27]\n\t"
    "s_add_u32 s60, s47, s46\n\t"
    "v_mfma_f32_16x16x32_bf16 v[24:27], a[232:235], v[220:223], v[24:27]\n\t"
    "s_mov_b32 m0, 25472\n\t"
    "s_waitcnt lgkmcnt(4)\n\t"
    "v_mfma_f32_16x16x32_bf16 v[24:27], a[236:239], v[224:227], v[24:27]\n\t"
    "buffer_load_dwordx4 v247, s[28:31], s53 offen offset:128 nt lds\n\t"
    "buffer_load_dwordx4 v247, s[28:31], s53 offen offset:2176 nt lds\n\t"
    "v_mfma_f32_16x16x32_bf16 v[28:31], a[252:255], v[224:227], v[28:31]\n\t"
    "s_sub_u32 s60, s60, 3\n\t"
    "v_mfma_f32_16x16x32_bf16 v[28:31], a[248:251], v[220:223], v[28:31]\n\t"
    "s_sub_u32 s61, s60, s46\n\t"
    "s_cselect_b32 s60, s60, s61\n\t"
    "v_mfma_f32_16x16x32_bf16 v[28:31], a[244:247], v[216:219], v[28:31]\n\t"
    "s_waitcnt lgkmcnt(0)\n\t"
    "v_mfma_f32_16x16x32_bf16 v[28:31], a[240:243], v[212:215], v[28:31]\n\t"
    "v_mfma_f32_16x16x32_bf16 v[32:35], a[128:131], v[228:231], v[32:35]\n\t"
    "s_mul_i32 s49, s60, 16384\n\t"
    "v_mul_f32 v0, s89, v0\n\t"
    "v_mul_f32 v1, s89, v1\n\t"
    "v_mfma_f32_16x16x32_bf16 v[32:35], a[132:135], v[232:235], v[32:35]\n\t"
    "s_mov_b32 s53, 28672\n\t"
    "v_mul_f32 v2, s89, v2\n\t"
    "v_mul_f32 v3, s89, v3\n\t"
    "v_mfma_f32_16x16x32_bf16 v[32:35], a[136:139], v[236:239], v[32:35]\n\t"
    "s_mov_b32 m0, 28672\n\t"
    "v_mul_f32 v4, s89, v4\n\t"
    "v_mul_f32 v5, s89, v5\n\t"
    "v_mfma_f32_16x16x32_bf16 v[32:35], a[140:143], v[240:243], v[32:35]\n\t"
    "buffer_load_dwordx4 v247, s[28:31], s53 offen offset:0 nt lds\n\t"
    "buffer_load_dwordx4 v247, s[28:31], s53 offen offset:2048 nt lds\n\t"
    "v_mfma_f32_16x16x32_bf16 v[36:39], a[156:159], v[240:243], v[36:39]\n\t"
    "s_add_u32 s60, s47, s46\n\t"
    "v_mul_f32 v6, s89, v6\n\t"
    "v_mul_f32 v7, s89, v7\n\t"
    "v_mfma_f32_16x16x32_bf16 v[36:39], a[152:155], v[236:239], v[36:39]\n\t"
    "s_sub_u32 s60, s60, 2\n\t"
    "v_cvt_pk_bf16_f32 v0, v0, v1\n\t"
    "v_cvt_pk_bf16_f32 v1, v2, v3\n\t"
    "v_mfma_f32_16x16x32_bf16 v[36:39], a[148:151], v[232:235], v[36:39]\n\t"
    "s_mov_b32 m0, 29568\n\t"
    "v_cvt_pk_bf16_f32 v2, v4, v5\n\t"
    "v_cvt_pk_bf16_f32 v3, v6, v7\n\t"
    "v_mfma_f32_16x16x32_bf16 v[36:39], a[144:147], v[228:231], v[36:39]\n\t"
    "buffer_load_dwordx4 v247, s[28:31], s53 offen offset:128 nt lds\n\t"
    "buffer_load_dwordx4 v247, s[28:31], s53 offen offset:2176 nt lds\n\t"
    "v_mfma_f32_16x16x32_bf16 v[40:43], a[160:163], v[228:231], v[40:43]\n\t"
    "s_sub_u32 s61, s60, s46\n\t"
    "s_cselect_b32 s60, s60, s61\n\t"
    "v_mul_f32 v8, s89, v8\n\t"
    "v_mul_f32 v9, s89, v9\n\t"
    "v_mfma_f32_16x16x32_bf16 v[40:43], a[164:167], v[232:235], v[40:43]\n\t"
    "s_mov_b32 s53, 0\n\t"
    "v_mul_f32 v10, s89, v10\n\t"
    "v_mul_f32 v11, s89, v11\n\t"
    "v_mfma_f32_16x16x32_bf16 v[40:43], a[168:171], v[236:239], v[40:43]\n\t"
    "s_mov_b32 m0, 65536\n\t"
    "v_mul_f32 v12, s89, v12\n\t"
    "v_mul_f32 v13, s89, v13\n\t"
    "v_mfma_f32_16x16x32_bf16 v[40:43], a[172:175], v[240:243], v[40:43]\n\t"
    "buffer_load_dwordx4 v247, s[16:19], s53 offen offset:0 nt lds\n\t"
    "buffer_load_dwordx4 v247, s[16:19], s53 offen offset:2048 nt lds\n\t"
    "v_mfma_f32_16x16x32_bf16 v[44:47], a[188:191], v[240:243], v[44:47]\n\t"
    "s_mul_i32 s50, s60, 16384\n\t"
    "v_mul_f32 v14, s89, v14\n\t"
    "v_mul_f32 v15, s89, v15\n\t"
    "v_mfma_f32_16x16x32_bf16 v[44:47], a[184:187], v[236:239], v[44:47]\n\t"
    "s_add_u32 s60, s47, s46\n\t"
    "v_permlane16_swap_b32 v0, v2\n\t"
    "v_mfma_f32_16x16x32_bf16 v[44:47], a[180:183], v[232:235], v[44:47]\n\t"
    "s_waitcnt vmcnt(38)\n\t"
    "s_mov_b32 m0, 66432\n\t"
    "ds_read_b128 v[212:215], v246 offset:8192\n\t"
    "v_mfma_f32_16x16x32_bf16 v[44:47], a[176:179], v[228:231], v[44:47]\n\t"
    "buffer_load_dwordx4 v247, s[16:19], s53 offen offset:128 nt lds\n\t"
    "buffer_load_dwordx4 v247, s[16:19], s53 offen offset:2176 nt lds\n\t"
    "ds_read_b128 v[216:219], v246 offset:8704\n\t"
    "v_mfma_f32_16x16x32_bf16 v[48:51], a[192:195], v[228:231], v[48:51]\n\t"
    "s_sub_u32 s60, s60, 1\n\t"
    "ds_read_b128 v[220:223], v246 offset:9216\n\t"
    "v_mfma_f32_16x16x32_bf16 v[48:51], a[196:199], v[232:235], v[48:51]\n\t"
    "s_sub_u32 s61, s60, s46\n\t"
    "s_cselect_b32 s60, s60, s61\n\t"
    "ds_read_b128 v[224:227], v246 offset:9728\n\t"
    "v_mfma_f32_16x16x32_bf16 v[48:51], a[200:203], v[236:239], v[48:51]\n\t"
    "s_mul_i32 s59, s60, 16384\n\t"
    "v_permlane16_swap_b32 v1, v3\n\t"
    "v_mfma_f32_16x16x32_bf16 v[48:51], a[204:207], v[240:243], v[48:51]\n\t"
    "s_cmp_ge_u32 s95, s8\n\t"
    "s_cmov_b32 s49, s50\n\t"
    "v_cvt_pk_bf16_f32 v4, v8, v9\n\t"
    "v_cvt_pk_bf16_f32 v5, v10, v11\n\t"
    "v_mfma_f32_16x16x32_bf16 v[52:55], a[220:223], v[240:243], v[52:55]\n\t"
    "s_lshl_b32 s60, s41, 2\n\t"
    "v_cvt_pk_bf16_f32 v6, v12, v13\n\t"
    "v_cvt_pk_bf16_f32 v7, v14, v15\n\t"
    "v_mfma_f32_16x16x32_bf16 v[52:55], a[216:219], v[236:239], v[52:55]\n\t"
    "s_mov_b32 s53, 4096\n\t"
    "v_mul_f32 v16, s89, v16\n\t"
    "v_mul_f32 v17, s89, v17\n\t"
    "v_mfma_f32_16x16x32_bf16 v[52:55], a[212:215], v[232:235], v[52:55]\n\t"
    "s_mov_b32 m0, 69632\n\t"
    "v_mul_f32 v18, s89, v18\n\t"
    "v_mul_f32 v19, s89, v19\n\t"
    "v_mfma_f32_16x16x32_bf16 v[52:55], a[208:211], v[228:231], v[52:55]\n\t"
    "buffer_load_dwordx4 v247, s[16:19], s53 offen offset:0 nt lds\n\t"
    "buffer_load_dwordx4 v247, s[16:19], s53 offen offset:2048 nt lds\n\t"
    "v_mfma_f32_16x16x32_bf16 v[56:59], a[224:227], v[228:231], v[56:59]\n\t"
    "s_add_u32 s60, s60, s95\n\t"
    "v_mul_f32 v20, s89, v20\n\t"
    "v_mul_f32 v21, s89, v21\n\t"
    "v_mfma_f32_16x16x32_bf16 v[56:59], a[228:231], v[232:235], v[56:59]\n\t"
    "s_lshl_b32 s60, s60, 1\n\t"
    "v_mul_f32 v22, s89, v22\n\t"
    "v_mul_f32 v23, s89, v23\n\t"
    "v_mfma_f32_16x16x32_bf16 v[56:59], a[232:235], v[236:239], v[56:59]\n\t"
    "s_mov_b32 m0, 70528\n\t"
    "v_permlane16_swap_b32 v4, v6\n\t"
    "v_mfma_f32_16x16x32_bf16 v[56:59], a[236:239], v[240:243], v[56:59]\n\t"
    "buffer_load_dwordx4 v247, s[16:19], s53 offen offset:128 nt lds\n\t"
    "buffer_load_dwordx4 v247, s[16:19], s53 offen offset:2176 nt lds\n\t"
    "v_mfma_f32_16x16x32_bf16 v[60:63], a[252:255], v[240:243], v[60:63]\n\t"
    "s_add_u32 s60, s60, s45\n\t"
    "v_permlane16_swap_b32 v5, v7\n\t"
    "v_mfma_f32_16x16x32_bf16 v[60:63], a[248:251], v[236:239], v[60:63]\n\t"
    "s_mul_i32 s60, s60, 327680\n\t"
    "v_cvt_pk_bf16_f32 v8, v16, v17\n\t"
    "v_cvt_pk_bf16_f32 v9, v18, v19\n\t"
    "v_mfma_f32_16x16x32_bf16 v[60:63], a[244:247], v[232:235], v[60:63]\n\t"
    "s_waitcnt lgkmcnt(0)\n\t"
    "v_cvt_pk_bf16_f32 v10, v20, v21\n\t"
    "v_cvt_pk_bf16_f32 v11, v22, v23\n\t"
    "v_mfma_f32_16x16x32_bf16 v[60:63], a[240:243], v[228:231], v[60:63]\n\t"
    "v_mul_f32 v24, s89, v24\n\t"
    "v_mul_f32 v25, s89, v25\n\t"
    "v_mul_f32 v26, s89, v26\n\t"
    "v_mul_f32 v27, s89, v27\n\t"
    "v_mul_f32 v28, s89, v28\n\t"
    "v_mul_f32 v29, s89, v29\n\t"
    "v_mul_f32 v30, s89, v30\n\t"
    "v_mul_f32 v31, s89, v31\n\t"
    "v_permlane16_swap_b32 v8, v10\n\t"
    "v_permlane16_swap_b32 v9, v11\n\t"
    "v_cvt_pk_bf16_f32 v12, v24, v25\n\t"
    "v_cvt_pk_bf16_f32 v13, v26, v27\n\t"
    "v_cvt_pk_bf16_f32 v14, v28, v29\n\t"
    "v_cvt_pk_bf16_f32 v15, v30, v31\n\t"
    "s_nop 1\n\t"
    "v_permlane16_swap_b32 v12, v14\n\t"
    "v_permlane16_swap_b32 v13, v15\n\t"
    "v_mfma_f32_16x16x32_bf16 v[64:67], a[128:131], v[212:215], v[64:67]\n\t"
    "s_add_u32 s60, s60, s51\n\t"
    "v_mul_f32 v32, s89, v32\n\t"
    "v_mul_f32 v33, s89, v33\n\t"
    "v_mfma_f32_16x16x32_bf16 v[64:67], a[132:135], v[216:219], v[64:67]\n\t"
    "s_mov_b32 s53, 8192\n\t"
    "v_mul_f32 v34, s89, v34\n\t"
    "v_mul_f32 v35, s89, v35\n\t"
    "v_mfma_f32_16x16x32_bf16 v[64:67], a[136:139], v[220:223], v[64:67]\n\t"
    "s_mov_b32 m0, 73728\n\t"
    "v_mul_f32 v36, s89, v36\n\t"
    "v_mul_f32 v37, s89, v37\n\t"
    "v_mfma_f32_16x16x32_bf16 v[64:67], a[140:143], v[224:227], v[64:67]\n\t"
    "buffer_load_dwordx4 v247, s[16:19], s53 offen offset:0 nt lds\n\t"
    "buffer_load_dwordx4 v247, s[16:19], s53 offen offset:2048 nt lds\n\t"
    "v_mfma_f32_16x16x32_bf16 v[68:71], a[156:159], v[224:227], v[68:71]\n\t"
    "s_add_u32 s20, s80, s60\n\t"
    "s_addc_u32 s21, s81, 0\n\t"
    "v_mul_f32 v38, s89, v38\n\t"
    "v_mul_f32 v39, s89, v39\n\t"
    "v_mfma_f32_16x16x32_bf16 v[68:71], a[152:155], v[220:223], v[68:71]\n\t"
    "s_mov_b32 s22, 303104\n\t"
    "v_cvt_pk_bf16_f32 v32, v32, v33\n\t"
    "v_cvt_pk_bf16_f32 v33, v34, v35\n\t"
    "v_mfma_f32_16x16x32_bf16 v[68:71], a[148:151], v[216:219], v[68:71]\n\t"
    "s_mov_b32 m0, 74624\n\t"
    "v_cvt_pk_bf16_f32 v34, v36, v37\n\t"
    "v_cvt_pk_bf16_f32 v35, v38, v39\n\t"
    "v_mfma_f32_16x16x32_bf16 v[68:71], a[144:147], v[212:215], v[68:71]\n\t"
    "buffer_load_dwordx4 v247, s[16:19], s53 offen offset:128 nt lds\n\t"
    "buffer_load_dwordx4 v247, s[16:19], s53 offen offset:2176 nt lds\n\t"
    "v_mfma_f32_16x16x32_bf16 v[72:75], a[160:163], v[212:215], v[72:75]\n\t"
    "s_mul_i32 s90, s45, 131072\n\t"
    "v_mul_f32 v40, s89, v40\n\t"
    "v_mul_f32 v41, s89, v41\n\t"
    "v_mfma_f32_16x16x32_bf16 v[72:75], a[164:167], v[216:219], v[72:75]\n\t"
    "s_mov_b32 s53, 12288\n\t"
    "v_mul_f32 v42, s89, v42\n\t"
    "v_mul_f32 v43, s89, v43\n\t"
    "v_mfma_f32_16x16x32_bf16 v[72:75], a[168:171], v[220:223], v[72:75]\n\t"
    "s_mov_b32 m0, 77824\n\t"
    "v_mul_f32 v44, s89, v44\n\t"
    "v_mul_f32 v45, s89, v45\n\t"
    "v_mfma_f32_16x16x32_bf16 v[72:75], a[172:175], v[224:227], v[72:75]\n\t"
    "buffer_load_dwordx4 v247, s[16:19], s53 offen offset:0 nt lds\n\t"
    "buffer_load_dwordx4 v247, s[16:19], s53 offen offset:2048 nt lds\n\t"
    "v_mfma_f32_16x16x32_bf16 v[76:79], a[188:191], v[224:227], v[76:79]\n\t"
    "s_add_u32 s90, s90, s51\n\t"
    "v_mul_f32 v46, s89, v46\n\t"
    "v_mul_f32 v47, s89, v47\n\t"
    "v_mfma_f32_16x16x32_bf16 v[76:79], a[184:187], v[220:223], v[76:79]\n\t"
    "s_mul_i32 s91, s45, 4294868992\n\t"
    "v_permlane16_swap_b32 v32, v34\n\t"
    "v_mfma_f32_16x16x32_bf16 v[76:79], a[180:183], v[216:219], v[76:79]\n\t"
    "s_waitcnt vmcnt(38)\n\t"
    "s_mov_b32 m0, 78720\n\t"
    "ds_read_b128 v[228:231], v246 offset:12288\n\t"
    "v_mfma_f32_16x16x32_bf16 v[76:79], a[176:179], v[212:215], v[76:79]\n\t"
    "buffer_load_dwordx4 v247, s[16:19], s53 offen offset:128 nt lds\n\t"
    "buffer_load_dwordx4 v247, s[16:19], s53 offen offset:2176 nt lds\n\t"
    "ds_read_b128 v[232:235], v246 offset:12800\n\t"
    "v_mfma_f32_16x16x32_bf16 v[80:83], a[192:195], v[212:215], v[80:83]\n\t"
    "s_add_u32 s91, s91, s51\n\t"
    "ds_read_b128 v[236:239], v246 offset:13312\n\t"
    "v_mfma_f32_16x16x32_bf16 v[80:83], a[196:199], v[216:219], v[80:83]\n\t"
    "s_add_u32 s91, s91, 98304\n\t"
    "ds_read_b128 v[240:243], v246 offset:13824\n\t"
    "v_mfma_f32_16x16x32_bf16 v[80:83], a[200:203], v[220:223], v[80:83]\n\t"
    "s_mul_i32 s99, s45, 4294934528\n\t"
    "v_permlane16_swap_b32 v33, v35\n\t"
    "v_mfma_f32_16x16x32_bf16 v[80:83], a[204:207], v[224:227], v[80:83]\n\t"
    "s_add_u32 s99, s99, s51\n\t"
    "v_cvt_pk_bf16_f32 v36, v40, v41\n\t"
    "v_cvt_pk_bf16_f32 v37, v42, v43\n\t"
    "v_mfma_f32_16x16x32_bf16 v[84:87], a[220:223], v[224:227], v[84:87]\n\t"
    "s_add_u32 s99, s99, 131072\n\t"
    "v_cvt_pk_bf16_f32 v38, v44, v45\n\t"
    "v_cvt_pk_bf16_f32 v39, v46, v47\n\t"
    "v_mfma_f32_16x16x32_bf16 v[84:87], a[216:219], v[220:223], v[84:87]\n\t"
    "s_mov_b32 s53, 16384\n\t"
    "v_mul_f32 v48, s89, v48\n\t"
    "v_mul_f32 v49, s89, v49\n\t"
    "v_mfma_f32_16x16x32_bf16 v[84:87], a[212:215], v[216:219], v[84:87]\n\t"
    "s_mov_b32 m0, 81920\n\t"
    "v_mul_f32 v50, s89, v50\n\t"
    "v_mul_f32 v51, s89, v51\n\t"
    "v_mfma_f32_16x16x32_bf16 v[84:87], a[208:211], v[212:215], v[84:87]\n\t"
    "buffer_load_dwordx4 v247, s[16:19], s53 offen offset:0 nt lds\n\t"
    "buffer_load_dwordx4 v247, s[16:19], s53 offen offset:2048 nt lds\n\t"
    "v_mfma_f32_16x16x32_bf16 v[88:91], a[224:227], v[212:215], v[88:91]\n\t"
    "s_mov_b32 s48, 4294938624\n\t"
    "v_mul_f32 v52, s89, v52\n\t"
    "v_mul_f32 v53, s89, v53\n\t"
    "v_mfma_f32_16x16x32_bf16 v[88:91], a[228:231], v[216:219], v[88:91]\n\t"
    "s_mov_b32 s57, 1048576\n\t"
    "v_mul_f32 v54, s89, v54\n\t"
    "v_mul_f32 v55, s89, v55\n\t"
    "v_mfma_f32_16x16x32_bf16 v[88:91], a[232:235], v[220:223], v[88:91]\n\t"
    "s_mov_b32 m0, 82816\n\t"
    "v_permlane16_swap_b32 v36, v38\n\t"
    "v_mfma_f32_16x16x32_bf16 v[88:91], a[236:239], v[224:227], v[88:91]\n\t"
    "buffer_load_dwordx4 v247, s[16:19], s53 offen offset:128 nt lds\n\t"
    "buffer_load_dwordx4 v247, s[16:19], s53 offen offset:2176 nt lds\n\t"
    "v_mfma_f32_16x16x32_bf16 v[92:95], a[252:255], v[224:227], v[92:95]\n\t"
    "s_lshl_b32 s60, s41, 3\n\t"
    "v_permlane16_swap_b32 v37, v39\n\t"
    "v_mfma_f32_16x16x32_bf16 v[92:95], a[248:251], v[220:223], v[92:95]\n\t"
    "s_add_u32 s60, s60, s96\n\t"
    "v_cvt_pk_bf16_f32 v40, v48, v49\n\t"
    "v_cvt_pk_bf16_f32 v41, v50, v51\n\t"
    "v_mfma_f32_16x16x32_bf16 v[92:95], a[244:247], v[216:219], v[92:95]\n\t"
    "s_mul_i32 s60, s60, 160\n\t"
    "v_cvt_pk_bf16_f32 v42, v52, v53\n\t"
    "v_cvt_pk_bf16_f32 v43, v54, v55\n\t"
    "v_mfma_f32_16x16x32_bf16 v[92:95], a[240:243], v[212:215], v[92:95]\n\t"
    "s_add_u32 s60, s60, s40\n\t"
    "v_mul_f32 v56, s89, v56\n\t"
    "v_mul_f32 v57, s89, v57\n\t"
    "v_mfma_f32_16x16x32_bf16 v[180:183], a[32:35], v[0:3], v[180:183]\n\t"
    "s_mul_i32 s60, s60, 1024\n\t"
    "v_mul_f32 v58, s89, v58\n\t"
    "v_mul_f32 v59, s89, v59\n\t"
    "v_mfma_f32_16x16x32_bf16 v[180:183], a[36:39], v[4:7], v[180:183]\n\t"
    "s_add_u32 s60, s60, 25165824\n\t"
    "v_mul_f32 v60, s89, v60\n\t"
    "v_mul_f32 v61, s89, v61\n\t"
    "v_mfma_f32_16x16x32_bf16 v[180:183], a[40:43], v[8:11], v[180:183]\n\t"
    "s_waitcnt lgkmcnt(0)\n\t"
    "v_mul_f32 v62, s89, v62\n\t"
    "v_mul_f32 v63, s89, v63\n\t"
    "v_mfma_f32_16x16x32_bf16 v[180:183], a[44:47], v[12:15], v[180:183]\n\t"
    "v_permlane16_swap_b32 v40, v42\n\t"
    "v_permlane16_swap_b32 v41, v43\n\t"
    "v_cvt_pk_bf16_f32 v44, v56, v57\n\t"
    "v_cvt_pk_bf16_f32 v45, v58, v59\n\t"
    "v_cvt_pk_bf16_f32 v46, v60, v61\n\t"
    "v_cvt_pk_bf16_f32 v47, v62, v63\n\t"
    "s_nop 1\n\t"
    "v_permlane16_swap_b32 v44, v46\n\t"
    "v_permlane16_swap_b32 v45, v47\n\t"
    "v_mfma_f32_16x16x32_bf16 v[96:99], a[128:131], v[228:231], v[96:99]\n\t"
    "s_add_u32 s68, s80, s60\n\t"
    "s_addc_u32 s69, s81, 0\n\t"
    "v_mul_f32 v64, s89, v64\n\t"
    "v_mul_f32 v65, s89, v65\n\t"
    "v_mfma_f32_16x16x32_bf16 v[96:99], a[132:135], v[232:235], v[96:99]\n\t"
    "s_mov_b32 s53, 20480\n\t"
    "v_mul_f32 v66, s89, v66\n\t"
    "v_mul_f32 v67, s89, v67\n\t"
    "v_mfma_f32_16x16x32_bf16 v[96:99], a[136:139], v[236:239], v[96:99]\n\t"
    "s_mov_b32 m0, 86016\n\t"
    "v_mul_f32 v68, s89, v68\n\t"
    "v_mul_f32 v69, s89, v69\n\t"
    "v_mfma_f32_16x16x32_bf16 v[96:99], a[140:143], v[240:243], v[96:99]\n\t"
    "buffer_load_dwordx4 v247, s[16:19], s53 offen offset:0 nt lds\n\t"
    "buffer_load_dwordx4 v247, s[16:19], s53 offen offset:2048 nt lds\n\t"
    "v_mfma_f32_16x16x32_bf16 v[100:103], a[156:159], v[240:243], v[100:103]\n\t"
    "s_mov_b32 s70, 160768\n\t"
    "v_mul_f32 v70, s89, v70\n\t"
    "v_mul_f32 v71, s89, v71\n\t"
    "v_mfma_f32_16x16x32_bf16 v[100:103], a[152:155], v[236:239], v[100:103]\n\t"
    "s_mul_i32 s60, s41, 304\n\t"
    "v_cvt_pk_bf16_f32 v64, v64, v65\n\t"
    "v_cvt_pk_bf16_f32 v65, v66, v67\n\t"
    "v_mfma_f32_16x16x32_bf16 v[100:103], a[148:151], v[232:235], v[100:103]\n\t"
    "s_mov_b32 m0, 86912\n\t"
    "v_cvt_pk_bf16_f32 v66, v68, v69\n\t"
    "v_cvt_pk_bf16_f32 v67, v70, v71\n\t"
    "v_mfma_f32_16x16x32_bf16 v[100:103], a[144:147], v[228:231], v[100:103]\n\t"
    "buffer_load_dwordx4 v247, s[16:19], s53 offen offset:128 nt lds\n\t"
    "buffer_load_dwordx4 v247, s[16:19], s53 offen offset:2176 nt lds\n\t"
    "v_mfma_f32_16x16x32_bf16 v[104:107], a[160:163], v[228:231], v[104:107]\n\t"
    "s_mul_i32 s61, s45, 160\n\t"
    "v_mul_f32 v72, s89, v72\n\t"
    "v_mul_f32 v73, s89, v73\n\t"
    "v_mfma_f32_16x16x32_bf16 v[104:107], a[164:167], v[232:235], v[104:107]\n\t"
    "s_mov_b32 s53, 24576\n\t"
    "v_mul_f32 v74, s89, v74\n\t"
    "v_mul_f32 v75, s89, v75\n\t"
    "v_mfma_f32_16x16x32_bf16 v[104:107], a[168:171], v[236:239], v[104:107]\n\t"
    "s_mov_b32 m0, 90112\n\t"
    "v_mul_f32 v76, s89, v76\n\t"
    "v_mul_f32 v77, s89, v77\n\t"
    "v_mfma_f32_16x16x32_bf16 v[104:107], a[172:175], v[240:243], v[104:107]\n\t"
    "buffer_load_dwordx4 v247, s[16:19], s53 offen offset:0 nt lds\n\t"
    "buffer_load_dwordx4 v247, s[16:19], s53 offen offset:2048 nt lds\n\t"
    "v_mfma_f32_16x16x32_bf16 v[108:111], a[188:191], v[240:243], v[108:111]\n\t"
    "s_add_u32 s60, s60, s61\n\t"
    "v_mul_f32 v78, s89, v78\n\t"
    "v_mul_f32 v79, s89, v79\n\t"
    "v_mfma_f32_16x16x32_bf16 v[108:111], a[184:187], v[236:239], v[108:111]\n\t"
    "s_lshl_b32 s60, s60, 10\n\t"
    "v_permlane16_swap_b32 v64, v66\n\t"
    "v_mfma_f32_16x16x32_bf16 v[108:111], a[180:183], v[232:235], v[108:111]\n\t"
    "s_mov_b32 m0, 91008\n\t"
    "v_permlane16_swap_b32 v65, v67\n\t"
    "v_mfma_f32_16x16x32_bf16 v[108:111], a[176:179], v[228:231], v[108:111]\n\t"
    "buffer_load_dwordx4 v247, s[16:19], s53 offen offset:128 nt lds\n\t"
    "buffer_load_dwordx4 v247, s[16:19], s53 offen offset:2176 nt lds\n\t"
    "v_mfma_f32_16x16x32_bf16 v[112:115], a[192:195], v[228:231], v[112:115]\n\t"
    "s_and_b32 s61, s96, 3\n\t"
    "v_cvt_pk_bf16_f32 v68, v72, v73\n\t"
    "v_cvt_pk_bf16_f32 v69, v74, v75\n\t"
    "v_mfma_f32_16x16x32_bf16 v[112:115], a[196:199], v[232:235], v[112:115]\n\t"
    "s_lshl_b32 s61, s61, 2\n\t"
    "v_cvt_pk_bf16_f32 v70, v76, v77\n\t"
    "v_cvt_pk_bf16_f32 v71, v78, v79\n\t"
    "v_mfma_f32_16x16x32_bf16 v[112:115], a[200:203], v[236:239], v[112:115]\n\t"
    "s_waitcnt vmcnt(44)\n\t"
    "v_mul_f32 v80, s89, v80\n\t"
    "v_mul_f32 v81, s89, v81\n\t"
    "v_mfma_f32_16x16x32_bf16 v[112:115], a[204:207], v[240:243], v[112:115]\n\t"
    "s_barrier\n\t"
    "ds_read_b128 a[0:3], v252 offset:0\n\t"
    "v_mul_f32 v82, s89, v82\n\t"
    "v_mfma_f32_16x16x32_bf16 v[116:119], a[220:223], v[240:243], v[116:119]\n\t"
    "s_add_u32 s61, s61, s40\n\t"
    "ds_read_b128 a[4:7], v252 offset:512\n\t"
    "v_mul_f32 v83, s89, v83\n\t"
    "v_mfma_f32_16x16x32_bf16 v[116:119], a[216:219], v[236:239], v[116:119]\n\t"
    "s_mov_b32 s53, 28672\n\t"
    "ds_read_b128 a[8:11], v252 offset:1024\n\t"
    "v_mul_f32 v84, s89, v84\n\t"
    "v_mfma_f32_16x16x32_bf16 v[116:119], a[212:215], v[232:235], v[116:119]\n\t"
    "s_mov_b32 m0, 94208\n\t"
    "ds_read_b128 a[12:15], v252 offset:1536\n\t"
    "v_mul_f32 v85, s89, v85\n\t"
    "v_mfma_f32_16x16x32_bf16 v[116:119], a[208:211], v[228:231], v[116:119]\n\t"
    "buffer_load_dwordx4 v247, s[16:19], s53 offen offset:0 nt lds\n\t"
    "buffer_load_dwordx4 v247, s[16:19], s53 offen offset:2048 nt lds\n\t"
    "v_mfma_f32_16x16x32_bf16 v[120:123], a[224:227], v[228:231], v[120:123]\n\t"
    "s_lshl_b32 s61, s61, 6\n\t"
    "v_mul_f32 v86, s89, v86\n\t"
    "v_mul_f32 v87, s89, v87\n\t"
    "v_mfma_f32_16x16x32_bf16 v[120:123], a[228:231], v[232:235], v[120:123]\n\t"
    "s_add_u32 s60, s60, s61\n\t"
    "v_permlane16_swap_b32 v68, v70\n\t"
    "v_mfma_f32_16x16x32_bf16 v[120:123], a[232:235], v[236:239], v[120:123]\n\t"
    "s_mov_b32 m0, 95104\n\t"
    "v_permlane16_swap_b32 v69, v71\n\t"
    "v_mfma_f32_16x16x32_bf16 v[120:123], a[236:239], v[240:243], v[120:123]\n\t"
    "buffer_load_dwordx4 v247, s[16:19], s53 offen offset:128 nt lds\n\t"
    "buffer_load_dwordx4 v247, s[16:19], s53 offen offset:2176 nt lds\n\t"
    "v_mfma_f32_16x16x32_bf16 v[124:127], a[252:255], v[240:243], v[124:127]\n\t"
    "s_add_u32 s72, s78, s60\n\t"
    "s_addc_u32 s73, s79, 0\n\t"
    "v_cvt_pk_bf16_f32 v72, v80, v81\n\t"
    "v_cvt_pk_bf16_f32 v73, v82, v83\n\t"
    "v_mfma_f32_16x16x32_bf16 v[124:127], a[248:251], v[236:239], v[124:127]\n\t"
    "s_sub_u32 s74, 2490368, s60\n\t"
    "v_cvt_pk_bf16_f32 v74, v84, v85\n\t"
    "v_cvt_pk_bf16_f32 v75, v86, v87\n\t"
    "v_mfma_f32_16x16x32_bf16 v[124:127], a[244:247], v[232:235], v[124:127]\n\t"
    "v_mul_f32 v88, s89, v88\n\t"
    "v_mul_f32 v89, s89, v89\n\t"
    "v_mfma_f32_16x16x32_bf16 v[124:127], a[240:243], v[228:231], v[124:127]\n\t"
    "v_mul_f32 v90, s89, v90\n\t"
    "v_mul_f32 v91, s89, v91\n\t"
    "v_mfma_f32_16x16x32_bf16 v[184:187], a[32:35], v[32:35], v[184:187]\n\t"
    "v_mul_f32 v92, s89, v92\n\t"
    "v_mul_f32 v93, s89, v93\n\t"
    "v_mfma_f32_16x16x32_bf16 v[184:187], a[36:39], v[36:39], v[184:187]\n\t"
    "v_mul_f32 v94, s89, v94\n\t"
    "v_mul_f32 v95, s89, v95\n\t"
    "v_mfma_f32_16x16x32_bf16 v[184:187], a[40:43], v[40:43], v[184:187]\n\t"
    "v_permlane16_swap_b32 v72, v74\n\t"
    "v_mfma_f32_16x16x32_bf16 v[184:187], a[44:47], v[44:47], v[184:187]\n\t"
    "v_permlane16_swap_b32 v73, v75\n\t"
    "v_cvt_pk_bf16_f32 v76, v88, v89\n\t"
    "v_cvt_pk_bf16_f32 v77, v90, v91\n\t"
    "v_cvt_pk_bf16_f32 v78, v92, v93\n\t"
    "v_cvt_pk_bf16_f32 v79, v94, v95\n\t"
    "s_nop 1\n\t"
    "v_permlane16_swap_b32 v76, v78\n\t"
    "v_permlane16_swap_b32 v77, v79\n\t"
    "v_mfma_f32_16x16x32_bf16 v[188:191], a[32:35], v[64:67], v[188:191]\n\t"
    "s_lshl_b32 s60, s95, 20\n\t"
    "v_mul_f32 v96, s89, v96\n\t"
    "v_mul_f32 v97, s89, v97\n\t"
    "v_mfma_f32_16x16x32_bf16 v[188:191], a[36:39], v[68:71], v[188:191]\n\t"
    "s_and_b32 s61, s96, 3\n\t"
    "v_mul_f32 v98, s89, v98\n\t"
    "v_mul_f32 v99, s89, v99\n\t"
    "v_mfma_f32_16x16x32_bf16 v[188:191], a[40:43], v[72:75], v[188:191]\n\t"
    "s_waitcnt lgkmcnt(0)\n\t"
    "v_mul_f32 v100, s89, v100\n\t"
    "v_mul_f32 v101, s89, v101\n\t"
    "v_mfma_f32_16x16x32_bf16 v[188:191], a[44:47], v[76:79], v[188:191]\n\t"
    "s_lshl_b32 s61, s61, 8\n\t"
    "v_mul_f32 v102, s89, v102\n\t"
    "v_mul_f32 v103, s89, v103\n\t"
    "v_cvt_pk_bf16_f32 v96, v96, v97\n\t"
    "v_cvt_pk_bf16_f32 v97, v98, v99\n\t"
    "v_cvt_pk_bf16_f32 v98, v100, v101\n\t"
    "v_cvt_pk_bf16_f32 v99, v102, v103\n\t"
    "v_mul_f32 v104, s89, v104\n\t"
    "v_mul_f32 v105, s89, v105\n\t"
    "v_mul_f32 v106, s89, v106\n\t"
    "v_mul_f32 v107, s89, v107\n\t"
    "v_mul_f32 v108, s89, v108\n\t"
    "v_mul_f32 v109, s89, v109\n\t"
    "v_mul_f32 v110, s89, v110\n\t"
    "v_mul_f32 v111, s89, v111\n\t"
    "v_mfma_f32_16x16x32_bf16 v[160:163], a[0:3], v[0:3], v[180:183]\n\t"
    "s_add_u32 s60, s60, s61\n\t"
    "v_permlane16_swap_b32 v96, v98\n\t"
    "v_mfma_f32_16x16x32_bf16 v[160:163], a[4:7], v[4:7], v[160:163]\n\t"
    "s_add_u32 s24, s76, s60\n\t"
    "s_addc_u32 s25, s77, 0\n\t"
    "v_permlane16_swap_b32 v97, v99\n\t"
    "v_mfma_f32_16x16x32_bf16 v[160:163], a[8:11], v[8:11], v[160:163]\n\t"
    "s_sub_u32 s26, 0x100000, s61\n\t"
    "v_cvt_pk_bf16_f32 v100, v104, v105\n\t"
    "v_cvt_pk_bf16_f32 v101, v106, v107\n\t"
    "v_mfma_f32_16x16x32_bf16 v[160:163], a[12:15], v[12:15], v[160:163]\n\t"
    "s_mov_b32 s54, 7\n\t"
    "ds_read_b128 a[16:19], v252 offset:4096\n\t"
    "ds_read_b128 a[20:23], v252 offset:4608\n\t"
    "v_cvt_pk_bf16_f32 v102, v108, v109\n\t"
    "v_cvt_pk_bf16_f32 v103, v110, v111\n\t"
    "v_mul_f32 v112, s89, v112\n\t"
    "v_mul_f32 v113, s89, v113\n\t"
    "v_mul_f32 v114, s89, v114\n\t"
    "v_mul_f32 v115, s89, v115\n\t"
    "v_mul_f32 v116, s89, v116\n\t"
    "v_mul_f32 v117, s89, v117\n\t"
    "v_mul_f32 v118, s89, v118\n\t"
    "v_mul_f32 v119, s89, v119\n\t"
    "v_permlane16_swap_b32 v100, v102\n\t"
    "v_permlane16_swap_b32 v101, v103\n\t"
    "v_cvt_pk_bf16_f32 v104, v112, v113\n\t"
    "v_cvt_pk_bf16_f32 v105, v114, v115\n\t"
    "v_cvt_pk_bf16_f32 v106, v116, v117\n\t"
    "v_mfma_f32_16x16x32_bf16 v[164:167], a[12:15], v[44:47], v[184:187]\n\t"
    "s_lshl_b32 s93, s40, 9\n\t"
    "v_cvt_pk_bf16_f32 v107, v118, v119\n\t"
    "v_mul_f32 v120, s89, v120\n\t"
    "v_mfma_f32_16x16x32_bf16 v[164:167], a[8:11], v[40:43], v[164:167]\n\t"
    "s_sub_u32 s92, s93, 0x10000\n\t"
    "v_mul_f32 v121, s89, v121\n\t"
    "v_mul_f32 v122, s89, v122\n\t"
    "v_mfma_f32_16x16x32_bf16 v[164:167], a[4:7], v[36:39], v[164:167]\n\t"
    "s_add_u32 s94, s93, 0x10000\n\t"
    "v_mul_f32 v123, s89, v123\n\t"
    "v_mul_f32 v124, s89, v124\n\t"
    "v_mfma_f32_16x16x32_bf16 v[164:167], a[0:3], v[32:35], v[164:167]\n\t"
    "s_mov_b32 s55, 0x8000\n\t"
    "ds_read_b128 a[24:27], v252 offset:5120\n\t"
    "v_mul_f32 v125, s89, v125\n\t"
    "v_mul_f32 v126, s89, v126\n\t"
    "v_mul_f32 v127, s89, v127\n\t"
    "v_permlane16_swap_b32 v104, v106\n\t"
    "v_permlane16_swap_b32 v105, v107\n\t"
    "v_cvt_pk_bf16_f32 v108, v120, v121\n\t"
    "v_cvt_pk_bf16_f32 v109, v122, v123\n\t"
    "v_cvt_pk_bf16_f32 v110, v124, v125\n\t"
    "v_cvt_pk_bf16_f32 v111, v126, v127\n\t"
    "s_nop 1\n\t"
    "v_permlane16_swap_b32 v108, v110\n\t"
    "v_permlane16_swap_b32 v109, v111\n\t"
    "s_nop 1\n\t"
    "v_mfma_f32_16x16x32_bf16 v[192:195], a[32:35], v[96:99], v[192:195]\n\t"
    "s_mov_b32 s56, 0xc000\n\t"
    "v_add_u32 v198, 6144, v255\n\t"
    "v_lshrrev_b32 v144, 1, v249\n\t"
    "v_mfma_f32_16x16x32_bf16 v[192:195], a[36:39], v[100:103], v[192:195]\n\t"
    "s_mov_b32 s43, 0\n\t"
    "v_and_b32 v145, 1, v249\n\t"
    "v_lshlrev_b32 v145, 7, v145\n\t"
    "v_mfma_f32_16x16x32_bf16 v[192:195], a[40:43], v[104:107], v[192:195]\n\t"
    "s_mov_b32 s44, 0x20000\n\t"
    "v_lshl_add_u32 v144, v144, 10, v145\n\t"
    "v_add_u32 v145, 44, v249\n\t"
    "v_mfma_f32_16x16x32_bf16 v[192:195], a[44:47], v[108:111], v[192:195]\n\t"
    "s_mov_b32 s53, 32768\n\t"
    "v_lshrrev_b32 v145, 6, v145\n\t"
    "v_lshl_add_u32 v144, v145, 30, v144\n\t"
    "v_mfma_f32_16x16x32_bf16 v[168:171], a[0:3], v[64:67], v[188:191]\n\t"
    "s_mov_b32 m0, 32768\n\t"
    "v_exp_f32 v160, v160\n\t"
    "v_mfma_f32_16x16x32_bf16 v[168:171], a[4:7], v[68:71], v[168:171]\n\t"
    "buffer_load_dwordx4 v247, s[28:31], s53 offen offset:0 nt lds\n\t"
    "buffer_load_dwordx4 v247, s[28:31], s53 offen offset:2048 nt lds\n\t"
    "v_exp_f32 v161, v161\n\t"
    "v_mfma_f32_16x16x32_bf16 v[168:171], a[8:11], v[72:75], v[168:171]\n\t"
    "s_mov_b32 m0, 33664\n\t"
    "v_exp_f32 v162, v162\n\t"
    "v_mfma_f32_16x16x32_bf16 v[168:171], a[12:15], v[76:79], v[168:171]\n\t"
    "buffer_load_dwordx4 v247, s[28:31], s53 offen offset:128 nt lds\n\t"
    "buffer_load_dwordx4 v247, s[28:31], s53 offen offset:2176 nt lds\n\t"
    "ds_read_b128 a[28:31], v252 offset:5632\n\t"
    "v_exp_f32 v163, v163\n\t"
    "v_add_f32 v200, v200, v160\n\t"
    "v_add_f32 v201, v201, v161\n\t"
    "v_cvt_pk_bf16_f32 v16, v160, v161\n\t"
    "v_add_f32 v200, v200, v162\n\t"
    "v_add_f32 v201, v201, v163\n\t"
    "v_cvt_pk_bf16_f32 v17, v162, v163\n\t"
    "v_mfma_f32_16x16x32_bf16 v[172:175], a[12:15], v[108:111], v[192:195]\n\t"
    "s_mov_b32 s53, 36864\n\t"
    "s_mov_b32 m0, 36864\n\t"
    "v_exp_f32 v164, v164\n\t"
    "v_mfma_f32_16x16x32_bf16 v[172:175], a[8:11], v[104:107], v[172:175]\n\t"
    "buffer_load_dwordx4 v247, s[28:31], s53 offen offset:0 nt lds\n\t"
    "buffer_load_dwordx4 v247, s[28:31], s53 offen offset:2048 nt lds\n\t"
    "v_exp_f32 v165, v165\n\t"
    "v_mfma_f32_16x16x32_bf16 v[172:175], a[4:7], v[100:103], v[172:175]\n\t"
    "s_mov_b32 m0, 37760\n\t"
    "v_exp_f32 v166, v166\n\t"
    "v_mfma_f32_16x16x32_bf16 v[172:175], a[0:3], v[96:99], v[172:175]\n\t"
    "buffer_load_dwordx4 v247, s[28:31], s53 offen offset:128 nt lds\n\t"
    "buffer_load_dwordx4 v247, s[28:31], s53 offen offset:2176 nt lds\n\t"
    "v_exp_f32 v167, v167\n\t"
    "v_mfma_f32_32x32x16_bf16 a[96:111], a[48:51], a[48:51], 0\n\t"
    "v_add_f32 v202, v202, v164\n\t"
    "v_add_f32 v203, v203, v165\n\t"
    "v_cvt_pk_bf16_f32 v48, v164, v165\n\t"
    "v_add_f32 v202, v202, v166\n\t"
    "v_add_f32 v203, v203, v167\n\t"
    "v_cvt_pk_bf16_f32 v49, v166, v167\n\t"
    "s_waitcnt lgkmcnt(0)\n\t"
    "v_mfma_f32_16x16x32_bf16 v[160:163], a[16:19], v[0:3], v[180:183]\n\t"
    "s_mov_b32 s53, 40960\n\t"
    "s_mov_b32 m0, 40960\n\t"
    "v_exp_f32 v168, v168\n\t"
    "v_mfma_f32_16x16x32_bf16 v[160:163], a[20:23], v[4:7], v[160:163]\n\t"
    "buffer_load_dwordx4 v247, s[28:31], s53 offen offset:0 nt lds\n\t"
    "buffer_load_dwordx4 v247, s[28:31], s53 offen offset:2048 nt lds\n\t"
    "v_exp_f32 v169, v169\n\t"
    "v_mfma_f32_16x16x32_bf16 v[160:163], a[24:27], v[8:11], v[160:163]\n\t"
    "s_mov_b32 m0, 41856\n\t"
    "v_exp_f32 v170, v170\n\t"
    "v_mfma_f32_16x16x32_bf16 v[160:163], a[28:31], v[12:15], v[160:163]\n\t"
    "buffer_load_dwordx4 v247, s[28:31], s53 offen offset:128 nt lds\n\t"
    "buffer_load_dwordx4 v247, s[28:31], s53 offen offset:2176 nt lds\n\t"
    "ds_read_b128 a[0:3], v252 offset:8192\n\t"
    "ds_read_b128 a[4:7], v252 offset:8704\n\t"
    "v_exp_f32 v171, v171\n\t"
    "v_mfma_f32_32x32x16_bf16 a[112:127], a[48:51], a[48:51], 0\n\t"
    "v_add_f32 v204, v204, v168\n\t"
    "v_add_f32 v205, v205, v169\n\t"
    "v_cvt_pk_bf16_f32 v80, v168, v169\n\t"
    "v_add_f32 v204, v204, v170\n\t"
    "v_add_f32 v205, v205, v171\n\t"
    "v_cvt_pk_bf16_f32 v81, v170, v171\n\t"
    "v_mfma_f32_16x16x32_bf16 v[164:167], a[28:31], v[44:47], v[184:187]\n\t"
    "s_mov_b32 s53, 45056\n\t"
    "s_mov_b32 m0, 45056\n\t"
    "v_exp_f32 v172, v172\n\t"
    "v_mfma_f32_16x16x32_bf16 v[164:167], a[24:27], v[40:43], v[164:167]\n\t"
    "buffer_load_dwordx4 v247, s[28:31], s53 offen offset:0 nt lds\n\t"
    "buffer_load_dwordx4 v247, s[28:31], s53 offen offset:2048 nt lds\n\t"
    "v_exp_f32 v173, v173\n\t"
    "v_mfma_f32_16x16x32_bf16 v[164:167], a[20:23], v[36:39], v[164:167]\n\t"
    "s_mov_b32 m0, 45952\n\t"
    "v_exp_f32 v174, v174\n\t"
    "v_mfma_f32_16x16x32_bf16 v[164:167], a[16:19], v[32:35], v[164:167]\n\t"
    "buffer_load_dwordx4 v247, s[28:31], s53 offen offset:128 nt lds\n\t"
    "buffer_load_dwordx4 v247, s[28:31], s53 offen offset:2176 nt lds\n\t"
    "ds_read_b128 a[8:11], v252 offset:9216\n\t"
    "v_exp_f32 v175, v175\n\t"
    "v_mfma_f32_32x32x16_bf16 a[128:143], a[48:51], a[48:51], 0\n\t"
    "v_add_f32 v206, v206, v172\n\t"
    "v_add_f32 v207, v207, v173\n\t"
    "v_cvt_pk_bf16_f32 v112, v172, v173\n\t"
    "v_add_f32 v206, v206, v174\n\t"
    "v_add_f32 v207, v207, v175\n\t"
    "v_cvt_pk_bf16_f32 v113, v174, v175\n\t"
    "v_mfma_f32_16x16x32_bf16 v[168:171], a[16:19], v[64:67], v[188:191]\n\t"
    "s_mov_b32 s53, 32768\n\t"
    "s_mov_b32 m0, 98304\n\t"
    "v_exp_f32 v160, v160\n\t"
    "v_mfma_f32_16x16x32_bf16 v[168:171], a[20:23], v[68:71], v[168:171]\n\t"
    "buffer_load_dwordx4 v247, s[16:19], s53 offen offset:0 nt lds\n\t"
    "buffer_load_dwordx4 v247, s[16:19], s53 offen offset:2048 nt lds\n\t"
    "v_exp_f32 v161, v161\n\t"
    "v_mfma_f32_16x16x32_bf16 v[168:171], a[24:27], v[72:75], v[168:171]\n\t"
    "s_mov_b32 m0, 99200\n\t"
    "v_exp_f32 v162, v162\n\t"
    "v_mfma_f32_16x16x32_bf16 v[168:171], a[28:31], v[76:79], v[168:171]\n\t"
    "buffer_load_dwordx4 v247, s[16:19], s53 offen offset:128 nt lds\n\t"
    "buffer_load_dwordx4 v247, s[16:19], s53 offen offset:2176 nt lds\n\t"
    "ds_read_b128 a[12:15], v252 offset:9728\n\t"
    "v_exp_f32 v163, v163\n\t"
    "v_mfma_f32_32x32x16_bf16 a[144:159], a[48:51], a[48:51], 0\n\t"
    "v_add_f32 v200, v200, v160\n\t"
    "v_add_f32 v201, v201, v161\n\t"
    "v_cvt_pk_bf16_f32 v18, v160, v161\n\t"
    "v_add_f32 v200, v200, v162\n\t"
    "v_add_f32 v201, v201, v163\n\t"
    "v_cvt_pk_bf16_f32 v19, v162, v163\n\t"
    "v_mfma_f32_16x16x32_bf16 v[172:175], a[28:31], v[108:111], v[192:195]\n\t"
    "s_lshr_b32 s62, %1, 3\n\t"
    "v_exp_f32 v164, v164\n\t"
    "v_mfma_f32_16x16x32_bf16 v[172:175], a[24:27], v[104:107], v[172:175]\n\t"
    "s_mov_b32 s53, 36864\n\t"
    "v_exp_f32 v165, v165\n\t"
    "v_mfma_f32_16x16x32_bf16 v[172:175], a[20:23], v[100:103], v[172:175]\n\t"
    "s_mov_b32 m0, 102400\n\t"
    "v_exp_f32 v166, v166\n\t"
    "v_mfma_f32_16x16x32_bf16 v[172:175], a[16:19], v[96:99], v[172:175]\n\t"
    "buffer_load_dwordx4 v247, s[16:19], s53 offen offset:0 nt lds\n\t"
    "buffer_load_dwordx4 v247, s[16:19], s53 offen offset:2048 nt lds\n\t"
    "v_exp_f32 v167, v167\n\t"
    "v_mfma_f32_32x32x16_bf16 a[160:175], a[48:51], a[48:51], 0\n\t"
    "v_add_f32 v202, v202, v164\n\t"
    "v_add_f32 v203, v203, v165\n\t"
    "v_cvt_pk_bf16_f32 v50, v164, v165\n\t"
    "v_add_f32 v202, v202, v166\n\t"
    "v_add_f32 v203, v203, v167\n\t"
    "v_cvt_pk_bf16_f32 v51, v166, v167\n\t"
    "s_waitcnt lgkmcnt(0)\n\t"
    "v_mfma_f32_16x16x32_bf16 v[160:163], a[0:3], v[0:3], v[180:183]\n\t"
    "s_mul_i32 s62, s62, 2560\n\t"
    "v_exp_f32 v168, v168\n\t"
    "v_mfma_f32_16x16x32_bf16 v[160:163], a[4:7], v[4:7], v[160:163]\n\t"
    "s_and_b32 s60, s41, 3\n\t"
    "v_exp_f32 v169, v169\n\t"
    "v_mfma_f32_16x16x32_bf16 v[160:163], a[8:11], v[8:11], v[160:163]\n\t"
    "s_mov_b32 m0, 103296\n\t"
    "v_exp_f32 v170, v170\n\t"
    "v_mfma_f32_16x16x32_bf16 v[160:163], a[12:15], v[12:15], v[160:163]\n\t"
    "buffer_load_dwordx4 v247, s[16:19], s53 offen offset:128 nt lds\n\t"
    "buffer_load_dwordx4 v247, s[16:19], s53 offen offset:2176 nt lds\n\t"
    "ds_read_b128 a[16:19], v252 offset:12288\n\t"
    "ds_read_b128 a[20:23], v252 offset:12800\n\t"
    "v_exp_f32 v171, v171\n\t"
    "v_mfma_f32_32x32x16_bf16 a[176:191], a[48:51], a[48:51], 0\n\t"
    "v_add_f32 v204, v204, v168\n\t"
    "v_add_f32 v205, v205, v169\n\t"
    "v_cvt_pk_bf16_f32 v82, v168, v169\n\t"
    "v_add_f32 v204, v204, v170\n\t"
    "v_add_f32 v205, v205, v171\n\t"
    "v_cvt_pk_bf16_f32 v83, v170, v171\n\t"
    "v_mfma_f32_16x16x32_bf16 v[164:167], a[12:15], v[44:47], v[184:187]\n\t"
    "s_lshr_b32 s61, s96, 2\n\t"
    "v_exp_f32 v172, v172\n\t"
    "v_mfma_f32_16x16x32_bf16 v[164:167], a[8:11], v[40:43], v[164:167]\n\t"
    "s_mov_b32 s53, 40960\n\t"
    "v_exp_f32 v173, v173\n\t"
    "v_mfma_f32_16x16x32_bf16 v[164:167], a[4:7], v[36:39], v[164:167]\n\t"
    "s_mov_b32 m0, 106496\n\t"
    "v_exp_f32 v174, v174\n\t"
    "v_mfma_f32_16x16x32_bf16 v[164:167], a[0:3], v[32:35], v[164:167]\n\t"
    "buffer_load_dwordx4 v247, s[16:19], s53 offen offset:0 nt lds\n\t"
    "buffer_load_dwordx4 v247, s[16:19], s53 offen offset:2048 nt lds\n\t"
    "ds_read_b128 a[24:27], v252 offset:13312\n\t"
    "v_exp_f32 v175, v175\n\t"
    "v_mfma_f32_32x32x16_bf16 a[192:207], a[48:51], a[48:51], 0\n\t"
    "v_add_f32 v206, v206, v172\n\t"
    "v_add_f32 v207, v207, v173\n\t"
    "v_cvt_pk_bf16_f32 v114, v172, v173\n\t"
    "v_add_f32 v206, v206, v174\n\t"
    "v_add_f32 v207, v207, v175\n\t"
    "v_cvt_pk_bf16_f32 v115, v174, v175\n\t"
    "v_mfma_f32_16x16x32_bf16 v[168:171], a[0:3], v[64:67], v[188:191]\n\t"
    "s_lshl1_add_u32 s60, s60, s61\n\t"
    "v_exp_f32 v160, v160\n\t"
    "v_mfma_f32_16x16x32_bf16 v[168:171], a[4:7], v[68:71], v[168:171]\n\t"
    "s_lshl_b32 s89, s60, 17\n\t"
    "v_exp_f32 v161, v161\n\t"
    "v_mfma_f32_16x16x32_bf16 v[168:171], a[8:11], v[72:75], v[168:171]\n\t"
    "s_mov_b32 m0, 107392\n\t"
    "v_exp_f32 v162, v162\n\t"
    "v_mfma_f32_16x16x32_bf16 v[168:171], a[12:15], v[76:79], v[168:171]\n\t"
    "buffer_load_dwordx4 v247, s[16:19], s53 offen offset:128 nt lds\n\t"
    "buffer_load_dwordx4 v247, s[16:19], s53 offen offset:2176 nt lds\n\t"
    "ds_read_b128 a[28:31], v252 offset:13824\n\t"
    "v_exp_f32 v163, v163\n\t"
    "v_mfma_f32_32x32x16_bf16 a[208:223], a[48:51], a[48:51], 0\n\t"
    "v_add_f32 v200, v200, v160\n\t"
    "v_add_f32 v201, v201, v161\n\t"
    "v_cvt_pk_bf16_f32 v20, v160, v161\n\t"
    "v_add_f32 v200, v200, v162\n\t"
    "v_add_f32 v201, v201, v163\n\t"
    "v_cvt_pk_bf16_f32 v21, v162, v163\n\t"
    "v_mfma_f32_16x16x32_bf16 v[172:175], a[12:15], v[108:111], v[192:195]\n\t"
    "v_exp_f32 v164, v164\n\t"
    "v_mfma_f32_16x16x32_bf16 v[172:175], a[8:11], v[104:107], v[172:175]\n\t"
    "s_mov_b32 s53, 45056\n\t"
    "v_exp_f32 v165, v165\n\t"
    "v_mfma_f32_16x16x32_bf16 v[172:175], a[4:7], v[100:103], v[172:175]\n\t"
    "s_mov_b32 m0, 110592\n\t"
    "v_exp_f32 v166, v166\n\t"
    "v_mfma_f32_16x16x32_bf16 v[172:175], a[0:3], v[96:99], v[172:175]\n\t"
    "buffer_load_dwordx4 v247, s[16:19], s53 offen offset:0 nt lds\n\t"
    "buffer_load_dwordx4 v247, s[16:19], s53 offen offset:2048 nt lds\n\t"
    "v_exp_f32 v167, v167\n\t"
    "v_add_f32 v202, v202, v164\n\t"
    "v_add_f32 v203, v203, v165\n\t"
    "v_cvt_pk_bf16_f32 v52, v164, v165\n\t"
    "v_add_f32 v202, v202, v166\n\t"
    "v_add_f32 v203, v203, v167\n\t"
    "v_cvt_pk_bf16_f32 v53, v166, v167\n\t"
    "s_waitcnt lgkmcnt(0)\n\t"
    "v_mfma_f32_16x16x32_bf16 v[160:163], a[16:19], v[0:3], v[180:183]\n\t"
    "v_exp_f32 v168, v168\n\t"
    "v_mfma_f32_16x16x32_bf16 v[160:163], a[20:23], v[4:7], v[160:163]\n\t"
    "v_exp_f32 v169, v169\n\t"
    "v_mfma_f32_16x16x32_bf16 v[160:163], a[24:27], v[8:11], v[160:163]\n\t"
    "s_waitcnt vmcnt(46)\n\t"
    "v_exp_f32 v170, v170\n\t"
    "v_mfma_f32_16x16x32_bf16 v[160:163], a[28:31], v[12:15], v[160:163]\n\t"
    "s_barrier\n\t"
    "v_exp_f32 v171, v171\n\t"
    "v_add_f32 v204, v204, v168\n\t"
    "v_add_f32 v205, v205, v169\n\t"
    "v_cvt_pk_bf16_f32 v84, v168, v169\n\t"
    "v_add_f32 v204, v204, v170\n\t"
    "v_add_f32 v205, v205, v171\n\t"
    "v_cvt_pk_bf16_f32 v85, v170, v171\n\t"
    "v_lshl_add_u32 v147, v249, 7, s62\n\t"
    "v_subrev_u32 v144, 2048, v144\n\t"
    "v_mfma_f32_16x16x32_bf16 v[164:167], a[28:31], v[44:47], v[184:187]\n\t"
    "s_mov_b32 m0, 111488\n\t"
    "v_exp_f32 v172, v172\n\t"
    "v_mfma_f32_16x16x32_bf16 v[164:167], a[24:27], v[40:43], v[164:167]\n\t"
    "buffer_load_dwordx4 v247, s[16:19], s53 offen offset:128 nt lds\n\t"
    "buffer_load_dwordx4 v247, s[16:19], s53 offen offset:2176 nt lds\n\t"
    "v_exp_f32 v173, v173\n\t"
    "v_mfma_f32_16x16x32_bf16 v[164:167], a[20:23], v[36:39], v[164:167]\n\t"
    "s_mov_b32 s53, 0\n\t"
    "s_mov_b32 m0, 131072\n\t"
    "v_exp_f32 v174, v174\n\t"
    "v_mfma_f32_16x16x32_bf16 v[164:167], a[16:19], v[32:35], v[164:167]\n\t"
    "buffer_load_dwordx4 v255, s[24:27], s53 offen offset:0 lds\n\t"
    "buffer_load_dwordx4 v198, s[24:27], s53 offen offset:2048 lds\n\t"
    "ds_read_b128 a[0:3], v252 offset:16384\n\t"
    "ds_read_b128 a[4:7], v252 offset:16896\n\t"
    "v_exp_f32 v175, v175\n\t"
    "v_add_f32 v206, v206, v172\n\t"
    "v_add_f32 v207, v207, v173\n\t"
    "v_cvt_pk_bf16_f32 v116, v172, v173\n\t"
    "v_add_f32 v206, v206, v174\n\t"
    "v_add_f32 v207, v207, v175\n\t"
    "v_cvt_pk_bf16_f32 v117, v174, v175\n\t"
    "v_mfma_f32_16x16x32_bf16 v[168:171], a[16:19], v[64:67], v[188:191]\n\t"
    "v_exp_f32 v160, v160\n\t"
    "v_mfma_f32_16x16x32_bf16 v[168:171], a[20:23], v[68:71], v[168:171]\n\t"
    "v_exp_f32 v161, v161\n\t"
    "v_mfma_f32_16x16x32_bf16 v[168:171], a[24:27], v[72:75], v[168:171]\n\t"
    "s_mov_b32 m0, 131968\n\t"
    "v_exp_f32 v162, v162\n\t"
    "v_mfma_f32_16x16x32_bf16 v[168:171], a[28:31], v[76:79], v[168:171]\n\t"
    "buffer_load_dwordx4 v255, s[24:27], s53 offen offset:128 lds\n\t"
    "buffer_load_dwordx4 v198, s[24:27], s53 offen offset:2176 lds\n\t"
    "ds_read_b128 a[8:11], v252 offset:17408\n\t"
    "ds_read_b128 a[12:15], v252 offset:17920\n\t"
    "v_exp_f32 v163, v163\n\t"
    "v_add_f32 v200, v200, v160\n\t"
    "v_add_f32 v201, v201, v161\n\t"
    "v_cvt_pk_bf16_f32 v22, v160, v161\n\t"
    "v_add_f32 v200, v200, v162\n\t"
    "v_add_f32 v201, v201, v163\n\t"
    "v_cvt_pk_bf16_f32 v23, v162, v163\n\t"
    "v_mfma_f32_16x16x32_bf16 v[172:175], a[28:31], v[108:111], v[192:195]\n\t"
    "v_exp_f32 v164, v164\n\t"
    "v_mfma_f32_16x16x32_bf16 v[172:175], a[24:27], v[104:107], v[172:175]\n\t"
    "v_exp_f32 v165, v165\n\t"
    "v_mfma_f32_16x16x32_bf16 v[172:175], a[20:23], v[100:103], v[172:175]\n\t"
    "v_exp_f32 v166, v166\n\t"
    "v_mfma_f32_16x16x32_bf16 v[172:175], a[16:19], v[96:99], v[172:175]\n\t"
    "ds_read_b64_tr_b16 a[16:17], v253 offset:0\n\t"
    "ds_read_b64_tr_b16 a[18:19], v253 offset:4096\n\t"
    "ds_read_b64_tr_b16 a[20:21], v253 offset:8192\n\t"
    "v_exp_f32 v167, v167\n\t"
    "v_add_f32 v202, v202, v164\n\t"
    "v_add_f32 v203, v203, v165\n\t"
    "v_cvt_pk_bf16_f32 v54, v164, v165\n\t"
    "v_add_f32 v202, v202, v166\n\t"
    "v_add_f32 v203, v203, v167\n\t"
    "v_cvt_pk_bf16_f32 v55, v166, v167\n\t"
    "v_exp_f32 v168, v168\n\t"
    "v_exp_f32 v169, v169\n\t"
    "v_exp_f32 v170, v170\n\t"
    "v_exp_f32 v171, v171\n\t"
    "v_add_f32 v204, v204, v168\n\t"
    "v_add_f32 v205, v205, v169\n\t"
    "v_cvt_pk_bf16_f32 v86, v168, v169\n\t"
    "s_waitcnt lgkmcnt(3)\n\t"
    ".p2align 6\n\t"
    "L_loopB_%=:\n\t"
    "ds_read_b64_tr_b16 a[22:23], v253 offset:12288\n\t"
    "v_add_f32 v204, v204, v170\n\t"
    "v_mfma_f32_16x16x32_bf16 v[160:163], a[0:3], v[0:3], v[180:183]\n\t"
    "s_add_u32 s43, s43, 0x4000\n\t"
    "v_exp_f32 v172, v172\n\t"
    "v_mfma_f32_16x16x32_bf16 v[160:163], a[4:7], v[4:7], v[160:163]\n\t"
    "s_add_u32 s44, s44, 0x1000\n\t"
    "v_add_f32 v205, v205, v171\n\t"
    "v_cvt_pk_bf16_f32 v87, v170, v171\n\t"
    "v_mfma_f32_16x16x32_bf16 v[160:163], a[8:11], v[8:11], v[160:163]\n\t"
    "v_exp_f32 v173, v173\n\t"
    "v_mfma_f32_16x16x32_bf16 v[160:163], a[12:15], v[12:15], v[160:163]\n\t"
    "buffer_load_dword v146, v144, s[24:27], s89 offen\n\t"
    "ds_read_b64_tr_b16 a[24:25], v254 offset:0\n\t"
    "v_add_f32 v206, v206, v172\n\t"
    "v_add_u32 v144, 10240, v144\n\t"
    "v_mfma_f32_16x16x32_bf16 v[164:167], a[12:15], v[44:47], v[184:187]\n\t"
    "s_sub_u32 s54, s54, 1\n\t"
    "v_exp_f32 v174, v174\n\t"
    "v_mfma_f32_16x16x32_bf16 v[164:167], a[8:11], v[40:43], v[164:167]\n\t"
    "v_add_f32 v207, v207, v173\n\t"
    "v_cvt_pk_bf16_f32 v118, v172, v173\n\t"
    "v_mfma_f32_16x16x32_bf16 v[164:167], a[4:7], v[36:39], v[164:167]\n\t"
    "s_add_u32 m0, s44, 0\n\t"
    "v_exp_f32 v175, v175\n\t"
    "v_mfma_f32_16x16x32_bf16 v[164:167], a[0:3], v[32:35], v[164:167]\n\t"
    "buffer_load_dwordx4 v255, s[24:27], s43 offen offset:0 lds\n\t"
    "buffer_load_dwordx4 v198, s[24:27], s43 offen offset:2048 lds\n\t"
    "ds_read_b64_tr_b16 a[26:27], v254 offset:4096\n\t"
    "ds_read_b64_tr_b16 a[28:29], v254 offset:8192\n\t"
    "v_add_f32 v206, v206, v174\n\t"
    "v_mfma_f32_16x16x32_bf16 v[168:171], a[0:3], v[64:67], v[188:191]\n\t"
    "v_exp_f32 v160, v160\n\t"
    "v_mfma_f32_16x16x32_bf16 v[168:171], a[4:7], v[68:71], v[168:171]\n\t"
    "v_add_f32 v207, v207, v175\n\t"
    "v_cvt_pk_bf16_f32 v119, v174, v175\n\t"
    "v_mfma_f32_16x16x32_bf16 v[168:171], a[8:11], v[72:75], v[168:171]\n\t"
    "s_add_u32 m0, s44, 896\n\t"
    "v_exp_f32 v161, v161\n\t"
    "v_mfma_f32_16x16x32_bf16 v[168:171], a[12:15], v[76:79], v[168:171]\n\t"
    "buffer_load_dwordx4 v255, s[24:27], s43 offen offset:128 lds\n\t"
    "buffer_load_dwordx4 v198, s[24:27], s43 offen offset:2176 lds\n\t"
    "ds_read_b64_tr_b16 a[30:31], v254 offset:12288\n\t"
    "v_add_f32 v200, v200, v160\n\t"
    "v_mfma_f32_16x16x32_bf16 v[172:175], a[12:15], v[108:111], v[192:195]\n\t"
    "v_exp_f32 v162, v162\n\t"
    "v_mfma_f32_16x16x32_bf16 v[172:175], a[8:11], v[104:107], v[172:175]\n\t"
    "s_add_u32 s53, s55, 16384\n\t"
    "v_add_f32 v201, v201, v161\n\t"
    "v_cvt_pk_bf16_f32 v24, v160, v161\n\t"
    "v_mfma_f32_16x16x32_bf16 v[172:175], a[4:7], v[100:103], v[172:175]\n\t"
    "s_add_u32 m0, s56, 0\n\t"
    "s_waitcnt lgkmcnt(4)\n\t"
    "v_exp_f32 v163, v163\n\t"
    "v_mfma_f32_16x16x32_bf16 v[172:175], a[0:3], v[96:99], v[172:175]\n\t"
    "buffer_load_dwordx4 v247, s[28:31], s53 offen offset:0 nt lds\n\t"
    "ds_read_b128 a[12:15], v252 offset:22016\n\t"
    "v_add_f32 v200, v200, v162\n\t"
    "v_mfma_f32_16x16x32_bf16 a[96:99], a[16:19], v[16:19], a[96:99]\n\t"
    "v_exp_f32 v164, v164\n\t"
    "v_mfma_f32_16x16x32_bf16 a[96:99], a[20:23], v[20:23], a[96:99]\n\t"
    "buffer_load_dwordx4 v247, s[28:31], s53 offen offset:2048 nt lds\n\t"
    "v_add_f32 v201, v201, v163\n\t"
    "v_cvt_pk_bf16_f32 v25, v162, v163\n\t"
    "v_mfma_f32_16x16x32_bf16 a[128:131], a[20:23], v[52:55], a[128:131]\n\t"
    "s_add_u32 m0, s56, 896\n\t"
    "v_exp_f32 v165, v165\n\t"
    "v_mfma_f32_16x16x32_bf16 a[128:131], a[16:19], v[48:51], a[128:131]\n\t"
    "buffer_load_dwordx4 v247, s[28:31], s53 offen offset:128 nt lds\n\t"
    "ds_read_b128 a[0:3], v252 offset:20480\n\t"
    "ds_read_b128 a[4:7], v252 offset:20992\n\t"
    "v_add_f32 v202, v202, v164\n\t"
    "v_mfma_f32_16x16x32_bf16 a[160:163], a[16:19], v[80:83], a[160:163]\n\t"
    "s_add_u32 s63, s55, 20480\n\t"
    "v_exp_f32 v166, v166\n\t"
    "v_mfma_f32_16x16x32_bf16 a[160:163], a[20:23], v[84:87], a[160:163]\n\t"
    "buffer_load_dwordx4 v247, s[28:31], s53 offen offset:2176 nt lds\n\t"
    "v_add_f32 v203, v203, v165\n\t"
    "v_cvt_pk_bf16_f32 v56, v164, v165\n\t"
    "v_mfma_f32_16x16x32_bf16 a[192:195], a[20:23], v[116:119], a[192:195]\n\t"
    "s_add_u32 m0, s56, 4096\n\t"
    "s_waitcnt lgkmcnt(3)\n\t"
    "v_exp_f32 v167, v167\n\t"
    "v_mfma_f32_16x16x32_bf16 a[192:195], a[16:19], v[112:115], a[192:195]\n\t"
    "buffer_load_dwordx4 v247, s[28:31], s63 offen offset:0 nt lds\n\t"
    "ds_read_b128 a[8:11], v252 offset:21504\n\t"
    "ds_read_b64_tr_b16 a[16:17], v253 offset:512\n\t"
    "v_add_f32 v202, v202, v166\n\t"
    "v_mfma_f32_16x16x32_bf16 a[196:199], a[24:27], v[112:115], a[196:199]\n\t"
    "v_exp_f32 v168, v168\n\t"
    "v_mfma_f32_16x16x32_bf16 a[196:199], a[28:31], v[116:119], a[196:199]\n\t"
    "buffer_load_dwordx4 v247, s[28:31], s63 offen offset:2048 nt lds\n\t"
    "v_add_f32 v203, v203, v167\n\t"
    "v_cvt_pk_bf16_f32 v57, v166, v167\n\t"
    "v_mfma_f32_16x16x32_bf16 a[164:167], a[28:31], v[84:87], a[164:167]\n\t"
    "s_add_u32 m0, s56, 4992\n\t"
    "v_exp_f32 v169, v169\n\t"
    "v_mfma_f32_16x16x32_bf16 a[164:167], a[24:27], v[80:83], a[164:167]\n\t"
    "buffer_load_dwordx4 v247, s[28:31], s63 offen offset:128 nt lds\n\t"
    "ds_read_b64_tr_b16 a[18:19], v253 offset:4608\n\t"
    "ds_read_b64_tr_b16 a[20:21], v253 offset:8704\n\t"
    "v_add_f32 v204, v204, v168\n\t"
    "v_mfma_f32_16x16x32_bf16 a[132:135], a[24:27], v[48:51], a[132:135]\n\t"
    "s_add_u32 s53, s55, 24576\n\t"
    "v_exp_f32 v170, v170\n\t"
    "v_mfma_f32_16x16x32_bf16 a[132:135], a[28:31], v[52:55], a[132:135]\n\t"
    "buffer_load_dwordx4 v247, s[28:31], s63 offen offset:2176 nt lds\n\t"
    "v_add_f32 v205, v205, v169\n\t"
    "v_cvt_pk_bf16_f32 v88, v168, v169\n\t"
    "v_mfma_f32_16x16x32_bf16 a[100:103], a[28:31], v[20:23], a[100:103]\n\t"
    "s_waitcnt lgkmcnt(3)\n\t"
    "v_exp_f32 v171, v171\n\t"
    "v_mfma_f32_16x16x32_bf16 a[100:103], a[24:27], v[16:19], a[100:103]\n\t"
    "ds_read_b64_tr_b16 a[22:23], v253 offset:12800\n\t"
    "v_add_f32 v204, v204, v170\n\t"
    "v_mfma_f32_16x16x32_bf16 v[160:163], a[0:3], v[0:3], v[180:183]\n\t"
    "v_exp_f32 v172, v172\n\t"
    "v_mfma_f32_16x16x32_bf16 v[160:163], a[4:7], v[4:7], v[160:163]\n\t"
    "v_add_f32 v205, v205, v171\n\t"
    "v_cvt_pk_bf16_f32 v89, v170, v171\n\t"
    "v_mfma_f32_16x16x32_bf16 v[160:163], a[8:11], v[8:11], v[160:163]\n\t"
    "s_add_u32 m0, s56, 8192\n\t"
    "v_exp_f32 v173, v173\n\t"
    "v_mfma_f32_16x16x32_bf16 v[160:163], a[12:15], v[12:15], v[160:163]\n\t"
    "buffer_load_dwordx4 v247, s[28:31], s53 offen offset:0 nt lds\n\t"
    "ds_read_b64_tr_b16 a[24:25], v254 offset:512\n\t"
    "v_add_f32 v206, v206, v172\n\t"
    "v_mfma_f32_16x16x32_bf16 v[164:167], a[12:15], v[44:47], v[184:187]\n\t"
    "v_exp_f32 v174, v174\n\t"
    "v_mfma_f32_16x16x32_bf16 v[164:167], a[8:11], v[40:43], v[164:167]\n\t"
    "v_add_f32 v207, v207, v173\n\t"
    "v_cvt_pk_bf16_f32 v120, v172, v173\n\t"
    "v_mfma_f32_16x16x32_bf16 v[164:167], a[4:7], v[36:39], v[164:167]\n\t"
    "v_exp_f32 v175, v175\n\t"
    "v_mfma_f32_16x16x32_bf16 v[164:167], a[0:3], v[32:35], v[164:167]\n\t"
    "buffer_load_dwordx4 v247, s[28:31], s53 offen offset:2048 nt lds\n\t"
    "ds_read_b64_tr_b16 a[26:27], v254 offset:4608\n\t"
    "ds_read_b64_tr_b16 a[28:29], v254 offset:8704\n\t"
    "v_add_f32 v206, v206, v174\n\t"
    "v_mfma_f32_16x16x32_bf16 v[168:171], a[0:3], v[64:67], v[188:191]\n\t"
    "v_exp_f32 v160, v160\n\t"
    "v_mfma_f32_16x16x32_bf16 v[168:171], a[4:7], v[68:71], v[168:171]\n\t"
    "v_add_f32 v207, v207, v175\n\t"
    "v_cvt_pk_bf16_f32 v121, v174, v175\n\t"
    "v_mfma_f32_16x16x32_bf16 v[168:171], a[8:11], v[72:75], v[168:171]\n\t"
    "s_add_u32 m0, s56, 9088\n\t"
    "v_exp_f32 v161, v161\n\t"
    "v_mfma_f32_16x16x32_bf16 v[168:171], a[12:15], v[76:79], v[168:171]\n\t"
    "buffer_load_dwordx4 v247, s[28:31], s53 offen offset:128 nt lds\n\t"
    "ds_read_b64_tr_b16 a[30:31], v254 offset:12800\n\t"
    "v_add_f32 v200, v200, v160\n\t"
    "v_mfma_f32_16x16x32_bf16 v[172:175], a[12:15], v[108:111], v[192:195]\n\t"
    "v_exp_f32 v162, v162\n\t"
    "v_mfma_f32_16x16x32_bf16 v[172:175], a[8:11], v[104:107], v[172:175]\n\t"
    "v_add_f32 v201, v201, v161\n\t"
    "v_cvt_pk_bf16_f32 v26, v160, v161\n\t"
    "v_mfma_f32_16x16x32_bf16 v[172:175], a[4:7], v[100:103], v[172:175]\n\t"
    "s_add_u32 s63, s55, 28672\n\t"
    "s_waitcnt lgkmcnt(4)\n\t"
    "v_exp_f32 v163, v163\n\t"
    "v_mfma_f32_16x16x32_bf16 v[172:175], a[0:3], v[96:99], v[172:175]\n\t"
    "buffer_load_dwordx4 v247, s[28:31], s53 offen offset:2176 nt lds\n\t"
    "ds_read_b128 a[12:15], v252 offset:26112\n\t"
    "v_add_f32 v200, v200, v162\n\t"
    "v_mfma_f32_16x16x32_bf16 a[104:107], a[16:19], v[16:19], a[104:107]\n\t"
    "s_add_u32 m0, s56, 12288\n\t"
    "v_exp_f32 v164, v164\n\t"
    "v_mfma_f32_16x16x32_bf16 a[104:107], a[20:23], v[20:23], a[104:107]\n\t"
    "buffer_load_dwordx4 v247, s[28:31], s63 offen offset:0 nt lds\n\t"
    "v_add_f32 v201, v201, v163\n\t"
    "v_cvt_pk_bf16_f32 v27, v162, v163\n\t"
    "v_mfma_f32_16x16x32_bf16 a[136:139], a[20:23], v[52:55], a[136:139]\n\t"
    "v_exp_f32 v165, v165\n\t"
    "v_mfma_f32_16x16x32_bf16 a[136:139], a[16:19], v[48:51], a[136:139]\n\t"
    "buffer_load_dwordx4 v247, s[28:31], s63 offen offset:2048 nt lds\n\t"
    "ds_read_b128 a[0:3], v252 offset:24576\n\t"
    "ds_read_b128 a[4:7], v252 offset:25088\n\t"
    "v_add_f32 v202, v202, v164\n\t"
    "v_mfma_f32_16x16x32_bf16 a[168:171], a[16:19], v[80:83], a[168:171]\n\t"
    "s_add_u32 m0, s56, 13184\n\t"
    "v_exp_f32 v166, v166\n\t"
    "v_mfma_f32_16x16x32_bf16 a[168:171], a[20:23], v[84:87], a[168:171]\n\t"
    "buffer_load_dwordx4 v247, s[28:31], s63 offen offset:128 nt lds\n\t"
    "v_add_f32 v203, v203, v165\n\t"
    "v_cvt_pk_bf16_f32 v58, v164, v165\n\t"
    "v_mfma_f32_16x16x32_bf16 a[200:203], a[20:23], v[116:119], a[200:203]\n\t"
    "s_add_u32 s53, s55, 16384\n\t"
    "s_waitcnt lgkmcnt(3)\n\t"
    "v_exp_f32 v167, v167\n\t"
    "v_mfma_f32_16x16x32_bf16 a[200:203], a[16:19], v[112:115], a[200:203]\n\t"
    "buffer_load_dwordx4 v247, s[28:31], s63 offen offset:2176 nt lds\n\t"
    "ds_read_b128 a[8:11], v252 offset:25600\n\t"
    "ds_read_b64_tr_b16 a[16:17], v253 offset:1024\n\t"
    "v_add_f32 v202, v202, v166\n\t"
    "v_mfma_f32_16x16x32_bf16 a[204:207], a[24:27], v[112:115], a[204:207]\n\t"
    "v_exp_f32 v168, v168\n\t"
    "v_mfma_f32_16x16x32_bf16 a[204:207], a[28:31], v[116:119], a[204:207]\n\t"
    "v_add_f32 v203, v203, v167\n\t"
    "v_cvt_pk_bf16_f32 v59, v166, v167\n\t"
    "v_mfma_f32_16x16x32_bf16 a[172:175], a[28:31], v[84:87], a[172:175]\n\t"
    "s_add_u32 m0, s56, 65536\n\t"
    "v_exp_f32 v169, v169\n\t"
    "v_mfma_f32_16x16x32_bf16 a[172:175], a[24:27], v[80:83], a[172:175]\n\t"
    "buffer_load_dwordx4 v247, s[16:19], s53 offen offset:0 lds\n\t"
    "ds_read_b64_tr_b16 a[18:19], v253 offset:5120\n\t"
    "ds_read_b64_tr_b16 a[20:21], v253 offset:9216\n\t"
    "v_add_f32 v204, v204, v168\n\t"
    "v_mfma_f32_16x16x32_bf16 a[140:143], a[24:27], v[48:51], a[140:143]\n\t"
    "v_exp_f32 v170, v170\n\t"
    "v_mfma_f32_16x16x32_bf16 a[140:143], a[28:31], v[52:55], a[140:143]\n\t"
    "buffer_load_dwordx4 v247, s[16:19], s53 offen offset:2048 lds\n\t"
    "v_add_f32 v205, v205, v169\n\t"
    "v_cvt_pk_bf16_f32 v90, v168, v169\n\t"
    "v_mfma_f32_16x16x32_bf16 a[108:111], a[28:31], v[20:23], a[108:111]\n\t"
    "s_add_u32 m0, s56, 66432\n\t"
    "s_waitcnt lgkmcnt(3)\n\t"
    "v_exp_f32 v171, v171\n\t"
    "v_mfma_f32_16x16x32_bf16 a[108:111], a[24:27], v[16:19], a[108:111]\n\t"
    "buffer_load_dwordx4 v247, s[16:19], s53 offen offset:128 lds\n\t"
    "ds_read_b64_tr_b16 a[22:23], v253 offset:13312\n\t"
    "v_add_f32 v204, v204, v170\n\t"
    "v_mfma_f32_16x16x32_bf16 v[160:163], a[0:3], v[0:3], v[180:183]\n\t"
    "v_exp_f32 v172, v172\n\t"
    "v_mfma_f32_16x16x32_bf16 v[160:163], a[4:7], v[4:7], v[160:163]\n\t"
    "v_add_f32 v205, v205, v171\n\t"
    "v_cvt_pk_bf16_f32 v91, v170, v171\n\t"
    "v_mfma_f32_16x16x32_bf16 v[160:163], a[8:11], v[8:11], v[160:163]\n\t"
    "s_add_u32 s63, s55, 20480\n\t"
    "v_exp_f32 v173, v173\n\t"
    "v_mfma_f32_16x16x32_bf16 v[160:163], a[12:15], v[12:15], v[160:163]\n\t"
    "buffer_load_dwordx4 v247, s[16:19], s53 offen offset:2176 lds\n\t"
    "ds_read_b64_tr_b16 a[24:25], v254 offset:1024\n\t"
    "v_add_f32 v206, v206, v172\n\t"
    "v_mfma_f32_16x16x32_bf16 v[164:167], a[12:15], v[44:47], v[184:187]\n\t"
    "v_exp_f32 v174, v174\n\t"
    "v_mfma_f32_16x16x32_bf16 v[164:167], a[8:11], v[40:43], v[164:167]\n\t"
    "v_add_f32 v207, v207, v173\n\t"
    "v_cvt_pk_bf16_f32 v122, v172, v173\n\t"
    "v_mfma_f32_16x16x32_bf16 v[164:167], a[4:7], v[36:39], v[164:167]\n\t"
    "s_add_u32 m0, s56, 69632\n\t"
    "v_exp_f32 v175, v175\n\t"
    "v_mfma_f32_16x16x32_bf16 v[164:167], a[0:3], v[32:35], v[164:167]\n\t"
    "buffer_load_dwordx4 v247, s[16:19], s63 offen offset:0 lds\n\t"
    "ds_read_b64_tr_b16 a[26:27], v254 offset:5120\n\t"
    "ds_read_b64_tr_b16 a[28:29], v254 offset:9216\n\t"
    "v_add_f32 v206, v206, v174\n\t"
    "v_mfma_f32_16x16x32_bf16 v[168:171], a[0:3], v[64:67], v[188:191]\n\t"
    "v_exp_f32 v160, v160\n\t"
    "v_mfma_f32_16x16x32_bf16 v[168:171], a[4:7], v[68:71], v[168:171]\n\t"
    "v_add_f32 v207, v207, v175\n\t"
    "v_cvt_pk_bf16_f32 v123, v174, v175\n\t"
    "v_mfma_f32_16x16x32_bf16 v[168:171], a[8:11], v[72:75], v[168:171]\n\t"
    "v_exp_f32 v161, v161\n\t"
    "v_mfma_f32_16x16x32_bf16 v[168:171], a[12:15], v[76:79], v[168:171]\n\t"
    "buffer_load_dwordx4 v247, s[16:19], s63 offen offset:2048 lds\n\t"
    "ds_read_b64_tr_b16 a[30:31], v254 offset:13312\n\t"
    "v_add_f32 v200, v200, v160\n\t"
    "v_mfma_f32_16x16x32_bf16 v[172:175], a[12:15], v[108:111], v[192:195]\n\t"
    "v_exp_f32 v162, v162\n\t"
    "v_mfma_f32_16x16x32_bf16 v[172:175], a[8:11], v[104:107], v[172:175]\n\t"
    "v_add_f32 v201, v201, v161\n\t"
    "v_cvt_pk_bf16_f32 v28, v160, v161\n\t"
    "v_mfma_f32_16x16x32_bf16 v[172:175], a[4:7], v[100:103], v[172:175]\n\t"
    "s_add_u32 m0, s56, 70528\n\t"
    "s_waitcnt lgkmcnt(4)\n\t"
    "v_exp_f32 v163, v163\n\t"
    "v_mfma_f32_16x16x32_bf16 v[172:175], a[0:3], v[96:99], v[172:175]\n\t"
    "buffer_load_dwordx4 v247, s[16:19], s63 offen offset:128 lds\n\t"
    "ds_read_b128 a[12:15], v252 offset:30208\n\t"
    "v_add_f32 v200, v200, v162\n\t"
    "v_mfma_f32_16x16x32_bf16 a[112:115], a[16:19], v[16:19], a[112:115]\n\t"
    "s_add_u32 s53, s55, 24576\n\t"
    "v_exp_f32 v164, v164\n\t"
    "v_mfma_f32_16x16x32_bf16 a[112:115], a[20:23], v[20:23], a[112:115]\n\t"
    "buffer_load_dwordx4 v247, s[16:19], s63 offen offset:2176 lds\n\t"
    "v_add_f32 v201, v201, v163\n\t"
    "v_cvt_pk_bf16_f32 v29, v162, v163\n\t"
    "v_mfma_f32_16x16x32_bf16 a[144:147], a[20:23], v[52:55], a[144:147]\n\t"
    "v_exp_f32 v165, v165\n\t"
    "v_mfma_f32_16x16x32_bf16 a[144:147], a[16:19], v[48:51], a[144:147]\n\t"
    "ds_read_b128 a[0:3], v252 offset:28672\n\t"
    "ds_read_b128 a[4:7], v252 offset:29184\n\t"
    "v_add_f32 v202, v202, v164\n\t"
    "v_mfma_f32_16x16x32_bf16 a[176:179], a[16:19], v[80:83], a[176:179]\n\t"
    "s_add_u32 m0, s56, 73728\n\t"
    "v_exp_f32 v166, v166\n\t"
    "v_mfma_f32_16x16x32_bf16 a[176:179], a[20:23], v[84:87], a[176:179]\n\t"
    "buffer_load_dwordx4 v247, s[16:19], s53 offen offset:0 lds\n\t"
    "v_add_f32 v203, v203, v165\n\t"
    "v_cvt_pk_bf16_f32 v60, v164, v165\n\t"
    "v_mfma_f32_16x16x32_bf16 a[208:211], a[20:23], v[116:119], a[208:211]\n\t"
    "s_waitcnt lgkmcnt(3)\n\t"
    "v_exp_f32 v167, v167\n\t"
    "v_mfma_f32_16x16x32_bf16 a[208:211], a[16:19], v[112:115], a[208:211]\n\t"
    "buffer_load_dwordx4 v247, s[16:19], s53 offen offset:2048 lds\n\t"
    "ds_read_b128 a[8:11], v252 offset:29696\n\t"
    "ds_read_b64_tr_b16 a[16:17], v253 offset:1536\n\t"
    "v_add_f32 v202, v202, v166\n\t"
    "v_mfma_f32_16x16x32_bf16 a[212:215], a[24:27], v[112:115], a[212:215]\n\t"
    "s_add_u32 m0, s56, 74624\n\t"
    "v_exp_f32 v168, v168\n\t"
    "v_mfma_f32_16x16x32_bf16 a[212:215], a[28:31], v[116:119], a[212:215]\n\t"
    "buffer_load_dwordx4 v247, s[16:19], s53 offen offset:128 lds\n\t"
    "v_add_f32 v203, v203, v167\n\t"
    "v_cvt_pk_bf16_f32 v61, v166, v167\n\t"
    "v_mfma_f32_16x16x32_bf16 a[180:183], a[28:31], v[84:87], a[180:183]\n\t"
    "s_add_u32 s63, s55, 28672\n\t"
    "v_exp_f32 v169, v169\n\t"
    "v_mfma_f32_16x16x32_bf16 a[180:183], a[24:27], v[80:83], a[180:183]\n\t"
    "buffer_load_dwordx4 v247, s[16:19], s53 offen offset:2176 lds\n\t"
    "ds_read_b64_tr_b16 a[18:19], v253 offset:5632\n\t"
    "ds_read_b64_tr_b16 a[20:21], v253 offset:9728\n\t"
    "v_add_f32 v204, v204, v168\n\t"
    "v_mfma_f32_16x16x32_bf16 a[148:151], a[24:27], v[48:51], a[148:151]\n\t"
    "s_add_u32 m0, s56, 77824\n\t"
    "v_exp_f32 v170, v170\n\t"
    "v_mfma_f32_16x16x32_bf16 a[148:151], a[28:31], v[52:55], a[148:151]\n\t"
    "buffer_load_dwordx4 v247, s[16:19], s63 offen offset:0 lds\n\t"
    "v_add_f32 v205, v205, v169\n\t"
    "v_cvt_pk_bf16_f32 v92, v168, v169\n\t"
    "v_mfma_f32_16x16x32_bf16 a[116:119], a[28:31], v[20:23], a[116:119]\n\t"
    "s_waitcnt lgkmcnt(3)\n\t"
    "v_exp_f32 v171, v171\n\t"
    "v_mfma_f32_16x16x32_bf16 a[116:119], a[24:27], v[16:19], a[116:119]\n\t"
    "buffer_load_dwordx4 v247, s[16:19], s63 offen offset:2048 lds\n\t"
    "ds_read_b64_tr_b16 a[22:23], v253 offset:13824\n\t"
    "v_add_f32 v204, v204, v170\n\t"
    "v_mfma_f32_16x16x32_bf16 v[160:163], a[0:3], v[0:3], v[180:183]\n\t"
    "v_exp_f32 v172, v172\n\t"
    "v_mfma_f32_16x16x32_bf16 v[160:163], a[4:7], v[4:7], v[160:163]\n\t"
    "v_add_f32 v205, v205, v171\n\t"
    "v_cvt_pk_bf16_f32 v93, v170, v171\n\t"
    "v_mfma_f32_16x16x32_bf16 v[160:163], a[8:11], v[8:11], v[160:163]\n\t"
    "s_add_u32 m0, s56, 78720\n\t"
    "v_exp_f32 v173, v173\n\t"
    "v_mfma_f32_16x16x32_bf16 v[160:163], a[12:15], v[12:15], v[160:163]\n\t"
    "buffer_load_dwordx4 v247, s[16:19], s63 offen offset:128 lds\n\t"
    "ds_read_b64_tr_b16 a[24:25], v254 offset:1536\n\t"
    "v_add_f32 v206, v206, v172\n\t"
    "v_xor_b32 v252, 0x8000, v252\n\t"
    "v_mfma_f32_16x16x32_bf16 v[164:167], a[12:15], v[44:47], v[184:187]\n\t"
    "v_exp_f32 v174, v174\n\t"
    "v_mfma_f32_16x16x32_bf16 v[164:167], a[8:11], v[40:43], v[164:167]\n\t"
    "v_add_f32 v207, v207, v173\n\t"
    "v_cvt_pk_bf16_f32 v124, v172, v173\n\t"
    "v_mfma_f32_16x16x32_bf16 v[164:167], a[4:7], v[36:39], v[164:167]\n\t"
    "v_exp_f32 v175, v175\n\t"
    "v_mfma_f32_16x16x32_bf16 v[164:167], a[0:3], v[32:35], v[164:167]\n\t"
    "buffer_load_dwordx4 v247, s[16:19], s63 offen offset:2176 lds\n\t"
    "ds_read_b64_tr_b16 a[26:27], v254 offset:5632\n\t"
    "ds_read_b64_tr_b16 a[28:29], v254 offset:9728\n\t"
    "v_add_f32 v206, v206, v174\n\t"
    "v_mfma_f32_16x16x32_bf16 v[168:171], a[0:3], v[64:67], v[188:191]\n\t"
    "v_exp_f32 v160, v160\n\t"
    "v_mfma_f32_16x16x32_bf16 v[168:171], a[4:7], v[68:71], v[168:171]\n\t"
    "v_add_f32 v207, v207, v175\n\t"
    "v_cvt_pk_bf16_f32 v125, v174, v175\n\t"
    "v_mfma_f32_16x16x32_bf16 v[168:171], a[8:11], v[72:75], v[168:171]\n\t"
    "v_exp_f32 v161, v161\n\t"
    "v_mfma_f32_16x16x32_bf16 v[168:171], a[12:15], v[76:79], v[168:171]\n\t"
    "ds_read_b64_tr_b16 a[30:31], v254 offset:13824\n\t"
    "v_add_f32 v200, v200, v160\n\t"
    "v_mfma_f32_16x16x32_bf16 v[172:175], a[12:15], v[108:111], v[192:195]\n\t"
    "v_exp_f32 v162, v162\n\t"
    "v_mfma_f32_16x16x32_bf16 v[172:175], a[8:11], v[104:107], v[172:175]\n\t"
    "v_add_f32 v201, v201, v161\n\t"
    "v_cvt_pk_bf16_f32 v30, v160, v161\n\t"
    "v_mfma_f32_16x16x32_bf16 v[172:175], a[4:7], v[100:103], v[172:175]\n\t"
    "s_waitcnt lgkmcnt(4)\n\t"
    "v_exp_f32 v163, v163\n\t"
    "v_mfma_f32_16x16x32_bf16 v[172:175], a[0:3], v[96:99], v[172:175]\n\t"
    "s_waitcnt lgkmcnt(0)\n\t"
    "s_waitcnt vmcnt(32)\n\t"
    "s_barrier\n\t"
    "ds_read_b128 a[12:15], v252 offset:1536\n\t"
    "v_add_f32 v200, v200, v162\n\t"
    "v_mfma_f32_16x16x32_bf16 a[120:123], a[16:19], v[16:19], a[120:123]\n\t"
    "s_add_u32 s55, s55, 0x4000\n\t"
    "v_exp_f32 v164, v164\n\t"
    "v_mfma_f32_16x16x32_bf16 a[120:123], a[20:23], v[20:23], a[120:123]\n\t"
    "v_add_f32 v201, v201, v163\n\t"
    "v_cvt_pk_bf16_f32 v31, v162, v163\n\t"
    "v_mfma_f32_16x16x32_bf16 a[152:155], a[20:23], v[52:55], a[152:155]\n\t"
    "s_add_u32 s56, s55, 0x4000\n\t"
    "v_exp_f32 v165, v165\n\t"
    "v_mfma_f32_16x16x32_bf16 a[152:155], a[16:19], v[48:51], a[152:155]\n\t"
    "ds_read_b128 a[0:3], v252 offset:0\n\t"
    "ds_read_b128 a[4:7], v252 offset:512\n\t"
    "v_add_f32 v202, v202, v164\n\t"
    "v_mfma_f32_16x16x32_bf16 a[184:187], a[16:19], v[80:83], a[184:187]\n\t"
    "s_and_b32 s56, s56, 0xffff\n\t"
    "v_exp_f32 v166, v166\n\t"
    "v_mfma_f32_16x16x32_bf16 a[184:187], a[20:23], v[84:87], a[184:187]\n\t"
    "v_add_f32 v203, v203, v165\n\t"
    "v_cvt_pk_bf16_f32 v62, v164, v165\n\t"
    "v_mfma_f32_16x16x32_bf16 a[216:219], a[20:23], v[116:119], a[216:219]\n\t"
    "v_exp_f32 v167, v167\n\t"
    "v_mfma_f32_16x16x32_bf16 a[216:219], a[16:19], v[112:115], a[216:219]\n\t"
    "ds_read_b128 a[8:11], v252 offset:1024\n\t"
    "ds_read_b64_tr_b16 a[16:17], v253 offset:16384\n\t"
    "v_add_f32 v202, v202, v166\n\t"
    "v_mfma_f32_16x16x32_bf16 a[220:223], a[24:27], v[112:115], a[220:223]\n\t"
    "v_exp_f32 v168, v168\n\t"
    "v_mfma_f32_16x16x32_bf16 a[220:223], a[28:31], v[116:119], a[220:223]\n\t"
    "v_add_f32 v203, v203, v167\n\t"
    "v_cvt_pk_bf16_f32 v63, v166, v167\n\t"
    "v_mfma_f32_16x16x32_bf16 a[188:191], a[28:31], v[84:87], a[188:191]\n\t"
    "v_exp_f32 v169, v169\n\t"
    "v_mfma_f32_16x16x32_bf16 a[188:191], a[24:27], v[80:83], a[188:191]\n\t"
    "ds_read_b64_tr_b16 a[18:19], v253 offset:20480\n\t"
    "ds_read_b64_tr_b16 a[20:21], v253 offset:24576\n\t"
    "v_add_f32 v204, v204, v168\n\t"
    "v_mfma_f32_16x16x32_bf16 a[156:159], a[24:27], v[48:51], a[156:159]\n\t"
    "s_cmp_eq_u32 s54, 0\n\t"
    "v_exp_f32 v170, v170\n\t"
    "v_mfma_f32_16x16x32_bf16 a[156:159], a[28:31], v[52:55], a[156:159]\n\t"
    "v_add_f32 v205, v205, v169\n\t"
    "v_cvt_pk_bf16_f32 v94, v168, v169\n\t"
    "v_mfma_f32_16x16x32_bf16 a[124:127], a[28:31], v[20:23], a[124:127]\n\t"
    "s_waitcnt lgkmcnt(3)\n\t"
    "v_exp_f32 v171, v171\n\t"
    "v_mfma_f32_16x16x32_bf16 a[124:127], a[24:27], v[16:19], a[124:127]\n\t"
    "s_cbranch_scc1 L_exitB_%=\n\t"
    "ds_read_b64_tr_b16 a[22:23], v253 offset:28672\n\t"
    "v_add_f32 v204, v204, v170\n\t"
    "v_mfma_f32_16x16x32_bf16 v[160:163], a[0:3], v[0:3], v[180:183]\n\t"
    "v_exp_f32 v172, v172\n\t"
    "v_mfma_f32_16x16x32_bf16 v[160:163], a[4:7], v[4:7], v[160:163]\n\t"
    "v_add_f32 v205, v205, v171\n\t"
    "v_cvt_pk_bf16_f32 v95, v170, v171\n\t"
    "v_mfma_f32_16x16x32_bf16 v[160:163], a[8:11], v[8:11], v[160:163]\n\t"
    "v_exp_f32 v173, v173\n\t"
    "v_mfma_f32_16x16x32_bf16 v[160:163], a[12:15], v[12:15], v[160:163]\n\t"
    "buffer_load_dword v145, v144, s[24:27], s89 offen\n\t"
    "ds_read_b64_tr_b16 a[24:25], v254 offset:16384\n\t"
    "v_add_f32 v206, v206, v172\n\t"
    "v_add_u32 v144, 10240, v144\n\t"
    "v_mfma_f32_16x16x32_bf16 v[164:167], a[12:15], v[44:47], v[184:187]\n\t"
    "v_exp_f32 v174, v174\n\t"
    "v_mfma_f32_16x16x32_bf16 v[164:167], a[8:11], v[40:43], v[164:167]\n\t"
    "v_add_f32 v207, v207, v173\n\t"
    "v_cvt_pk_bf16_f32 v126, v172, v173\n\t"
    "v_mfma_f32_16x16x32_bf16 v[164:167], a[4:7], v[36:39], v[164:167]\n\t"
    "v_exp_f32 v175, v175\n\t"
    "v_mfma_f32_16x16x32_bf16 v[164:167], a[0:3], v[32:35], v[164:167]\n\t"
    "s_mov_b64 exec, 0xfffff\n\t"
    "global_load_dword v148, v147, s[6:7]\n\t"
    "s_mov_b64 exec, -1\n\t"
    "ds_read_b64_tr_b16 a[26:27], v254 offset:20480\n\t"
    "ds_read_b64_tr_b16 a[28:29], v254 offset:24576\n\t"
    "v_add_f32 v206, v206, v174\n\t"
    "v_mfma_f32_16x16x32_bf16 v[168:171], a[0:3], v[64:67], v[188:191]\n\t"
    "v_exp_f32 v160, v160\n\t"
    "v_mfma_f32_16x16x32_bf16 v[168:171], a[4:7], v[68:71], v[168:171]\n\t"
    "s_add_u32 s53, s55, 16384\n\t"
    "v_add_f32 v207, v207, v175\n\t"
    "v_cvt_pk_bf16_f32 v127, v174, v175\n\t"
    "v_mfma_f32_16x16x32_bf16 v[168:171], a[8:11], v[72:75], v[168:171]\n\t"
    "s_add_u32 m0, s56, 0\n\t"
    "v_exp_f32 v161, v161\n\t"
    "v_mfma_f32_16x16x32_bf16 v[168:171], a[12:15], v[76:79], v[168:171]\n\t"
    "buffer_load_dwordx4 v247, s[28:31], s53 offen offset:0 nt lds\n\t"
    "ds_read_b64_tr_b16 a[30:31], v254 offset:28672\n\t"
    "v_add_f32 v200, v200, v160\n\t"
    "v_mfma_f32_16x16x32_bf16 v[172:175], a[12:15], v[108:111], v[192:195]\n\t"
    "v_exp_f32 v162, v162\n\t"
    "v_mfma_f32_16x16x32_bf16 v[172:175], a[8:11], v[104:107], v[172:175]\n\t"
    "v_add_f32 v201, v201, v161\n\t"
    "v_cvt_pk_bf16_f32 v16, v160, v161\n\t"
    "v_mfma_f32_16x16x32_bf16 v[172:175], a[4:7], v[100:103], v[172:175]\n\t"
    "s_waitcnt lgkmcnt(4)\n\t"
    "v_exp_f32 v163, v163\n\t"
    "v_mfma_f32_16x16x32_bf16 v[172:175], a[0:3], v[96:99], v[172:175]\n\t"
    "buffer_load_dwordx4 v247, s[28:31], s53 offen offset:2048 nt lds\n\t"
    "ds_read_b128 a[12:15], v252 offset:5632\n\t"
    "v_add_f32 v200, v200, v162\n\t"
    "v_mfma_f32_16x16x32_bf16 a[96:99], a[16:19], v[24:27], a[96:99]\n\t"
    "s_add_u32 m0, s56, 896\n\t"
    "v_exp_f32 v164, v164\n\t"
    "v_mfma_f32_16x16x32_bf16 a[96:99], a[20:23], v[28:31], a[96:99]\n\t"
    "buffer_load_dwordx4 v247, s[28:31], s53 offen offset:128 nt lds\n\t"
    "v_add_f32 v201, v201, v163\n\t"
    "v_cvt_pk_bf16_f32 v17, v162, v163\n\t"
    "v_mfma_f32_16x16x32_bf16 a[128:131], a[20:23], v[60:63], a[128:131]\n\t"
    "s_add_u32 s63, s55, 20480\n\t"
    "v_exp_f32 v165, v165\n\t"
    "v_mfma_f32_16x16x32_bf16 a[128:131], a[16:19], v[56:59], a[128:131]\n\t"
    "buffer_load_dwordx4 v247, s[28:31], s53 offen offset:2176 nt lds\n\t"
    "ds_read_b128 a[0:3], v252 offset:4096\n\t"
    "ds_read_b128 a[4:7], v252 offset:4608\n\t"
    "v_add_f32 v202, v202, v164\n\t"
    "v_mfma_f32_16x16x32_bf16 a[160:163], a[16:19], v[88:91], a[160:163]\n\t"
    "s_add_u32 m0, s56, 4096\n\t"
    "v_exp_f32 v166, v166\n\t"
    "v_mfma_f32_16x16x32_bf16 a[160:163], a[20:23], v[92:95], a[160:163]\n\t"
    "buffer_load_dwordx4 v247, s[28:31], s63 offen offset:0 nt lds\n\t"
    "v_add_f32 v203, v203, v165\n\t"
    "v_cvt_pk_bf16_f32 v48, v164, v165\n\t"
    "v_mfma_f32_16x16x32_bf16 a[192:195], a[20:23], v[124:127], a[192:195]\n\t"
    "s_waitcnt lgkmcnt(3)\n\t"
    "v_exp_f32 v167, v167\n\t"
    "v_mfma_f32_16x16x32_bf16 a[192:195], a[16:19], v[120:123], a[192:195]\n\t"
    "buffer_load_dwordx4 v247, s[28:31], s63 offen offset:2048 nt lds\n\t"
    "ds_read_b128 a[8:11], v252 offset:5120\n\t"
    "ds_read_b64_tr_b16 a[16:17], v253 offset:16896\n\t"
    "v_add_f32 v202, v202, v166\n\t"
    "v_mfma_f32_16x16x32_bf16 a[196:199], a[24:27], v[120:123], a[196:199]\n\t"
    "s_add_u32 m0, s56, 4992\n\t"
    "v_exp_f32 v168, v168\n\t"
    "v_mfma_f32_16x16x32_bf16 a[196:199], a[28:31], v[124:127], a[196:199]\n\t"
    "buffer_load_dwordx4 v247, s[28:31], s63 offen offset:128 nt lds\n\t"
    "v_add_f32 v203, v203, v167\n\t"
    "v_cvt_pk_bf16_f32 v49, v166, v167\n\t"
    "v_mfma_f32_16x16x32_bf16 a[164:167], a[28:31], v[92:95], a[164:167]\n\t"
    "v_exp_f32 v169, v169\n\t"
    "v_mfma_f32_16x16x32_bf16 a[164:167], a[24:27], v[88:91], a[164:167]\n\t"
    "ds_read_b64_tr_b16 a[18:19], v253 offset:20992\n\t"
    "ds_read_b64_tr_b16 a[20:21], v253 offset:25088\n\t"
    "v_add_f32 v204, v204, v168\n\t"
    "v_mfma_f32_16x16x32_bf16 a[132:135], a[24:27], v[56:59], a[132:135]\n\t"
    "s_add_u32 s53, s55, 24576\n\t"
    "v_exp_f32 v170, v170\n\t"
    "v_mfma_f32_16x16x32_bf16 a[132:135], a[28:31], v[60:63], a[132:135]\n\t"
    "buffer_load_dwordx4 v247, s[28:31], s63 offen offset:2176 nt lds\n\t"
    "v_add_f32 v205, v205, v169\n\t"
    "v_cvt_pk_bf16_f32 v80, v168, v169\n\t"
    "v_mfma_f32_16x16x32_bf16 a[100:103], a[28:31], v[28:31], a[100:103]\n\t"
    "s_add_u32 m0, s56, 8192\n\t"
    "s_waitcnt lgkmcnt(3)\n\t"
    "v_exp_f32 v171, v171\n\t"
    "v_mfma_f32_16x16x32_bf16 a[100:103], a[24:27], v[24:27], a[100:103]\n\t"
    "buffer_load_dwordx4 v247, s[28:31], s53 offen offset:0 nt lds\n\t"
    "ds_read_b64_tr_b16 a[22:23], v253 offset:29184\n\t"
    "v_add_f32 v204, v204, v170\n\t"
    "v_mfma_f32_16x16x32_bf16 v[160:163], a[0:3], v[0:3], v[180:183]\n\t"
    "v_exp_f32 v172, v172\n\t"
    "v_mfma_f32_16x16x32_bf16 v[160:163], a[4:7], v[4:7], v[160:163]\n\t"
    "v_add_f32 v205, v205, v171\n\t"
    "v_cvt_pk_bf16_f32 v81, v170, v171\n\t"
    "v_mfma_f32_16x16x32_bf16 v[160:163], a[8:11], v[8:11], v[160:163]\n\t"
    "v_exp_f32 v173, v173\n\t"
    "v_mfma_f32_16x16x32_bf16 v[160:163], a[12:15], v[12:15], v[160:163]\n\t"
    "buffer_load_dwordx4 v247, s[28:31], s53 offen offset:2048 nt lds\n\t"
    "ds_read_b64_tr_b16 a[24:25], v254 offset:16896\n\t"
    "v_add_f32 v206, v206, v172\n\t"
    "v_mfma_f32_16x16x32_bf16 v[164:167], a[12:15], v[44:47], v[184:187]\n\t"
    "v_exp_f32 v174, v174\n\t"
    "v_mfma_f32_16x16x32_bf16 v[164:167], a[8:11], v[40:43], v[164:167]\n\t"
    "v_add_f32 v207, v207, v173\n\t"
    "v_cvt_pk_bf16_f32 v112, v172, v173\n\t"
    "v_mfma_f32_16x16x32_bf16 v[164:167], a[4:7], v[36:39], v[164:167]\n\t"
    "s_add_u32 m0, s56, 9088\n\t"
    "v_exp_f32 v175, v175\n\t"
    "v_mfma_f32_16x16x32_bf16 v[164:167], a[0:3], v[32:35], v[164:167]\n\t"
    "buffer_load_dwordx4 v247, s[28:31], s53 offen offset:128 nt lds\n\t"
    "ds_read_b64_tr_b16 a[26:27], v254 offset:20992\n\t"
    "ds_read_b64_tr_b16 a[28:29], v254 offset:25088\n\t"
    "v_add_f32 v206, v206, v174\n\t"
    "v_mfma_f32_16x16x32_bf16 v[168:171], a[0:3], v[64:67], v[188:191]\n\t"
    "v_exp_f32 v160, v160\n\t"
    "v_mfma_f32_16x16x32_bf16 v[168:171], a[4:7], v[68:71], v[168:171]\n\t"
    "v_add_f32 v207, v207, v175\n\t"
    "v_cvt_pk_bf16_f32 v113, v174, v175\n\t"
    "v_mfma_f32_16x16x32_bf16 v[168:171], a[8:11], v[72:75], v[168:171]\n\t"
    "s_add_u32 s63, s55, 28672\n\t"
    "v_exp_f32 v161, v161\n\t"
    "v_mfma_f32_16x16x32_bf16 v[168:171], a[12:15], v[76:79], v[168:171]\n\t"
    "buffer_load_dwordx4 v247, s[28:31], s53 offen offset:2176 nt lds\n\t"
    "ds_read_b64_tr_b16 a[30:31], v254 offset:29184\n\t"
    "v_add_f32 v200, v200, v160\n\t"
    "v_mfma_f32_16x16x32_bf16 v[172:175], a[12:15], v[108:111], v[192:195]\n\t"
    "v_exp_f32 v162, v162\n\t"
    "v_mfma_f32_16x16x32_bf16 v[172:175], a[8:11], v[104:107], v[172:175]\n\t"
    "v_add_f32 v201, v201, v161\n\t"
    "v_cvt_pk_bf16_f32 v18, v160, v161\n\t"
    "v_mfma_f32_16x16x32_bf16 v[172:175], a[4:7], v[100:103], v[172:175]\n\t"
    "s_add_u32 m0, s56, 12288\n\t"
    "s_waitcnt lgkmcnt(4)\n\t"
    "v_exp_f32 v163, v163\n\t"
    "v_mfma_f32_16x16x32_bf16 v[172:175], a[0:3], v[96:99], v[172:175]\n\t"
    "buffer_load_dwordx4 v247, s[28:31], s63 offen offset:0 nt lds\n\t"
    "ds_read_b128 a[12:15], v252 offset:9728\n\t"
    "v_add_f32 v200, v200, v162\n\t"
    "v_mfma_f32_16x16x32_bf16 a[104:107], a[16:19], v[24:27], a[104:107]\n\t"
    "v_exp_f32 v164, v164\n\t"
    "v_mfma_f32_16x16x32_bf16 a[104:107], a[20:23], v[28:31], a[104:107]\n\t"
    "v_add_f32 v201, v201, v163\n\t"
    "v_cvt_pk_bf16_f32 v19, v162, v163\n\t"
    "v_mfma_f32_16x16x32_bf16 a[136:139], a[20:23], v[60:63], a[136:139]\n\t"
    "v_exp_f32 v165, v165\n\t"
    "v_mfma_f32_16x16x32_bf16 a[136:139], a[16:19], v[56:59], a[136:139]\n\t"
    "buffer_load_dwordx4 v247, s[28:31], s63 offen offset:2048 nt lds\n\t"
    "ds_read_b128 a[0:3], v252 offset:8192\n\t"
    "ds_read_b128 a[4:7], v252 offset:8704\n\t"
    "v_add_f32 v202, v202, v164\n\t"
    "v_mfma_f32_16x16x32_bf16 a[168:171], a[16:19], v[88:91], a[168:171]\n\t"
    "s_add_u32 m0, s56, 13184\n\t"
    "v_exp_f32 v166, v166\n\t"
    "v_mfma_f32_16x16x32_bf16 a[168:171], a[20:23], v[92:95], a[168:171]\n\t"
    "buffer_load_dwordx4 v247, s[28:31], s63 offen offset:128 nt lds\n\t"
    "v_add_f32 v203, v203, v165\n\t"
    "v_cvt_pk_bf16_f32 v50, v164, v165\n\t"
    "v_mfma_f32_16x16x32_bf16 a[200:203], a[20:23], v[124:127], a[200:203]\n\t"
    "s_add_u32 s53, s55, 16384\n\t"
    "s_waitcnt lgkmcnt(3)\n\t"
    "v_exp_f32 v167, v167\n\t"
    "v_mfma_f32_16x16x32_bf16 a[200:203], a[16:19], v[120:123], a[200:203]\n\t"
    "buffer_load_dwordx4 v247, s[28:31], s63 offen offset:2176 nt lds\n\t"
    "ds_read_b128 a[8:11], v252 offset:9216\n\t"
    "ds_read_b64_tr_b16 a[16:17], v253 offset:17408\n\t"
    "v_add_f32 v202, v202, v166\n\t"
    "v_mfma_f32_16x16x32_bf16 a[204:207], a[24:27], v[120:123], a[204:207]\n\t"
    "s_add_u32 m0, s56, 65536\n\t"
    "v_exp_f32 v168, v168\n\t"
    "v_mfma_f32_16x16x32_bf16 a[204:207], a[28:31], v[124:127], a[204:207]\n\t"
    "buffer_load_dwordx4 v247, s[16:19], s53 offen offset:0 lds\n\t"
    "v_add_f32 v203, v203, v167\n\t"
    "v_cvt_pk_bf16_f32 v51, v166, v167\n\t"
    "v_mfma_f32_16x16x32_bf16 a[172:175], a[28:31], v[92:95], a[172:175]\n\t"
    "v_exp_f32 v169, v169\n\t"
    "v_mfma_f32_16x16x32_bf16 a[172:175], a[24:27], v[88:91], a[172:175]\n\t"
    "buffer_load_dwordx4 v247, s[16:19], s53 offen offset:2048 lds\n\t"
    "ds_read_b64_tr_b16 a[18:19], v253 offset:21504\n\t"
    "ds_read_b64_tr_b16 a[20:21], v253 offset:25600\n\t"
    "v_add_f32 v204, v204, v168\n\t"
    "v_mfma_f32_16x16x32_bf16 a[140:143], a[24:27], v[56:59], a[140:143]\n\t"
    "s_add_u32 m0, s56, 66432\n\t"
    "v_exp_f32 v170, v170\n\t"
    "v_mfma_f32_16x16x32_bf16 a[140:143], a[28:31], v[60:63], a[140:143]\n\t"
    "buffer_load_dwordx4 v247, s[16:19], s53 offen offset:128 lds\n\t"
    "v_add_f32 v205, v205, v169\n\t"
    "v_cvt_pk_bf16_f32 v82, v168, v169\n\t"
    "v_mfma_f32_16x16x32_bf16 a[108:111], a[28:31], v[28:31], a[108:111]\n\t"
    "s_add_u32 s63, s55, 20480\n\t"
    "s_waitcnt lgkmcnt(3)\n\t"
    "v_exp_f32 v171, v171\n\t"
    "v_mfma_f32_16x16x32_bf16 a[108:111], a[24:27], v[24:27], a[108:111]\n\t"
    "buffer_load_dwordx4 v247, s[16:19], s53 offen offset:2176 lds\n\t"
    "ds_read_b64_tr_b16 a[22:23], v253 offset:29696\n\t"
    "v_add_f32 v204, v204, v170\n\t"
    "v_mfma_f32_16x16x32_bf16 v[160:163], a[0:3], v[0:3], v[180:183]\n\t"
    "v_exp_f32 v172, v172\n\t"
    "v_mfma_f32_16x16x32_bf16 v[160:163], a[4:7], v[4:7], v[160:163]\n\t"
    "v_add_f32 v205, v205, v171\n\t"
    "v_cvt_pk_bf16_f32 v83, v170, v171\n\t"
    "v_mfma_f32_16x16x32_bf16 v[160:163], a[8:11], v[8:11], v[160:163]\n\t"
    "v_exp_f32 v173, v173\n\t"
    "v_mfma_f32_16x16x32_bf16 v[160:163], a[12:15], v[12:15], v[160:163]\n\t"
    "ds_read_b64_tr_b16 a[24:25], v254 offset:17408\n\t"
    "v_add_f32 v206, v206, v172\n\t"
    "v_mfma_f32_16x16x32_bf16 v[164:167], a[12:15], v[44:47], v[184:187]\n\t"
    "v_exp_f32 v174, v174\n\t"
    "v_mfma_f32_16x16x32_bf16 v[164:167], a[8:11], v[40:43], v[164:167]\n\t"
    "v_add_f32 v207, v207, v173\n\t"
    "v_cvt_pk_bf16_f32 v114, v172, v173\n\t"
    "v_mfma_f32_16x16x32_bf16 v[164:167], a[4:7], v[36:39], v[164:167]\n\t"
    "s_add_u32 m0, s56, 69632\n\t"
    "v_exp_f32 v175, v175\n\t"
    "v_mfma_f32_16x16x32_bf16 v[164:167], a[0:3], v[32:35], v[164:167]\n\t"
    "buffer_load_dwordx4 v247, s[16:19], s63 offen offset:0 lds\n\t"
    "ds_read_b64_tr_b16 a[26:27], v254 offset:21504\n\t"
    "ds_read_b64_tr_b16 a[28:29], v254 offset:25600\n\t"
    "v_add_f32 v206, v206, v174\n\t"
    "v_mfma_f32_16x16x32_bf16 v[168:171], a[0:3], v[64:67], v[188:191]\n\t"
    "v_exp_f32 v160, v160\n\t"
    "v_mfma_f32_16x16x32_bf16 v[168:171], a[4:7], v[68:71], v[168:171]\n\t"
    "v_add_f32 v207, v207, v175\n\t"
    "v_cvt_pk_bf16_f32 v115, v174, v175\n\t"
    "v_mfma_f32_16x16x32_bf16 v[168:171], a[8:11], v[72:75], v[168:171]\n\t"
    "v_exp_f32 v161, v161\n\t"
    "v_mfma_f32_16x16x32_bf16 v[168:171], a[12:15], v[76:79], v[168:171]\n\t"
    "buffer_load_dwordx4 v247, s[16:19], s63 offen offset:2048 lds\n\t"
    "ds_read_b64_tr_b16 a[30:31], v254 offset:29696\n\t"
    "v_add_f32 v200, v200, v160\n\t"
    "v_mfma_f32_16x16x32_bf16 v[172:175], a[12:15], v[108:111], v[192:195]\n\t"
    "v_exp_f32 v162, v162\n\t"
    "v_mfma_f32_16x16x32_bf16 v[172:175], a[8:11], v[104:107], v[172:175]\n\t"
    "v_add_f32 v201, v201, v161\n\t"
    "v_cvt_pk_bf16_f32 v20, v160, v161\n\t"
    "v_mfma_f32_16x16x32_bf16 v[172:175], a[4:7], v[100:103], v[172:175]\n\t"
    "s_add_u32 m0, s56, 70528\n\t"
    "s_waitcnt lgkmcnt(4)\n\t"
    "v_exp_f32 v163, v163\n\t"
    "v_mfma_f32_16x16x32_bf16 v[172:175], a[0:3], v[96:99], v[172:175]\n\t"
    "buffer_load_dwordx4 v247, s[16:19], s63 offen offset:128 lds\n\t"
    "ds_read_b128 a[12:15], v252 offset:13824\n\t"
    "v_add_f32 v200, v200, v162\n\t"
    "v_mfma_f32_16x16x32_bf16 a[112:115], a[16:19], v[24:27], a[112:115]\n\t"
    "s_add_u32 s53, s55, 24576\n\t"
    "v_exp_f32 v164, v164\n\t"
    "v_mfma_f32_16x16x32_bf16 a[112:115], a[20:23], v[28:31], a[112:115]\n\t"
    "buffer_load_dwordx4 v247, s[16:19], s63 offen offset:2176 lds\n\t"
    "v_add_f32 v201, v201, v163\n\t"
    "v_cvt_pk_bf16_f32 v21, v162, v163\n\t"
    "v_mfma_f32_16x16x32_bf16 a[144:147], a[20:23], v[60:63], a[144:147]\n\t"
    "s_add_u32 m0, s56, 73728\n\t"
    "v_exp_f32 v165, v165\n\t"
    "v_mfma_f32_16x16x32_bf16 a[144:147], a[16:19], v[56:59], a[144:147]\n\t"
    "buffer_load_dwordx4 v247, s[16:19], s53 offen offset:0 lds\n\t"
    "ds_read_b128 a[0:3], v252 offset:12288\n\t"
    "ds_read_b128 a[4:7], v252 offset:12800\n\t"
    "v_add_f32 v202, v202, v164\n\t"
    "v_mfma_f32_16x16x32_bf16 a[176:179], a[16:19], v[88:91], a[176:179]\n\t"
    "v_exp_f32 v166, v166\n\t"
    "v_mfma_f32_16x16x32_bf16 a[176:179], a[20:23], v[92:95], a[176:179]\n\t"
    "buffer_load_dwordx4 v247, s[16:19], s53 offen offset:2048 lds\n\t"
    "v_add_f32 v203, v203, v165\n\t"
    "v_cvt_pk_bf16_f32 v52, v164, v165\n\t"
    "v_mfma_f32_16x16x32_bf16 a[208:211], a[20:23], v[124:127], a[208:211]\n\t"
    "s_waitcnt lgkmcnt(3)\n\t"
    "v_exp_f32 v167, v167\n\t"
    "v_mfma_f32_16x16x32_bf16 a[208:211], a[16:19], v[120:123], a[208:211]\n\t"
    "ds_read_b128 a[8:11], v252 offset:13312\n\t"
    "ds_read_b64_tr_b16 a[16:17], v253 offset:17920\n\t"
    "v_add_f32 v202, v202, v166\n\t"
    "v_mfma_f32_16x16x32_bf16 a[212:215], a[24:27], v[120:123], a[212:215]\n\t"
    "s_add_u32 m0, s56, 74624\n\t"
    "v_exp_f32 v168, v168\n\t"
    "v_mfma_f32_16x16x32_bf16 a[212:215], a[28:31], v[124:127], a[212:215]\n\t"
    "buffer_load_dwordx4 v247, s[16:19], s53 offen offset:128 lds\n\t"
    "v_add_f32 v203, v203, v167\n\t"
    "v_cvt_pk_bf16_f32 v53, v166, v167\n\t"
    "v_mfma_f32_16x16x32_bf16 a[180:183], a[28:31], v[92:95], a[180:183]\n\t"
    "s_add_u32 s63, s55, 28672\n\t"
    "v_exp_f32 v169, v169\n\t"
    "v_mfma_f32_16x16x32_bf16 a[180:183], a[24:27], v[88:91], a[180:183]\n\t"
    "buffer_load_dwordx4 v247, s[16:19], s53 offen offset:2176 lds\n\t"
    "ds_read_b64_tr_b16 a[18:19], v253 offset:22016\n\t"
    "ds_read_b64_tr_b16 a[20:21], v253 offset:26112\n\t"
    "v_add_f32 v204, v204, v168\n\t"
    "v_mfma_f32_16x16x32_bf16 a[148:151], a[24:27], v[56:59], a[148:151]\n\t"
    "s_add_u32 m0, s56, 77824\n\t"
    "v_exp_f32 v170, v170\n\t"
    "v_mfma_f32_16x16x32_bf16 a[148:151], a[28:31], v[60:63], a[148:151]\n\t"
    "buffer_load_dwordx4 v247, s[16:19], s63 offen offset:0 lds\n\t"
    "v_add_f32 v205, v205, v169\n\t"
    "v_cvt_pk_bf16_f32 v84, v168, v169\n\t"
    "v_mfma_f32_16x16x32_bf16 a[116:119], a[28:31], v[28:31], a[116:119]\n\t"
    "s_waitcnt lgkmcnt(3)\n\t"
    "v_exp_f32 v171, v171\n\t"
    "v_mfma_f32_16x16x32_bf16 a[116:119], a[24:27], v[24:27], a[116:119]\n\t"
    "buffer_load_dwordx4 v247, s[16:19], s63 offen offset:2048 lds\n\t"
    "ds_read_b64_tr_b16 a[22:23], v253 offset:30208\n\t"
    "v_add_f32 v204, v204, v170\n\t"
    "v_mfma_f32_16x16x32_bf16 v[160:163], a[0:3], v[0:3], v[180:183]\n\t"
    "v_exp_f32 v172, v172\n\t"
    "v_mfma_f32_16x16x32_bf16 v[160:163], a[4:7], v[4:7], v[160:163]\n\t"
    "v_add_f32 v205, v205, v171\n\t"
    "v_cvt_pk_bf16_f32 v85, v170, v171\n\t"
    "v_mfma_f32_16x16x32_bf16 v[160:163], a[8:11], v[8:11], v[160:163]\n\t"
    "s_add_u32 m0, s56, 78720\n\t"
    "v_exp_f32 v173, v173\n\t"
    "v_mfma_f32_16x16x32_bf16 v[160:163], a[12:15], v[12:15], v[160:163]\n\t"
    "buffer_load_dwordx4 v247, s[16:19], s63 offen offset:128 lds\n\t"
    "ds_read_b64_tr_b16 a[24:25], v254 offset:17920\n\t"
    "v_add_f32 v206, v206, v172\n\t"
    "v_mfma_f32_16x16x32_bf16 v[164:167], a[12:15], v[44:47], v[184:187]\n\t"
    "v_exp_f32 v174, v174\n\t"
    "v_mfma_f32_16x16x32_bf16 v[164:167], a[8:11], v[40:43], v[164:167]\n\t"
    "v_add_f32 v207, v207, v173\n\t"
    "v_cvt_pk_bf16_f32 v116, v172, v173\n\t"
    "v_mfma_f32_16x16x32_bf16 v[164:167], a[4:7], v[36:39], v[164:167]\n\t"
    "v_exp_f32 v175, v175\n\t"
    "v_mfma_f32_16x16x32_bf16 v[164:167], a[0:3], v[32:35], v[164:167]\n\t"
    "buffer_load_dwordx4 v247, s[16:19], s63 offen offset:2176 lds\n\t"
    "ds_read_b64_tr_b16 a[26:27], v254 offset:22016\n\t"
    "ds_read_b64_tr_b16 a[28:29], v254 offset:26112\n\t"
    "v_add_f32 v206, v206, v174\n\t"
    "v_mfma_f32_16x16x32_bf16 v[168:171], a[0:3], v[64:67], v[188:191]\n\t"
    "v_exp_f32 v160, v160\n\t"
    "v_mfma_f32_16x16x32_bf16 v[168:171], a[4:7], v[68:71], v[168:171]\n\t"
    "v_add_f32 v207, v207, v175\n\t"
    "v_cvt_pk_bf16_f32 v117, v174, v175\n\t"
    "v_mfma_f32_16x16x32_bf16 v[168:171], a[8:11], v[72:75], v[168:171]\n\t"
    "v_exp_f32 v161, v161\n\t"
    "v_mfma_f32_16x16x32_bf16 v[168:171], a[12:15], v[76:79], v[168:171]\n\t"
    "ds_read_b64_tr_b16 a[30:31], v254 offset:30208\n\t"
    "v_add_f32 v200, v200, v160\n\t"
    "v_mfma_f32_16x16x32_bf16 v[172:175], a[12:15], v[108:111], v[192:195]\n\t"
    "v_exp_f32 v162, v162\n\t"
    "v_mfma_f32_16x16x32_bf16 v[172:175], a[8:11], v[104:107], v[172:175]\n\t"
    "v_add_f32 v201, v201, v161\n\t"
    "v_cvt_pk_bf16_f32 v22, v160, v161\n\t"
    "v_mfma_f32_16x16x32_bf16 v[172:175], a[4:7], v[100:103], v[172:175]\n\t"
    "s_waitcnt lgkmcnt(4)\n\t"
    "v_exp_f32 v163, v163\n\t"
    "v_mfma_f32_16x16x32_bf16 v[172:175], a[0:3], v[96:99], v[172:175]\n\t"
    "s_waitcnt lgkmcnt(0)\n\t"
    "s_waitcnt vmcnt(48)\n\t"
    "s_barrier\n\t"
    "ds_read_b128 a[12:15], v252 offset:17920\n\t"
    "v_add_f32 v200, v200, v162\n\t"
    "v_xor_b32 v253, 0x8000, v253\n\t"
    "v_mfma_f32_16x16x32_bf16 a[120:123], a[16:19], v[24:27], a[120:123]\n\t"
    "s_add_u32 s55, s55, 0x4000\n\t"
    "v_exp_f32 v164, v164\n\t"
    "v_mfma_f32_16x16x32_bf16 a[120:123], a[20:23], v[28:31], a[120:123]\n\t"
    "v_add_f32 v201, v201, v163\n\t"
    "v_cvt_pk_bf16_f32 v23, v162, v163\n\t"
    "v_mfma_f32_16x16x32_bf16 a[152:155], a[20:23], v[60:63], a[152:155]\n\t"
    "s_add_u32 s56, s55, 0x4000\n\t"
    "v_exp_f32 v165, v165\n\t"
    "v_mfma_f32_16x16x32_bf16 a[152:155], a[16:19], v[56:59], a[152:155]\n\t"
    "ds_read_b128 a[0:3], v252 offset:16384\n\t"
    "ds_read_b128 a[4:7], v252 offset:16896\n\t"
    "v_add_f32 v202, v202, v164\n\t"
    "v_mfma_f32_16x16x32_bf16 a[184:187], a[16:19], v[88:91], a[184:187]\n\t"
    "s_and_b32 s56, s56, 0xffff\n\t"
    "v_exp_f32 v166, v166\n\t"
    "v_mfma_f32_16x16x32_bf16 a[184:187], a[20:23], v[92:95], a[184:187]\n\t"
    "v_add_f32 v203, v203, v165\n\t"
    "v_cvt_pk_bf16_f32 v54, v164, v165\n\t"
    "v_mfma_f32_16x16x32_bf16 a[216:219], a[20:23], v[124:127], a[216:219]\n\t"
    "v_exp_f32 v167, v167\n\t"
    "v_mfma_f32_16x16x32_bf16 a[216:219], a[16:19], v[120:123], a[216:219]\n\t"
    "ds_read_b128 a[8:11], v252 offset:17408\n\t"
    "ds_read_b64_tr_b16 a[16:17], v253 offset:0\n\t"
    "v_add_f32 v202, v202, v166\n\t"
    "v_xor_b32 v254, 0x8000, v254\n\t"
    "v_mfma_f32_16x16x32_bf16 a[220:223], a[24:27], v[120:123], a[220:223]\n\t"
    "v_exp_f32 v168, v168\n\t"
    "v_mfma_f32_16x16x32_bf16 a[220:223], a[28:31], v[124:127], a[220:223]\n\t"
    "v_add_f32 v203, v203, v167\n\t"
    "v_cvt_pk_bf16_f32 v55, v166, v167\n\t"
    "v_mfma_f32_16x16x32_bf16 a[188:191], a[28:31], v[92:95], a[188:191]\n\t"
    "v_exp_f32 v169, v169\n\t"
    "v_mfma_f32_16x16x32_bf16 a[188:191], a[24:27], v[88:91], a[188:191]\n\t"
    "ds_read_b64_tr_b16 a[18:19], v253 offset:4096\n\t"
    "ds_read_b64_tr_b16 a[20:21], v253 offset:8192\n\t"
    "v_add_f32 v204, v204, v168\n\t"
    "v_mfma_f32_16x16x32_bf16 a[156:159], a[24:27], v[56:59], a[156:159]\n\t"
    "v_exp_f32 v170, v170\n\t"
    "v_mfma_f32_16x16x32_bf16 a[156:159], a[28:31], v[60:63], a[156:159]\n\t"
    "v_add_f32 v205, v205, v169\n\t"
    "v_cvt_pk_bf16_f32 v86, v168, v169\n\t"
    "v_mfma_f32_16x16x32_bf16 a[124:127], a[28:31], v[28:31], a[124:127]\n\t"
    "s_waitcnt lgkmcnt(3)\n\t"
    "v_exp_f32 v171, v171\n\t"
    "v_mfma_f32_16x16x32_bf16 a[124:127], a[24:27], v[24:27], a[124:127]\n\t"
    "s_branch L_loopB_%=\n\t"
    "L_exitB_%=:\n\t"
    "ds_read_b64_tr_b16 a[22:23], v253 offset:28672\n\t"
    "v_add_f32 v204, v204, v170\n\t"
    "v_mfma_f32_16x16x32_bf16 v[160:163], a[0:3], v[0:3], v[180:183]\n\t"
    "v_add_f32 v205, v205, v171\n\t"
    "v_cvt_pk_bf16_f32 v95, v170, v171\n\t"
    "v_mfma_f32_16x16x32_bf16 v[160:163], a[4:7], v[4:7], v[160:163]\n\t"
    "v_add_u32 v253, s94, v253\n\t"
    "v_add_u32 v254, s94, v254\n\t"
    "v_mfma_f32_16x16x32_bf16 v[160:163], a[8:11], v[8:11], v[160:163]\n\t"
    "v_mfma_f32_16x16x32_bf16 v[160:163], a[12:15], v[12:15], v[160:163]\n\t"
    "s_waitcnt lgkmcnt(0)\n\t"
    "ds_read_b128 a[16:19], v252 offset:4096\n\t"
    "ds_read_b128 a[20:23], v252 offset:4608\n\t"
    "v_mfma_f32_16x16x32_bf16 v[164:167], a[12:15], v[44:47], v[184:187]\n\t"
    "v_exp_f32 v172, v172\n\t"
    "v_mfma_f32_16x16x32_bf16 v[164:167], a[8:11], v[40:43], v[164:167]\n\t"
    "v_exp_f32 v173, v173\n\t"
    "v_mfma_f32_16x16x32_bf16 v[164:167], a[4:7], v[36:39], v[164:167]\n\t"
    "v_exp_f32 v174, v174\n\t"
    "v_mfma_f32_16x16x32_bf16 v[164:167], a[0:3], v[32:35], v[164:167]\n\t"
    "ds_read_b128 a[24:27], v252 offset:5120\n\t"
    "ds_read_b64_tr_b16 a[32:33], v253 offset:0\n\t"
    "ds_read_b64_tr_b16 a[34:35], v253 offset:4096\n\t"
    "ds_read_b64_tr_b16 a[36:37], v253 offset:8192\n\t"
    "ds_read_b64_tr_b16 a[38:39], v253 offset:12288\n\t"
    "v_exp_f32 v175, v175\n\t"
    "v_add_f32 v206, v206, v172\n\t"
    "v_add_f32 v207, v207, v173\n\t"
    "v_cvt_pk_bf16_f32 v126, v172, v173\n\t"
    "v_add_f32 v206, v206, v174\n\t"
    "v_add_f32 v207, v207, v175\n\t"
    "v_cvt_pk_bf16_f32 v127, v174, v175\n\t"
    "v_mfma_f32_16x16x32_bf16 v[168:171], a[0:3], v[64:67], v[188:191]\n\t"
    "v_exp_f32 v160, v160\n\t"
    "v_mfma_f32_16x16x32_bf16 v[168:171], a[4:7], v[68:71], v[168:171]\n\t"
    "s_mov_b32 s53, 131072\n\t"
    "v_exp_f32 v161, v161\n\t"
    "v_mfma_f32_16x16x32_bf16 v[168:171], a[8:11], v[72:75], v[168:171]\n\t"
    "s_mov_b32 m0, 0\n\t"
    "v_exp_f32 v162, v162\n\t"
    "v_mfma_f32_16x16x32_bf16 v[168:171], a[12:15], v[76:79], v[168:171]\n\t"
    "buffer_load_dwordx4 v255, s[24:27], s53 offen offset:0 lds\n\t"
    "buffer_load_dwordx4 v198, s[24:27], s53 offen offset:2048 lds\n\t"
    "ds_read_b128 a[28:31], v252 offset:5632\n\t"
    "ds_read_b64_tr_b16 a[40:41], v254 offset:0\n\t"
    "ds_read_b64_tr_b16 a[42:43], v254 offset:4096\n\t"
    "ds_read_b64_tr_b16 a[44:45], v254 offset:8192\n\t"
    "ds_read_b64_tr_b16 a[46:47], v254 offset:12288\n\t"
    "v_exp_f32 v163, v163\n\t"
    "v_add_f32 v200, v200, v160\n\t"
    "v_add_f32 v201, v201, v161\n\t"
    "v_cvt_pk_bf16_f32 v16, v160, v161\n\t"
    "v_add_f32 v200, v200, v162\n\t"
    "v_add_f32 v201, v201, v163\n\t"
    "v_cvt_pk_bf16_f32 v17, v162, v163\n\t"
    "v_mfma_f32_16x16x32_bf16 v[172:175], a[12:15], v[108:111], v[192:195]\n\t"
    "s_mov_b32 m0, 896\n\t"
    "v_exp_f32 v164, v164\n\t"
    "v_mfma_f32_16x16x32_bf16 v[172:175], a[8:11], v[104:107], v[172:175]\n\t"
    "buffer_load_dwordx4 v255, s[24:27], s53 offen offset:128 lds\n\t"
    "buffer_load_dwordx4 v198, s[24:27], s53 offen offset:2176 lds\n\t"
    "v_exp_f32 v165, v165\n\t"
    "v_mfma_f32_16x16x32_bf16 v[172:175], a[4:7], v[100:103], v[172:175]\n\t"
    "s_mov_b32 s53, 147456\n\t"
    "s_mov_b32 m0, 4096\n\t"
    "v_exp_f32 v166, v166\n\t"
    "v_mfma_f32_16x16x32_bf16 v[172:175], a[0:3], v[96:99], v[172:175]\n\t"
    "buffer_load_dwordx4 v255, s[24:27], s53 offen offset:0 lds\n\t"
    "buffer_load_dwordx4 v198, s[24:27], s53 offen offset:2048 lds\n\t"
    "ds_read_b64_tr_b16 a[48:49], v253 offset:16384\n\t"
    "ds_read_b64_tr_b16 a[50:51], v253 offset:20480\n\t"
    "ds_read_b64_tr_b16 a[52:53], v253 offset:24576\n\t"
    "ds_read_b64_tr_b16 a[54:55], v253 offset:28672\n\t"
    "v_exp_f32 v167, v167\n\t"
    "v_add_f32 v202, v202, v164\n\t"
    "v_add_f32 v203, v203, v165\n\t"
    "v_cvt_pk_bf16_f32 v48, v164, v165\n\t"
    "v_add_f32 v202, v202, v166\n\t"
    "v_add_f32 v203, v203, v167\n\t"
    "v_cvt_pk_bf16_f32 v49, v166, v167\n\t"
    "s_waitcnt lgkmcnt(8)\n\t"
    "v_mfma_f32_16x16x32_bf16 v[160:163], a[16:19], v[0:3], v[180:183]\n\t"
    "v_exp_f32 v168, v168\n\t"
    "v_mfma_f32_16x16x32_bf16 v[160:163], a[20:23], v[4:7], v[160:163]\n\t"
    "v_exp_f32 v169, v169\n\t"
    "v_mfma_f32_16x16x32_bf16 v[160:163], a[24:27], v[8:11], v[160:163]\n\t"
    "s_mov_b32 m0, 4992\n\t"
    "v_exp_f32 v170, v170\n\t"
    "v_mfma_f32_16x16x32_bf16 v[160:163], a[28:31], v[12:15], v[160:163]\n\t"
    "buffer_load_dwordx4 v255, s[24:27], s53 offen offset:128 lds\n\t"
    "buffer_load_dwordx4 v198, s[24:27], s53 offen offset:2176 lds\n\t"
    "ds_read_b128 a[0:3], v252 offset:8192\n\t"
    "ds_read_b128 a[4:7], v252 offset:8704\n\t"
    "ds_read_b64_tr_b16 a[56:57], v254 offset:16384\n\t"
    "ds_read_b64_tr_b16 a[58:59], v254 offset:20480\n\t"
    "ds_read_b64_tr_b16 a[60:61], v254 offset:24576\n\t"
    "ds_read_b64_tr_b16 a[62:63], v254 offset:28672\n\t"
    "v_exp_f32 v171, v171\n\t"
    "v_add_f32 v204, v204, v168\n\t"
    "v_add_f32 v205, v205, v169\n\t"
    "v_cvt_pk_bf16_f32 v80, v168, v169\n\t"
    "v_add_f32 v204, v204, v170\n\t"
    "v_add_f32 v205, v205, v171\n\t"
    "v_cvt_pk_bf16_f32 v81, v170, v171\n\t"
    "v_mfma_f32_16x16x32_bf16 v[164:167], a[28:31], v[44:47], v[184:187]\n\t"
    "s_mov_b32 s53, 163840\n\t"
    "s_mov_b32 m0, 8192\n\t"
    "v_exp_f32 v172, v172\n\t"
    "v_mfma_f32_16x16x32_bf16 v[164:167], a[24:27], v[40:43], v[164:167]\n\t"
    "buffer_load_dwordx4 v255, s[24:27], s53 offen offset:0 lds\n\t"
    "buffer_load_dwordx4 v198, s[24:27], s53 offen offset:2048 lds\n\t"
    "v_exp_f32 v173, v173\n\t"
    "v_mfma_f32_16x16x32_bf16 v[164:167], a[20:23], v[36:39], v[164:167]\n\t"
    "s_mov_b32 m0, 9088\n\t"
    "v_exp_f32 v174, v174\n\t"
    "v_mfma_f32_16x16x32_bf16 v[164:167], a[16:19], v[32:35], v[164:167]\n\t"
    "buffer_load_dwordx4 v255, s[24:27], s53 offen offset:128 lds\n\t"
    "buffer_load_dwordx4 v198, s[24:27], s53 offen offset:2176 lds\n\t"
    "ds_read_b128 a[8:11], v252 offset:9216\n\t"
    "v_exp_f32 v175, v175\n\t"
    "v_add_f32 v206, v206, v172\n\t"
    "v_add_f32 v207, v207, v173\n\t"
    "v_cvt_pk_bf16_f32 v112, v172, v173\n\t"
    "v_add_f32 v206, v206, v174\n\t"
    "v_add_f32 v207, v207, v175\n\t"
    "v_cvt_pk_bf16_f32 v113, v174, v175\n\t"
    "v_mfma_f32_16x16x32_bf16 v[168:171], a[16:19], v[64:67], v[188:191]\n\t"
    "v_exp_f32 v160, v160\n\t"
    "v_mfma_f32_16x16x32_bf16 v[168:171], a[20:23], v[68:71], v[168:171]\n\t"
    "s_mov_b32 s53, 180224\n\t"
    "v_exp_f32 v161, v161\n\t"
    "v_mfma_f32_16x16x32_bf16 v[168:171], a[24:27], v[72:75], v[168:171]\n\t"
    "s_mov_b32 m0, 12288\n\t"
    "v_exp_f32 v162, v162\n\t"
    "v_mfma_f32_16x16x32_bf16 v[168:171], a[28:31], v[76:79], v[168:171]\n\t"
    "buffer_load_dwordx4 v255, s[24:27], s53 offen offset:0 lds\n\t"
    "buffer_load_dwordx4 v198, s[24:27], s53 offen offset:2048 lds\n\t"
    "ds_read_b128 a[12:15], v252 offset:9728\n\t"
    "v_subrev_u32 v253, 0x20000, v253\n\t"
    "v_subrev_u32 v254, 0x20000, v254\n\t"
    "v_exp_f32 v163, v163\n\t"
    "v_add_f32 v200, v200, v160\n\t"
    "v_add_f32 v201, v201, v161\n\t"
    "v_cvt_pk_bf16_f32 v18, v160, v161\n\t"
    "v_add_f32 v200, v200, v162\n\t"
    "v_add_f32 v201, v201, v163\n\t"
    "v_cvt_pk_bf16_f32 v19, v162, v163\n\t"
    "v_mfma_f32_16x16x32_bf16 v[172:175], a[28:31], v[108:111], v[192:195]\n\t"
    "s_mov_b32 m0, 13184\n\t"
    "v_exp_f32 v164, v164\n\t"
    "v_mfma_f32_16x16x32_bf16 v[172:175], a[24:27], v[104:107], v[172:175]\n\t"
    "buffer_load_dwordx4 v255, s[24:27], s53 offen offset:128 lds\n\t"
    "buffer_load_dwordx4 v198, s[24:27], s53 offen offset:2176 lds\n\t"
    "v_exp_f32 v165, v165\n\t"
    "v_mfma_f32_16x16x32_bf16 v[172:175], a[20:23], v[100:103], v[172:175]\n\t"
    "s_mov_b32 s53, 196608\n\t"
    "s_mov_b32 m0, 16384\n\t"
    "v_exp_f32 v166, v166\n\t"
    "v_mfma_f32_16x16x32_bf16 v[172:175], a[16:19], v[96:99], v[172:175]\n\t"
    "buffer_load_dwordx4 v255, s[24:27], s53 offen offset:0 lds\n\t"
    "buffer_load_dwordx4 v198, s[24:27], s53 offen offset:2048 lds\n\t"
    "v_exp_f32 v167, v167\n\t"
    "v_add_f32 v202, v202, v164\n\t"
    "v_add_f32 v203, v203, v165\n\t"
    "v_cvt_pk_bf16_f32 v50, v164, v165\n\t"
    "v_add_f32 v202, v202, v166\n\t"
    "v_add_f32 v203, v203, v167\n\t"
    "v_cvt_pk_bf16_f32 v51, v166, v167\n\t"
    "s_waitcnt lgkmcnt(0)\n\t"
    "v_mfma_f32_16x16x32_bf16 v[160:163], a[0:3], v[0:3], v[180:183]\n\t"
    "v_exp_f32 v168, v168\n\t"
    "v_mfma_f32_16x16x32_bf16 v[160:163], a[4:7], v[4:7], v[160:163]\n\t"
    "v_exp_f32 v169, v169\n\t"
    "v_mfma_f32_16x16x32_bf16 v[160:163], a[8:11], v[8:11], v[160:163]\n\t"
    "s_mov_b32 m0, 17280\n\t"
    "v_exp_f32 v170, v170\n\t"
    "v_mfma_f32_16x16x32_bf16 v[160:163], a[12:15], v[12:15], v[160:163]\n\t"
    "buffer_load_dwordx4 v255, s[24:27], s53 offen offset:128 lds\n\t"
    "buffer_load_dwordx4 v198, s[24:27], s53 offen offset:2176 lds\n\t"
    "ds_read_b128 a[16:19], v252 offset:12288\n\t"
    "ds_read_b128 a[20:23], v252 offset:12800\n\t"
    "v_exp_f32 v171, v171\n\t"
    "v_add_f32 v204, v204, v168\n\t"
    "v_add_f32 v205, v205, v169\n\t"
    "v_cvt_pk_bf16_f32 v82, v168, v169\n\t"
    "v_add_f32 v204, v204, v170\n\t"
    "v_add_f32 v205, v205, v171\n\t"
    "v_cvt_pk_bf16_f32 v83, v170, v171\n\t"
    "v_mfma_f32_16x16x32_bf16 v[164:167], a[12:15], v[44:47], v[184:187]\n\t"
    "s_mov_b32 s53, 212992\n\t"
    "s_mov_b32 m0, 20480\n\t"
    "v_exp_f32 v172, v172\n\t"
    "v_mfma_f32_16x16x32_bf16 v[164:167], a[8:11], v[40:43], v[164:167]\n\t"
    "buffer_load_dwordx4 v255, s[24:27], s53 offen offset:0 lds\n\t"
    "buffer_load_dwordx4 v198, s[24:27], s53 offen offset:2048 lds\n\t"
    "v_exp_f32 v173, v173\n\t"
    "v_mfma_f32_16x16x32_bf16 v[164:167], a[4:7], v[36:39], v[164:167]\n\t"
    "s_mov_b32 m0, 21376\n\t"
    "v_exp_f32 v174, v174\n\t"
    "v_mfma_f32_16x16x32_bf16 v[164:167], a[0:3], v[32:35], v[164:167]\n\t"
    "buffer_load_dwordx4 v255, s[24:27], s53 offen offset:128 lds\n\t"
    "buffer_load_dwordx4 v198, s[24:27], s53 offen offset:2176 lds\n\t"
    "ds_read_b128 a[24:27], v252 offset:13312\n\t"
    "v_exp_f32 v175, v175\n\t"
    "v_add_f32 v206, v206, v172\n\t"
    "v_add_f32 v207, v207, v173\n\t"
    "v_cvt_pk_bf16_f32 v114, v172, v173\n\t"
    "v_add_f32 v206, v206, v174\n\t"
    "v_add_f32 v207, v207, v175\n\t"
    "v_cvt_pk_bf16_f32 v115, v174, v175\n\t"
    "v_mfma_f32_16x16x32_bf16 v[168:171], a[0:3], v[64:67], v[188:191]\n\t"
    "v_exp_f32 v160, v160\n\t"
    "v_mfma_f32_16x16x32_bf16 v[168:171], a[4:7], v[68:71], v[168:171]\n\t"
    "s_mov_b32 s53, 229376\n\t"
    "v_exp_f32 v161, v161\n\t"
    "v_mfma_f32_16x16x32_bf16 v[168:171], a[8:11], v[72:75], v[168:171]\n\t"
    "s_mov_b32 m0, 24576\n\t"
    "v_exp_f32 v162, v162\n\t"
    "v_mfma_f32_16x16x32_bf16 v[168:171], a[12:15], v[76:79], v[168:171]\n\t"
    "buffer_load_dwordx4 v255, s[24:27], s53 offen offset:0 lds\n\t"
    "buffer_load_dwordx4 v198, s[24:27], s53 offen offset:2048 lds\n\t"
    "ds_read_b128 a[28:31], v252 offset:13824\n\t"
    "v_exp_f32 v163, v163\n\t"
    "v_add_f32 v200, v200, v160\n\t"
    "v_add_f32 v201, v201, v161\n\t"
    "v_cvt_pk_bf16_f32 v20, v160, v161\n\t"
    "v_add_f32 v200, v200, v162\n\t"
    "v_add_f32 v201, v201, v163\n\t"
    "v_cvt_pk_bf16_f32 v21, v162, v163\n\t"
    "v_mfma_f32_16x16x32_bf16 v[172:175], a[12:15], v[108:111], v[192:195]\n\t"
    "s_mov_b32 m0, 25472\n\t"
    "v_exp_f32 v164, v164\n\t"
    "v_mfma_f32_16x16x32_bf16 v[172:175], a[8:11], v[104:107], v[172:175]\n\t"
    "buffer_load_dwordx4 v255, s[24:27], s53 offen offset:128 lds\n\t"
    "buffer_load_dwordx4 v198, s[24:27], s53 offen offset:2176 lds\n\t"
    "v_exp_f32 v165, v165\n\t"
    "v_mfma_f32_16x16x32_bf16 v[172:175], a[4:7], v[100:103], v[172:175]\n\t"
    "s_mov_b32 s53, 245760\n\t"
    "s_mov_b32 m0, 28672\n\t"
    "v_exp_f32 v166, v166\n\t"
    "v_mfma_f32_16x16x32_bf16 v[172:175], a[0:3], v[96:99], v[172:175]\n\t"
    "buffer_load_dwordx4 v255, s[24:27], s53 offen offset:0 lds\n\t"
    "buffer_load_dwordx4 v198, s[24:27], s53 offen offset:2048 lds\n\t"
    "v_exp_f32 v167, v167\n\t"
    "v_add_f32 v202, v202, v164\n\t"
    "v_add_f32 v203, v203, v165\n\t"
    "v_cvt_pk_bf16_f32 v52, v164, v165\n\t"
    "v_add_f32 v202, v202, v166\n\t"
    "v_add_f32 v203, v203, v167\n\t"
    "v_cvt_pk_bf16_f32 v53, v166, v167\n\t"
    "s_waitcnt lgkmcnt(0)\n\t"
    "v_mfma_f32_16x16x32_bf16 v[160:163], a[16:19], v[0:3], v[180:183]\n\t"
    "v_exp_f32 v168, v168\n\t"
    "v_mfma_f32_16x16x32_bf16 v[160:163], a[20:23], v[4:7], v[160:163]\n\t"
    "v_exp_f32 v169, v169\n\t"
    "v_mfma_f32_16x16x32_bf16 v[160:163], a[24:27], v[8:11], v[160:163]\n\t"
    "s_waitcnt vmcnt(46) lgkmcnt(6)\n\t"
    "v_exp_f32 v170, v170\n\t"
    "v_mfma_f32_16x16x32_bf16 v[160:163], a[28:31], v[12:15], v[160:163]\n\t"
    "s_barrier\n\t"
    "ds_read_b128 a[0:3], v252 offset:16384\n\t"
    "ds_read_b128 a[4:7], v252 offset:16896\n\t"
    "v_exp_f32 v171, v171\n\t"
    "v_add_f32 v204, v204, v168\n\t"
    "v_add_f32 v205, v205, v169\n\t"
    "v_cvt_pk_bf16_f32 v84, v168, v169\n\t"
    "v_add_f32 v204, v204, v170\n\t"
    "v_add_f32 v205, v205, v171\n\t"
    "v_cvt_pk_bf16_f32 v85, v170, v171\n\t"
    "v_mfma_f32_16x16x32_bf16 v[164:167], a[28:31], v[44:47], v[184:187]\n\t"
    "s_mov_b32 m0, 29568\n\t"
    "v_exp_f32 v172, v172\n\t"
    "v_mfma_f32_16x16x32_bf16 v[164:167], a[24:27], v[40:43], v[164:167]\n\t"
    "buffer_load_dwordx4 v255, s[24:27], s53 offen offset:128 lds\n\t"
    "buffer_load_dwordx4 v198, s[24:27], s53 offen offset:2176 lds\n\t"
    "v_exp_f32 v173, v173\n\t"
    "v_mfma_f32_16x16x32_bf16 v[164:167], a[20:23], v[36:39], v[164:167]\n\t"
    "s_add_u32 s53, s98, 393216\n\t"
    "s_add_u32 m0, s97, 65536\n\t"
    "v_exp_f32 v174, v174\n\t"
    "v_mfma_f32_16x16x32_bf16 v[164:167], a[16:19], v[32:35], v[164:167]\n\t"
    "buffer_load_dwordx4 v255, s[24:27], s53 offen offset:0 lds\n\t"
    "buffer_load_dwordx4 v198, s[24:27], s53 offen offset:2048 lds\n\t"
    "ds_read_b128 a[8:11], v252 offset:17408\n\t"
    "v_exp_f32 v175, v175\n\t"
    "v_add_f32 v206, v206, v172\n\t"
    "v_add_f32 v207, v207, v173\n\t"
    "v_cvt_pk_bf16_f32 v116, v172, v173\n\t"
    "v_add_f32 v206, v206, v174\n\t"
    "v_add_f32 v207, v207, v175\n\t"
    "v_cvt_pk_bf16_f32 v117, v174, v175\n\t"
    "v_mfma_f32_16x16x32_bf16 v[168:171], a[16:19], v[64:67], v[188:191]\n\t"
    "v_exp_f32 v160, v160\n\t"
    "v_mfma_f32_16x16x32_bf16 v[168:171], a[20:23], v[68:71], v[168:171]\n\t"
    "v_exp_f32 v161, v161\n\t"
    "v_mfma_f32_16x16x32_bf16 v[168:171], a[24:27], v[72:75], v[168:171]\n\t"
    "s_add_u32 m0, s97, 66432\n\t"
    "v_exp_f32 v162, v162\n\t"
    "v_mfma_f32_16x16x32_bf16 v[168:171], a[28:31], v[76:79], v[168:171]\n\t"
    "buffer_load_dwordx4 v255, s[24:27], s53 offen offset:128 lds\n\t"
    "buffer_load_dwordx4 v198, s[24:27], s53 offen offset:2176 lds\n\t"
    "ds_read_b128 a[12:15], v252 offset:17920\n\t"
    "v_exp_f32 v163, v163\n\t"
    "v_add_f32 v200, v200, v160\n\t"
    "v_add_f32 v201, v201, v161\n\t"
    "v_cvt_pk_bf16_f32 v22, v160, v161\n\t"
    "v_add_f32 v200, v200, v162\n\t"
    "v_add_f32 v201, v201, v163\n\t"
    "v_cvt_pk_bf16_f32 v23, v162, v163\n\t"
    "v_mfma_f32_16x16x32_bf16 v[172:175], a[28:31], v[108:111], v[192:195]\n\t"
    "s_add_u32 s53, s98, 262144\n\t"
    "s_add_u32 m0, s97, 131072\n\t"
    "v_exp_f32 v164, v164\n\t"
    "v_mfma_f32_16x16x32_bf16 v[172:175], a[24:27], v[104:107], v[172:175]\n\t"
    "buffer_load_dwordx4 v255, s[24:27], s53 offen offset:0 lds\n\t"
    "buffer_load_dwordx4 v198, s[24:27], s53 offen offset:2048 lds\n\t"
    "v_exp_f32 v165, v165\n\t"
    "v_mfma_f32_16x16x32_bf16 v[172:175], a[20:23], v[100:103], v[172:175]\n\t"
    "s_add_u32 m0, s97, 131968\n\t"
    "v_exp_f32 v166, v166\n\t"
    "v_mfma_f32_16x16x32_bf16 v[172:175], a[16:19], v[96:99], v[172:175]\n\t"
    "buffer_load_dwordx4 v255, s[24:27], s53 offen offset:128 lds\n\t"
    "buffer_load_dwordx4 v198, s[24:27], s53 offen offset:2176 lds\n\t"
    "v_exp_f32 v167, v167\n\t"
    "v_add_f32 v202, v202, v164\n\t"
    "v_add_f32 v203, v203, v165\n\t"
    "v_cvt_pk_bf16_f32 v54, v164, v165\n\t"
    "v_add_f32 v202, v202, v166\n\t"
    "v_add_f32 v203, v203, v167\n\t"
    "v_cvt_pk_bf16_f32 v55, v166, v167\n\t"
    "s_waitcnt lgkmcnt(0)\n\t"
    "v_mfma_f32_16x16x32_bf16 v[160:163], a[0:3], v[0:3], v[180:183]\n\t"
    "v_exp_f32 v168, v168\n\t"
    "v_mfma_f32_16x16x32_bf16 v[160:163], a[4:7], v[4:7], v[160:163]\n\t"
    "s_add_u32 s53, s98, 327680\n\t"
    "v_exp_f32 v169, v169\n\t"
    "v_mfma_f32_16x16x32_bf16 v[160:163], a[8:11], v[8:11], v[160:163]\n\t"
    "s_add_u32 m0, s97, 147456\n\t"
    "v_exp_f32 v170, v170\n\t"
    "v_mfma_f32_16x16x32_bf16 v[160:163], a[12:15], v[12:15], v[160:163]\n\t"
    "buffer_load_dwordx4 v255, s[24:27], s53 offen offset:0 lds\n\t"
    "buffer_load_dwordx4 v198, s[24:27], s53 offen offset:2048 lds\n\t"
    "ds_read_b128 a[16:19], v252 offset:20480\n\t"
    "ds_read_b128 a[20:23], v252 offset:20992\n\t"
    "v_exp_f32 v171, v171\n\t"
    "v_add_f32 v204, v204, v168\n\t"
    "v_add_f32 v205, v205, v169\n\t"
    "v_cvt_pk_bf16_f32 v86, v168, v169\n\t"
    "v_add_f32 v204, v204, v170\n\t"
    "v_add_f32 v205, v205, v171\n\t"
    "v_cvt_pk_bf16_f32 v87, v170, v171\n\t"
    "v_mfma_f32_16x16x32_bf16 v[164:167], a[12:15], v[44:47], v[184:187]\n\t"
    "s_add_u32 m0, s97, 148352\n\t"
    "v_exp_f32 v172, v172\n\t"
    "v_mfma_f32_16x16x32_bf16 v[164:167], a[8:11], v[40:43], v[164:167]\n\t"
    "buffer_load_dwordx4 v255, s[24:27], s53 offen offset:128 lds\n\t"
    "buffer_load_dwordx4 v198, s[24:27], s53 offen offset:2176 lds\n\t"
    "v_exp_f32 v173, v173\n\t"
    "v_mfma_f32_16x16x32_bf16 v[164:167], a[4:7], v[36:39], v[164:167]\n\t"
    "s_mov_b32 s53, 458752\n\t"
    "s_mov_b32 m0, 32768\n\t"
    "v_exp_f32 v174, v174\n\t"
    "v_mfma_f32_16x16x32_bf16 v[164:167], a[0:3], v[32:35], v[164:167]\n\t"
    "buffer_load_dwordx4 v255, s[24:27], s53 offen offset:0 lds\n\t"
    "buffer_load_dwordx4 v198, s[24:27], s53 offen offset:2048 lds\n\t"
    "ds_read_b128 a[24:27], v252 offset:21504\n\t"
    "v_exp_f32 v175, v175\n\t"
    "v_add_f32 v206, v206, v172\n\t"
    "v_add_f32 v207, v207, v173\n\t"
    "v_cvt_pk_bf16_f32 v118, v172, v173\n\t"
    "v_add_f32 v206, v206, v174\n\t"
    "v_add_f32 v207, v207, v175\n\t"
    "v_cvt_pk_bf16_f32 v119, v174, v175\n\t"
    "v_mfma_f32_16x16x32_bf16 v[168:171], a[0:3], v[64:67], v[188:191]\n\t"
    "v_exp_f32 v160, v160\n\t"
    "v_mfma_f32_16x16x32_bf16 v[168:171], a[4:7], v[68:71], v[168:171]\n\t"
    "v_exp_f32 v161, v161\n\t"
    "v_mfma_f32_16x16x32_bf16 v[168:171], a[8:11], v[72:75], v[168:171]\n\t"
    "s_waitcnt vmcnt(14)\n\t"
    "v_exp_f32 v162, v162\n\t"
    "v_mfma_f32_16x16x32_bf16 v[168:171], a[12:15], v[76:79], v[168:171]\n\t"
    "s_barrier\n\t"
    "ds_read_b128 a[28:31], v252 offset:22016\n\t"
    "ds_read_b64_tr_b16 a[64:65], v253 offset:0\n\t"
    "ds_read_b64_tr_b16 a[66:67], v253 offset:4096\n\t"
    "ds_read_b64_tr_b16 a[68:69], v253 offset:8192\n\t"
    "ds_read_b64_tr_b16 a[70:71], v253 offset:12288\n\t"
    "v_exp_f32 v163, v163\n\t"
    "v_add_f32 v200, v200, v160\n\t"
    "v_add_f32 v201, v201, v161\n\t"
    "v_cvt_pk_bf16_f32 v212, v160, v161\n\t"
    "v_add_f32 v200, v200, v162\n\t"
    "v_add_f32 v201, v201, v163\n\t"
    "v_cvt_pk_bf16_f32 v213, v162, v163\n\t"
    "v_mfma_f32_16x16x32_bf16 v[172:175], a[12:15], v[108:111], v[192:195]\n\t"
    "s_mov_b32 m0, 33664\n\t"
    "v_exp_f32 v164, v164\n\t"
    "v_mfma_f32_16x16x32_bf16 v[172:175], a[8:11], v[104:107], v[172:175]\n\t"
    "buffer_load_dwordx4 v255, s[24:27], s53 offen offset:128 lds\n\t"
    "buffer_load_dwordx4 v198, s[24:27], s53 offen offset:2176 lds\n\t"
    "v_exp_f32 v165, v165\n\t"
    "v_mfma_f32_16x16x32_bf16 v[172:175], a[4:7], v[100:103], v[172:175]\n\t"
    "s_mov_b32 s53, 475136\n\t"
    "s_mov_b32 m0, 36864\n\t"
    "v_exp_f32 v166, v166\n\t"
    "v_mfma_f32_16x16x32_bf16 v[172:175], a[0:3], v[96:99], v[172:175]\n\t"
    "buffer_load_dwordx4 v255, s[24:27], s53 offen offset:0 lds\n\t"
    "buffer_load_dwordx4 v198, s[24:27], s53 offen offset:2048 lds\n\t"
    "ds_read_b64_tr_b16 a[72:73], v254 offset:0\n\t"
    "ds_read_b64_tr_b16 a[74:75], v254 offset:4096\n\t"
    "ds_read_b64_tr_b16 a[76:77], v254 offset:8192\n\t"
    "ds_read_b64_tr_b16 a[78:79], v254 offset:12288\n\t"
    "v_exp_f32 v167, v167\n\t"
    "v_add_f32 v202, v202, v164\n\t"
    "v_add_f32 v203, v203, v165\n\t"
    "v_cvt_pk_bf16_f32 v220, v164, v165\n\t"
    "v_add_f32 v202, v202, v166\n\t"
    "v_add_f32 v203, v203, v167\n\t"
    "v_cvt_pk_bf16_f32 v221, v166, v167\n\t"
    "s_waitcnt lgkmcnt(8)\n\t"
    "v_mfma_f32_16x16x32_bf16 v[160:163], a[16:19], v[0:3], v[180:183]\n\t"
    "v_exp_f32 v168, v168\n\t"
    "v_mfma_f32_16x16x32_bf16 v[160:163], a[20:23], v[4:7], v[160:163]\n\t"
    "v_exp_f32 v169, v169\n\t"
    "v_mfma_f32_16x16x32_bf16 v[160:163], a[24:27], v[8:11], v[160:163]\n\t"
    "s_mov_b32 m0, 37760\n\t"
    "v_exp_f32 v170, v170\n\t"
    "v_mfma_f32_16x16x32_bf16 v[160:163], a[28:31], v[12:15], v[160:163]\n\t"
    "buffer_load_dwordx4 v255, s[24:27], s53 offen offset:128 lds\n\t"
    "buffer_load_dwordx4 v198, s[24:27], s53 offen offset:2176 lds\n\t"
    "ds_read_b128 a[0:3], v252 offset:24576\n\t"
    "ds_read_b128 a[4:7], v252 offset:25088\n\t"
    "ds_read_b64_tr_b16 a[80:81], v253 offset:16384\n\t"
    "ds_read_b64_tr_b16 a[82:83], v253 offset:20480\n\t"
    "ds_read_b64_tr_b16 a[84:85], v253 offset:24576\n\t"
    "ds_read_b64_tr_b16 a[86:87], v253 offset:28672\n\t"
    "v_exp_f32 v171, v171\n\t"
    "v_add_f32 v204, v204, v168\n\t"
    "v_add_f32 v205, v205, v169\n\t"
    "v_cvt_pk_bf16_f32 v228, v168, v169\n\t"
    "v_add_f32 v204, v204, v170\n\t"
    "v_add_f32 v205, v205, v171\n\t"
    "v_cvt_pk_bf16_f32 v229, v170, v171\n\t"
    "v_mfma_f32_16x16x32_bf16 v[164:167], a[28:31], v[44:47], v[184:187]\n\t"
    "s_mov_b32 s53, 491520\n\t"
    "s_mov_b32 m0, 40960\n\t"
    "v_exp_f32 v172, v172\n\t"
    "v_mfma_f32_16x16x32_bf16 v[164:167], a[24:27], v[40:43], v[164:167]\n\t"
    "buffer_load_dwordx4 v255, s[24:27], s53 offen offset:0 lds\n\t"
    "buffer_load_dwordx4 v198, s[24:27], s53 offen offset:2048 lds\n\t"
    "v_exp_f32 v173, v173\n\t"
    "v_mfma_f32_16x16x32_bf16 v[164:167], a[20:23], v[36:39], v[164:167]\n\t"
    "s_mov_b32 m0, 41856\n\t"
    "v_exp_f32 v174, v174\n\t"
    "v_mfma_f32_16x16x32_bf16 v[164:167], a[16:19], v[32:35], v[164:167]\n\t"
    "buffer_load_dwordx4 v255, s[24:27], s53 offen offset:128 lds\n\t"
    "buffer_load_dwordx4 v198, s[24:27], s53 offen offset:2176 lds\n\t"
    "ds_read_b128 a[8:11], v252 offset:25600\n\t"
    "ds_read_b64_tr_b16 a[88:89], v254 offset:16384\n\t"
    "ds_read_b64_tr_b16 a[90:91], v254 offset:20480\n\t"
    "ds_read_b64_tr_b16 a[92:93], v254 offset:24576\n\t"
    "ds_read_b64_tr_b16 a[94:95], v254 offset:28672\n\t"
    "v_exp_f32 v175, v175\n\t"
    "v_add_f32 v206, v206, v172\n\t"
    "v_add_f32 v207, v207, v173\n\t"
    "v_cvt_pk_bf16_f32 v236, v172, v173\n\t"
    "v_add_f32 v206, v206, v174\n\t"
    "v_add_f32 v207, v207, v175\n\t"
    "v_cvt_pk_bf16_f32 v237, v174, v175\n\t"
    "v_mfma_f32_16x16x32_bf16 v[168:171], a[16:19], v[64:67], v[188:191]\n\t"
    "v_exp_f32 v160, v160\n\t"
    "v_mfma_f32_16x16x32_bf16 v[168:171], a[20:23], v[68:71], v[168:171]\n\t"
    "s_mov_b32 s53, 507904\n\t"
    "v_exp_f32 v161, v161\n\t"
    "v_mfma_f32_16x16x32_bf16 v[168:171], a[24:27], v[72:75], v[168:171]\n\t"
    "s_mov_b32 m0, 45056\n\t"
    "v_exp_f32 v162, v162\n\t"
    "v_mfma_f32_16x16x32_bf16 v[168:171], a[28:31], v[76:79], v[168:171]\n\t"
    "buffer_load_dwordx4 v255, s[24:27], s53 offen offset:0 lds\n\t"
    "buffer_load_dwordx4 v198, s[24:27], s53 offen offset:2048 lds\n\t"
    "ds_read_b128 a[12:15], v252 offset:26112\n\t"
    "v_exp_f32 v163, v163\n\t"
    "v_add_f32 v200, v200, v160\n\t"
    "v_add_f32 v201, v201, v161\n\t"
    "v_cvt_pk_bf16_f32 v214, v160, v161\n\t"
    "v_add_f32 v200, v200, v162\n\t"
    "v_add_f32 v201, v201, v163\n\t"
    "v_cvt_pk_bf16_f32 v215, v162, v163\n\t"
    "v_mfma_f32_16x16x32_bf16 v[172:175], a[28:31], v[108:111], v[192:195]\n\t"
    "s_mov_b32 m0, 45952\n\t"
    "v_exp_f32 v164, v164\n\t"
    "v_mfma_f32_16x16x32_bf16 v[172:175], a[24:27], v[104:107], v[172:175]\n\t"
    "buffer_load_dwordx4 v255, s[24:27], s53 offen offset:128 lds\n\t"
    "buffer_load_dwordx4 v198, s[24:27], s53 offen offset:2176 lds\n\t"
    "v_exp_f32 v165, v165\n\t"
    "v_mfma_f32_16x16x32_bf16 v[172:175], a[20:23], v[100:103], v[172:175]\n\t"
    "v_exp_f32 v166, v166\n\t"
    "v_mfma_f32_16x16x32_bf16 v[172:175], a[16:19], v[96:99], v[172:175]\n\t"
    "v_subrev_u32 v253, s92, v253\n\t"
    "v_subrev_u32 v254, s92, v254\n\t"
    "v_exp_f32 v167, v167\n\t"
    "v_add_f32 v202, v202, v164\n\t"
    "v_add_f32 v203, v203, v165\n\t"
    "v_cvt_pk_bf16_f32 v222, v164, v165\n\t"
    "v_add_f32 v202, v202, v166\n\t"
    "v_add_f32 v203, v203, v167\n\t"
    "v_cvt_pk_bf16_f32 v223, v166, v167\n\t"
    "s_waitcnt lgkmcnt(0)\n\t"
    "v_mfma_f32_16x16x32_bf16 v[160:163], a[0:3], v[0:3], v[180:183]\n\t"
    "v_exp_f32 v168, v168\n\t"
    "v_mfma_f32_16x16x32_bf16 v[160:163], a[4:7], v[4:7], v[160:163]\n\t"
    "v_exp_f32 v169, v169\n\t"
    "v_mfma_f32_16x16x32_bf16 v[160:163], a[8:11], v[8:11], v[160:163]\n\t"
    "v_exp_f32 v170, v170\n\t"
    "v_mfma_f32_16x16x32_bf16 v[160:163], a[12:15], v[12:15], v[160:163]\n\t"
    "ds_read_b128 a[16:19], v252 offset:28672\n\t"
    "ds_read_b128 a[20:23], v252 offset:29184\n\t"
    "v_exp_f32 v171, v171\n\t"
    "v_add_f32 v204, v204, v168\n\t"
    "v_add_f32 v205, v205, v169\n\t"
    "v_cvt_pk_bf16_f32 v230, v168, v169\n\t"
    "v_add_f32 v204, v204, v170\n\t"
    "v_add_f32 v205, v205, v171\n\t"
    "v_cvt_pk_bf16_f32 v231, v170, v171\n\t"
    "v_mfma_f32_16x16x32_bf16 v[164:167], a[12:15], v[44:47], v[184:187]\n\t"
    "v_exp_f32 v172, v172\n\t"
    "v_mfma_f32_16x16x32_bf16 v[164:167], a[8:11], v[40:43], v[164:167]\n\t"
    "v_exp_f32 v173, v173\n\t"
    "v_mfma_f32_16x16x32_bf16 v[164:167], a[4:7], v[36:39], v[164:167]\n\t"
    "v_exp_f32 v174, v174\n\t"
    "v_mfma_f32_16x16x32_bf16 v[164:167], a[0:3], v[32:35], v[164:167]\n\t"
    "ds_read_b128 a[24:27], v252 offset:29696\n\t"
    "v_exp_f32 v175, v175\n\t"
    "v_add_f32 v206, v206, v172\n\t"
    "v_add_f32 v207, v207, v173\n\t"
    "v_cvt_pk_bf16_f32 v238, v172, v173\n\t"
    "v_add_f32 v206, v206, v174\n\t"
    "v_add_f32 v207, v207, v175\n\t"
    "v_cvt_pk_bf16_f32 v239, v174, v175\n\t"
    "v_mfma_f32_16x16x32_bf16 v[168:171], a[0:3], v[64:67], v[188:191]\n\t"
    "v_exp_f32 v160, v160\n\t"
    "v_mfma_f32_16x16x32_bf16 v[168:171], a[4:7], v[68:71], v[168:171]\n\t"
    "v_exp_f32 v161, v161\n\t"
    "v_mfma_f32_16x16x32_bf16 v[168:171], a[8:11], v[72:75], v[168:171]\n\t"
    "v_exp_f32 v162, v162\n\t"
    "v_mfma_f32_16x16x32_bf16 v[168:171], a[12:15], v[76:79], v[168:171]\n\t"
    "ds_read_b128 a[28:31], v252 offset:30208\n\t"
    "v_exp_f32 v163, v163\n\t"
    "v_add_f32 v200, v200, v160\n\t"
    "v_add_f32 v201, v201, v161\n\t"
    "v_cvt_pk_bf16_f32 v216, v160, v161\n\t"
    "v_add_f32 v200, v200, v162\n\t"
    "v_add_f32 v201, v201, v163\n\t"
    "v_cvt_pk_bf16_f32 v217, v162, v163\n\t"
    "v_mfma_f32_16x16x32_bf16 v[172:175], a[12:15], v[108:111], v[192:195]\n\t"
    "v_exp_f32 v164, v164\n\t"
    "v_mfma_f32_16x16x32_bf16 v[172:175], a[8:11], v[104:107], v[172:175]\n\t"
    "v_exp_f32 v165, v165\n\t"
    "v_mfma_f32_16x16x32_bf16 v[172:175], a[4:7], v[100:103], v[172:175]\n\t"
    "v_exp_f32 v166, v166\n\t"
    "v_mfma_f32_16x16x32_bf16 v[172:175], a[0:3], v[96:99], v[172:175]\n\t"
    "v_exp_f32 v167, v167\n\t"
    "v_add_f32 v202, v202, v164\n\t"
    "v_add_f32 v203, v203, v165\n\t"
    "v_cvt_pk_bf16_f32 v224, v164, v165\n\t"
    "v_add_f32 v202, v202, v166\n\t"
    "v_add_f32 v203, v203, v167\n\t"
    "v_cvt_pk_bf16_f32 v225, v166, v167\n\t"
    "s_waitcnt lgkmcnt(0)\n\t"
    "v_mfma_f32_16x16x32_bf16 v[160:163], a[16:19], v[0:3], v[180:183]\n\t"
    "v_exp_f32 v168, v168\n\t"
    "v_mfma_f32_16x16x32_bf16 v[160:163], a[20:23], v[4:7], v[160:163]\n\t"
    "v_exp_f32 v169, v169\n\t"
    "v_mfma_f32_16x16x32_bf16 v[160:163], a[24:27], v[8:11], v[160:163]\n\t"
    "s_waitcnt vmcnt(0) lgkmcnt(5)\n\t"
    "v_exp_f32 v170, v170\n\t"
    "v_mfma_f32_16x16x32_bf16 v[160:163], a[28:31], v[12:15], v[160:163]\n\t"
    "s_barrier\n\t"
    "s_add_u32 s53, s98, 524288\n\t"
    "v_exp_f32 v171, v171\n\t"
    "v_add_f32 v204, v204, v168\n\t"
    "v_add_f32 v205, v205, v169\n\t"
    "v_cvt_pk_bf16_f32 v232, v168, v169\n\t"
    "v_add_f32 v204, v204, v170\n\t"
    "v_add_f32 v205, v205, v171\n\t"
    "v_cvt_pk_bf16_f32 v233, v170, v171\n\t"
    "v_mfma_f32_16x16x32_bf16 v[164:167], a[28:31], v[44:47], v[184:187]\n\t"
    "s_add_u32 m0, s97, 0\n\t"
    "v_exp_f32 v172, v172\n\t"
    "v_mfma_f32_16x16x32_bf16 v[164:167], a[24:27], v[40:43], v[164:167]\n\t"
    "buffer_load_dwordx4 v255, s[24:27], s53 offen offset:0 lds\n\t"
    "buffer_load_dwordx4 v198, s[24:27], s53 offen offset:2048 lds\n\t"
    "v_exp_f32 v173, v173\n\t"
    "v_mfma_f32_16x16x32_bf16 v[164:167], a[20:23], v[36:39], v[164:167]\n\t"
    "s_add_u32 m0, s97, 896\n\t"
    "v_exp_f32 v174, v174\n\t"
    "v_mfma_f32_16x16x32_bf16 v[164:167], a[16:19], v[32:35], v[164:167]\n\t"
    "buffer_load_dwordx4 v255, s[24:27], s53 offen offset:128 lds\n\t"
    "buffer_load_dwordx4 v198, s[24:27], s53 offen offset:2176 lds\n\t"
    "ds_read_b64_tr_b16 a[0:1], v253 offset:16384\n\t"
    "ds_read_b64_tr_b16 a[2:3], v253 offset:20480\n\t"
    "ds_read_b64_tr_b16 a[4:5], v253 offset:24576\n\t"
    "ds_read_b64_tr_b16 a[6:7], v253 offset:28672\n\t"
    "v_exp_f32 v175, v175\n\t"
    "v_add_f32 v206, v206, v172\n\t"
    "v_add_f32 v207, v207, v173\n\t"
    "v_cvt_pk_bf16_f32 v240, v172, v173\n\t"
    "v_add_f32 v206, v206, v174\n\t"
    "v_add_f32 v207, v207, v175\n\t"
    "v_cvt_pk_bf16_f32 v241, v174, v175\n\t"
    "v_mfma_f32_16x16x32_bf16 v[168:171], a[16:19], v[64:67], v[188:191]\n\t"
    "s_add_u32 s53, s98, 589824\n\t"
    "s_add_u32 m0, s97, 16384\n\t"
    "v_exp_f32 v160, v160\n\t"
    "v_mfma_f32_16x16x32_bf16 v[168:171], a[20:23], v[68:71], v[168:171]\n\t"
    "buffer_load_dwordx4 v255, s[24:27], s53 offen offset:0 lds\n\t"
    "buffer_load_dwordx4 v198, s[24:27], s53 offen offset:2048 lds\n\t"
    "v_exp_f32 v161, v161\n\t"
    "v_mfma_f32_16x16x32_bf16 v[168:171], a[24:27], v[72:75], v[168:171]\n\t"
    "s_add_u32 m0, s97, 17280\n\t"
    "v_exp_f32 v162, v162\n\t"
    "v_mfma_f32_16x16x32_bf16 v[168:171], a[28:31], v[76:79], v[168:171]\n\t"
    "buffer_load_dwordx4 v255, s[24:27], s53 offen offset:128 lds\n\t"
    "buffer_load_dwordx4 v198, s[24:27], s53 offen offset:2176 lds\n\t"
    "ds_read_b64_tr_b16 a[12:13], v253 offset:40960\n\t"
    "ds_read_b64_tr_b16 a[14:15], v253 offset:45056\n\t"
    "ds_read_b64_tr_b16 a[8:9], v253 offset:32768\n\t"
    "ds_read_b64_tr_b16 a[10:11], v253 offset:36864\n\t"
    "v_exp_f32 v163, v163\n\t"
    "v_add_f32 v200, v200, v160\n\t"
    "v_add_f32 v201, v201, v161\n\t"
    "v_cvt_pk_bf16_f32 v218, v160, v161\n\t"
    "v_add_f32 v200, v200, v162\n\t"
    "v_add_f32 v201, v201, v163\n\t"
    "v_cvt_pk_bf16_f32 v219, v162, v163\n\t"
    "v_mfma_f32_16x16x32_bf16 v[172:175], a[28:31], v[108:111], v[192:195]\n\t"
    "s_add_u32 s53, s98, 655360\n\t"
    "s_add_u32 m0, s97, 49152\n\t"
    "v_exp_f32 v164, v164\n\t"
    "v_mfma_f32_16x16x32_bf16 v[172:175], a[24:27], v[104:107], v[172:175]\n\t"
    "buffer_load_dwordx4 v255, s[24:27], s53 offen offset:0 lds\n\t"
    "buffer_load_dwordx4 v198, s[24:27], s53 offen offset:2048 lds\n\t"
    "v_exp_f32 v165, v165\n\t"
    "v_mfma_f32_16x16x32_bf16 v[172:175], a[20:23], v[100:103], v[172:175]\n\t"
    "s_add_u32 m0, s97, 50048\n\t"
    "v_exp_f32 v166, v166\n\t"
    "v_mfma_f32_16x16x32_bf16 v[172:175], a[16:19], v[96:99], v[172:175]\n\t"
    "buffer_load_dwordx4 v255, s[24:27], s53 offen offset:128 lds\n\t"
    "buffer_load_dwordx4 v198, s[24:27], s53 offen offset:2176 lds\n\t"
    "ds_read_b64_tr_b16 v[180:181], v253 offset:49152\n\t"
    "ds_read_b64_tr_b16 v[182:183], v253 offset:53248\n\t"
    "ds_read_b64_tr_b16 v[184:185], v253 offset:57344\n\t"
    "ds_read_b64_tr_b16 v[186:187], v253 offset:61440\n\t"
    "v_exp_f32 v167, v167\n\t"
    "v_add_f32 v202, v202, v164\n\t"
    "v_add_f32 v203, v203, v165\n\t"
    "v_cvt_pk_bf16_f32 v226, v164, v165\n\t"
    "v_add_f32 v202, v202, v166\n\t"
    "v_add_f32 v203, v203, v167\n\t"
    "v_cvt_pk_bf16_f32 v227, v166, v167\n\t"
    "L_rejoinB_%=:\n\t"
    "s_waitcnt lgkmcnt(6)\n\t"
    "v_mfma_f32_16x16x32_bf16 a[96:99], a[0:3], v[24:27], a[96:99]\n\t"
    "ds_read_b64_tr_b16 a[16:17], v254 offset:16384\n\t"
    "v_mfma_f32_16x16x32_bf16 a[96:99], a[4:7], v[28:31], a[96:99]\n\t"
    "s_waitcnt lgkmcnt(1)\n\t"
    "ds_read_b64_tr_b16 a[18:19], v254 offset:20480\n\t"
    "v_mbcnt_lo_u32_b32 v197, -1, 0\n\t"
    "v_mfma_f32_16x16x32_bf16 a[96:99], a[12:15], v[20:23], a[96:99]\n\t"
    "ds_read_b64_tr_b16 a[20:21], v254 offset:24576\n\t"
    "v_mbcnt_hi_u32_b32 v197, -1, v197\n\t"
    "v_mfma_f32_16x16x32_bf16 a[96:99], a[8:11], v[16:19], a[96:99]\n\t"
    "ds_read_b64_tr_b16 a[22:23], v254 offset:28672\n\t"
    "v_lshlrev_b32 v196, 4, v197\n\t"
    "v_mfma_f32_16x16x32_bf16 a[96:99], v[180:183], v[212:215], a[96:99]\n\t"
    "ds_read_b64_tr_b16 a[28:29], v254 offset:40960\n\t"
    "v_add_f32 v200, v200, v201\n\t"
    "v_mfma_f32_16x16x32_bf16 a[96:99], v[184:187], v[216:219], a[96:99]\n\t"
    "ds_read_b64_tr_b16 a[30:31], v254 offset:45056\n\t"
    "v_mov_b32 v201, v200\n\t"
    "v_mfma_f32_16x16x32_bf16 a[128:131], v[184:187], v[224:227], a[128:131]\n\t"
    "ds_read_b64_tr_b16 a[24:25], v254 offset:32768\n\t"
    "v_add_f32 v202, v202, v203\n\t"
    "v_mfma_f32_16x16x32_bf16 a[128:131], v[180:183], v[220:223], a[128:131]\n\t"
    "ds_read_b64_tr_b16 a[26:27], v254 offset:36864\n\t"
    "v_mov_b32 v203, v202\n\t"
    "v_mfma_f32_16x16x32_bf16 a[128:131], a[8:11], v[48:51], a[128:131]\n\t"
    "ds_read_b64_tr_b16 v[188:189], v254 offset:49152\n\t"
    "v_mfma_f32_16x16x32_bf16 a[128:131], a[12:15], v[52:55], a[128:131]\n\t"
    "ds_read_b64_tr_b16 v[190:191], v254 offset:53248\n\t"
    "v_mfma_f32_16x16x32_bf16 a[128:131], a[4:7], v[60:63], a[128:131]\n\t"
    "ds_read_b64_tr_b16 v[192:193], v254 offset:57344\n\t"
    "v_mfma_f32_16x16x32_bf16 a[128:131], a[0:3], v[56:59], a[128:131]\n\t"
    "ds_read_b64_tr_b16 v[194:195], v254 offset:61440\n\t"
    "v_permlane32_swap_b32 v200, v201\n\t"
    "v_add_f32 v200, v200, v201\n\t"
    "v_mov_b32 v201, v200\n\t"
    "v_permlane32_swap_b32 v202, v203\n\t"
    "v_add_f32 v202, v202, v203\n\t"
    "v_mov_b32 v203, v202\n\t"
    "s_waitcnt lgkmcnt(6)\n\t"
    "v_mfma_f32_16x16x32_bf16 a[132:135], a[16:19], v[56:59], a[132:135]\n\t"
    "ds_read_b64_tr_b16 a[0:1], v253 offset:16896\n\t"
    "v_mfma_f32_16x16x32_bf16 a[132:135], a[20:23], v[60:63], a[132:135]\n\t"
    "s_waitcnt lgkmcnt(1)\n\t"
    "ds_read_b64_tr_b16 a[2:3], v253 offset:20992\n\t"
    "v_mfma_f32_16x16x32_bf16 a[132:135], a[28:31], v[52:55], a[132:135]\n\t"
    "ds_read_b64_tr_b16 a[4:5], v253 offset:25088\n\t"
    "v_mfma_f32_16x16x32_bf16 a[132:135], a[24:27], v[48:51], a[132:135]\n\t"
    "ds_read_b64_tr_b16 a[6:7], v253 offset:29184\n\t"
    "v_mfma_f32_16x16x32_bf16 a[132:135], v[188:191], v[220:223], a[132:135]\n\t"
    "ds_read_b64_tr_b16 a[12:13], v253 offset:41472\n\t"
    "v_mfma_f32_16x16x32_bf16 a[132:135], v[192:195], v[224:227], a[132:135]\n\t"
    "ds_read_b64_tr_b16 a[14:15], v253 offset:45568\n\t"
    "v_permlane16_swap_b32 v200, v201\n\t"
    "v_add_f32 v200, v200, v201\n\t"
    "v_rcp_f32 v201, v200\n\t"
    "v_permlane16_swap_b32 v202, v203\n\t"
    "v_add_f32 v202, v202, v203\n\t"
    "v_rcp_f32 v203, v202\n\t"
    "v_mfma_f32_16x16x32_bf16 a[100:103], v[192:195], v[216:219], a[100:103]\n\t"
    "ds_read_b64_tr_b16 a[8:9], v253 offset:33280\n\t"
    "v_accvgpr_read_b32 v160, a128\n\t"
    "v_mfma_f32_16x16x32_bf16 a[100:103], v[188:191], v[212:215], a[100:103]\n\t"
    "ds_read_b64_tr_b16 a[10:11], v253 offset:37376\n\t"
    "v_accvgpr_read_b32 v161, a129\n\t"
    "v_mfma_f32_16x16x32_bf16 a[100:103], a[24:27], v[16:19], a[100:103]\n\t"
    "ds_read_b64_tr_b16 v[180:181], v253 offset:49664\n\t"
    "v_accvgpr_read_b32 v162, a130\n\t"
    "v_mfma_f32_16x16x32_bf16 a[100:103], a[28:31], v[20:23], a[100:103]\n\t"
    "ds_read_b64_tr_b16 v[182:183], v253 offset:53760\n\t"
    "v_accvgpr_read_b32 v163, a131\n\t"
    "v_mfma_f32_16x16x32_bf16 a[100:103], a[20:23], v[28:31], a[100:103]\n\t"
    "ds_read_b64_tr_b16 v[184:185], v253 offset:57856\n\t"
    "v_accvgpr_read_b32 v164, a132\n\t"
    "v_mfma_f32_16x16x32_bf16 a[100:103], a[16:19], v[24:27], a[100:103]\n\t"
    "ds_read_b64_tr_b16 v[186:187], v253 offset:61952\n\t"
    "v_accvgpr_read_b32 v165, a133\n\t"
    "v_accvgpr_read_b32 v166, a134\n\t"
    "v_accvgpr_read_b32 v167, a135\n\t"
    "v_mul_f32 v160, v203, v160\n\t"
    "v_mul_f32 v161, v203, v161\n\t"
    "v_mul_f32 v162, v203, v162\n\t"
    "v_mul_f32 v163, v203, v163\n\t"
    "v_mul_f32 v164, v203, v164\n\t"
    "v_mul_f32 v165, v203, v165\n\t"
    "v_mul_f32 v166, v203, v166\n\t"
    "v_mul_f32 v167, v203, v167\n\t"
    "v_cvt_pk_bf16_f32 v160, v160, v161\n\t"
    "v_cvt_pk_bf16_f32 v161, v162, v163\n\t"
    "v_cvt_pk_bf16_f32 v162, v164, v165\n\t"
    "v_cvt_pk_bf16_f32 v163, v166, v167\n\t"
    "buffer_store_dwordx4 v[160:163], v196, s[36:39], s85 offen offset:0 sc1\n\t"
    "s_waitcnt lgkmcnt(6)\n\t"
    "v_mfma_f32_16x16x32_bf16 a[104:107], a[0:3], v[24:27], a[104:107]\n\t"
    "ds_read_b64_tr_b16 a[16:17], v254 offset:16896\n\t"
    "v_accvgpr_read_b32 v160, a96\n\t"
    "v_mfma_f32_16x16x32_bf16 a[104:107], a[4:7], v[28:31], a[104:107]\n\t"
    "s_waitcnt lgkmcnt(1)\n\t"
    "ds_read_b64_tr_b16 a[18:19], v254 offset:20992\n\t"
    "v_accvgpr_read_b32 v161, a97\n\t"
    "v_mfma_f32_16x16x32_bf16 a[104:107], a[12:15], v[20:23], a[104:107]\n\t"
    "ds_read_b64_tr_b16 a[20:21], v254 offset:25088\n\t"
    "v_accvgpr_read_b32 v162, a98\n\t"
    "v_mfma_f32_16x16x32_bf16 a[104:107], a[8:11], v[16:19], a[104:107]\n\t"
    "ds_read_b64_tr_b16 a[22:23], v254 offset:29184\n\t"
    "v_accvgpr_read_b32 v163, a99\n\t"
    "v_mfma_f32_16x16x32_bf16 a[104:107], v[180:183], v[212:215], a[104:107]\n\t"
    "ds_read_b64_tr_b16 a[28:29], v254 offset:41472\n\t"
    "v_accvgpr_read_b32 v164, a100\n\t"
    "v_mfma_f32_16x16x32_bf16 a[104:107], v[184:187], v[216:219], a[104:107]\n\t"
    "ds_read_b64_tr_b16 a[30:31], v254 offset:45568\n\t"
    "v_accvgpr_read_b32 v165, a101\n\t"
    "v_accvgpr_read_b32 v166, a102\n\t"
    "v_accvgpr_read_b32 v167, a103\n\t"
    "v_mul_f32 v160, v201, v160\n\t"
    "v_mul_f32 v161, v201, v161\n\t"
    "v_mul_f32 v162, v201, v162\n\t"
    "v_mul_f32 v163, v201, v163\n\t"
    "v_mul_f32 v164, v201, v164\n\t"
    "v_mul_f32 v165, v201, v165\n\t"
    "v_mul_f32 v166, v201, v166\n\t"
    "v_mul_f32 v167, v201, v167\n\t"
    "v_cvt_pk_bf16_f32 v160, v160, v161\n\t"
    "v_cvt_pk_bf16_f32 v161, v162, v163\n\t"
    "v_cvt_pk_bf16_f32 v162, v164, v165\n\t"
    "v_cvt_pk_bf16_f32 v163, v166, v167\n\t"
    "buffer_store_dwordx4 v[160:163], v196, s[36:39], s84 offen offset:0 sc1\n\t"
    "v_mfma_f32_16x16x32_bf16 a[136:139], v[184:187], v[224:227], a[136:139]\n\t"
    "ds_read_b64_tr_b16 a[24:25], v254 offset:33280\n\t"
    "v_mfma_f32_16x16x32_bf16 a[136:139], v[180:183], v[220:223], a[136:139]\n\t"
    "ds_read_b64_tr_b16 a[26:27], v254 offset:37376\n\t"
    "v_mfma_f32_16x16x32_bf16 a[136:139], a[8:11], v[48:51], a[136:139]\n\t"
    "ds_read_b64_tr_b16 v[188:189], v254 offset:49664\n\t"
    "v_mfma_f32_16x16x32_bf16 a[136:139], a[12:15], v[52:55], a[136:139]\n\t"
    "ds_read_b64_tr_b16 v[190:191], v254 offset:53760\n\t"
    "v_mfma_f32_16x16x32_bf16 a[136:139], a[4:7], v[60:63], a[136:139]\n\t"
    "ds_read_b64_tr_b16 v[192:193], v254 offset:57856\n\t"
    "v_mfma_f32_16x16x32_bf16 a[136:139], a[0:3], v[56:59], a[136:139]\n\t"
    "ds_read_b64_tr_b16 v[194:195], v254 offset:61952\n\t"
    "v_exp_f32 v168, v168\n\t"
    "v_exp_f32 v169, v169\n\t"
    "v_exp_f32 v170, v170\n\t"
    "v_exp_f32 v171, v171\n\t"
    "v_add_f32 v204, v204, v168\n\t"
    "v_add_f32 v205, v205, v169\n\t"
    "v_cvt_pk_bf16_f32 v234, v168, v169\n\t"
    "v_add_f32 v204, v204, v170\n\t"
    "v_add_f32 v205, v205, v171\n\t"
    "v_cvt_pk_bf16_f32 v235, v170, v171\n\t"
    "v_exp_f32 v172, v172\n\t"
    "v_exp_f32 v173, v173\n\t"
    "v_exp_f32 v174, v174\n\t"
    "v_exp_f32 v175, v175\n\t"
    "v_add_f32 v206, v206, v172\n\t"
    "v_add_f32 v207, v207, v173\n\t"
    "v_cvt_pk_bf16_f32 v242, v172, v173\n\t"
    "v_add_f32 v206, v206, v174\n\t"
    "v_add_f32 v207, v207, v175\n\t"
    "v_cvt_pk_bf16_f32 v243, v174, v175\n\t"
    "v_add_f32 v204, v204, v205\n\t"
    "s_waitcnt lgkmcnt(6)\n\t"
    "v_mfma_f32_16x16x32_bf16 a[140:143], a[16:19], v[56:59], a[140:143]\n\t"
    "ds_read_b64_tr_b16 a[0:1], v253 offset:17408\n\t"
    "v_mov_b32 v205, v204\n\t"
    "v_mfma_f32_16x16x32_bf16 a[140:143], a[20:23], v[60:63], a[140:143]\n\t"
    "s_waitcnt lgkmcnt(1)\n\t"
    "ds_read_b64_tr_b16 a[2:3], v253 offset:21504\n\t"
    "v_add_f32 v206, v206, v207\n\t"
    "v_mfma_f32_16x16x32_bf16 a[140:143], a[28:31], v[52:55], a[140:143]\n\t"
    "ds_read_b64_tr_b16 a[4:5], v253 offset:25600\n\t"
    "v_mov_b32 v207, v206\n\t"
    "v_mfma_f32_16x16x32_bf16 a[140:143], a[24:27], v[48:51], a[140:143]\n\t"
    "ds_read_b64_tr_b16 a[6:7], v253 offset:29696\n\t"
    "v_mfma_f32_16x16x32_bf16 a[140:143], v[188:191], v[220:223], a[140:143]\n\t"
    "ds_read_b64_tr_b16 a[12:13], v253 offset:41984\n\t"
    "v_mfma_f32_16x16x32_bf16 a[140:143], v[192:195], v[224:227], a[140:143]\n\t"
    "ds_read_b64_tr_b16 a[14:15], v253 offset:46080\n\t"
    "v_permlane32_swap_b32 v204, v205\n\t"
    "v_add_f32 v204, v204, v205\n\t"
    "v_mov_b32 v205, v204\n\t"
    "v_permlane32_swap_b32 v206, v207\n\t"
    "v_add_f32 v206, v206, v207\n\t"
    "v_mov_b32 v207, v206\n\t"
    "v_permlane16_swap_b32 v204, v205\n\t"
    "v_add_f32 v204, v204, v205\n\t"
    "v_rcp_f32 v205, v204\n\t"
    "v_permlane16_swap_b32 v206, v207\n\t"
    "v_add_f32 v206, v206, v207\n\t"
    "v_rcp_f32 v207, v206\n\t"
    "v_max3_f32 v176, v200, v202, v204\n\t"
    "v_min3_f32 v177, v200, v202, v204\n\t"
    "v_max3_f32 v176, v176, v206, v206\n\t"
    "v_min3_f32 v177, v177, v206, v206\n\t"
    "v_cmp_nlt_f32 s[66:67], s5, v177\n\t"
    "v_cmp_ngt_f32 vcc, 0x5f800000, v176\n\t"
    "s_nop 0\n\t"
    "s_or_b64 s[66:67], s[66:67], vcc\n\t"
    "s_cbranch_scc1 L_fbB_%=\n\t"
    "L_safeB_%=:\n\t"
    "v_mfma_f32_16x16x32_bf16 a[108:111], v[192:195], v[216:219], a[108:111]\n\t"
    "ds_read_b64_tr_b16 a[8:9], v253 offset:33792\n\t"
    "v_accvgpr_read_b32 v160, a136\n\t"
    "v_mfma_f32_16x16x32_bf16 a[108:111], v[188:191], v[212:215], a[108:111]\n\t"
    "ds_read_b64_tr_b16 a[10:11], v253 offset:37888\n\t"
    "v_accvgpr_read_b32 v161, a137\n\t"
    "v_mfma_f32_16x16x32_bf16 a[108:111], a[24:27], v[16:19], a[108:111]\n\t"
    "ds_read_b64_tr_b16 v[180:181], v253 offset:50176\n\t"
    "v_accvgpr_read_b32 v162, a138\n\t"
    "v_mfma_f32_16x16x32_bf16 a[108:111], a[28:31], v[20:23], a[108:111]\n\t"
    "ds_read_b64_tr_b16 v[182:183], v253 offset:54272\n\t"
    "v_accvgpr_read_b32 v163, a139\n\t"
    "v_mfma_f32_16x16x32_bf16 a[108:111], a[20:23], v[28:31], a[108:111]\n\t"
    "ds_read_b64_tr_b16 v[184:185], v253 offset:58368\n\t"
    "v_accvgpr_read_b32 v164, a140\n\t"
    "v_mfma_f32_16x16x32_bf16 a[108:111], a[16:19], v[24:27], a[108:111]\n\t"
    "ds_read_b64_tr_b16 v[186:187], v253 offset:62464\n\t"
    "v_accvgpr_read_b32 v165, a141\n\t"
    "v_accvgpr_read_b32 v166, a142\n\t"
    "v_accvgpr_read_b32 v167, a143\n\t"
    "v_mul_f32 v160, v203, v160\n\t"
    "v_mul_f32 v161, v203, v161\n\t"
    "s_waitcnt lgkmcnt(6)\n\t"
    "v_mfma_f32_16x16x32_bf16 a[112:115], a[0:3], v[24:27], a[112:115]\n\t"
    "ds_read_b64_tr_b16 a[16:17], v254 offset:17408\n\t"
    "v_mul_f32 v162, v203, v162\n\t"
    "v_mfma_f32_16x16x32_bf16 a[112:115], a[4:7], v[28:31], a[112:115]\n\t"
    "s_waitcnt lgkmcnt(1)\n\t"
    "ds_read_b64_tr_b16 a[18:19], v254 offset:21504\n\t"
    "v_mul_f32 v163, v203, v163\n\t"
    "v_mfma_f32_16x16x32_bf16 a[112:115], a[12:15], v[20:23], a[112:115]\n\t"
    "ds_read_b64_tr_b16 a[20:21], v254 offset:25600\n\t"
    "v_mul_f32 v164, v203, v164\n\t"
    "v_mfma_f32_16x16x32_bf16 a[112:115], a[8:11], v[16:19], a[112:115]\n\t"
    "ds_read_b64_tr_b16 a[22:23], v254 offset:29696\n\t"
    "v_mul_f32 v165, v203, v165\n\t"
    "v_mfma_f32_16x16x32_bf16 a[112:115], v[180:183], v[212:215], a[112:115]\n\t"
    "ds_read_b64_tr_b16 a[28:29], v254 offset:41984\n\t"
    "v_mul_f32 v166, v203, v166\n\t"
    "v_mfma_f32_16x16x32_bf16 a[112:115], v[184:187], v[216:219], a[112:115]\n\t"
    "ds_read_b64_tr_b16 a[30:31], v254 offset:46080\n\t"
    "v_mul_f32 v167, v203, v167\n\t"
    "v_cvt_pk_bf16_f32 v104, v160, v161\n\t"
    "v_cvt_pk_bf16_f32 v105, v162, v163\n\t"
    "v_cvt_pk_bf16_f32 v106, v164, v165\n\t"
    "v_cvt_pk_bf16_f32 v107, v166, v167\n\t"
    "v_add_u32 v128, s92, v253\n\t"
    "v_add_u32 v129, s92, v254\n\t"
    "v_add_u32 v130, s93, v253\n\t"
    "v_add_u32 v131, s93, v254\n\t"
    "v_add_u32 v132, s94, v253\n\t"
    "v_add_u32 v133, s94, v254\n\t"
    "v_mul_u32_u24 v137, 128, v197\n\t"
    "v_add_u32 v137, s64, v137\n\t"
    "v_and_b32 v138, 3, v197\n\t"
    "s_mov_b32 exec_lo, 0\n\t"
    "v_lshl_add_u32 v137, v138, 2, s65\n\t"
    "s_mov_b32 exec_lo, -1\n\t"
    "v_subrev_u32 v138, s51, v196\n\t"
    "v_add_u32 v141, s90, v138\n\t"
    "v_add_u32 v142, s91, v138\n\t"
    "v_add_u32 v143, s99, v138\n\t"
    "v_bfe_u32 v138, v197, 4, 1\n\t"
    "v_lshrrev_b32 v139, 5, v197\n\t"
    "v_lshl_add_u32 v138, v138, 1, v139\n\t"
    "v_lshlrev_b32 v138, 4, v138\n\t"
    "v_and_b32 v136, 15, v197\n\t"
    "v_lshl_add_u32 v136, v136, 10, v138\n\t"
    "v_mfma_f32_16x16x32_bf16 a[144:147], v[184:187], v[224:227], a[144:147]\n\t"
    "ds_read_b64_tr_b16 a[24:25], v254 offset:33792\n\t"
    "v_accvgpr_read_b32 v168, a104\n\t"
    "v_mfma_f32_16x16x32_bf16 a[144:147], v[180:183], v[220:223], a[144:147]\n\t"
    "ds_read_b64_tr_b16 a[26:27], v254 offset:37888\n\t"
    "v_accvgpr_read_b32 v169, a105\n\t"
    "v_mfma_f32_16x16x32_bf16 a[144:147], a[8:11], v[48:51], a[144:147]\n\t"
    "ds_read_b64_tr_b16 v[188:189], v254 offset:50176\n\t"
    "v_accvgpr_read_b32 v170, a106\n\t"
    "v_mfma_f32_16x16x32_bf16 a[144:147], a[12:15], v[52:55], a[144:147]\n\t"
    "ds_read_b64_tr_b16 v[190:191], v254 offset:54272\n\t"
    "v_accvgpr_read_b32 v171, a107\n\t"
    "v_mfma_f32_16x16x32_bf16 a[144:147], a[4:7], v[60:63], a[144:147]\n\t"
    "ds_read_b64_tr_b16 v[192:193], v254 offset:58368\n\t"
    "v_accvgpr_read_b32 v172, a108\n\t"
    "v_mfma_f32_16x16x32_bf16 a[144:147], a[0:3], v[56:59], a[144:147]\n\t"
    "buffer_store_dwordx4 v[104:107], v196, s[36:39], s85 offen offset:1024 sc1\n\t"
    "ds_read_b64_tr_b16 v[194:195], v254 offset:62464\n\t"
    "ds_read_b64_tr_b16 v[0:1], v132 offset:0\n\t"
    "ds_read_b64_tr_b16 v[2:3], v132 offset:4096\n\t"
    "ds_read_b64_tr_b16 v[4:5], v132 offset:8192\n\t"
    "ds_read_b64_tr_b16 v[6:7], v132 offset:12288\n\t"
    "v_accvgpr_read_b32 v173, a109\n\t"
    "v_accvgpr_read_b32 v174, a110\n\t"
    "v_accvgpr_read_b32 v175, a111\n\t"
    "v_mul_f32 v168, v201, v168\n\t"
    "v_mul_f32 v169, v201, v169\n\t"
    "s_waitcnt lgkmcnt(10)\n\t"
    "v_mfma_f32_16x16x32_bf16 a[148:151], a[16:19], v[56:59], a[148:151]\n\t"
    "ds_read_b64_tr_b16 a[0:1], v253 offset:17920\n\t"
    "v_mul_f32 v170, v201, v170\n\t"
    "v_mfma_f32_16x16x32_bf16 a[148:151], a[20:23], v[60:63], a[148:151]\n\t"
    "s_waitcnt lgkmcnt(5)\n\t"
    "ds_read_b64_tr_b16 a[2:3], v253 offset:22016\n\t"
    "v_mul_f32 v171, v201, v171\n\t"
    "v_mfma_f32_16x16x32_bf16 a[148:151], a[28:31], v[52:55], a[148:151]\n\t"
    "ds_read_b64_tr_b16 a[4:5], v253 offset:26112\n\t"
    "v_mul_f32 v172, v201, v172\n\t"
    "v_mfma_f32_16x16x32_bf16 a[148:151], a[24:27], v[48:51], a[148:151]\n\t"
    "ds_read_b64_tr_b16 a[6:7], v253 offset:30208\n\t"
    "v_mul_f32 v173, v201, v173\n\t"
    "v_mfma_f32_16x16x32_bf16 a[148:151], v[188:191], v[220:223], a[148:151]\n\t"
    "ds_read_b64_tr_b16 a[12:13], v253 offset:42496\n\t"
    "v_mul_f32 v174, v201, v174\n\t"
    "v_mfma_f32_16x16x32_bf16 a[148:151], v[192:195], v[224:227], a[148:151]\n\t"
    "ds_read_b64_tr_b16 a[14:15], v253 offset:46592\n\t"
    "ds_read_b64_tr_b16 v[8:9], v133 offset:0\n\t"
    "ds_read_b64_tr_b16 v[10:11], v133 offset:4096\n\t"
    "ds_read_b64_tr_b16 v[12:13], v133 offset:8192\n\t"
    "ds_read_b64_tr_b16 v[14:15], v133 offset:12288\n\t"
    "v_mul_f32 v175, v201, v175\n\t"
    "v_cvt_pk_bf16_f32 v108, v168, v169\n\t"
    "v_cvt_pk_bf16_f32 v109, v170, v171\n\t"
    "v_cvt_pk_bf16_f32 v110, v172, v173\n\t"
    "v_cvt_pk_bf16_f32 v111, v174, v175\n\t"
    "v_mfma_f32_16x16x32_bf16 a[116:119], v[192:195], v[216:219], a[116:119]\n\t"
    "ds_read_b64_tr_b16 a[8:9], v253 offset:34304\n\t"
    "v_accvgpr_read_b32 v160, a144\n\t"
    "v_mfma_f32_16x16x32_bf16 a[116:119], v[188:191], v[212:215], a[116:119]\n\t"
    "ds_read_b64_tr_b16 a[10:11], v253 offset:38400\n\t"
    "v_accvgpr_read_b32 v161, a145\n\t"
    "v_mfma_f32_16x16x32_bf16 a[116:119], a[24:27], v[16:19], a[116:119]\n\t"
    "ds_read_b64_tr_b16 v[180:181], v253 offset:50688\n\t"
    "v_accvgpr_read_b32 v162, a146\n\t"
    "v_mfma_f32_16x16x32_bf16 a[116:119], a[28:31], v[20:23], a[116:119]\n\t"
    "ds_read_b64_tr_b16 v[182:183], v253 offset:54784\n\t"
    "v_accvgpr_read_b32 v163, a147\n\t"
    "v_mfma_f32_16x16x32_bf16 a[116:119], a[20:23], v[28:31], a[116:119]\n\t"
    "ds_read_b64_tr_b16 v[184:185], v253 offset:58880\n\t"
    "v_accvgpr_read_b32 v164, a148\n\t"
    "v_mfma_f32_16x16x32_bf16 a[116:119], a[16:19], v[24:27], a[116:119]\n\t"
    "buffer_store_dwordx4 v[108:111], v196, s[36:39], s84 offen offset:1024 sc1\n\t"
    "ds_read_b64_tr_b16 v[186:187], v253 offset:62976\n\t"
    "ds_read_b64_tr_b16 v[32:33], v132 offset:16384\n\t"
    "ds_read_b64_tr_b16 v[34:35], v132 offset:20480\n\t"
    "ds_read_b64_tr_b16 v[36:37], v132 offset:24576\n\t"
    "ds_read_b64_tr_b16 v[38:39], v132 offset:28672\n\t"
    "v_accvgpr_read_b32 v165, a149\n\t"
    "v_accvgpr_read_b32 v166, a150\n\t"
    "v_accvgpr_read_b32 v167, a151\n\t"
    "v_mul_f32 v160, v203, v160\n\t"
    "v_mul_f32 v161, v203, v161\n\t"
    "s_waitcnt lgkmcnt(14)\n\t"
    "v_mfma_f32_16x16x32_bf16 a[120:123], a[0:3], v[24:27], a[120:123]\n\t"
    "ds_read_b64_tr_b16 a[16:17], v254 offset:17920\n\t"
    "v_mul_f32 v162, v203, v162\n\t"
    "v_mfma_f32_16x16x32_bf16 a[120:123], a[4:7], v[28:31], a[120:123]\n\t"
    "s_waitcnt lgkmcnt(5)\n\t"
    "ds_read_b64_tr_b16 a[18:19], v254 offset:22016\n\t"
    "v_mul_f32 v163, v203, v163\n\t"
    "v_mfma_f32_16x16x32_bf16 a[120:123], a[12:15], v[20:23], a[120:123]\n\t"
    "ds_read_b64_tr_b16 a[20:21], v254 offset:26112\n\t"
    "v_mul_f32 v164, v203, v164\n\t"
    "v_mfma_f32_16x16x32_bf16 a[120:123], a[8:11], v[16:19], a[120:123]\n\t"
    "ds_read_b64_tr_b16 a[22:23], v254 offset:30208\n\t"
    "v_mul_f32 v165, v203, v165\n\t"
    "v_mfma_f32_16x16x32_bf16 a[120:123], v[180:183], v[212:215], a[120:123]\n\t"
    "ds_read_b64_tr_b16 a[28:29], v254 offset:42496\n\t"
    "v_mul_f32 v166, v203, v166\n\t"
    "v_mfma_f32_16x16x32_bf16 a[120:123], v[184:187], v[216:219], a[120:123]\n\t"
    "ds_read_b64_tr_b16 a[30:31], v254 offset:46592\n\t"
    "ds_read_b64_tr_b16 v[40:41], v133 offset:16384\n\t"
    "ds_read_b64_tr_b16 v[42:43], v133 offset:20480\n\t"
    "ds_read_b64_tr_b16 v[44:45], v133 offset:24576\n\t"
    "v_mul_f32 v167, v203, v167\n\t"
    "v_cvt_pk_bf16_f32 v96, v160, v161\n\t"
    "v_cvt_pk_bf16_f32 v97, v162, v163\n\t"
    "v_cvt_pk_bf16_f32 v98, v164, v165\n\t"
    "v_cvt_pk_bf16_f32 v99, v166, v167\n\t"
    "v_mfma_f32_16x16x32_bf16 a[152:155], v[184:187], v[224:227], a[152:155]\n\t"
    "ds_read_b64_tr_b16 a[24:25], v254 offset:34304\n\t"
    "v_accvgpr_read_b32 v168, a112\n\t"
    "v_mfma_f32_16x16x32_bf16 a[152:155], v[180:183], v[220:223], a[152:155]\n\t"
    "ds_read_b64_tr_b16 a[26:27], v254 offset:38400\n\t"
    "v_accvgpr_read_b32 v169, a113\n\t"
    "v_mfma_f32_16x16x32_bf16 a[152:155], a[8:11], v[48:51], a[152:155]\n\t"
    "ds_read_b64_tr_b16 v[188:189], v254 offset:50688\n\t"
    "v_accvgpr_read_b32 v170, a114\n\t"
    "v_mfma_f32_16x16x32_bf16 a[152:155], a[12:15], v[52:55], a[152:155]\n\t"
    "ds_read_b64_tr_b16 v[190:191], v254 offset:54784\n\t"
    "v_accvgpr_read_b32 v171, a115\n\t"
    "v_mfma_f32_16x16x32_bf16 a[152:155], a[4:7], v[60:63], a[152:155]\n\t"
    "ds_read_b64_tr_b16 v[192:193], v254 offset:58880\n\t"
    "v_accvgpr_read_b32 v172, a116\n\t"
    "v_mfma_f32_16x16x32_bf16 a[152:155], a[0:3], v[56:59], a[152:155]\n\t"
    "buffer_store_dwordx4 v[96:99], v196, s[36:39], s85 offen offset:2048 sc1\n\t"
    "ds_read_b64_tr_b16 v[194:195], v254 offset:62976\n\t"
    "ds_read_b64_tr_b16 v[46:47], v133 offset:28672\n\t"
    "ds_read_b64_tr_b16 v[64:65], v130 offset:0\n\t"
    "ds_read_b64_tr_b16 v[66:67], v130 offset:4096\n\t"
    "v_accvgpr_read_b32 v173, a117\n\t"
    "v_accvgpr_read_b32 v174, a118\n\t"
    "v_accvgpr_read_b32 v175, a119\n\t"
    "v_mul_f32 v168, v201, v168\n\t"
    "v_mul_f32 v169, v201, v169\n\t"
    "s_waitcnt lgkmcnt(12)\n\t"
    "v_mfma_f32_16x16x32_bf16 a[156:159], a[16:19], v[56:59], a[156:159]\n\t"
    "ds_read_b64_tr_b16 a[0:1], v253 offset:16384\n\t"
    "v_mul_f32 v170, v201, v170\n\t"
    "v_mfma_f32_16x16x32_bf16 a[156:159], a[20:23], v[60:63], a[156:159]\n\t"
    "s_waitcnt lgkmcnt(4)\n\t"
    "ds_read_b64_tr_b16 a[2:3], v253 offset:20480\n\t"
    "v_mul_f32 v171, v201, v171\n\t"
    "v_mfma_f32_16x16x32_bf16 a[156:159], a[28:31], v[52:55], a[156:159]\n\t"
    "ds_read_b64_tr_b16 a[4:5], v253 offset:24576\n\t"
    "v_mul_f32 v172, v201, v172\n\t"
    "v_mfma_f32_16x16x32_bf16 a[156:159], a[24:27], v[48:51], a[156:159]\n\t"
    "ds_read_b64_tr_b16 a[6:7], v253 offset:28672\n\t"
    "v_mul_f32 v173, v201, v173\n\t"
    "v_mfma_f32_16x16x32_bf16 a[156:159], v[188:191], v[220:223], a[156:159]\n\t"
    "ds_read_b64_tr_b16 a[12:13], v253 offset:40960\n\t"
    "v_mul_f32 v174, v201, v174\n\t"
    "v_mfma_f32_16x16x32_bf16 a[156:159], v[192:195], v[224:227], a[156:159]\n\t"
    "ds_read_b64_tr_b16 a[14:15], v253 offset:45056\n\t"
    "ds_read_b64_tr_b16 v[68:69], v130 offset:8192\n\t"
    "ds_read_b64_tr_b16 v[70:71], v130 offset:12288\n\t"
    "ds_read_b64_tr_b16 v[72:73], v131 offset:0\n\t"
    "v_mul_f32 v175, v201, v175\n\t"
    "v_cvt_pk_bf16_f32 v100, v168, v169\n\t"
    "v_cvt_pk_bf16_f32 v101, v170, v171\n\t"
    "v_cvt_pk_bf16_f32 v102, v172, v173\n\t"
    "v_cvt_pk_bf16_f32 v103, v174, v175\n\t"
    "v_mfma_f32_16x16x32_bf16 a[124:127], v[192:195], v[216:219], a[124:127]\n\t"
    "ds_read_b64_tr_b16 a[8:9], v253 offset:32768\n\t"
    "v_accvgpr_read_b32 v160, a152\n\t"
    "v_mfma_f32_16x16x32_bf16 a[124:127], v[188:191], v[212:215], a[124:127]\n\t"
    "ds_read_b64_tr_b16 a[10:11], v253 offset:36864\n\t"
    "v_accvgpr_read_b32 v161, a153\n\t"
    "v_mfma_f32_16x16x32_bf16 a[124:127], a[24:27], v[16:19], a[124:127]\n\t"
    "ds_read_b64_tr_b16 v[180:181], v253 offset:49152\n\t"
    "v_accvgpr_read_b32 v162, a154\n\t"
    "v_mfma_f32_16x16x32_bf16 a[124:127], a[28:31], v[20:23], a[124:127]\n\t"
    "ds_read_b64_tr_b16 v[182:183], v253 offset:53248\n\t"
    "v_accvgpr_read_b32 v163, a155\n\t"
    "v_mfma_f32_16x16x32_bf16 a[124:127], a[20:23], v[28:31], a[124:127]\n\t"
    "ds_read_b64_tr_b16 v[184:185], v253 offset:57344\n\t"
    "v_accvgpr_read_b32 v164, a156\n\t"
    "v_mfma_f32_16x16x32_bf16 a[124:127], a[16:19], v[24:27], a[124:127]\n\t"
    "buffer_store_dwordx4 v[100:103], v196, s[36:39], s84 offen offset:2048 sc1\n\t"
    "ds_read_b64_tr_b16 v[186:187], v253 offset:61440\n\t"
    "ds_read_b64_tr_b16 v[74:75], v131 offset:4096\n\t"
    "ds_read_b64_tr_b16 v[76:77], v131 offset:8192\n\t"
    "ds_read_b64_tr_b16 v[78:79], v131 offset:12288\n\t"
    "v_accvgpr_read_b32 v165, a157\n\t"
    "v_accvgpr_read_b32 v166, a158\n\t"
    "v_accvgpr_read_b32 v167, a159\n\t"
    "v_mul_f32 v160, v203, v160\n\t"
    "v_mul_f32 v161, v203, v161\n\t"
    "s_waitcnt lgkmcnt(12)\n\t"
    "v_mfma_f32_16x16x32_bf16 a[160:163], a[0:3], v[88:91], a[160:163]\n\t"
    "ds_read_b64_tr_b16 a[16:17], v254 offset:16384\n\t"
    "v_mul_f32 v162, v203, v162\n\t"
    "v_mfma_f32_16x16x32_bf16 a[160:163], a[4:7], v[92:95], a[160:163]\n\t"
    "s_waitcnt lgkmcnt(4)\n\t"
    "ds_read_b64_tr_b16 a[18:19], v254 offset:20480\n\t"
    "v_mul_f32 v163, v203, v163\n\t"
    "v_mfma_f32_16x16x32_bf16 a[160:163], a[12:15], v[84:87], a[160:163]\n\t"
    "ds_read_b64_tr_b16 a[20:21], v254 offset:24576\n\t"
    "v_mul_f32 v164, v203, v164\n\t"
    "v_mfma_f32_16x16x32_bf16 a[160:163], a[8:11], v[80:83], a[160:163]\n\t"
    "ds_read_b64_tr_b16 a[22:23], v254 offset:28672\n\t"
    "v_mul_f32 v165, v203, v165\n\t"
    "v_mfma_f32_16x16x32_bf16 a[160:163], v[180:183], v[228:231], a[160:163]\n\t"
    "ds_read_b64_tr_b16 a[28:29], v254 offset:40960\n\t"
    "v_mul_f32 v166, v203, v166\n\t"
    "v_mfma_f32_16x16x32_bf16 a[160:163], v[184:187], v[232:235], a[160:163]\n\t"
    "ds_read_b64_tr_b16 a[30:31], v254 offset:45056\n\t"
    "v_mul_f32 v167, v203, v167\n\t"
    "v_cvt_pk_bf16_f32 v104, v160, v161\n\t"
    "v_cvt_pk_bf16_f32 v105, v162, v163\n\t"
    "v_cvt_pk_bf16_f32 v106, v164, v165\n\t"
    "v_cvt_pk_bf16_f32 v107, v166, v167\n\t"
    "v_mfma_f32_16x16x32_bf16 a[192:195], v[184:187], v[240:243], a[192:195]\n\t"
    "ds_read_b64_tr_b16 a[24:25], v254 offset:32768\n\t"
    "v_accvgpr_read_b32 v168, a120\n\t"
    "v_mfma_f32_16x16x32_bf16 a[192:195], v[180:183], v[236:239], a[192:195]\n\t"
    "ds_read_b64_tr_b16 a[26:27], v254 offset:36864\n\t"
    "v_accvgpr_read_b32 v169, a121\n\t"
    "v_mfma_f32_16x16x32_bf16 a[192:195], a[8:11], v[112:115], a[192:195]\n\t"
    "ds_read_b64_tr_b16 v[188:189], v254 offset:49152\n\t"
    "v_accvgpr_read_b32 v170, a122\n\t"
    "v_mfma_f32_16x16x32_bf16 a[192:195], a[12:15], v[116:119], a[192:195]\n\t"
    "ds_read_b64_tr_b16 v[190:191], v254 offset:53248\n\t"
    "v_accvgpr_read_b32 v171, a123\n\t"
    "v_mfma_f32_16x16x32_bf16 a[192:195], a[4:7], v[124:127], a[192:195]\n\t"
    "ds_read_b64_tr_b16 v[192:193], v254 offset:57344\n\t"
    "v_accvgpr_read_b32 v172, a124\n\t"
    "v_mfma_f32_16x16x32_bf16 a[192:195], a[0:3], v[120:123], a[192:195]\n\t"
    "buffer_store_dwordx4 v[104:107], v196, s[36:39], s85 offen offset:3072 sc1\n\t"
    "ds_read_b64_tr_b16 v[194:195], v254 offset:61440\n\t"
    "v_accvgpr_read_b32 v173, a125\n\t"
    "v_accvgpr_read_b32 v174, a126\n\t"
    "v_accvgpr_read_b32 v175, a127\n\t"
    "v_mul_f32 v168, v201, v168\n\t"
    "v_mul_f32 v169, v201, v169\n\t"
    "s_waitcnt lgkmcnt(6)\n\t"
    "v_mfma_f32_16x16x32_bf16 a[196:199], a[16:19], v[120:123], a[196:199]\n\t"
    "ds_read_b64_tr_b16 a[0:1], v253 offset:16896\n\t"
    "v_mul_f32 v170, v201, v170\n\t"
    "v_mfma_f32_16x16x32_bf16 a[196:199], a[20:23], v[124:127], a[196:199]\n\t"
    "s_waitcnt lgkmcnt(1)\n\t"
    "ds_read_b64_tr_b16 a[2:3], v253 offset:20992\n\t"
    "v_mul_f32 v171, v201, v171\n\t"
    "v_mfma_f32_16x16x32_bf16 a[196:199], a[28:31], v[116:119], a[196:199]\n\t"
    "ds_read_b64_tr_b16 a[4:5], v253 offset:25088\n\t"
    "v_mul_f32 v172, v201, v172\n\t"
    "v_mfma_f32_16x16x32_bf16 a[196:199], a[24:27], v[112:115], a[196:199]\n\t"
    "ds_read_b64_tr_b16 a[6:7], v253 offset:29184\n\t"
    "v_mul_f32 v173, v201, v173\n\t"
    "v_mfma_f32_16x16x32_bf16 a[196:199], v[188:191], v[236:239], a[196:199]\n\t"
    "ds_read_b64_tr_b16 a[12:13], v253 offset:41472\n\t"
    "v_mul_f32 v174, v201, v174\n\t"
    "v_mfma_f32_16x16x32_bf16 a[196:199], v[192:195], v[240:243], a[196:199]\n\t"
    "ds_read_b64_tr_b16 a[14:15], v253 offset:45568\n\t"
    "v_mul_f32 v175, v201, v175\n\t"
    "v_cvt_pk_bf16_f32 v108, v168, v169\n\t"
    "v_cvt_pk_bf16_f32 v109, v170, v171\n\t"
    "v_cvt_pk_bf16_f32 v110, v172, v173\n\t"
    "v_cvt_pk_bf16_f32 v111, v174, v175\n\t"
    "v_mfma_f32_16x16x32_bf16 a[164:167], v[192:195], v[232:235], a[164:167]\n\t"
    "ds_read_b64_tr_b16 a[8:9], v253 offset:33280\n\t"
    "v_accvgpr_read_b32 v160, a192\n\t"
    "v_mfma_f32_16x16x32_bf16 a[164:167], v[188:191], v[228:231], a[164:167]\n\t"
    "ds_read_b64_tr_b16 a[10:11], v253 offset:37376\n\t"
    "v_accvgpr_read_b32 v161, a193\n\t"
    "v_mfma_f32_16x16x32_bf16 a[164:167], a[24:27], v[80:83], a[164:167]\n\t"
    "ds_read_b64_tr_b16 v[180:181], v253 offset:49664\n\t"
    "v_accvgpr_read_b32 v162, a194\n\t"
    "v_mfma_f32_16x16x32_bf16 a[164:167], a[28:31], v[84:87], a[164:167]\n\t"
    "ds_read_b64_tr_b16 v[182:183], v253 offset:53760\n\t"
    "v_accvgpr_read_b32 v163, a195\n\t"
    "v_mfma_f32_16x16x32_bf16 a[164:167], a[20:23], v[92:95], a[164:167]\n\t"
    "ds_read_b64_tr_b16 v[184:185], v253 offset:57856\n\t"
    "v_accvgpr_read_b32 v164, a196\n\t"
    "v_mfma_f32_16x16x32_bf16 a[164:167], a[16:19], v[88:91], a[164:167]\n\t"
    "buffer_store_dwordx4 v[108:111], v196, s[36:39], s84 offen offset:3072 sc1\n\t"
    "ds_read_b64_tr_b16 v[186:187], v253 offset:61952\n\t"
    "ds_read_b64_tr_b16 v[96:97], v128 offset:32768\n\t"
    "ds_read_b64_tr_b16 v[98:99], v128 offset:36864\n\t"
    "ds_read_b64_tr_b16 v[100:101], v128 offset:40960\n\t"
    "ds_read_b64_tr_b16 v[102:103], v128 offset:45056\n\t"
    "ds_read_b64_tr_b16 v[104:105], v129 offset:32768\n\t"
    "ds_read_b64_tr_b16 v[106:107], v129 offset:36864\n\t"
    "ds_read_b64_tr_b16 v[108:109], v129 offset:40960\n\t"
    "ds_read_b64_tr_b16 v[110:111], v129 offset:45056\n\t"
    "v_accvgpr_read_b32 v165, a197\n\t"
    "v_accvgpr_read_b32 v166, a198\n\t"
    "v_accvgpr_read_b32 v167, a199\n\t"
    "v_mul_f32 v160, v207, v160\n\t"
    "v_mul_f32 v161, v207, v161\n\t"
    "s_waitcnt lgkmcnt(14)\n\t"
    "v_mfma_f32_16x16x32_bf16 a[168:171], a[0:3], v[88:91], a[168:171]\n\t"
    "ds_read_b64_tr_b16 a[16:17], v254 offset:16896\n\t"
    "v_mul_f32 v162, v207, v162\n\t"
    "v_mfma_f32_16x16x32_bf16 a[168:171], a[4:7], v[92:95], a[168:171]\n\t"
    "s_waitcnt lgkmcnt(9)\n\t"
    "ds_read_b64_tr_b16 a[18:19], v254 offset:20992\n\t"
    "v_mul_f32 v163, v207, v163\n\t"
    "v_mfma_f32_16x16x32_bf16 a[168:171], a[12:15], v[84:87], a[168:171]\n\t"
    "ds_read_b64_tr_b16 a[20:21], v254 offset:25088\n\t"
    "v_mul_f32 v164, v207, v164\n\t"
    "v_mfma_f32_16x16x32_bf16 a[168:171], a[8:11], v[80:83], a[168:171]\n\t"
    "ds_read_b64_tr_b16 a[22:23], v254 offset:29184\n\t"
    "v_mul_f32 v165, v207, v165\n\t"
    "v_mfma_f32_16x16x32_bf16 a[168:171], v[180:183], v[228:231], a[168:171]\n\t"
    "ds_read_b64_tr_b16 a[28:29], v254 offset:41472\n\t"
    "v_mul_f32 v166, v207, v166\n\t"
    "v_mfma_f32_16x16x32_bf16 a[168:171], v[184:187], v[232:235], a[168:171]\n\t"
    "s_waitcnt vmcnt(8) lgkmcnt(5)\n\t"
    "s_barrier\n\t"
    "ds_read_b64_tr_b16 a[30:31], v254 offset:45568\n\t"
    "v_mul_f32 v167, v207, v167\n\t"
    "v_cvt_pk_bf16_f32 v16, v160, v161\n\t"
    "v_cvt_pk_bf16_f32 v17, v162, v163\n\t"
    "v_cvt_pk_bf16_f32 v18, v164, v165\n\t"
    "v_cvt_pk_bf16_f32 v19, v166, v167\n\t"
    "v_mfma_f32_16x16x32_bf16 a[200:203], v[184:187], v[240:243], a[200:203]\n\t"
    "ds_read_b64_tr_b16 a[24:25], v254 offset:33280\n\t"
    "v_accvgpr_read_b32 v168, a160\n\t"
    "v_mfma_f32_16x16x32_bf16 a[200:203], v[180:183], v[236:239], a[200:203]\n\t"
    "ds_read_b64_tr_b16 a[26:27], v254 offset:37376\n\t"
    "v_accvgpr_read_b32 v169, a161\n\t"
    "v_mfma_f32_16x16x32_bf16 a[200:203], a[8:11], v[112:115], a[200:203]\n\t"
    "ds_read_b64_tr_b16 v[188:189], v254 offset:49664\n\t"
    "v_accvgpr_read_b32 v170, a162\n\t"
    "v_mfma_f32_16x16x32_bf16 a[200:203], a[12:15], v[116:119], a[200:203]\n\t"
    "s_add_u32 s53, s98, 720896\n\t"
    "ds_read_b64_tr_b16 v[190:191], v254 offset:53760\n\t"
    "v_accvgpr_read_b32 v171, a163\n\t"
    "v_mfma_f32_16x16x32_bf16 a[200:203], a[4:7], v[124:127], a[200:203]\n\t"
    "s_add_u32 m0, s97, 131072\n\t"
    "ds_read_b64_tr_b16 v[192:193], v254 offset:57856\n\t"
    "v_accvgpr_read_b32 v172, a164\n\t"
    "v_mfma_f32_16x16x32_bf16 a[200:203], a[0:3], v[120:123], a[200:203]\n\t"
    "buffer_store_dwordx4 v[16:19], v196, s[36:39], s87 offen offset:0 sc1\n\t"
    "buffer_load_dwordx4 v255, s[24:27], s53 offen offset:0 lds\n\t"
    "buffer_load_dwordx4 v198, s[24:27], s53 offen offset:2048 lds\n\t"
    "ds_read_b64_tr_b16 v[194:195], v254 offset:61952\n\t"
    "ds_read_b64_tr_b16 a[96:97], v128 offset:0\n\t"
    "ds_read_b64_tr_b16 a[98:99], v128 offset:4096\n\t"
    "ds_read_b64_tr_b16 a[100:101], v128 offset:8192\n\t"
    "v_accvgpr_read_b32 v173, a165\n\t"
    "v_accvgpr_read_b32 v174, a166\n\t"
    "v_accvgpr_read_b32 v175, a167\n\t"
    "v_mul_f32 v168, v205, v168\n\t"
    "v_mul_f32 v169, v205, v169\n\t"
    "s_waitcnt lgkmcnt(9)\n\t"
    "s_waitcnt vmcnt(3)\n\t"
    "s_lshl_b32 s60, s96, 2\n\t"
    "s_add_u32 s60, s60, s40\n\t"
    "s_lshl_b32 exec_lo, 1, s60\n\t"
    "s_mov_b32 exec_hi, 0\n\t"
    "v_add_u32 v139, 1, v211\n\t"
    "buffer_store_dword v139, v137, s[12:15], 0 offen sc1\n\t"
    "s_mov_b64 exec, -1\n\t"
    "v_mfma_f32_16x16x32_bf16 a[204:207], a[16:19], v[120:123], a[204:207]\n\t"
    "ds_read_b64_tr_b16 a[0:1], v253 offset:17408\n\t"
    "v_mul_f32 v170, v205, v170\n\t"
    "v_mfma_f32_16x16x32_bf16 a[204:207], a[20:23], v[124:127], a[204:207]\n\t"
    "s_waitcnt lgkmcnt(4)\n\t"
    "ds_read_b64_tr_b16 a[2:3], v253 offset:21504\n\t"
    "v_mul_f32 v171, v205, v171\n\t"
    "v_mfma_f32_16x16x32_bf16 a[204:207], a[28:31], v[116:119], a[204:207]\n\t"
    "ds_read_b64_tr_b16 a[4:5], v253 offset:25600\n\t"
    "v_mul_f32 v172, v205, v172\n\t"
    "v_mfma_f32_16x16x32_bf16 a[204:207], a[24:27], v[112:115], a[204:207]\n\t"
    "ds_read_b64_tr_b16 a[6:7], v253 offset:29696\n\t"
    "v_mul_f32 v173, v205, v173\n\t"
    "v_mfma_f32_16x16x32_bf16 a[204:207], v[188:191], v[236:239], a[204:207]\n\t"
    "s_add_u32 m0, s97, 131968\n\t"
    "ds_read_b64_tr_b16 a[12:13], v253 offset:41984\n\t"
    "v_mul_f32 v174, v205, v174\n\t"
    "v_mfma_f32_16x16x32_bf16 a[204:207], v[192:195], v[240:243], a[204:207]\n\t"
    "buffer_load_dwordx4 v255, s[24:27], s53 offen offset:128 lds\n\t"
    "buffer_load_dwordx4 v198, s[24:27], s53 offen offset:2176 lds\n\t"
    "ds_read_b64_tr_b16 a[14:15], v253 offset:46080\n\t"
    "ds_read_b64_tr_b16 a[102:103], v128 offset:12288\n\t"
    "ds_read_b64_tr_b16 a[104:105], v129 offset:0\n\t"
    "ds_read_b64_tr_b16 a[106:107], v129 offset:4096\n\t"
    "v_mul_f32 v175, v205, v175\n\t"
    "v_cvt_pk_bf16_f32 v20, v168, v169\n\t"
    "v_cvt_pk_bf16_f32 v21, v170, v171\n\t"
    "v_cvt_pk_bf16_f32 v22, v172, v173\n\t"
    "v_cvt_pk_bf16_f32 v23, v174, v175\n\t"
    "v_mfma_f32_16x16x32_bf16 a[172:175], v[192:195], v[232:235], a[172:175]\n\t"
    "ds_read_b64_tr_b16 a[8:9], v253 offset:33792\n\t"
    "v_accvgpr_read_b32 v160, a200\n\t"
    "v_mfma_f32_16x16x32_bf16 a[172:175], v[188:191], v[228:231], a[172:175]\n\t"
    "ds_read_b64_tr_b16 a[10:11], v253 offset:37888\n\t"
    "v_accvgpr_read_b32 v161, a201\n\t"
    "v_mfma_f32_16x16x32_bf16 a[172:175], a[24:27], v[80:83], a[172:175]\n\t"
    "ds_read_b64_tr_b16 v[180:181], v253 offset:50176\n\t"
    "v_accvgpr_read_b32 v162, a202\n\t"
    "v_mfma_f32_16x16x32_bf16 a[172:175], a[28:31], v[84:87], a[172:175]\n\t"
    "s_add_u32 s53, s98, 786432\n\t"
    "ds_read_b64_tr_b16 v[182:183], v253 offset:54272\n\t"
    "v_accvgpr_read_b32 v163, a203\n\t"
    "v_mfma_f32_16x16x32_bf16 a[172:175], a[20:23], v[92:95], a[172:175]\n\t"
    "s_add_u32 m0, s97, 147456\n\t"
    "ds_read_b64_tr_b16 v[184:185], v253 offset:58368\n\t"
    "v_accvgpr_read_b32 v164, a204\n\t"
    "v_mfma_f32_16x16x32_bf16 a[172:175], a[16:19], v[88:91], a[172:175]\n\t"
    "buffer_store_dwordx4 v[20:23], v196, s[36:39], s86 offen offset:0 sc1\n\t"
    "buffer_load_dwordx4 v255, s[24:27], s53 offen offset:0 lds\n\t"
    "buffer_load_dwordx4 v198, s[24:27], s53 offen offset:2048 lds\n\t"
    "ds_read_b64_tr_b16 v[186:187], v253 offset:62464\n\t"
    "ds_read_b64_tr_b16 a[108:109], v129 offset:8192\n\t"
    "ds_read_b64_tr_b16 a[110:111], v129 offset:12288\n\t"
    "ds_read_b64_tr_b16 a[112:113], v128 offset:16384\n\t"
    "v_accvgpr_read_b32 v165, a205\n\t"
    "v_accvgpr_read_b32 v166, a206\n\t"
    "v_accvgpr_read_b32 v167, a207\n\t"
    "v_mul_f32 v160, v207, v160\n\t"
    "v_mul_f32 v161, v207, v161\n\t"
    "s_waitcnt lgkmcnt(12)\n\t"
    "v_mfma_f32_16x16x32_bf16 a[176:179], a[0:3], v[88:91], a[176:179]\n\t"
    "ds_read_b64_tr_b16 a[16:17], v254 offset:17408\n\t"
    "v_mul_f32 v162, v207, v162\n\t"
    "v_mfma_f32_16x16x32_bf16 a[176:179], a[4:7], v[92:95], a[176:179]\n\t"
    "s_waitcnt lgkmcnt(4)\n\t"
    "ds_read_b64_tr_b16 a[18:19], v254 offset:21504\n\t"
    "v_mul_f32 v163, v207, v163\n\t"
    "v_mfma_f32_16x16x32_bf16 a[176:179], a[12:15], v[84:87], a[176:179]\n\t"
    "ds_read_b64_tr_b16 a[20:21], v254 offset:25600\n\t"
    "v_mul_f32 v164, v207, v164\n\t"
    "v_mfma_f32_16x16x32_bf16 a[176:179], a[8:11], v[80:83], a[176:179]\n\t"
    "ds_read_b64_tr_b16 a[22:23], v254 offset:29696\n\t"
    "v_mul_f32 v165, v207, v165\n\t"
    "v_mfma_f32_16x16x32_bf16 a[176:179], v[180:183], v[228:231], a[176:179]\n\t"
    "s_add_u32 m0, s97, 148352\n\t"
    "ds_read_b64_tr_b16 a[28:29], v254 offset:41984\n\t"
    "v_mul_f32 v166, v207, v166\n\t"
    "v_mfma_f32_16x16x32_bf16 a[176:179], v[184:187], v[232:235], a[176:179]\n\t"
    "buffer_load_dwordx4 v255, s[24:27], s53 offen offset:128 lds\n\t"
    "buffer_load_dwordx4 v198, s[24:27], s53 offen offset:2176 lds\n\t"
    "ds_read_b64_tr_b16 a[30:31], v254 offset:46080\n\t"
    "ds_read_b64_tr_b16 a[114:115], v128 offset:20480\n\t"
    "ds_read_b64_tr_b16 a[116:117], v128 offset:24576\n\t"
    "ds_read_b64_tr_b16 a[118:119], v128 offset:28672\n\t"
    "v_mul_f32 v167, v207, v167\n\t"
    "v_cvt_pk_bf16_f32 v24, v160, v161\n\t"
    "v_cvt_pk_bf16_f32 v25, v162, v163\n\t"
    "v_cvt_pk_bf16_f32 v26, v164, v165\n\t"
    "v_cvt_pk_bf16_f32 v27, v166, v167\n\t"
    "v_mfma_f32_16x16x32_bf16 a[208:211], v[184:187], v[240:243], a[208:211]\n\t"
    "ds_read_b64_tr_b16 a[24:25], v254 offset:33792\n\t"
    "v_accvgpr_read_b32 v168, a168\n\t"
    "v_mfma_f32_16x16x32_bf16 a[208:211], v[180:183], v[236:239], a[208:211]\n\t"
    "ds_read_b64_tr_b16 a[26:27], v254 offset:37888\n\t"
    "v_accvgpr_read_b32 v169, a169\n\t"
    "v_mfma_f32_16x16x32_bf16 a[208:211], a[8:11], v[112:115], a[208:211]\n\t"
    "ds_read_b64_tr_b16 v[188:189], v254 offset:50176\n\t"
    "v_accvgpr_read_b32 v170, a170\n\t"
    "v_mfma_f32_16x16x32_bf16 a[208:211], a[12:15], v[116:119], a[208:211]\n\t"
    "s_add_u32 s53, s98, 851968\n\t"
    "ds_read_b64_tr_b16 v[190:191], v254 offset:54272\n\t"
    "v_accvgpr_read_b32 v171, a171\n\t"
    "v_mfma_f32_16x16x32_bf16 a[208:211], a[4:7], v[124:127], a[208:211]\n\t"
    "s_add_u32 m0, s97, 65536\n\t"
    "ds_read_b64_tr_b16 v[192:193], v254 offset:58368\n\t"
    "v_accvgpr_read_b32 v172, a172\n\t"
    "v_mfma_f32_16x16x32_bf16 a[208:211], a[0:3], v[120:123], a[208:211]\n\t"
    "buffer_store_dwordx4 v[24:27], v196, s[36:39], s87 offen offset:1024 sc1\n\t"
    "buffer_load_dwordx4 v255, s[24:27], s53 offen offset:0 lds\n\t"
    "buffer_load_dwordx4 v198, s[24:27], s53 offen offset:2048 lds\n\t"
    "ds_read_b64_tr_b16 v[194:195], v254 offset:62464\n\t"
    "ds_read_b64_tr_b16 a[120:121], v129 offset:16384\n\t"
    "ds_read_b64_tr_b16 a[122:123], v129 offset:20480\n\t"
    "ds_read_b64_tr_b16 a[124:125], v129 offset:24576\n\t"
    "v_accvgpr_read_b32 v173, a173\n\t"
    "v_accvgpr_read_b32 v174, a174\n\t"
    "v_accvgpr_read_b32 v175, a175\n\t"
    "v_mul_f32 v168, v205, v168\n\t"
    "v_mul_f32 v169, v205, v169\n\t"
    "s_waitcnt lgkmcnt(12)\n\t"
    "v_mfma_f32_16x16x32_bf16 a[212:215], a[16:19], v[120:123], a[212:215]\n\t"
    "ds_read_b64_tr_b16 a[0:1], v253 offset:17920\n\t"
    "v_mul_f32 v170, v205, v170\n\t"
    "v_mfma_f32_16x16x32_bf16 a[212:215], a[20:23], v[124:127], a[212:215]\n\t"
    "s_waitcnt lgkmcnt(4)\n\t"
    "ds_read_b64_tr_b16 a[2:3], v253 offset:22016\n\t"
    "v_mul_f32 v171, v205, v171\n\t"
    "v_mfma_f32_16x16x32_bf16 a[212:215], a[28:31], v[116:119], a[212:215]\n\t"
    "ds_read_b64_tr_b16 a[4:5], v253 offset:26112\n\t"
    "v_mul_f32 v172, v205, v172\n\t"
    "v_mfma_f32_16x16x32_bf16 a[212:215], a[24:27], v[112:115], a[212:215]\n\t"
    "ds_read_b64_tr_b16 a[6:7], v253 offset:30208\n\t"
    "v_mul_f32 v173, v205, v173\n\t"
    "v_mfma_f32_16x16x32_bf16 a[212:215], v[188:191], v[236:239], a[212:215]\n\t"
    "s_add_u32 m0, s97, 66432\n\t"
    "ds_read_b64_tr_b16 a[12:13], v253 offset:42496\n\t"
    "v_mul_f32 v174, v205, v174\n\t"
    "v_mfma_f32_16x16x32_bf16 a[212:215], v[192:195], v[240:243], a[212:215]\n\t"
    "buffer_load_dwordx4 v255, s[24:27], s53 offen offset:128 lds\n\t"
    "buffer_load_dwordx4 v198, s[24:27], s53 offen offset:2176 lds\n\t"
    "ds_read_b64_tr_b16 a[14:15], v253 offset:46592\n\t"
    "ds_read_b64_tr_b16 a[126:127], v129 offset:28672\n\t"
    "ds_read_b64_tr_b16 a[128:129], v128 offset:49152\n\t"
    "ds_read_b64_tr_b16 a[130:131], v128 offset:53248\n\t"
    "v_mul_f32 v175, v205, v175\n\t"
    "v_cvt_pk_bf16_f32 v28, v168, v169\n\t"
    "v_cvt_pk_bf16_f32 v29, v170, v171\n\t"
    "v_cvt_pk_bf16_f32 v30, v172, v173\n\t"
    "v_cvt_pk_bf16_f32 v31, v174, v175\n\t"
    "v_mfma_f32_16x16x32_bf16 a[180:183], v[192:195], v[232:235], a[180:183]\n\t"
    "ds_read_b64_tr_b16 a[8:9], v253 offset:34304\n\t"
    "v_accvgpr_read_b32 v160, a208\n\t"
    "v_mfma_f32_16x16x32_bf16 a[180:183], v[188:191], v[228:231], a[180:183]\n\t"
    "ds_read_b64_tr_b16 a[10:11], v253 offset:38400\n\t"
    "v_accvgpr_read_b32 v161, a209\n\t"
    "v_mfma_f32_16x16x32_bf16 a[180:183], a[24:27], v[80:83], a[180:183]\n\t"
    "ds_read_b64_tr_b16 v[180:181], v253 offset:50688\n\t"
    "v_accvgpr_read_b32 v162, a210\n\t"
    "v_mfma_f32_16x16x32_bf16 a[180:183], a[28:31], v[84:87], a[180:183]\n\t"
    "s_add_u32 s53, s98, 917504\n\t"
    "ds_read_b64_tr_b16 v[182:183], v253 offset:54784\n\t"
    "v_accvgpr_read_b32 v163, a211\n\t"
    "v_mfma_f32_16x16x32_bf16 a[180:183], a[20:23], v[92:95], a[180:183]\n\t"
    "s_add_u32 m0, s97, 32768\n\t"
    "ds_read_b64_tr_b16 v[184:185], v253 offset:58880\n\t"
    "v_accvgpr_read_b32 v164, a212\n\t"
    "v_mfma_f32_16x16x32_bf16 a[180:183], a[16:19], v[88:91], a[180:183]\n\t"
    "buffer_store_dwordx4 v[28:31], v196, s[36:39], s86 offen offset:1024 sc1\n\t"
    "buffer_load_dwordx4 v255, s[24:27], s53 offen offset:0 lds\n\t"
    "buffer_load_dwordx4 v198, s[24:27], s53 offen offset:2048 lds\n\t"
    "ds_read_b64_tr_b16 v[186:187], v253 offset:62976\n\t"
    "ds_read_b64_tr_b16 a[132:133], v128 offset:57344\n\t"
    "ds_read_b64_tr_b16 a[134:135], v128 offset:61440\n\t"
    "v_accvgpr_read_b32 v165, a213\n\t"
    "v_accvgpr_read_b32 v166, a214\n\t"
    "v_accvgpr_read_b32 v167, a215\n\t"
    "v_mul_f32 v160, v207, v160\n\t"
    "v_mul_f32 v161, v207, v161\n\t"
    "s_waitcnt lgkmcnt(11)\n\t"
    "v_mfma_f32_16x16x32_bf16 a[184:187], a[0:3], v[88:91], a[184:187]\n\t"
    "ds_read_b64_tr_b16 a[16:17], v254 offset:17920\n\t"
    "v_mul_f32 v162, v207, v162\n\t"
    "v_mfma_f32_16x16x32_bf16 a[184:187], a[4:7], v[92:95], a[184:187]\n\t"
    "s_waitcnt lgkmcnt(3)\n\t"
    "ds_read_b64_tr_b16 a[18:19], v254 offset:22016\n\t"
    "v_mul_f32 v163, v207, v163\n\t"
    "v_mfma_f32_16x16x32_bf16 a[184:187], a[12:15], v[84:87], a[184:187]\n\t"
    "ds_read_b64_tr_b16 a[20:21], v254 offset:26112\n\t"
    "v_mul_f32 v164, v207, v164\n\t"
    "v_mfma_f32_16x16x32_bf16 a[184:187], a[8:11], v[80:83], a[184:187]\n\t"
    "ds_read_b64_tr_b16 a[22:23], v254 offset:30208\n\t"
    "v_mul_f32 v165, v207, v165\n\t"
    "v_mfma_f32_16x16x32_bf16 a[184:187], v[180:183], v[228:231], a[184:187]\n\t"
    "s_add_u32 m0, s97, 33664\n\t"
    "ds_read_b64_tr_b16 a[28:29], v254 offset:42496\n\t"
    "v_mul_f32 v166, v207, v166\n\t"
    "v_mfma_f32_16x16x32_bf16 a[184:187], v[184:187], v[232:235], a[184:187]\n\t"
    "buffer_load_dwordx4 v255, s[24:27], s53 offen offset:128 lds\n\t"
    "buffer_load_dwordx4 v198, s[24:27], s53 offen offset:2176 lds\n\t"
    "ds_read_b64_tr_b16 a[30:31], v254 offset:46592\n\t"
    "ds_read_b64_tr_b16 a[136:137], v129 offset:49152\n\t"
    "ds_read_b64_tr_b16 a[138:139], v129 offset:53248\n\t"
    "v_mul_f32 v167, v207, v167\n\t"
    "v_cvt_pk_bf16_f32 v16, v160, v161\n\t"
    "v_cvt_pk_bf16_f32 v17, v162, v163\n\t"
    "v_cvt_pk_bf16_f32 v18, v164, v165\n\t"
    "v_cvt_pk_bf16_f32 v19, v166, v167\n\t"
    "v_mfma_f32_16x16x32_bf16 a[216:219], v[184:187], v[240:243], a[216:219]\n\t"
    "ds_read_b64_tr_b16 a[24:25], v254 offset:34304\n\t"
    "v_accvgpr_read_b32 v168, a176\n\t"
    "v_mfma_f32_16x16x32_bf16 a[216:219], v[180:183], v[236:239], a[216:219]\n\t"
    "ds_read_b64_tr_b16 a[26:27], v254 offset:38400\n\t"
    "v_accvgpr_read_b32 v169, a177\n\t"
    "v_mfma_f32_16x16x32_bf16 a[216:219], a[8:11], v[112:115], a[216:219]\n\t"
    "ds_read_b64_tr_b16 v[188:189], v254 offset:50688\n\t"
    "v_accvgpr_read_b32 v170, a178\n\t"
    "v_mfma_f32_16x16x32_bf16 a[216:219], a[12:15], v[116:119], a[216:219]\n\t"
    "ds_read_b64_tr_b16 v[190:191], v254 offset:54784\n\t"
    "v_accvgpr_read_b32 v171, a179\n\t"
    "v_mfma_f32_16x16x32_bf16 a[216:219], a[4:7], v[124:127], a[216:219]\n\t"
    "ds_read_b64_tr_b16 v[192:193], v254 offset:58880\n\t"
    "v_accvgpr_read_b32 v172, a180\n\t"
    "v_mfma_f32_16x16x32_bf16 a[216:219], a[0:3], v[120:123], a[216:219]\n\t"
    "buffer_store_dwordx4 v[16:19], v196, s[36:39], s87 offen offset:2048 sc1\n\t"
    "ds_read_b64_tr_b16 v[194:195], v254 offset:62976\n\t"
    "ds_read_b64_tr_b16 a[140:141], v129 offset:57344\n\t"
    "ds_read_b64_tr_b16 a[142:143], v129 offset:61440\n\t"
    "v_accvgpr_read_b32 v173, a181\n\t"
    "v_accvgpr_read_b32 v174, a182\n\t"
    "v_accvgpr_read_b32 v175, a183\n\t"
    "v_mul_f32 v168, v205, v168\n\t"
    "v_mul_f32 v169, v205, v169\n\t"
    "s_waitcnt lgkmcnt(10)\n\t"
    "s_mov_b32 exec_hi, 0\n\t"
    "buffer_load_dword v138, v137, s[12:15], 0 offen sc1\n\t"
    "s_mov_b64 exec, -1\n\t"
    "v_mfma_f32_16x16x32_bf16 a[220:223], a[16:19], v[120:123], a[220:223]\n\t"
    "s_nop 0\n\t"
    "v_mul_f32 v170, v205, v170\n\t"
    "v_mul_f32 v171, v205, v171\n\t"
    "v_mfma_f32_16x16x32_bf16 a[220:223], a[20:23], v[124:127], a[220:223]\n\t"
    "s_waitcnt lgkmcnt(2)\n\t"
    "v_mul_f32 v172, v205, v172\n\t"
    "v_mul_f32 v173, v205, v173\n\t"
    "v_mfma_f32_16x16x32_bf16 a[220:223], a[28:31], v[116:119], a[220:223]\n\t"
    "v_mul_f32 v174, v205, v174\n\t"
    "v_mul_f32 v175, v205, v175\n\t"
    "v_mfma_f32_16x16x32_bf16 a[220:223], a[24:27], v[112:115], a[220:223]\n\t"
    "v_cvt_pk_bf16_f32 v20, v168, v169\n\t"
    "v_cvt_pk_bf16_f32 v21, v170, v171\n\t"
    "v_mfma_f32_16x16x32_bf16 a[220:223], v[188:191], v[236:239], a[220:223]\n\t"
    "v_cvt_pk_bf16_f32 v22, v172, v173\n\t"
    "v_cvt_pk_bf16_f32 v23, v174, v175\n\t"
    "v_mfma_f32_16x16x32_bf16 a[220:223], v[192:195], v[240:243], a[220:223]\n\t"
    "v_mfma_f32_16x16x32_bf16 a[188:191], v[192:195], v[232:235], a[188:191]\n\t"
    "s_nop 0\n\t"
    "v_accvgpr_read_b32 v160, a216\n\t"
    "v_accvgpr_read_b32 v161, a217\n\t"
    "v_mfma_f32_16x16x32_bf16 a[188:191], v[188:191], v[228:231], a[188:191]\n\t"
    "v_accvgpr_read_b32 v162, a218\n\t"
    "v_accvgpr_read_b32 v163, a219\n\t"
    "v_mfma_f32_16x16x32_bf16 a[188:191], a[24:27], v[80:83], a[188:191]\n\t"
    "v_accvgpr_read_b32 v164, a220\n\t"
    "v_accvgpr_read_b32 v165, a221\n\t"
    "v_mfma_f32_16x16x32_bf16 a[188:191], a[28:31], v[84:87], a[188:191]\n\t"
    "v_accvgpr_read_b32 v166, a222\n\t"
    "v_accvgpr_read_b32 v167, a223\n\t"
    "v_mfma_f32_16x16x32_bf16 a[188:191], a[20:23], v[92:95], a[188:191]\n\t"
    "v_mul_f32 v160, v207, v160\n\t"
    "v_mul_f32 v161, v207, v161\n\t"
    "v_mfma_f32_16x16x32_bf16 a[188:191], a[16:19], v[88:91], a[188:191]\n\t"
    "buffer_store_dwordx4 v[20:23], v196, s[36:39], s86 offen offset:2048 sc1\n\t"
    "v_mul_f32 v162, v207, v162\n\t"
    "v_mul_f32 v163, v207, v163\n\t"
    "v_mul_f32 v164, v207, v164\n\t"
    "v_mul_f32 v165, v207, v165\n\t"
    "v_mul_f32 v166, v207, v166\n\t"
    "v_mul_f32 v167, v207, v167\n\t"
    "v_cvt_pk_bf16_f32 v24, v160, v161\n\t"
    "v_cvt_pk_bf16_f32 v25, v162, v163\n\t"
    "v_cvt_pk_bf16_f32 v26, v164, v165\n\t"
    "v_cvt_pk_bf16_f32 v27, v166, v167\n\t"
    "buffer_store_dwordx4 v[24:27], v196, s[36:39], s87 offen offset:3072 sc1\n\t"
    "v_accvgpr_read_b32 v168, a184\n\t"
    "v_accvgpr_read_b32 v169, a185\n\t"
    "v_accvgpr_read_b32 v170, a186\n\t"
    "v_accvgpr_read_b32 v171, a187\n\t"
    "v_accvgpr_read_b32 v172, a188\n\t"
    "v_accvgpr_read_b32 v173, a189\n\t"
    "v_accvgpr_read_b32 v174, a190\n\t"
    "v_accvgpr_read_b32 v175, a191\n\t"
    "v_mul_f32 v168, v205, v168\n\t"
    "v_mul_f32 v169, v205, v169\n\t"
    "v_mul_f32 v170, v205, v170\n\t"
    "v_mul_f32 v171, v205, v171\n\t"
    "v_mul_f32 v172, v205, v172\n\t"
    "v_mul_f32 v173, v205, v173\n\t"
    "v_mul_f32 v174, v205, v174\n\t"
    "v_mul_f32 v175, v205, v175\n\t"
    "v_cvt_pk_bf16_f32 v28, v168, v169\n\t"
    "v_cvt_pk_bf16_f32 v29, v170, v171\n\t"
    "v_cvt_pk_bf16_f32 v30, v172, v173\n\t"
    "v_cvt_pk_bf16_f32 v31, v174, v175\n\t"
    "buffer_store_dwordx4 v[28:31], v196, s[36:39], s86 offen offset:3072 sc1\n\t"
    "s_waitcnt vmcnt(3) lgkmcnt(0)\n\t"
    "s_barrier\n\t"
    "s_mov_b32 exec_hi, 0\n\t"
    "L_chk1aB_%=:\n\t"
    "v_sub_u32 v138, v138, v211\n\t"
    "v_cmp_gt_u32 vcc, 1, v138\n\t"
    "s_cbranch_vccnz L_spin1aB_%=\n\t"
    "L_got1aB_%=:\n\t"
    "s_mov_b64 exec, -1\n\t"
    "s_add_u32 m0, s97, 81920\n\t"
    "s_add_u32 s53, s98, 983040\n\t"
    "buffer_load_dwordx4 v255, s[24:27], s53 offen offset:0 lds\n\t"
    "buffer_load_dwordx4 v198, s[24:27], s53 offen offset:2048 lds\n\t"
    "s_add_u32 m0, s97, 82816\n\t"
    "s_nop 0\n\t"
    "buffer_load_dwordx4 v255, s[24:27], s53 offen offset:128 lds\n\t"
    "buffer_load_dwordx4 v198, s[24:27], s53 offen offset:2176 lds\n\t"
    "s_add_u32 m0, s51, 0\n\t"
    "s_add_u32 s48, s48, 28672\n\t"
    "buffer_load_dwordx4 v196, s[20:23], s48 offen offset:0 sc1 lds\n\t"
    "buffer_load_dwordx4 v196, s[20:23], s48 offen offset:1024 sc1 lds\n\t"
    "buffer_load_dwordx4 v196, s[20:23], s48 offen offset:2048 sc1 lds\n\t"
    "buffer_load_dwordx4 v196, s[20:23], s48 offen offset:3072 sc1 lds\n\t"
    "s_add_u32 m0, s51, 4096\n\t"
    "s_add_u32 s48, s48, 4096\n\t"
    "buffer_load_dwordx4 v196, s[20:23], s48 offen offset:0 sc1 lds\n\t"
    "buffer_load_dwordx4 v196, s[20:23], s48 offen offset:1024 sc1 lds\n\t"
    "buffer_load_dwordx4 v196, s[20:23], s48 offen offset:2048 sc1 lds\n\t"
    "buffer_load_dwordx4 v196, s[20:23], s48 offen offset:3072 sc1 lds\n\t"
    "ds_read_b64_tr_b16 a[144:145], v132 offset:0\n\t"
    "ds_read_b64_tr_b16 a[146:147], v132 offset:4096\n\t"
    "ds_read_b64_tr_b16 a[148:149], v132 offset:8192\n\t"
    "ds_read_b64_tr_b16 a[150:151], v132 offset:12288\n\t"
    "ds_read_b64_tr_b16 a[152:153], v133 offset:0\n\t"
    "ds_read_b64_tr_b16 a[154:155], v133 offset:4096\n\t"
    "ds_read_b64_tr_b16 a[156:157], v133 offset:8192\n\t"
    "ds_read_b64_tr_b16 a[158:159], v133 offset:12288\n\t"
    "ds_read_b64_tr_b16 a[160:161], v132 offset:16384\n\t"
    "ds_read_b64_tr_b16 a[162:163], v132 offset:20480\n\t"
    "ds_read_b64_tr_b16 a[164:165], v132 offset:24576\n\t"
    "ds_read_b64_tr_b16 a[166:167], v132 offset:28672\n\t"
    "ds_read_b64_tr_b16 a[168:169], v133 offset:16384\n\t"
    "ds_read_b64_tr_b16 a[170:171], v133 offset:20480\n\t"
    "ds_read_b64_tr_b16 a[172:173], v133 offset:24576\n\t"
    "ds_read_b64_tr_b16 a[174:175], v133 offset:28672\n\t"
    "ds_read_b64_tr_b16 a[176:177], v130 offset:0\n\t"
    "ds_read_b64_tr_b16 a[178:179], v130 offset:4096\n\t"
    "ds_read_b64_tr_b16 a[180:181], v130 offset:8192\n\t"
    "ds_read_b64_tr_b16 a[182:183], v130 offset:12288\n\t"
    "ds_read_b64_tr_b16 a[184:185], v131 offset:0\n\t"
    "ds_read_b64_tr_b16 a[186:187], v131 offset:4096\n\t"
    "ds_read_b64_tr_b16 a[188:189], v131 offset:8192\n\t"
    "ds_read_b64_tr_b16 a[190:191], v131 offset:12288\n\t"
    "ds_read_b64_tr_b16 a[192:193], v128 offset:32768\n\t"
    "ds_read_b64_tr_b16 a[194:195], v128 offset:36864\n\t"
    "ds_read_b64_tr_b16 a[196:197], v128 offset:40960\n\t"
    "ds_read_b64_tr_b16 a[198:199], v128 offset:45056\n\t"
    "ds_read_b64_tr_b16 a[200:201], v129 offset:32768\n\t"
    "ds_read_b64_tr_b16 a[202:203], v129 offset:36864\n\t"
    "ds_read_b64_tr_b16 a[204:205], v129 offset:40960\n\t"
    "ds_read_b64_tr_b16 a[206:207], v129 offset:45056\n\t"
    "s_add_u32 m0, s51, 98304\n\t"
    "s_add_u32 s48, s48, 28672\n\t"
    "buffer_load_dwordx4 v196, s[20:23], s48 offen offset:0 sc1 lds\n\t"
    "buffer_load_dwordx4 v196, s[20:23], s48 offen offset:1024 sc1 lds\n\t"
    "buffer_load_dwordx4 v196, s[20:23], s48 offen offset:2048 sc1 lds\n\t"
    "buffer_load_dwordx4 v196, s[20:23], s48 offen offset:3072 sc1 lds\n\t"
    "s_add_u32 m0, s51, 102400\n\t"
    "s_add_u32 s48, s48, 4096\n\t"
    "buffer_load_dwordx4 v196, s[20:23], s48 offen offset:0 sc1 lds\n\t"
    "buffer_load_dwordx4 v196, s[20:23], s48 offen offset:1024 sc1 lds\n\t"
    "buffer_load_dwordx4 v196, s[20:23], s48 offen offset:2048 sc1 lds\n\t"
    "buffer_load_dwordx4 v196, s[20:23], s48 offen offset:3072 sc1 lds\n\t"
    "s_waitcnt vmcnt(8) lgkmcnt(0)\n\t"
    "s_barrier\n\t"
    "s_branch L_tail_%=\n\t"
    "L_cold_%=:\n\t"
    ".p2align 12\n\t"
    "L_fbA_%=:\n\t"
    "s_cmp_eq_u32 s58, 1\n\t"
    "s_cbranch_scc1 L_safeA_%=\n\t"
    "s_mov_b32 s58, 1\n\t"
    "s_waitcnt vmcnt(0) lgkmcnt(0)\n\t"
    "s_nop 7\n\t"
    "v_mov_b32 v238, v197\n\t"
    "v_bfe_u32 v232, v238, 4, 1\n\t"
    "v_lshrrev_b32 v233, 5, v238\n\t"
    "v_lshl_add_u32 v232, v232, 1, v233\n\t"
    "v_lshlrev_b32 v232, 4, v232\n\t"
    "v_and_b32 v228, 15, v238\n\t"
    "v_lshl_add_u32 v228, v228, 8, v232\n\t"
    "v_bfe_u32 v232, v238, 2, 3\n\t"
    "v_mul_u32_u24 v232, 768, v232\n\t"
    "v_sub_u32 v239, v255, v232\n\t"
    "s_add_u32 s61, s97, 0xc000\n\t"
    "s_sub_u32 s62, 0x10000, s61\n\t"
    "v_lshl_add_u32 v232, v233, 10, s62\n\t"
    "v_sub_u32 v236, v253, v232\n\t"
    "v_sub_u32 v237, v254, v232\n\t"
    "v_mov_b32 v180, 0xff800000\n\t"
    "v_mov_b32 v181, 0xff800000\n\t"
    "v_mov_b32 v182, 0xff800000\n\t"
    "v_mov_b32 v183, 0xff800000\n\t"
    "v_mov_b32 v184, 0xff800000\n\t"
    "v_mov_b32 v232, 0\n\t"
    "v_mov_b32 v233, 0\n\t"
    "v_mov_b32 v234, 0\n\t"
    "v_mov_b32 v235, 0\n\t"
    "v_mov_b32 v200, 0\n\t"
    "v_mov_b32 v201, 0\n\t"
    "v_mov_b32 v202, 0\n\t"
    "v_mov_b32 v203, 0\n\t"
    "v_mov_b32 v204, 0\n\t"
    "v_mov_b32 v205, 0\n\t"
    "v_mov_b32 v206, 0\n\t"
    "v_mov_b32 v207, 0\n\t"
    "v_mov_b32 v208, 0\n\t"
    "v_mov_b32 v209, 0\n\t"
    "v_mfma_f32_32x32x16_bf16 a[96:111], v[232:235], v[232:235], 0\n\t"
    "v_mfma_f32_32x32x16_bf16 a[112:127], v[232:235], v[232:235], 0\n\t"
    "v_mfma_f32_32x32x16_bf16 a[128:143], v[232:235], v[232:235], 0\n\t"
    "v_mfma_f32_32x32x16_bf16 a[144:159], v[232:235], v[232:235], 0\n\t"
    "v_mfma_f32_32x32x16_bf16 a[160:175], v[232:235], v[232:235], 0\n\t"
    "v_mfma_f32_32x32x16_bf16 a[176:191], v[232:235], v[232:235], 0\n\t"
    "v_mfma_f32_32x32x16_bf16 a[192:207], v[232:235], v[232:235], 0\n\t"
    "v_mfma_f32_32x32x16_bf16 a[208:223], v[232:235], v[232:235], 0\n\t"
    "v_mfma_f32_32x32x16_bf16 a[224:239], v[232:235], v[232:235], 0\n\t"
    "v_mfma_f32_32x32x16_bf16 a[240:255], v[232:235], v[232:235], 0\n\t"
    "s_mov_b32 s53, 0\n\t"
    "L_fb1A_%=:\n\t"
    "buffer_load_dwordx4 v[212:215], v228, s[28:31], s53 offen offset:0\n\t"
    "buffer_load_dwordx4 v[216:219], v228, s[28:31], s53 offen offset:64\n\t"
    "buffer_load_dwordx4 v[220:223], v228, s[28:31], s53 offen offset:128\n\t"
    "buffer_load_dwordx4 v[224:227], v228, s[28:31], s53 offen offset:192\n\t"
    "s_waitcnt vmcnt(0)\n\t"
    "v_mfma_f32_16x16x32_bf16 v[160:163], v[212:215], v[0:3], 0\n\t"
    "v_mfma_f32_16x16x32_bf16 v[160:163], v[216:219], v[4:7], v[160:163]\n\t"
    "v_mfma_f32_16x16x32_bf16 v[160:163], v[220:223], v[8:11], v[160:163]\n\t"
    "v_mfma_f32_16x16x32_bf16 v[160:163], v[224:227], v[12:15], v[160:163]\n\t"
    "v_mfma_f32_16x16x32_bf16 v[164:167], v[212:215], v[32:35], 0\n\t"
    "v_mfma_f32_16x16x32_bf16 v[164:167], v[216:219], v[36:39], v[164:167]\n\t"
    "v_mfma_f32_16x16x32_bf16 v[164:167], v[220:223], v[40:43], v[164:167]\n\t"
    "v_mfma_f32_16x16x32_bf16 v[164:167], v[224:227], v[44:47], v[164:167]\n\t"
    "v_mfma_f32_16x16x32_bf16 v[168:171], v[212:215], v[64:67], 0\n\t"
    "v_mfma_f32_16x16x32_bf16 v[168:171], v[216:219], v[68:71], v[168:171]\n\t"
    "v_mfma_f32_16x16x32_bf16 v[168:171], v[220:223], v[72:75], v[168:171]\n\t"
    "v_mfma_f32_16x16x32_bf16 v[168:171], v[224:227], v[76:79], v[168:171]\n\t"
    "v_mfma_f32_16x16x32_bf16 v[172:175], v[212:215], v[96:99], 0\n\t"
    "v_mfma_f32_16x16x32_bf16 v[172:175], v[216:219], v[100:103], v[172:175]\n\t"
    "v_mfma_f32_16x16x32_bf16 v[172:175], v[220:223], v[104:107], v[172:175]\n\t"
    "v_mfma_f32_16x16x32_bf16 v[172:175], v[224:227], v[108:111], v[172:175]\n\t"
    "v_mfma_f32_16x16x32_bf16 v[176:179], v[212:215], v[128:131], 0\n\t"
    "v_mfma_f32_16x16x32_bf16 v[176:179], v[216:219], v[132:135], v[176:179]\n\t"
    "v_mfma_f32_16x16x32_bf16 v[176:179], v[220:223], v[136:139], v[176:179]\n\t"
    "v_mfma_f32_16x16x32_bf16 v[176:179], v[224:227], v[140:143], v[176:179]\n\t"
    "s_nop 7\n\t"
    "s_add_u32 s53, s53, 0x1000\n\t"
    "v_max3_f32 v180, v180, v160, v161\n\t"
    "v_max3_f32 v180, v180, v162, v163\n\t"
    "v_max3_f32 v181, v181, v164, v165\n\t"
    "v_max3_f32 v181, v181, v166, v167\n\t"
    "v_max3_f32 v182, v182, v168, v169\n\t"
    "v_max3_f32 v182, v182, v170, v171\n\t"
    "v_max3_f32 v183, v183, v172, v173\n\t"
    "v_max3_f32 v183, v183, v174, v175\n\t"
    "v_max3_f32 v184, v184, v176, v177\n\t"
    "v_max3_f32 v184, v184, v178, v179\n\t"
    "s_cmp_lt_u32 s53, 0x40000\n\t"
    "s_cbranch_scc1 L_fb1A_%=\n\t"
    "v_mov_b32 v160, v180\n\t"
    "v_mov_b32 v161, v181\n\t"
    "v_mov_b32 v162, v182\n\t"
    "v_mov_b32 v163, v183\n\t"
    "v_mov_b32 v164, v184\n\t"
    "s_nop 1\n\t"
    "v_permlane32_swap_b32 v180, v160\n\t"
    "v_permlane32_swap_b32 v181, v161\n\t"
    "v_permlane32_swap_b32 v182, v162\n\t"
    "v_permlane32_swap_b32 v183, v163\n\t"
    "v_permlane32_swap_b32 v184, v164\n\t"
    "v_max_f32 v180, v180, v160\n\t"
    "v_max_f32 v181, v181, v161\n\t"
    "v_max_f32 v182, v182, v162\n\t"
    "v_max_f32 v183, v183, v163\n\t"
    "v_max_f32 v184, v184, v164\n\t"
    "v_mov_b32 v160, v180\n\t"
    "v_mov_b32 v161, v181\n\t"
    "v_mov_b32 v162, v182\n\t"
    "v_mov_b32 v163, v183\n\t"
    "v_mov_b32 v164, v184\n\t"
    "s_nop 1\n\t"
    "v_permlane16_swap_b32 v180, v160\n\t"
    "v_permlane16_swap_b32 v181, v161\n\t"
    "v_permlane16_swap_b32 v182, v162\n\t"
    "v_permlane16_swap_b32 v183, v163\n\t"
    "v_permlane16_swap_b32 v184, v164\n\t"
    "v_max_f32 v180, v180, v160\n\t"
    "v_max_f32 v181, v181, v161\n\t"
    "v_max_f32 v182, v182, v162\n\t"
    "v_max_f32 v183, v183, v163\n\t"
    "v_max_f32 v184, v184, v164\n\t"
    "s_mov_b32 s53, 0\n\t"
    "L_fb2A_%=:\n\t"
    "s_mov_b32 s60, s53\n\t"
    "buffer_load_dwordx4 v[212:215], v228, s[28:31], s53 offen offset:0\n\t"
    "buffer_load_dwordx4 v[216:219], v228, s[28:31], s53 offen offset:64\n\t"
    "buffer_load_dwordx4 v[220:223], v228, s[28:31], s53 offen offset:128\n\t"
    "buffer_load_dwordx4 v[224:227], v228, s[28:31], s53 offen offset:192\n\t"
    "s_waitcnt vmcnt(0)\n\t"
    "v_mfma_f32_16x16x32_bf16 v[160:163], v[212:215], v[0:3], 0\n\t"
    "v_mfma_f32_16x16x32_bf16 v[160:163], v[216:219], v[4:7], v[160:163]\n\t"
    "v_mfma_f32_16x16x32_bf16 v[160:163], v[220:223], v[8:11], v[160:163]\n\t"
    "v_mfma_f32_16x16x32_bf16 v[160:163], v[224:227], v[12:15], v[160:163]\n\t"
    "v_mfma_f32_16x16x32_bf16 v[164:167], v[212:215], v[32:35], 0\n\t"
    "v_mfma_f32_16x16x32_bf16 v[164:167], v[216:219], v[36:39], v[164:167]\n\t"
    "v_mfma_f32_16x16x32_bf16 v[164:167], v[220:223], v[40:43], v[164:167]\n\t"
    "v_mfma_f32_16x16x32_bf16 v[164:167], v[224:227], v[44:47], v[164:167]\n\t"
    "v_mfma_f32_16x16x32_bf16 v[168:171], v[212:215], v[64:67], 0\n\t"
    "v_mfma_f32_16x16x32_bf16 v[168:171], v[216:219], v[68:71], v[168:171]\n\t"
    "v_mfma_f32_16x16x32_bf16 v[168:171], v[220:223], v[72:75], v[168:171]\n\t"
    "v_mfma_f32_16x16x32_bf16 v[168:171], v[224:227], v[76:79], v[168:171]\n\t"
    "v_mfma_f32_16x16x32_bf16 v[172:175], v[212:215], v[96:99], 0\n\t"
    "v_mfma_f32_16x16x32_bf16 v[172:175], v[216:219], v[100:103], v[172:175]\n\t"
    "v_mfma_f32_16x16x32_bf16 v[172:175], v[220:223], v[104:107], v[172:175]\n\t"
    "v_mfma_f32_16x16x32_bf16 v[172:175], v[224:227], v[108:111], v[172:175]\n\t"
    "v_mfma_f32_16x16x32_bf16 v[176:179], v[212:215], v[128:131], 0\n\t"
    "v_mfma_f32_16x16x32_bf16 v[176:179], v[216:219], v[132:135], v[176:179]\n\t"
    "v_mfma_f32_16x16x32_bf16 v[176:179], v[220:223], v[136:139], v[176:179]\n\t"
    "v_mfma_f32_16x16x32_bf16 v[176:179], v[224:227], v[140:143], v[176:179]\n\t"
    "s_nop 7\n\t"
    "s_add_u32 s53, s53, 0x1000\n\t"
    "v_sub_f32 v160, v160, v180\n\t"
    "v_sub_f32 v161, v161, v180\n\t"
    "v_sub_f32 v162, v162, v180\n\t"
    "v_sub_f32 v163, v163, v180\n\t"
    "v_exp_f32 v160, v160\n\t"
    "v_exp_f32 v161, v161\n\t"
    "v_exp_f32 v162, v162\n\t"
    "v_exp_f32 v163, v163\n\t"
    "s_nop 0\n\t"
    "v_add_f32 v200, v200, v160\n\t"
    "v_add_f32 v201, v201, v161\n\t"
    "v_add_f32 v200, v200, v162\n\t"
    "v_add_f32 v201, v201, v163\n\t"
    "v_cvt_pk_bf16_f32 v24, v160, v161\n\t"
    "v_cvt_pk_bf16_f32 v25, v162, v163\n\t"
    "v_sub_f32 v164, v164, v181\n\t"
    "v_sub_f32 v165, v165, v181\n\t"
    "v_sub_f32 v166, v166, v181\n\t"
    "v_sub_f32 v167, v167, v181\n\t"
    "v_exp_f32 v164, v164\n\t"
    "v_exp_f32 v165, v165\n\t"
    "v_exp_f32 v166, v166\n\t"
    "v_exp_f32 v167, v167\n\t"
    "s_nop 0\n\t"
    "v_add_f32 v202, v202, v164\n\t"
    "v_add_f32 v203, v203, v165\n\t"
    "v_add_f32 v202, v202, v166\n\t"
    "v_add_f32 v203, v203, v167\n\t"
    "v_cvt_pk_bf16_f32 v56, v164, v165\n\t"
    "v_cvt_pk_bf16_f32 v57, v166, v167\n\t"
    "v_sub_f32 v168, v168, v182\n\t"
    "v_sub_f32 v169, v169, v182\n\t"
    "v_sub_f32 v170, v170, v182\n\t"
    "v_sub_f32 v171, v171, v182\n\t"
    "v_exp_f32 v168, v168\n\t"
    "v_exp_f32 v169, v169\n\t"
    "v_exp_f32 v170, v170\n\t"
    "v_exp_f32 v171, v171\n\t"
    "s_nop 0\n\t"
    "v_add_f32 v204, v204, v168\n\t"
    "v_add_f32 v205, v205, v169\n\t"
    "v_add_f32 v204, v204, v170\n\t"
    "v_add_f32 v205, v205, v171\n\t"
    "v_cvt_pk_bf16_f32 v88, v168, v169\n\t"
    "v_cvt_pk_bf16_f32 v89, v170, v171\n\t"
    "v_sub_f32 v172, v172, v183\n\t"
    "v_sub_f32 v173, v173, v183\n\t"
    "v_sub_f32 v174, v174, v183\n\t"
    "v_sub_f32 v175, v175, v183\n\t"
    "v_exp_f32 v172, v172\n\t"
    "v_exp_f32 v173, v173\n\t"
    "v_exp_f32 v174, v174\n\t"
    "v_exp_f32 v175, v175\n\t"
    "s_nop 0\n\t"
    "v_add_f32 v206, v206, v172\n\t"
    "v_add_f32 v207, v207, v173\n\t"
    "v_add_f32 v206, v206, v174\n\t"
    "v_add_f32 v207, v207, v175\n\t"
    "v_cvt_pk_bf16_f32 v120, v172, v173\n\t"
    "v_cvt_pk_bf16_f32 v121, v174, v175\n\t"
    "v_sub_f32 v176, v176, v184\n\t"
    "v_sub_f32 v177, v177, v184\n\t"
    "v_sub_f32 v178, v178, v184\n\t"
    "v_sub_f32 v179, v179, v184\n\t"
    "v_exp_f32 v176, v176\n\t"
    "v_exp_f32 v177, v177\n\t"
    "v_exp_f32 v178, v178\n\t"
    "v_exp_f32 v179, v179\n\t"
    "s_nop 0\n\t"
    "v_add_f32 v208, v208, v176\n\t"
    "v_add_f32 v209, v209, v177\n\t"
    "v_add_f32 v208, v208, v178\n\t"
    "v_add_f32 v209, v209, v179\n\t"
    "v_cvt_pk_bf16_f32 v152, v176, v177\n\t"
    "v_cvt_pk_bf16_f32 v153, v178, v179\n\t"
    "buffer_load_dwordx4 v[212:215], v228, s[28:31], s53 offen offset:0\n\t"
    "buffer_load_dwordx4 v[216:219], v228, s[28:31], s53 offen offset:64\n\t"
    "buffer_load_dwordx4 v[220:223], v228, s[28:31], s53 offen offset:128\n\t"
    "buffer_load_dwordx4 v[224:227], v228, s[28:31], s53 offen offset:192\n\t"
    "s_waitcnt vmcnt(0)\n\t"
    "v_mfma_f32_16x16x32_bf16 v[160:163], v[212:215], v[0:3], 0\n\t"
    "v_mfma_f32_16x16x32_bf16 v[160:163], v[216:219], v[4:7], v[160:163]\n\t"
    "v_mfma_f32_16x16x32_bf16 v[160:163], v[220:223], v[8:11], v[160:163]\n\t"
    "v_mfma_f32_16x16x32_bf16 v[160:163], v[224:227], v[12:15], v[160:163]\n\t"
    "v_mfma_f32_16x16x32_bf16 v[164:167], v[212:215], v[32:35], 0\n\t"
    "v_mfma_f32_16x16x32_bf16 v[164:167], v[216:219], v[36:39], v[164:167]\n\t"
    "v_mfma_f32_16x16x32_bf16 v[164:167], v[220:223], v[40:43], v[164:167]\n\t"
    "v_mfma_f32_16x16x32_bf16 v[164:167], v[224:227], v[44:47], v[164:167]\n\t"
    "v_mfma_f32_16x16x32_bf16 v[168:171], v[212:215], v[64:67], 0\n\t"
    "v_mfma_f32_16x16x32_bf16 v[168:171], v[216:219], v[68:71], v[168:171]\n\t"
    "v_mfma_f32_16x16x32_bf16 v[168:171], v[220:223], v[72:75], v[168:171]\n\t"
    "v_mfma_f32_16x16x32_bf16 v[168:171], v[224:227], v[76:79], v[168:171]\n\t"
    "v_mfma_f32_16x16x32_bf16 v[172:175], v[212:215], v[96:99], 0\n\t"
    "v_mfma_f32_16x16x32_bf16 v[172:175], v[216:219], v[100:103], v[172:175]\n\t"
    "v_mfma_f32_16x16x32_bf16 v[172:175], v[220:223], v[104:107], v[172:175]\n\t"
    "v_mfma_f32_16x16x32_bf16 v[172:175], v[224:227], v[108:111], v[172:175]\n\t"
    "v_mfma_f32_16x16x32_bf16 v[176:179], v[212:215], v[128:131], 0\n\t"
    "v_mfma_f32_16x16x32_bf16 v[176:179], v[216:219], v[132:135], v[176:179]\n\t"
    "v_mfma_f32_16x16x32_bf16 v[176:179], v[220:223], v[136:139], v[176:179]\n\t"
    "v_mfma_f32_16x16x32_bf16 v[176:179], v[224:227], v[140:143], v[176:179]\n\t"
    "s_nop 7\n\t"
    "s_add_u32 s53, s53, 0x1000\n\t"
    "v_sub_f32 v160, v160, v180\n\t"
    "v_sub_f32 v161, v161, v180\n\t"
    "v_sub_f32 v162, v162, v180\n\t"
    "v_sub_f32 v163, v163, v180\n\t"
    "v_exp_f32 v160, v160\n\t"
    "v_exp_f32 v161, v161\n\t"
    "v_exp_f32 v162, v162\n\t"
    "v_exp_f32 v163, v163\n\t"
    "s_nop 0\n\t"
    "v_add_f32 v200, v200, v160\n\t"
    "v_add_f32 v201, v201, v161\n\t"
    "v_add_f32 v200, v200, v162\n\t"
    "v_add_f32 v201, v201, v163\n\t"
    "v_cvt_pk_bf16_f32 v26, v160, v161\n\t"
    "v_cvt_pk_bf16_f32 v27, v162, v163\n\t"
    "v_sub_f32 v164, v164, v181\n\t"
    "v_sub_f32 v165, v165, v181\n\t"
    "v_sub_f32 v166, v166, v181\n\t"
    "v_sub_f32 v167, v167, v181\n\t"
    "v_exp_f32 v164, v164\n\t"
    "v_exp_f32 v165, v165\n\t"
    "v_exp_f32 v166, v166\n\t"
    "v_exp_f32 v167, v167\n\t"
    "s_nop 0\n\t"
    "v_add_f32 v202, v202, v164\n\t"
    "v_add_f32 v203, v203, v165\n\t"
    "v_add_f32 v202, v202, v166\n\t"
    "v_add_f32 v203, v203, v167\n\t"
    "v_cvt_pk_bf16_f32 v58, v164, v165\n\t"
    "v_cvt_pk_bf16_f32 v59, v166, v167\n\t"
    "v_sub_f32 v168, v168, v182\n\t"
    "v_sub_f32 v169, v169, v182\n\t"
    "v_sub_f32 v170, v170, v182\n\t"
    "v_sub_f32 v171, v171, v182\n\t"
    "v_exp_f32 v168, v168\n\t"
    "v_exp_f32 v169, v169\n\t"
    "v_exp_f32 v170, v170\n\t"
    "v_exp_f32 v171, v171\n\t"
    "s_nop 0\n\t"
    "v_add_f32 v204, v204, v168\n\t"
    "v_add_f32 v205, v205, v169\n\t"
    "v_add_f32 v204, v204, v170\n\t"
    "v_add_f32 v205, v205, v171\n\t"
    "v_cvt_pk_bf16_f32 v90, v168, v169\n\t"
    "v_cvt_pk_bf16_f32 v91, v170, v171\n\t"
    "v_sub_f32 v172, v172, v183\n\t"
    "v_sub_f32 v173, v173, v183\n\t"
    "v_sub_f32 v174, v174, v183\n\t"
    "v_sub_f32 v175, v175, v183\n\t"
    "v_exp_f32 v172, v172\n\t"
    "v_exp_f32 v173, v173\n\t"
    "v_exp_f32 v174, v174\n\t"
    "v_exp_f32 v175, v175\n\t"
    "s_nop 0\n\t"
    "v_add_f32 v206, v206, v172\n\t"
    "v_add_f32 v207, v207, v173\n\t"
    "v_add_f32 v206, v206, v174\n\t"
    "v_add_f32 v207, v207, v175\n\t"
    "v_cvt_pk_bf16_f32 v122, v172, v173\n\t"
    "v_cvt_pk_bf16_f32 v123, v174, v175\n\t"
    "v_sub_f32 v176, v176, v184\n\t"
    "v_sub_f32 v177, v177, v184\n\t"
    "v_sub_f32 v178, v178, v184\n\t"
    "v_sub_f32 v179, v179, v184\n\t"
    "v_exp_f32 v176, v176\n\t"
    "v_exp_f32 v177, v177\n\t"
    "v_exp_f32 v178, v178\n\t"
    "v_exp_f32 v179, v179\n\t"
    "s_nop 0\n\t"
    "v_add_f32 v208, v208, v176\n\t"
    "v_add_f32 v209, v209, v177\n\t"
    "v_add_f32 v208, v208, v178\n\t"
    "v_add_f32 v209, v209, v179\n\t"
    "v_cvt_pk_bf16_f32 v154, v176, v177\n\t"
    "v_cvt_pk_bf16_f32 v155, v178, v179\n\t"
    "s_add_u32 m0, s61, 0\n\t"
    "s_add_u32 s62, s60, 0\n\t"
    "buffer_load_dwordx4 v239, s[16:19], s62 offen lds\n\t"
    "s_add_u32 m0, s61, 1024\n\t"
    "s_add_u32 s62, s60, 2048\n\t"
    "buffer_load_dwordx4 v239, s[16:19], s62 offen lds\n\t"
    "s_add_u32 m0, s61, 2048\n\t"
    "s_add_u32 s62, s60, 4096\n\t"
    "buffer_load_dwordx4 v239, s[16:19], s62 offen lds\n\t"
    "s_add_u32 m0, s61, 3072\n\t"
    "s_add_u32 s62, s60, 6144\n\t"
    "buffer_load_dwordx4 v239, s[16:19], s62 offen lds\n\t"
    "s_waitcnt vmcnt(0)\n\t"
    "ds_read_b64_tr_b16 a[0:1], v236 offset:0\n\t"
    "ds_read_b64_tr_b16 a[2:3], v236 offset:2048\n\t"
    "ds_read_b64_tr_b16 a[4:5], v237 offset:0\n\t"
    "ds_read_b64_tr_b16 a[6:7], v237 offset:2048\n\t"
    "ds_read_b64_tr_b16 a[8:9], v236 offset:512\n\t"
    "ds_read_b64_tr_b16 a[10:11], v236 offset:2560\n\t"
    "ds_read_b64_tr_b16 a[12:13], v237 offset:512\n\t"
    "ds_read_b64_tr_b16 a[14:15], v237 offset:2560\n\t"
    "s_waitcnt lgkmcnt(0)\n\t"
    "v_mfma_f32_16x16x32_bf16 a[96:99], a[0:3], v[24:27], a[96:99]\n\t"
    "v_mfma_f32_16x16x32_bf16 a[128:131], a[0:3], v[56:59], a[128:131]\n\t"
    "v_mfma_f32_16x16x32_bf16 a[160:163], a[0:3], v[88:91], a[160:163]\n\t"
    "v_mfma_f32_16x16x32_bf16 a[192:195], a[0:3], v[120:123], a[192:195]\n\t"
    "v_mfma_f32_16x16x32_bf16 a[224:227], a[0:3], v[152:155], a[224:227]\n\t"
    "v_mfma_f32_16x16x32_bf16 a[100:103], a[4:7], v[24:27], a[100:103]\n\t"
    "v_mfma_f32_16x16x32_bf16 a[132:135], a[4:7], v[56:59], a[132:135]\n\t"
    "v_mfma_f32_16x16x32_bf16 a[164:167], a[4:7], v[88:91], a[164:167]\n\t"
    "v_mfma_f32_16x16x32_bf16 a[196:199], a[4:7], v[120:123], a[196:199]\n\t"
    "v_mfma_f32_16x16x32_bf16 a[228:231], a[4:7], v[152:155], a[228:231]\n\t"
    "v_mfma_f32_16x16x32_bf16 a[104:107], a[8:11], v[24:27], a[104:107]\n\t"
    "v_mfma_f32_16x16x32_bf16 a[136:139], a[8:11], v[56:59], a[136:139]\n\t"
    "v_mfma_f32_16x16x32_bf16 a[168:171], a[8:11], v[88:91], a[168:171]\n\t"
    "v_mfma_f32_16x16x32_bf16 a[200:203], a[8:11], v[120:123], a[200:203]\n\t"
    "v_mfma_f32_16x16x32_bf16 a[232:235], a[8:11], v[152:155], a[232:235]\n\t"
    "v_mfma_f32_16x16x32_bf16 a[108:111], a[12:15], v[24:27], a[108:111]\n\t"
    "v_mfma_f32_16x16x32_bf16 a[140:143], a[12:15], v[56:59], a[140:143]\n\t"
    "v_mfma_f32_16x16x32_bf16 a[172:175], a[12:15], v[88:91], a[172:175]\n\t"
    "v_mfma_f32_16x16x32_bf16 a[204:207], a[12:15], v[120:123], a[204:207]\n\t"
    "v_mfma_f32_16x16x32_bf16 a[236:239], a[12:15], v[152:155], a[236:239]\n\t"
    "s_add_u32 m0, s61, 0\n\t"
    "s_add_u32 s62, s60, 128\n\t"
    "buffer_load_dwordx4 v239, s[16:19], s62 offen lds\n\t"
    "s_add_u32 m0, s61, 1024\n\t"
    "s_add_u32 s62, s60, 2176\n\t"
    "buffer_load_dwordx4 v239, s[16:19], s62 offen lds\n\t"
    "s_add_u32 m0, s61, 2048\n\t"
    "s_add_u32 s62, s60, 4224\n\t"
    "buffer_load_dwordx4 v239, s[16:19], s62 offen lds\n\t"
    "s_add_u32 m0, s61, 3072\n\t"
    "s_add_u32 s62, s60, 6272\n\t"
    "buffer_load_dwordx4 v239, s[16:19], s62 offen lds\n\t"
    "s_waitcnt vmcnt(0)\n\t"
    "ds_read_b64_tr_b16 a[0:1], v236 offset:0\n\t"
    "ds_read_b64_tr_b16 a[2:3], v236 offset:2048\n\t"
    "ds_read_b64_tr_b16 a[4:5], v237 offset:0\n\t"
    "ds_read_b64_tr_b16 a[6:7], v237 offset:2048\n\t"
    "ds_read_b64_tr_b16 a[8:9], v236 offset:512\n\t"
    "ds_read_b64_tr_b16 a[10:11], v236 offset:2560\n\t"
    "ds_read_b64_tr_b16 a[12:13], v237 offset:512\n\t"
    "ds_read_b64_tr_b16 a[14:15], v237 offset:2560\n\t"
    "s_waitcnt lgkmcnt(0)\n\t"
    "v_mfma_f32_16x16x32_bf16 a[112:115], a[0:3], v[24:27], a[112:115]\n\t"
    "v_mfma_f32_16x16x32_bf16 a[144:147], a[0:3], v[56:59], a[144:147]\n\t"
    "v_mfma_f32_16x16x32_bf16 a[176:179], a[0:3], v[88:91], a[176:179]\n\t"
    "v_mfma_f32_16x16x32_bf16 a[208:211], a[0:3], v[120:123], a[208:211]\n\t"
    "v_mfma_f32_16x16x32_bf16 a[240:243], a[0:3], v[152:155], a[240:243]\n\t"
    "v_mfma_f32_16x16x32_bf16 a[116:119], a[4:7], v[24:27], a[116:119]\n\t"
    "v_mfma_f32_16x16x32_bf16 a[148:151], a[4:7], v[56:59], a[148:151]\n\t"
    "v_mfma_f32_16x16x32_bf16 a[180:183], a[4:7], v[88:91], a[180:183]\n\t"
    "v_mfma_f32_16x16x32_bf16 a[212:215], a[4:7], v[120:123], a[212:215]\n\t"
    "v_mfma_f32_16x16x32_bf16 a[244:247], a[4:7], v[152:155], a[244:247]\n\t"
    "v_mfma_f32_16x16x32_bf16 a[120:123], a[8:11], v[24:27], a[120:123]\n\t"
    "v_mfma_f32_16x16x32_bf16 a[152:155], a[8:11], v[56:59], a[152:155]\n\t"
    "v_mfma_f32_16x16x32_bf16 a[184:187], a[8:11], v[88:91], a[184:187]\n\t"
    "v_mfma_f32_16x16x32_bf16 a[216:219], a[8:11], v[120:123], a[216:219]\n\t"
    "v_mfma_f32_16x16x32_bf16 a[248:251], a[8:11], v[152:155], a[248:251]\n\t"
    "v_mfma_f32_16x16x32_bf16 a[124:127], a[12:15], v[24:27], a[124:127]\n\t"
    "v_mfma_f32_16x16x32_bf16 a[156:159], a[12:15], v[56:59], a[156:159]\n\t"
    "v_mfma_f32_16x16x32_bf16 a[188:191], a[12:15], v[88:91], a[188:191]\n\t"
    "v_mfma_f32_16x16x32_bf16 a[220:223], a[12:15], v[120:123], a[220:223]\n\t"
    "v_mfma_f32_16x16x32_bf16 a[252:255], a[12:15], v[152:155], a[252:255]\n\t"
    "s_cmp_lt_u32 s53, 0x40000\n\t"
    "s_cbranch_scc1 L_fb2A_%=\n\t"
    "v_mov_b32 v16, 0\n\t"
    "v_mov_b32 v17, 0\n\t"
    "v_mov_b32 v18, 0\n\t"
    "v_mov_b32 v19, 0\n\t"
    "v_mov_b32 v20, 0\n\t"
    "v_mov_b32 v21, 0\n\t"
    "v_mov_b32 v22, 0\n\t"
    "v_mov_b32 v23, 0\n\t"
    "v_mov_b32 v24, 0\n\t"
    "v_mov_b32 v25, 0\n\t"
    "v_mov_b32 v26, 0\n\t"
    "v_mov_b32 v27, 0\n\t"
    "v_mov_b32 v28, 0\n\t"
    "v_mov_b32 v29, 0\n\t"
    "v_mov_b32 v30, 0\n\t"
    "v_mov_b32 v31, 0\n\t"
    "v_mov_b32 v212, 0\n\t"
    "v_mov_b32 v213, 0\n\t"
    "v_mov_b32 v214, 0\n\t"
    "v_mov_b32 v215, 0\n\t"
    "v_mov_b32 v216, 0\n\t"
    "v_mov_b32 v217, 0\n\t"
    "v_mov_b32 v218, 0\n\t"
    "v_mov_b32 v219, 0\n\t"
    "v_mov_b32 v48, 0\n\t"
    "v_mov_b32 v49, 0\n\t"
    "v_mov_b32 v50, 0\n\t"
    "v_mov_b32 v51, 0\n\t"
    "v_mov_b32 v52, 0\n\t"
    "v_mov_b32 v53, 0\n\t"
    "v_mov_b32 v54, 0\n\t"
    "v_mov_b32 v55, 0\n\t"
    "v_mov_b32 v56, 0\n\t"
    "v_mov_b32 v57, 0\n\t"
    "v_mov_b32 v58, 0\n\t"
    "v_mov_b32 v59, 0\n\t"
    "v_mov_b32 v60, 0\n\t"
    "v_mov_b32 v61, 0\n\t"
    "v_mov_b32 v62, 0\n\t"
    "v_mov_b32 v63, 0\n\t"
    "v_mov_b32 v220, 0\n\t"
    "v_mov_b32 v221, 0\n\t"
    "v_mov_b32 v222, 0\n\t"
    "v_mov_b32 v223, 0\n\t"
    "v_mov_b32 v224, 0\n\t"
    "v_mov_b32 v225, 0\n\t"
    "v_mov_b32 v226, 0\n\t"
    "v_mov_b32 v227, 0\n\t"
    "v_mov_b32 v80, 0\n\t"
    "v_mov_b32 v81, 0\n\t"
    "v_mov_b32 v82, 0\n\t"
    "v_mov_b32 v83, 0\n\t"
    "v_mov_b32 v84, 0\n\t"
    "v_mov_b32 v85, 0\n\t"
    "v_mov_b32 v86, 0\n\t"
    "v_mov_b32 v87, 0\n\t"
    "v_mov_b32 v88, 0\n\t"
    "v_mov_b32 v89, 0\n\t"
    "v_mov_b32 v90, 0\n\t"
    "v_mov_b32 v91, 0\n\t"
    "v_mov_b32 v92, 0\n\t"
    "v_mov_b32 v93, 0\n\t"
    "v_mov_b32 v94, 0\n\t"
    "v_mov_b32 v95, 0\n\t"
    "v_mov_b32 v228, 0\n\t"
    "v_mov_b32 v229, 0\n\t"
    "v_mov_b32 v230, 0\n\t"
    "v_mov_b32 v231, 0\n\t"
    "v_mov_b32 v232, 0\n\t"
    "v_mov_b32 v233, 0\n\t"
    "v_mov_b32 v234, 0\n\t"
    "v_mov_b32 v235, 0\n\t"
    "v_mov_b32 v112, 0\n\t"
    "v_mov_b32 v113, 0\n\t"
    "v_mov_b32 v114, 0\n\t"
    "v_mov_b32 v115, 0\n\t"
    "v_mov_b32 v116, 0\n\t"
    "v_mov_b32 v117, 0\n\t"
    "v_mov_b32 v118, 0\n\t"
    "v_mov_b32 v119, 0\n\t"
    "v_mov_b32 v120, 0\n\t"
    "v_mov_b32 v121, 0\n\t"
    "v_mov_b32 v122, 0\n\t"
    "v_mov_b32 v123, 0\n\t"
    "v_mov_b32 v124, 0\n\t"
    "v_mov_b32 v125, 0\n\t"
    "v_mov_b32 v126, 0\n\t"
    "v_mov_b32 v127, 0\n\t"
    "v_mov_b32 v236, 0\n\t"
    "v_mov_b32 v237, 0\n\t"
    "v_mov_b32 v238, 0\n\t"
    "v_mov_b32 v239, 0\n\t"
    "v_mov_b32 v240, 0\n\t"
    "v_mov_b32 v241, 0\n\t"
    "v_mov_b32 v242, 0\n\t"
    "v_mov_b32 v243, 0\n\t"
    "v_mov_b32 v144, 0\n\t"
    "v_mov_b32 v145, 0\n\t"
    "v_mov_b32 v146, 0\n\t"
    "v_mov_b32 v147, 0\n\t"
    "v_mov_b32 v148, 0\n\t"
    "v_mov_b32 v149, 0\n\t"
    "v_mov_b32 v150, 0\n\t"
    "v_mov_b32 v151, 0\n\t"
    "v_mov_b32 v152, 0\n\t"
    "v_mov_b32 v153, 0\n\t"
    "v_mov_b32 v154, 0\n\t"
    "v_mov_b32 v155, 0\n\t"
    "v_mov_b32 v156, 0\n\t"
    "v_mov_b32 v157, 0\n\t"
    "v_mov_b32 v158, 0\n\t"
    "v_mov_b32 v159, 0\n\t"
    "v_mov_b32 v244, 0\n\t"
    "v_mov_b32 v245, 0\n\t"
    "v_mov_b32 v246, 0\n\t"
    "v_mov_b32 v247, 0\n\t"
    "v_mov_b32 v248, 0\n\t"
    "v_mov_b32 v249, 0\n\t"
    "v_mov_b32 v250, 0\n\t"
    "v_mov_b32 v251, 0\n\t"
    "v_mov_b32 v172, 0xff800000\n\t"
    "v_mov_b32 v173, 0xff800000\n\t"
    "v_mov_b32 v174, 0xff800000\n\t"
    "v_mov_b32 v175, 0xff800000\n\t"
    "v_mov_b32 v176, 0xff800000\n\t"
    "v_mov_b32 v177, 0xff800000\n\t"
    "v_mov_b32 v178, 0xff800000\n\t"
    "v_mov_b32 v179, 0xff800000\n\t"
    "s_add_u32 m0, s97, 49152\n\t"
    "s_add_u32 s53, s98, 655360\n\t"
    "buffer_load_dwordx4 v255, s[24:27], s53 offen offset:0 lds\n\t"
    "s_add_u32 m0, s97, 50048\n\t"
    "s_nop 0\n\t"
    "buffer_load_dwordx4 v255, s[24:27], s53 offen offset:128 lds\n\t"
    "s_add_u32 m0, s97, 51200\n\t"
    "s_add_u32 s53, s98, 663552\n\t"
    "buffer_load_dwordx4 v255, s[24:27], s53 offen offset:0 lds\n\t"
    "s_add_u32 m0, s97, 52096\n\t"
    "s_nop 0\n\t"
    "buffer_load_dwordx4 v255, s[24:27], s53 offen offset:128 lds\n\t"
    "ds_read_b64_tr_b16 a[0:1], v253 offset:16384\n\t"
    "ds_read_b64_tr_b16 a[2:3], v253 offset:20480\n\t"
    "ds_read_b64_tr_b16 a[4:5], v253 offset:24576\n\t"
    "ds_read_b64_tr_b16 a[6:7], v253 offset:28672\n\t"
    "ds_read_b64_tr_b16 a[12:13], v253 offset:40960\n\t"
    "ds_read_b64_tr_b16 a[14:15], v253 offset:45056\n\t"
    "ds_read_b64_tr_b16 a[8:9], v253 offset:32768\n\t"
    "ds_read_b64_tr_b16 a[10:11], v253 offset:36864\n\t"
    "ds_read_b64_tr_b16 v[180:181], v253 offset:49152\n\t"
    "ds_read_b64_tr_b16 v[182:183], v253 offset:53248\n\t"
    "ds_read_b64_tr_b16 v[184:185], v253 offset:57344\n\t"
    "ds_read_b64_tr_b16 v[186:187], v253 offset:61440\n\t"
    "s_waitcnt vmcnt(0) lgkmcnt(0)\n\t"
    "s_branch L_rejoinA_%=\n\t"
    "L_fbB_%=:\n\t"
    "s_cmp_eq_u32 s58, 1\n\t"
    "s_cbranch_scc1 L_safeB_%=\n\t"
    "s_mov_b32 s58, 1\n\t"
    "s_waitcnt vmcnt(0) lgkmcnt(0)\n\t"
    "s_nop 7\n\t"
    "v_mov_b32 v238, v197\n\t"
    "v_bfe_u32 v232, v238, 4, 1\n\t"
    "v_lshrrev_b32 v233, 5, v238\n\t"
    "v_lshl_add_u32 v232, v232, 1, v233\n\t"
    "v_lshlrev_b32 v232, 4, v232\n\t"
    "v_and_b32 v228, 15, v238\n\t"
    "v_lshl_add_u32 v228, v228, 8, v232\n\t"
    "v_bfe_u32 v232, v238, 2, 3\n\t"
    "v_mul_u32_u24 v232, 768, v232\n\t"
    "v_sub_u32 v239, v255, v232\n\t"
    "s_add_u32 s61, s97, 0xc000\n\t"
    "s_sub_u32 s62, 0x10000, s61\n\t"
    "v_lshl_add_u32 v232, v233, 10, s62\n\t"
    "v_sub_u32 v236, v253, v232\n\t"
    "v_sub_u32 v237, v254, v232\n\t"
    "v_mov_b32 v180, 0xff800000\n\t"
    "v_mov_b32 v181, 0xff800000\n\t"
    "v_mov_b32 v182, 0xff800000\n\t"
    "v_mov_b32 v183, 0xff800000\n\t"
    "v_mov_b32 v232, 0\n\t"
    "v_mov_b32 v233, 0\n\t"
    "v_mov_b32 v234, 0\n\t"
    "v_mov_b32 v235, 0\n\t"
    "v_mov_b32 v200, 0\n\t"
    "v_mov_b32 v201, 0\n\t"
    "v_mov_b32 v202, 0\n\t"
    "v_mov_b32 v203, 0\n\t"
    "v_mov_b32 v204, 0\n\t"
    "v_mov_b32 v205, 0\n\t"
    "v_mov_b32 v206, 0\n\t"
    "v_mov_b32 v207, 0\n\t"
    "v_mfma_f32_32x32x16_bf16 a[96:111], v[232:235], v[232:235], 0\n\t"
    "v_mfma_f32_32x32x16_bf16 a[112:127], v[232:235], v[232:235], 0\n\t"
    "v_mfma_f32_32x32x16_bf16 a[128:143], v[232:235], v[232:235], 0\n\t"
    "v_mfma_f32_32x32x16_bf16 a[144:159], v[232:235], v[232:235], 0\n\t"
    "v_mfma_f32_32x32x16_bf16 a[160:175], v[232:235], v[232:235], 0\n\t"
    "v_mfma_f32_32x32x16_bf16 a[176:191], v[232:235], v[232:235], 0\n\t"
    "v_mfma_f32_32x32x16_bf16 a[192:207], v[232:235], v[232:235], 0\n\t"
    "v_mfma_f32_32x32x16_bf16 a[208:223], v[232:235], v[232:235], 0\n\t"
    "s_mov_b32 s53, 0\n\t"
    "L_fb1B_%=:\n\t"
    "buffer_load_dwordx4 v[212:215], v228, s[28:31], s53 offen offset:0\n\t"
    "buffer_load_dwordx4 v[216:219], v228, s[28:31], s53 offen offset:64\n\t"
    "buffer_load_dwordx4 v[220:223], v228, s[28:31], s53 offen offset:128\n\t"
    "buffer_load_dwordx4 v[224:227], v228, s[28:31], s53 offen offset:192\n\t"
    "s_waitcnt vmcnt(0)\n\t"
    "v_mfma_f32_16x16x32_bf16 v[160:163], v[212:215], v[0:3], 0\n\t"
    "v_mfma_f32_16x16x32_bf16 v[160:163], v[216:219], v[4:7], v[160:163]\n\t"
    "v_mfma_f32_16x16x32_bf16 v[160:163], v[220:223], v[8:11], v[160:163]\n\t"
    "v_mfma_f32_16x16x32_bf16 v[160:163], v[224:227], v[12:15], v[160:163]\n\t"
    "v_mfma_f32_16x16x32_bf16 v[164:167], v[212:215], v[32:35], 0\n\t"
    "v_mfma_f32_16x16x32_bf16 v[164:167], v[216:219], v[36:39], v[164:167]\n\t"
    "v_mfma_f32_16x16x32_bf16 v[164:167], v[220:223], v[40:43], v[164:167]\n\t"
    "v_mfma_f32_16x16x32_bf16 v[164:167], v[224:227], v[44:47], v[164:167]\n\t"
    "v_mfma_f32_16x16x32_bf16 v[168:171], v[212:215], v[64:67], 0\n\t"
    "v_mfma_f32_16x16x32_bf16 v[168:171], v[216:219], v[68:71], v[168:171]\n\t"
    "v_mfma_f32_16x16x32_bf16 v[168:171], v[220:223], v[72:75], v[168:171]\n\t"
    "v_mfma_f32_16x16x32_bf16 v[168:171], v[224:227], v[76:79], v[168:171]\n\t"
    "v_mfma_f32_16x16x32_bf16 v[172:175], v[212:215], v[96:99], 0\n\t"
    "v_mfma_f32_16x16x32_bf16 v[172:175], v[216:219], v[100:103], v[172:175]\n\t"
    "v_mfma_f32_16x16x32_bf16 v[172:175], v[220:223], v[104:107], v[172:175]\n\t"
    "v_mfma_f32_16x16x32_bf16 v[172:175], v[224:227], v[108:111], v[172:175]\n\t"
    "s_nop 7\n\t"
    "s_add_u32 s53, s53, 0x1000\n\t"
    "v_max3_f32 v180, v180, v160, v161\n\t"
    "v_max3_f32 v180, v180, v162, v163\n\t"
    "v_max3_f32 v181, v181, v164, v165\n\t"
    "v_max3_f32 v181, v181, v166, v167\n\t"
    "v_max3_f32 v182, v182, v168, v169\n\t"
    "v_max3_f32 v182, v182, v170, v171\n\t"
    "v_max3_f32 v183, v183, v172, v173\n\t"
    "v_max3_f32 v183, v183, v174, v175\n\t"
    "s_cmp_lt_u32 s53, 0x40000\n\t"
    "s_cbranch_scc1 L_fb1B_%=\n\t"
    "v_mov_b32 v160, v180\n\t"
    "v_mov_b32 v161, v181\n\t"
    "v_mov_b32 v162, v182\n\t"
    "v_mov_b32 v163, v183\n\t"
    "s_nop 1\n\t"
    "v_permlane32_swap_b32 v180, v160\n\t"
    "v_permlane32_swap_b32 v181, v161\n\t"
    "v_permlane32_swap_b32 v182, v162\n\t"
    "v_permlane32_swap_b32 v183, v163\n\t"
    "v_max_f32 v180, v180, v160\n\t"
    "v_max_f32 v181, v181, v161\n\t"
    "v_max_f32 v182, v182, v162\n\t"
    "v_max_f32 v183, v183, v163\n\t"
    "v_mov_b32 v160, v180\n\t"
    "v_mov_b32 v161, v181\n\t"
    "v_mov_b32 v162, v182\n\t"
    "v_mov_b32 v163, v183\n\t"
    "s_nop 1\n\t"
    "v_permlane16_swap_b32 v180, v160\n\t"
    "v_permlane16_swap_b32 v181, v161\n\t"
    "v_permlane16_swap_b32 v182, v162\n\t"
    "v_permlane16_swap_b32 v183, v163\n\t"
    "v_max_f32 v180, v180, v160\n\t"
    "v_max_f32 v181, v181, v161\n\t"
    "v_max_f32 v182, v182, v162\n\t"
    "v_max_f32 v183, v183, v163\n\t"
    "s_mov_b32 s53, 0\n\t"
    "L_fb2B_%=:\n\t"
    "s_mov_b32 s60, s53\n\t"
    "buffer_load_dwordx4 v[212:215], v228, s[28:31], s53 offen offset:0\n\t"
    "buffer_load_dwordx4 v[216:219], v228, s[28:31], s53 offen offset:64\n\t"
    "buffer_load_dwordx4 v[220:223], v228, s[28:31], s53 offen offset:128\n\t"
    "buffer_load_dwordx4 v[224:227], v228, s[28:31], s53 offen offset:192\n\t"
    "s_waitcnt vmcnt(0)\n\t"
    "v_mfma_f32_16x16x32_bf16 v[160:163], v[212:215], v[0:3], 0\n\t"
    "v_mfma_f32_16x16x32_bf16 v[160:163], v[216:219], v[4:7], v[160:163]\n\t"
    "v_mfma_f32_16x16x32_bf16 v[160:163], v[220:223], v[8:11], v[160:163]\n\t"
    "v_mfma_f32_16x16x32_bf16 v[160:163], v[224:227], v[12:15], v[160:163]\n\t"
    "v_mfma_f32_16x16x32_bf16 v[164:167], v[212:215], v[32:35], 0\n\t"
    "v_mfma_f32_16x16x32_bf16 v[164:167], v[216:219], v[36:39], v[164:167]\n\t"
    "v_mfma_f32_16x16x32_bf16 v[164:167], v[220:223], v[40:43], v[164:167]\n\t"
    "v_mfma_f32_16x16x32_bf16 v[164:167], v[224:227], v[44:47], v[164:167]\n\t"
    "v_mfma_f32_16x16x32_bf16 v[168:171], v[212:215], v[64:67], 0\n\t"
    "v_mfma_f32_16x16x32_bf16 v[168:171], v[216:219], v[68:71], v[168:171]\n\t"
    "v_mfma_f32_16x16x32_bf16 v[168:171], v[220:223], v[72:75], v[168:171]\n\t"
    "v_mfma_f32_16x16x32_bf16 v[168:171], v[224:227], v[76:79], v[168:171]\n\t"
    "v_mfma_f32_16x16x32_bf16 v[172:175], v[212:215], v[96:99], 0\n\t"
    "v_mfma_f32_16x16x32_bf16 v[172:175], v[216:219], v[100:103], v[172:175]\n\t"
    "v_mfma_f32_16x16x32_bf16 v[172:175], v[220:223], v[104:107], v[172:175]\n\t"
    "v_mfma_f32_16x16x32_bf16 v[172:175], v[224:227], v[108:111], v[172:175]\n\t"
    "s_nop 7\n\t"
    "s_add_u32 s53, s53, 0x1000\n\t"
    "v_sub_f32 v160, v160, v180\n\t"
    "v_sub_f32 v161, v161, v180\n\t"
    "v_sub_f32 v162, v162, v180\n\t"
    "v_sub_f32 v163, v163, v180\n\t"
    "v_exp_f32 v160, v160\n\t"
    "v_exp_f32 v161, v161\n\t"
    "v_exp_f32 v162, v162\n\t"
    "v_exp_f32 v163, v163\n\t"
    "s_nop 0\n\t"
    "v_add_f32 v200, v200, v160\n\t"
    "v_add_f32 v201, v201, v161\n\t"
    "v_add_f32 v200, v200, v162\n\t"
    "v_add_f32 v201, v201, v163\n\t"
    "v_cvt_pk_bf16_f32 v24, v160, v161\n\t"
    "v_cvt_pk_bf16_f32 v25, v162, v163\n\t"
    "v_sub_f32 v164, v164, v181\n\t"
    "v_sub_f32 v165, v165, v181\n\t"
    "v_sub_f32 v166, v166, v181\n\t"
    "v_sub_f32 v167, v167, v181\n\t"
    "v_exp_f32 v164, v164\n\t"
    "v_exp_f32 v165, v165\n\t"
    "v_exp_f32 v166, v166\n\t"
    "v_exp_f32 v167, v167\n\t"
    "s_nop 0\n\t"
    "v_add_f32 v202, v202, v164\n\t"
    "v_add_f32 v203, v203, v165\n\t"
    "v_add_f32 v202, v202, v166\n\t"
    "v_add_f32 v203, v203, v167\n\t"
    "v_cvt_pk_bf16_f32 v56, v164, v165\n\t"
    "v_cvt_pk_bf16_f32 v57, v166, v167\n\t"
    "v_sub_f32 v168, v168, v182\n\t"
    "v_sub_f32 v169, v169, v182\n\t"
    "v_sub_f32 v170, v170, v182\n\t"
    "v_sub_f32 v171, v171, v182\n\t"
    "v_exp_f32 v168, v168\n\t"
    "v_exp_f32 v169, v169\n\t"
    "v_exp_f32 v170, v170\n\t"
    "v_exp_f32 v171, v171\n\t"
    "s_nop 0\n\t"
    "v_add_f32 v204, v204, v168\n\t"
    "v_add_f32 v205, v205, v169\n\t"
    "v_add_f32 v204, v204, v170\n\t"
    "v_add_f32 v205, v205, v171\n\t"
    "v_cvt_pk_bf16_f32 v88, v168, v169\n\t"
    "v_cvt_pk_bf16_f32 v89, v170, v171\n\t"
    "v_sub_f32 v172, v172, v183\n\t"
    "v_sub_f32 v173, v173, v183\n\t"
    "v_sub_f32 v174, v174, v183\n\t"
    "v_sub_f32 v175, v175, v183\n\t"
    "v_exp_f32 v172, v172\n\t"
    "v_exp_f32 v173, v173\n\t"
    "v_exp_f32 v174, v174\n\t"
    "v_exp_f32 v175, v175\n\t"
    "s_nop 0\n\t"
    "v_add_f32 v206, v206, v172\n\t"
    "v_add_f32 v207, v207, v173\n\t"
    "v_add_f32 v206, v206, v174\n\t"
    "v_add_f32 v207, v207, v175\n\t"
    "v_cvt_pk_bf16_f32 v120, v172, v173\n\t"
    "v_cvt_pk_bf16_f32 v121, v174, v175\n\t"
    "buffer_load_dwordx4 v[212:215], v228, s[28:31], s53 offen offset:0\n\t"
    "buffer_load_dwordx4 v[216:219], v228, s[28:31], s53 offen offset:64\n\t"
    "buffer_load_dwordx4 v[220:223], v228, s[28:31], s53 offen offset:128\n\t"
    "buffer_load_dwordx4 v[224:227], v228, s[28:31], s53 offen offset:192\n\t"
    "s_waitcnt vmcnt(0)\n\t"
    "v_mfma_f32_16x16x32_bf16 v[160:163], v[212:215], v[0:3], 0\n\t"
    "v_mfma_f32_16x16x32_bf16 v[160:163], v[216:219], v[4:7], v[160:163]\n\t"
    "v_mfma_f32_16x16x32_bf16 v[160:163], v[220:223], v[8:11], v[160:163]\n\t"
    "v_mfma_f32_16x16x32_bf16 v[160:163], v[224:227], v[12:15], v[160:163]\n\t"
    "v_mfma_f32_16x16x32_bf16 v[164:167], v[212:215], v[32:35], 0\n\t"
    "v_mfma_f32_16x16x32_bf16 v[164:167], v[216:219], v[36:39], v[164:167]\n\t"
    "v_mfma_f32_16x16x32_bf16 v[164:167], v[220:223], v[40:43], v[164:167]\n\t"
    "v_mfma_f32_16x16x32_bf16 v[164:167], v[224:227], v[44:47], v[164:167]\n\t"
    "v_mfma_f32_16x16x32_bf16 v[168:171], v[212:215], v[64:67], 0\n\t"
    "v_mfma_f32_16x16x32_bf16 v[168:171], v[216:219], v[68:71], v[168:171]\n\t"
    "v_mfma_f32_16x16x32_bf16 v[168:171], v[220:223], v[72:75], v[168:171]\n\t"
    "v_mfma_f32_16x16x32_bf16 v[168:171], v[224:227], v[76:79], v[168:171]\n\t"
    "v_mfma_f32_16x16x32_bf16 v[172:175], v[212:215], v[96:99], 0\n\t"
    "v_mfma_f32_16x16x32_bf16 v[172:175], v[216:219], v[100:103], v[172:175]\n\t"
    "v_mfma_f32_16x16x32_bf16 v[172:175], v[220:223], v[104:107], v[172:175]\n\t"
    "v_mfma_f32_16x16x32_bf16 v[172:175], v[224:227], v[108:111], v[172:175]\n\t"
    "s_nop 7\n\t"
    "s_add_u32 s53, s53, 0x1000\n\t"
    "v_sub_f32 v160, v160, v180\n\t"
    "v_sub_f32 v161, v161, v180\n\t"
    "v_sub_f32 v162, v162, v180\n\t"
    "v_sub_f32 v163, v163, v180\n\t"
    "v_exp_f32 v160, v160\n\t"
    "v_exp_f32 v161, v161\n\t"
    "v_exp_f32 v162, v162\n\t"
    "v_exp_f32 v163, v163\n\t"
    "s_nop 0\n\t"
    "v_add_f32 v200, v200, v160\n\t"
    "v_add_f32 v201, v201, v161\n\t"
    "v_add_f32 v200, v200, v162\n\t"
    "v_add_f32 v201, v201, v163\n\t"
    "v_cvt_pk_bf16_f32 v26, v160, v161\n\t"
    "v_cvt_pk_bf16_f32 v27, v162, v163\n\t"
    "v_sub_f32 v164, v164, v181\n\t"
    "v_sub_f32 v165, v165, v181\n\t"
    "v_sub_f32 v166, v166, v181\n\t"
    "v_sub_f32 v167, v167, v181\n\t"
    "v_exp_f32 v164, v164\n\t"
    "v_exp_f32 v165, v165\n\t"
    "v_exp_f32 v166, v166\n\t"
    "v_exp_f32 v167, v167\n\t"
    "s_nop 0\n\t"
    "v_add_f32 v202, v202, v164\n\t"
    "v_add_f32 v203, v203, v165\n\t"
    "v_add_f32 v202, v202, v166\n\t"
    "v_add_f32 v203, v203, v167\n\t"
    "v_cvt_pk_bf16_f32 v58, v164, v165\n\t"
    "v_cvt_pk_bf16_f32 v59, v166, v167\n\t"
    "v_sub_f32 v168, v168, v182\n\t"
    "v_sub_f32 v169, v169, v182\n\t"
    "v_sub_f32 v170, v170, v182\n\t"
    "v_sub_f32 v171, v171, v182\n\t"
    "v_exp_f32 v168, v168\n\t"
    "v_exp_f32 v169, v169\n\t"
    "v_exp_f32 v170, v170\n\t"
    "v_exp_f32 v171, v171\n\t"
    "s_nop 0\n\t"
    "v_add_f32 v204, v204, v168\n\t"
    "v_add_f32 v205, v205, v169\n\t"
    "v_add_f32 v204, v204, v170\n\t"
    "v_add_f32 v205, v205, v171\n\t"
    "v_cvt_pk_bf16_f32 v90, v168, v169\n\t"
    "v_cvt_pk_bf16_f32 v91, v170, v171\n\t"
    "v_sub_f32 v172, v172, v183\n\t"
    "v_sub_f32 v173, v173, v183\n\t"
    "v_sub_f32 v174, v174, v183\n\t"
    "v_sub_f32 v175, v175, v183\n\t"
    "v_exp_f32 v172, v172\n\t"
    "v_exp_f32 v173, v173\n\t"
    "v_exp_f32 v174, v174\n\t"
    "v_exp_f32 v175, v175\n\t"
    "s_nop 0\n\t"
    "v_add_f32 v206, v206, v172\n\t"
    "v_add_f32 v207, v207, v173\n\t"
    "v_add_f32 v206, v206, v174\n\t"
    "v_add_f32 v207, v207, v175\n\t"
    "v_cvt_pk_bf16_f32 v122, v172, v173\n\t"
    "v_cvt_pk_bf16_f32 v123, v174, v175\n\t"
    "s_add_u32 m0, s61, 0\n\t"
    "s_add_u32 s62, s60, 0\n\t"
    "buffer_load_dwordx4 v239, s[16:19], s62 offen lds\n\t"
    "s_add_u32 m0, s61, 1024\n\t"
    "s_add_u32 s62, s60, 2048\n\t"
    "buffer_load_dwordx4 v239, s[16:19], s62 offen lds\n\t"
    "s_add_u32 m0, s61, 2048\n\t"
    "s_add_u32 s62, s60, 4096\n\t"
    "buffer_load_dwordx4 v239, s[16:19], s62 offen lds\n\t"
    "s_add_u32 m0, s61, 3072\n\t"
    "s_add_u32 s62, s60, 6144\n\t"
    "buffer_load_dwordx4 v239, s[16:19], s62 offen lds\n\t"
    "s_waitcnt vmcnt(0)\n\t"
    "ds_read_b64_tr_b16 a[0:1], v236 offset:0\n\t"
    "ds_read_b64_tr_b16 a[2:3], v236 offset:2048\n\t"
    "ds_read_b64_tr_b16 a[4:5], v237 offset:0\n\t"
    "ds_read_b64_tr_b16 a[6:7], v237 offset:2048\n\t"
    "ds_read_b64_tr_b16 a[8:9], v236 offset:512\n\t"
    "ds_read_b64_tr_b16 a[10:11], v236 offset:2560\n\t"
    "ds_read_b64_tr_b16 a[12:13], v237 offset:512\n\t"
    "ds_read_b64_tr_b16 a[14:15], v237 offset:2560\n\t"
    "s_waitcnt lgkmcnt(0)\n\t"
    "v_mfma_f32_16x16x32_bf16 a[96:99], a[0:3], v[24:27], a[96:99]\n\t"
    "v_mfma_f32_16x16x32_bf16 a[128:131], a[0:3], v[56:59], a[128:131]\n\t"
    "v_mfma_f32_16x16x32_bf16 a[160:163], a[0:3], v[88:91], a[160:163]\n\t"
    "v_mfma_f32_16x16x32_bf16 a[192:195], a[0:3], v[120:123], a[192:195]\n\t"
    "v_mfma_f32_16x16x32_bf16 a[100:103], a[4:7], v[24:27], a[100:103]\n\t"
    "v_mfma_f32_16x16x32_bf16 a[132:135], a[4:7], v[56:59], a[132:135]\n\t"
    "v_mfma_f32_16x16x32_bf16 a[164:167], a[4:7], v[88:91], a[164:167]\n\t"
    "v_mfma_f32_16x16x32_bf16 a[196:199], a[4:7], v[120:123], a[196:199]\n\t"
    "v_mfma_f32_16x16x32_bf16 a[104:107], a[8:11], v[24:27], a[104:107]\n\t"
    "v_mfma_f32_16x16x32_bf16 a[136:139], a[8:11], v[56:59], a[136:139]\n\t"
    "v_mfma_f32_16x16x32_bf16 a[168:171], a[8:11], v[88:91], a[168:171]\n\t"
    "v_mfma_f32_16x16x32_bf16 a[200:203], a[8:11], v[120:123], a[200:203]\n\t"
    "v_mfma_f32_16x16x32_bf16 a[108:111], a[12:15], v[24:27], a[108:111]\n\t"
    "v_mfma_f32_16x16x32_bf16 a[140:143], a[12:15], v[56:59], a[140:143]\n\t"
    "v_mfma_f32_16x16x32_bf16 a[172:175], a[12:15], v[88:91], a[172:175]\n\t"
    "v_mfma_f32_16x16x32_bf16 a[204:207], a[12:15], v[120:123], a[204:207]\n\t"
    "s_add_u32 m0, s61, 0\n\t"
    "s_add_u32 s62, s60, 128\n\t"
    "buffer_load_dwordx4 v239, s[16:19], s62 offen lds\n\t"
    "s_add_u32 m0, s61, 1024\n\t"
    "s_add_u32 s62, s60, 2176\n\t"
    "buffer_load_dwordx4 v239, s[16:19], s62 offen lds\n\t"
    "s_add_u32 m0, s61, 2048\n\t"
    "s_add_u32 s62, s60, 4224\n\t"
    "buffer_load_dwordx4 v239, s[16:19], s62 offen lds\n\t"
    "s_add_u32 m0, s61, 3072\n\t"
    "s_add_u32 s62, s60, 6272\n\t"
    "buffer_load_dwordx4 v239, s[16:19], s62 offen lds\n\t"
    "s_waitcnt vmcnt(0)\n\t"
    "ds_read_b64_tr_b16 a[0:1], v236 offset:0\n\t"
    "ds_read_b64_tr_b16 a[2:3], v236 offset:2048\n\t"
    "ds_read_b64_tr_b16 a[4:5], v237 offset:0\n\t"
    "ds_read_b64_tr_b16 a[6:7], v237 offset:2048\n\t"
    "ds_read_b64_tr_b16 a[8:9], v236 offset:512\n\t"
    "ds_read_b64_tr_b16 a[10:11], v236 offset:2560\n\t"
    "ds_read_b64_tr_b16 a[12:13], v237 offset:512\n\t"
    "ds_read_b64_tr_b16 a[14:15], v237 offset:2560\n\t"
    "s_waitcnt lgkmcnt(0)\n\t"
    "v_mfma_f32_16x16x32_bf16 a[112:115], a[0:3], v[24:27], a[112:115]\n\t"
    "v_mfma_f32_16x16x32_bf16 a[144:147], a[0:3], v[56:59], a[144:147]\n\t"
    "v_mfma_f32_16x16x32_bf16 a[176:179], a[0:3], v[88:91], a[176:179]\n\t"
    "v_mfma_f32_16x16x32_bf16 a[208:211], a[0:3], v[120:123], a[208:211]\n\t"
    "v_mfma_f32_16x16x32_bf16 a[116:119], a[4:7], v[24:27], a[116:119]\n\t"
    "v_mfma_f32_16x16x32_bf16 a[148:151], a[4:7], v[56:59], a[148:151]\n\t"
    "v_mfma_f32_16x16x32_bf16 a[180:183], a[4:7], v[88:91], a[180:183]\n\t"
    "v_mfma_f32_16x16x32_bf16 a[212:215], a[4:7], v[120:123], a[212:215]\n\t"
    "v_mfma_f32_16x16x32_bf16 a[120:123], a[8:11], v[24:27], a[120:123]\n\t"
    "v_mfma_f32_16x16x32_bf16 a[152:155], a[8:11], v[56:59], a[152:155]\n\t"
    "v_mfma_f32_16x16x32_bf16 a[184:187], a[8:11], v[88:91], a[184:187]\n\t"
    "v_mfma_f32_16x16x32_bf16 a[216:219], a[8:11], v[120:123], a[216:219]\n\t"
    "v_mfma_f32_16x16x32_bf16 a[124:127], a[12:15], v[24:27], a[124:127]\n\t"
    "v_mfma_f32_16x16x32_bf16 a[156:159], a[12:15], v[56:59], a[156:159]\n\t"
    "v_mfma_f32_16x16x32_bf16 a[188:191], a[12:15], v[88:91], a[188:191]\n\t"
    "v_mfma_f32_16x16x32_bf16 a[220:223], a[12:15], v[120:123], a[220:223]\n\t"
    "s_cmp_lt_u32 s53, 0x40000\n\t"
    "s_cbranch_scc1 L_fb2B_%=\n\t"
    "v_mov_b32 v16, 0\n\t"
    "v_mov_b32 v17, 0\n\t"
    "v_mov_b32 v18, 0\n\t"
    "v_mov_b32 v19, 0\n\t"
    "v_mov_b32 v20, 0\n\t"
    "v_mov_b32 v21, 0\n\t"
    "v_mov_b32 v22, 0\n\t"
    "v_mov_b32 v23, 0\n\t"
    "v_mov_b32 v24, 0\n\t"
    "v_mov_b32 v25, 0\n\t"
    "v_mov_b32 v26, 0\n\t"
    "v_mov_b32 v27, 0\n\t"
    "v_mov_b32 v28, 0\n\t"
    "v_mov_b32 v29, 0\n\t"
    "v_mov_b32 v30, 0\n\t"
    "v_mov_b32 v31, 0\n\t"
    "v_mov_b32 v212, 0\n\t"
    "v_mov_b32 v213, 0\n\t"
    "v_mov_b32 v214, 0\n\t"
    "v_mov_b32 v215, 0\n\t"
    "v_mov_b32 v216, 0\n\t"
    "v_mov_b32 v217, 0\n\t"
    "v_mov_b32 v218, 0\n\t"
    "v_mov_b32 v219, 0\n\t"
    "v_mov_b32 v48, 0\n\t"
    "v_mov_b32 v49, 0\n\t"
    "v_mov_b32 v50, 0\n\t"
    "v_mov_b32 v51, 0\n\t"
    "v_mov_b32 v52, 0\n\t"
    "v_mov_b32 v53, 0\n\t"
    "v_mov_b32 v54, 0\n\t"
    "v_mov_b32 v55, 0\n\t"
    "v_mov_b32 v56, 0\n\t"
    "v_mov_b32 v57, 0\n\t"
    "v_mov_b32 v58, 0\n\t"
    "v_mov_b32 v59, 0\n\t"
    "v_mov_b32 v60, 0\n\t"
    "v_mov_b32 v61, 0\n\t"
    "v_mov_b32 v62, 0\n\t"
    "v_mov_b32 v63, 0\n\t"
    "v_mov_b32 v220, 0\n\t"
    "v_mov_b32 v221, 0\n\t"
    "v_mov_b32 v222, 0\n\t"
    "v_mov_b32 v223, 0\n\t"
    "v_mov_b32 v224, 0\n\t"
    "v_mov_b32 v225, 0\n\t"
    "v_mov_b32 v226, 0\n\t"
    "v_mov_b32 v227, 0\n\t"
    "v_mov_b32 v80, 0\n\t"
    "v_mov_b32 v81, 0\n\t"
    "v_mov_b32 v82, 0\n\t"
    "v_mov_b32 v83, 0\n\t"
    "v_mov_b32 v84, 0\n\t"
    "v_mov_b32 v85, 0\n\t"
    "v_mov_b32 v86, 0\n\t"
    "v_mov_b32 v87, 0\n\t"
    "v_mov_b32 v88, 0\n\t"
    "v_mov_b32 v89, 0\n\t"
    "v_mov_b32 v90, 0\n\t"
    "v_mov_b32 v91, 0\n\t"
    "v_mov_b32 v92, 0\n\t"
    "v_mov_b32 v93, 0\n\t"
    "v_mov_b32 v94, 0\n\t"
    "v_mov_b32 v95, 0\n\t"
    "v_mov_b32 v228, 0\n\t"
    "v_mov_b32 v229, 0\n\t"
    "v_mov_b32 v230, 0\n\t"
    "v_mov_b32 v231, 0\n\t"
    "v_mov_b32 v232, 0\n\t"
    "v_mov_b32 v233, 0\n\t"
    "v_mov_b32 v234, 0\n\t"
    "v_mov_b32 v235, 0\n\t"
    "v_mov_b32 v112, 0\n\t"
    "v_mov_b32 v113, 0\n\t"
    "v_mov_b32 v114, 0\n\t"
    "v_mov_b32 v115, 0\n\t"
    "v_mov_b32 v116, 0\n\t"
    "v_mov_b32 v117, 0\n\t"
    "v_mov_b32 v118, 0\n\t"
    "v_mov_b32 v119, 0\n\t"
    "v_mov_b32 v120, 0\n\t"
    "v_mov_b32 v121, 0\n\t"
    "v_mov_b32 v122, 0\n\t"
    "v_mov_b32 v123, 0\n\t"
    "v_mov_b32 v124, 0\n\t"
    "v_mov_b32 v125, 0\n\t"
    "v_mov_b32 v126, 0\n\t"
    "v_mov_b32 v127, 0\n\t"
    "v_mov_b32 v236, 0\n\t"
    "v_mov_b32 v237, 0\n\t"
    "v_mov_b32 v238, 0\n\t"
    "v_mov_b32 v239, 0\n\t"
    "v_mov_b32 v240, 0\n\t"
    "v_mov_b32 v241, 0\n\t"
    "v_mov_b32 v242, 0\n\t"
    "v_mov_b32 v243, 0\n\t"
    "v_mov_b32 v168, 0xff800000\n\t"
    "v_mov_b32 v169, 0xff800000\n\t"
    "v_mov_b32 v170, 0xff800000\n\t"
    "v_mov_b32 v171, 0xff800000\n\t"
    "v_mov_b32 v172, 0xff800000\n\t"
    "v_mov_b32 v173, 0xff800000\n\t"
    "v_mov_b32 v174, 0xff800000\n\t"
    "v_mov_b32 v175, 0xff800000\n\t"
    "s_add_u32 m0, s97, 49152\n\t"
    "s_add_u32 s53, s98, 655360\n\t"
    "buffer_load_dwordx4 v255, s[24:27], s53 offen offset:0 lds\n\t"
    "s_add_u32 m0, s97, 50048\n\t"
    "s_nop 0\n\t"
    "buffer_load_dwordx4 v255, s[24:27], s53 offen offset:128 lds\n\t"
    "s_add_u32 m0, s97, 51200\n\t"
    "s_add_u32 s53, s98, 663552\n\t"
    "buffer_load_dwordx4 v255, s[24:27], s53 offen offset:0 lds\n\t"
    "s_add_u32 m0, s97, 52096\n\t"
    "s_nop 0\n\t"
    "buffer_load_dwordx4 v255, s[24:27], s53 offen offset:128 lds\n\t"
    "ds_read_b64_tr_b16 a[0:1], v253 offset:16384\n\t"
    "ds_read_b64_tr_b16 a[2:3], v253 offset:20480\n\t"
    "ds_read_b64_tr_b16 a[4:5], v253 offset:24576\n\t"
    "ds_read_b64_tr_b16 a[6:7], v253 offset:28672\n\t"
    "ds_read_b64_tr_b16 a[12:13], v253 offset:40960\n\t"
    "ds_read_b64_tr_b16 a[14:15], v253 offset:45056\n\t"
    "ds_read_b64_tr_b16 a[8:9], v253 offset:32768\n\t"
    "ds_read_b64_tr_b16 a[10:11], v253 offset:36864\n\t"
    "ds_read_b64_tr_b16 v[180:181], v253 offset:49152\n\t"
    "ds_read_b64_tr_b16 v[182:183], v253 offset:53248\n\t"
    "ds_read_b64_tr_b16 v[184:185], v253 offset:57344\n\t"
    "ds_read_b64_tr_b16 v[186:187], v253 offset:61440\n\t"
    "s_waitcnt vmcnt(0) lgkmcnt(0)\n\t"
    "s_branch L_rejoinB_%=\n\t"
    :
    : "s"(__builtin_amdgcn_kernarg_segment_ptr()), "s"(blockIdx.x), "s"(wid), "s"((unsigned)(size_t)smem)
    : "memory", "vcc", "scc", "v0", "v1", "v2", "v3", "v4", "v5", "v6", "v7", "v8", "v9", "v10", "v11", "v12", "v13", "v14", "v15", "v16", "v17", "v18", "v19", "v20", "v21", "v22", "v23", "v24", "v25", "v26", "v27", "v28", "v29", "v30", "v31", "v32", "v33", "v34", "v35", "v36", "v37", "v38", "v39", "v40", "v41", "v42", "v43", "v44", "v45", "v46", "v47", "v48", "v49", "v50", "v51", "v52", "v53", "v54", "v55", "v56", "v57", "v58", "v59", "v60", "v61", "v62", "v63", "v64", "v65", "v66", "v67", "v68", "v69", "v70", "v71", "v72", "v73", "v74", "v75", "v76", "v77", "v78", "v79", "v80", "v81", "v82", "v83", "v84", "v85", "v86", "v87", "v88", "v89", "v90", "v91", "v92", "v93", "v94", "v95", "v96", "v97", "v98", "v99", "v100", "v101", "v102", "v103", "v104", "v105", "v106", "v107", "v108", "v109", "v110", "v111", "v112", "v113", "v114", "v115", "v116", "v117", "v118", "v119", "v120", "v121", "v122", "v123", "v124", "v125", "v126", "v127", "v128", "v129", "v130", "v131", "v132", "v133", "v134", "v135", "v136", "v137", "v138", "v139", "v140", "v141", "v142", "v143", "v144", "v145", "v146", "v147", "v148", "v149", "v150", "v151", "v152", "v153", "v154", "v155", "v156", "v157", "v158", "v159", "v160", "v161", "v162", "v163", "v164", "v165", "v166", "v167", "v168", "v169", "v170", "v171", "v172", "v173", "v174", "v175", "v176", "v177", "v178", "v179", "v180", "v181", "v182", "v183", "v184", "v185", "v186", "v187", "v188", "v189", "v190", "v191", "v192", "v193", "v194", "v195", "v196", "v197", "v198", "v199", "v200", "v201", "v202", "v203", "v204", "v205", "v206", "v207", "v208", "v209", "v210", "v211", "v212", "v213", "v214", "v215", "v216", "v217", "v218", "v219", "v220", "v221", "v222", "v223", "v224", "v225", "v226", "v227", "v228", "v229", "v230", "v231", "v232", "v233", "v234", "v235", "v236", "v237", "v238", "v239", "v240", "v241", "v242", "v243", "v244", "v245", "v246", "v247", "v248", "v249", "v250", "v251", "v252", "v253", "v254", "v255", "a0", "a1", "a2", "a3", "a4", "a5", "a6", "a7", "a8", "a9", "a10", "a11", "a12", "a13", "a14", "a15", "a16", "a17", "a18", "a19", "a20", "a21", "a22", "a23", "a24", "a25", "a26", "a27", "a28", "a29", "a30", "a31", "a32", "a33", "a34", "a35", "a36", "a37", "a38", "a39", "a40", "a41", "a42", "a43", "a44", "a45", "a46", "a47", "a48", "a49", "a50", "a51", "a52", "a53", "a54", "a55", "a56", "a57", "a58", "a59", "a60", "a61", "a62", "a63", "a64", "a65", "a66", "a67", "a68", "a69", "a70", "a71", "a72", "a73", "a74", "a75", "a76", "a77", "a78", "a79", "a80", "a81", "a82", "a83", "a84", "a85", "a86", "a87", "a88", "a89", "a90", "a91", "a92", "a93", "a94", "a95", "a96", "a97", "a98", "a99", "a100", "a101", "a102", "a103", "a104", "a105", "a106", "a107", "a108", "a109", "a110", "a111", "a112", "a113", "a114", "a115", "a116", "a117", "a118", "a119", "a120", "a121", "a122", "a123", "a124", "a125", "a126", "a127", "a128", "a129", "a130", "a131", "a132", "a133", "a134", "a135", "a136", "a137", "a138", "a139", "a140", "a141", "a142", "a143", "a144", "a145", "a146", "a147", "a148", "a149", "a150", "a151", "a152", "a153", "a154", "a155", "a156", "a157", "a158", "a159", "a160", "a161", "a162", "a163", "a164", "a165", "a166", "a167", "a168", "a169", "a170", "a171", "a172", "a173", "a174", "a175", "a176", "a177", "a178", "a179", "a180", "a181", "a182", "a183", "a184", "a185", "a186", "a187", "a188", "a189", "a190", "a191", "a192", "a193", "a194", "a195", "a196", "a197", "a198", "a199", "a200", "a201", "a202", "a203", "a204", "a205", "a206", "a207", "a208", "a209", "a210", "a211", "a212", "a213", "a214", "a215", "a216", "a217", "a218", "a219", "a220", "a221", "a222", "a223", "a224", "a225", "a226", "a227", "a228", "a229", "a230", "a231", "a232", "a233", "a234", "a235", "a236", "a237", "a238", "a239", "a240", "a241", "a242", "a243", "a244", "a245", "a246", "a247", "a248", "a249", "a250", "a251", "a252", "a253", "a254", "a255", "s5", "s6", "s7", "s8", "s9", "s10", "s11", "s12", "s13", "s14", "s15", "s16", "s17", "s18", "s19", "s20", "s21", "s22", "s23", "s24", "s25", "s26", "s27", "s28", "s29", "s30", "s31", "s36", "s37", "s38", "s39", "s40", "s41", "s42", "s43", "s44", "s45", "s46", "s47", "s48", "s49", "s50", "s51", "s52", "s53", "s54", "s55", "s56", "s57", "s58", "s59", "s60", "s61", "s62", "s63", "s64", "s65", "s66", "s67", "s68", "s69", "s70", "s71", "s72", "s73", "s74", "s75", "s76", "s77", "s78", "s79", "s80", "s81", "s82", "s83", "s84", "s85", "s86", "s87", "s88", "s89", "s90", "s91", "s92", "s93", "s94", "s95", "s96", "s97", "s98", "s99");
}

extern "C" void kernel_launch(void* const* d_in, const int* in_sizes, int n_in, void* d_out, int out_size, void* d_ws, size_t ws_size, hipStream_t stream) {
  char* ws = (char*)d_ws;
  mha_block_kernel<<<256, 256, 0, stream>>>(d_in[0], d_in[1], d_in[2], d_in[3], d_in[4], d_out, ws, ws + (64u << 20));
}
```
